```python
import torch

def _softmax(x, axis=-1):
    return torch.softmax(x, dim=axis)
def _gelu(x):
    return torch.nn.functional.gelu(x, approximate="tanh")
def _silu(x):
    return torch.nn.functional.silu(x)
def _sigmoid(x):
    return torch.sigmoid(x)
def _layernorm(x, axis=-1, eps=1e-5):
    m = x.mean(dim=axis, keepdim=True)
    v = x.var(dim=axis, keepdim=True, unbiased=False)
    return (x - m) / torch.sqrt(v + eps)
def _scatter_add(tgt, idx, src, axis=0):
    return tgt.clone().index_add_(axis, idx, src)
def _segment_sum(src, seg, n):
    out = torch.zeros((n,) + src.shape[1:], dtype=src.dtype)
    return out.index_add_(0, seg, src)
def _pad(x, pw):
    flat = tuple(v for pair in reversed(pw) for v in pair)
    return torch.nn.functional.pad(x, flat)

kernel_name = 'standalone__paged_block_table_kv_pagedattention__sampling'

def setup_inputs():

    _w = (lambda a: (lambda t: t.to(torch.bfloat16) if t.is_floating_point() else t)(torch.as_tensor(a)))
    B, Hq, Hkv, Sq, Skv, D = 16, 32, 16, 16, 4096, 128
    bs = 16
    nbps = max(1, Skv // bs)
    num_phys = B * nbps + 4
    bt = torch.randperm(num_phys)[: B * nbps].reshape(B, nbps).to(torch.int64)
    cl = (nbps * bs - (torch.arange(1, B + 1, dtype=torch.int64) * 2654435761) % max(1, nbps * bs // 2)).to(torch.int64)
    return {
        "q": _w((lambda *s: torch.randn(*s, dtype=torch.bfloat16) * 0.8)(B, Hq, Sq, D)),
        "k_cache": _w((lambda *s: torch.randn(*s, dtype=torch.bfloat16) * 0.8)(num_phys, bs, Hkv, D)),
        "v_cache": _w((lambda *s: torch.randn(*s, dtype=torch.bfloat16) * 0.05)(num_phys, bs, Hkv, D)),
        "block_table": _w(bt),
        "context_lens": _w(cl),
    }

def reference(q, k_cache, v_cache, block_table, context_lens):
    B, Hq, Sq, D = q.shape
    bs = k_cache.shape[1]
    Hkv = k_cache.shape[2]
    nbps = block_table.shape[1]
    Skv = nbps * bs
    bt = block_table.reshape(-1)
    k = (lambda x, *d: x.permute(*d))(k_cache[bt].reshape(B, Skv, Hkv, D), 0, 2, 1, 3)
    v = (lambda x, *d: x.permute(*d))(v_cache[bt].reshape(B, Skv, Hkv, D), 0, 2, 1, 3)
    g = Hq // Hkv
    qg = q.reshape(B, Hkv, g, Sq, D)
    scores = torch.einsum("bhgqd,bhkd->bhgqk", qg, k) * (D ** -0.5)
    pos = torch.arange(Skv)
    msk = pos[None, :] < context_lens[:, None]
    scores = torch.where(msk[:, None, None, None, :], scores, -1e9)
    p = _softmax(scores, axis=-1)
    out = torch.einsum("bhgqk,bhkd->bhgqd", p, v)
    return out.reshape(B, Hq, Sq, D)
```

```cpp
#define BALANCE 1
#define PACE_X16 0
#include <hip/hip_runtime.h>
#include <hip/hip_bf16.h>

#define PACE_LEAD 1
#define KPOL "nt"
#define VPOL "nt"

namespace {
constexpr int NB = 16, HQ = 32, HKV = 16, SQ = 16, HD = 128, PAGE = 16, NBPS = 256, SKV = NBPS * PAGE;
constexpr unsigned PAGE_BYTES = PAGE * HKV * HD * 2;
constexpr unsigned INVALID = 0x80000000u;
constexpr int MAXENT = 448;
constexpr unsigned RING_W = 3 * 8192;
constexpr unsigned LDS_RING = 0;
constexpr unsigned LDS_MRG = 4 * RING_W;
constexpr unsigned LDS_ML = LDS_MRG + 4 * 3 * 4096;
constexpr unsigned LDS_PL = LDS_ML + 4 * 32 * 8;
constexpr unsigned LDS_TOTAL = LDS_PL + 4 * MAXENT * 4;
static_assert(LDS_TOTAL <= 163840, "lds");
constexpr unsigned SLOT_BYTES = 20480, SLOT_ML = 16384, SLOT_FLAG = 18432, SLOT_EPOCH = 18560;
constexpr size_t WS_NEED = (size_t)HKV * NB * NB * SLOT_BYTES;

typedef __attribute__((ext_vector_type(8))) __bf16 bf16x8;
typedef __attribute__((ext_vector_type(4))) __bf16 bf16x4;
typedef __attribute__((ext_vector_type(2))) __bf16 bf16x2;
typedef __attribute__((ext_vector_type(4))) short s16x4;
typedef __attribute__((ext_vector_type(16))) float f32x16;
typedef __attribute__((ext_vector_type(4))) float f32x4;
typedef __attribute__((ext_vector_type(2))) float f32x2;
typedef __attribute__((ext_vector_type(4))) unsigned u32x4;
typedef __attribute__((ext_vector_type(2))) unsigned u32x2;
#define LAS __attribute__((address_space(3)))
#define GAS __attribute__((address_space(1)))

#define DMA8(POL, rs, m0v, v0, v1, v2, v3, v4, v5, v6, v7)                                            \
  asm volatile("s_mov_b32 m0, %0\n\ts_nop 4\n\tbuffer_load_dwordx4 %2, %1, 0 offen " POL " lds\n\t"   \
               "s_add_u32 m0, m0, 1024\n\ts_nop 0\n\tbuffer_load_dwordx4 %3, %1, 0 offen " POL " lds\n\t" \
               "s_add_u32 m0, m0, 1024\n\ts_nop 0\n\tbuffer_load_dwordx4 %4, %1, 0 offen " POL " lds\n\t" \
               "s_add_u32 m0, m0, 1024\n\ts_nop 0\n\tbuffer_load_dwordx4 %5, %1, 0 offen " POL " lds\n\t" \
               "s_add_u32 m0, m0, 1024\n\ts_nop 0\n\tbuffer_load_dwordx4 %6, %1, 0 offen " POL " lds\n\t" \
               "s_add_u32 m0, m0, 1024\n\ts_nop 0\n\tbuffer_load_dwordx4 %7, %1, 0 offen " POL " lds\n\t" \
               "s_add_u32 m0, m0, 1024\n\ts_nop 0\n\tbuffer_load_dwordx4 %8, %1, 0 offen " POL " lds\n\t" \
               "s_add_u32 m0, m0, 1024\n\ts_nop 0\n\tbuffer_load_dwordx4 %9, %1, 0 offen " POL " lds"     \
               :: "s"(m0v), "s"(rs), "v"(v0), "v"(v1), "v"(v2), "v"(v3), "v"(v4), "v"(v5), "v"(v6), "v"(v7) \
               : "memory", "scc")

__device__ __forceinline__ float swap_max(float x) {
  auto r = __builtin_amdgcn_permlane32_swap(__float_as_uint(x), __float_as_uint(x), false, false);
  return fmaxf(__uint_as_float(r[0]), __uint_as_float(r[1]));
}
__device__ __forceinline__ float swap_sum(float x) {
  auto r = __builtin_amdgcn_permlane32_swap(__float_as_uint(x), __float_as_uint(x), false, false);
  return __uint_as_float(r[0]) + __uint_as_float(r[1]);
}
__device__ __forceinline__ unsigned pack2(float a, float b) {
  bf16x2 v = {(__bf16)a, (__bf16)b};
  return __builtin_bit_cast(unsigned, v);
}
#define LGKM0() asm volatile("s_waitcnt lgkmcnt(0)" ::: "memory")
#define VMWAIT(n) asm volatile("s_waitcnt vmcnt(" #n ")" ::: "memory")
#define BARRIER() do { asm volatile("s_waitcnt lgkmcnt(0)" ::: "memory"); __builtin_amdgcn_s_barrier(); asm volatile("" ::: "memory"); } while (0)

__global__ void __launch_bounds__(256, 1)
paged_attn(const void* __restrict__ qp, const void* __restrict__ kp, const void* __restrict__ vp, const int* __restrict__ bt,
           const int* __restrict__ lens, void* __restrict__ outp, char* __restrict__ ws, unsigned kvbytes, unsigned nphys) {
  extern __shared__ __attribute__((aligned(16))) char smem[];
  LAS char* lds = (LAS char*)smem;
  const unsigned t0 = (unsigned)__builtin_amdgcn_s_memrealtime();
  const int lane = threadIdx.x & 63;
  const int w = __builtin_amdgcn_readfirstlane(threadIdx.x >> 6);
  const int g = blockIdx.x;
  const int b = 2 * (g & 7) + ((g >> 3) & 1), h = g >> 4;
  constexpr float CS = 0.08838834764831845f * 1.4426950408889634f;

  int np[NB], tot = 0;
#pragma unroll
  for (int i = 0; i < NB; ++i) {
    int L = lens[i];
    L = (L <= 0 || L > SKV) ? SKV : L;
    np[i] = (L + PAGE - 1) / PAGE;
    tot += np[i];
  }
  const int S = BALANCE ? (tot + NB - 1) / NB : NBPS;
  int dlo = 0, elo = 0, npb = 0;
  {
    int dacc = 0, eacc = 0;
#pragma unroll
    for (int i = 0; i < NB; ++i) {
      if (i == b) { dlo = dacc; elo = eacc; npb = np[i]; }
      dacc += max(S - np[i], 0);
      eacc += max(np[i] - S, 0);
    }
  }
  const int dhi = dlo + max(S - npb, 0);
  const int ehi = elo + max(npb - S, 0);
  const int own = min(npb, S);

  unsigned helpmask = 0;
  int nent;
  {
    int ebatch[MAXENT / 64], epage[MAXENT / 64];
#pragma unroll
    for (int r = 0; r < MAXENT / 64; ++r) { ebatch[r] = 0; epage[r] = -1; }
    int pos = 0, eacc = 0;
#pragma unroll
    for (int c = 0; c < NB; ++c) {
      const int ex = max(np[c] - S, 0);
      const int lo = max(eacc, dlo), hi = min(eacc + ex, dhi);
      if (hi > lo) {
        helpmask |= 1u << c;
        const int p0 = S + (lo - eacc), n = hi - lo;
#pragma unroll
        for (int r = 0; r < MAXENT / 64; ++r) {
          const int k = lane + 64 * r - pos;
          if (k >= 0 && k < n) { ebatch[r] = c; epage[r] = p0 + k; }
        }
        pos += (n + 7) & ~7;
      }
      eacc += ex;
    }
#pragma unroll
    for (int r = 0; r < MAXENT / 64; ++r) {
      const int k = lane + 64 * r - pos;
      if (k >= 0 && k < own) { ebatch[r] = b; epage[r] = k; }
    }
    nent = pos + ((own + 7) & ~7);
    LAS unsigned* pl = (LAS unsigned*)(lds + LDS_PL + w * MAXENT * 4);
    unsigned ph[MAXENT / 64];
#pragma unroll
    for (int r = 0; r < MAXENT / 64; ++r) ph[r] = (unsigned)bt[ebatch[r] * NBPS + max(epage[r], 0)];
#pragma unroll
    for (int r = 0; r < MAXENT / 64; ++r)
      pl[lane + 64 * r] = (epage[r] >= 0 && ph[r] < nphys) ? ph[r] * PAGE_BYTES + h * (HD * 2) : INVALID;
  }
  LGKM0();

  const __amdgpu_buffer_rsrc_t rk = __builtin_amdgcn_make_buffer_rsrc((void*)kp, 0, (int)kvbytes, 0x00020000);
  const __amdgpu_buffer_rsrc_t rv = __builtin_amdgcn_make_buffer_rsrc((void*)vp, 0, (int)kvbytes, 0x00020000);
  const __amdgpu_buffer_rsrc_t rq = __builtin_amdgcn_make_buffer_rsrc((void*)qp, 0, NB * HQ * SQ * HD * 2, 0x00020000);
  const __amdgpu_buffer_rsrc_t rw = __builtin_amdgcn_make_buffer_rsrc((void*)ws, 0, (int)WS_NEED, 0x00020000);

  const unsigned rq4 = lane >> 4;
  const unsigned chs = ((unsigned)(lane & 15) ^ (rq4 << 2)) << 4;
  const unsigned vo_kv = (4 * w + rq4) * 4096 + chs;
  const unsigned vo_q = rq4 * 256 + chs;
  const unsigned ring = (unsigned)(size_t)(lds) + LDS_RING + w * RING_W;
  const unsigned plbase = LDS_PL + w * MAXENT * 4;

  unsigned imask = helpmask | (1u << 16);
  int ient = 0;
  int iend = 0;
  int iphase = 0;
  int ieacc = 0, ic = 0;
  int due = -PACE_LEAD * 4 * PACE_X16;
  int ileft = 0;
  unsigned islot = 0;
  u32x4 ea = {INVALID, INVALID, INVALID, INVALID}, eb = ea;
  (void)ieacc; (void)ic;

  auto piece_pages = [&](int c) -> int {
    if (c == 16) return own;
    int eacc = 0, n = 0;
#pragma unroll
    for (int i = 0; i < NB; ++i) {
      const int ex = max(np[i] - S, 0);
      if (i == c) n = min(eacc + ex, dhi) - max(eacc, dlo);
      eacc += ex;
    }
    return n;
  };
  auto piece_p0 = [&](int c) -> int {
    if (c == 16) return 0;
    int eacc = 0, p = 0;
#pragma unroll
    for (int i = 0; i < NB; ++i) {
      const int ex = max(np[i] - S, 0);
      if (i == c) p = S + max(eacc, dlo) - eacc;
      eacc += ex;
    }
    return p;
  };

  auto issue_next = [&]() {
    const unsigned m0v = ring + islot;
    islot = (islot == 2 * 8192) ? 0u : islot + 8192;
    if (iphase == 0) {
      unsigned qoff = INVALID;
      if (imask) {
        const int c = __builtin_ctz(imask);
        imask &= imask - 1;
        const int n = piece_pages(c);
        ileft = n;
        iend = ient + ((n + 7) & ~7);
        const int cb = (c == 16) ? b : c;
        if (lens[cb] > 0) qoff = (unsigned)((cb * HQ + 2 * h) * SQ * HD * 2);
        iphase = 1;
      }
      const unsigned q0 = qoff + vo_q;
      DMA8("", rq, m0v, q0, (q0 ^ 16) + 1024, (q0 ^ 32) + 2048, (q0 ^ 48) + 3072, q0 + 4096, (q0 ^ 16) + 5120, (q0 ^ 32) + 6144, (q0 ^ 48) + 7168);
    } else {
      if (PACE_X16 > 0) {
        while ((int)(((unsigned)__builtin_amdgcn_s_memrealtime() - t0) * 16u) < due) __builtin_amdgcn_s_sleep(1);
        due += min(ileft, 8) * (PACE_X16 / 2);
      }
      if (iphase == 1) {
        ea = *(LAS u32x4*)(lds + plbase + ient * 4);
        eb = *(LAS u32x4*)(lds + plbase + ient * 4 + 16);
        DMA8(KPOL, rk, m0v, ea[0] + vo_kv, ea[1] + (vo_kv ^ 16), ea[2] + (vo_kv ^ 32), ea[3] + (vo_kv ^ 48),
             eb[0] + vo_kv, eb[1] + (vo_kv ^ 16), eb[2] + (vo_kv ^ 32), eb[3] + (vo_kv ^ 48));
        iphase = 2;
      } else {
        DMA8(VPOL, rv, m0v, ea[0] + vo_kv, ea[1] + vo_kv, ea[2] + vo_kv, ea[3] + vo_kv,
             eb[0] + vo_kv, eb[1] + vo_kv, eb[2] + vo_kv, eb[3] + vo_kv);
        ient += 8;
        ileft -= 8;
        iphase = (ient == iend) ? 0 : 1;
      }
    }
  };

  issue_next(); issue_next(); issue_next();

  const int r32 = lane & 31, hi = lane >> 5;
  const unsigned fk = (((unsigned)r32 & 3) << 2) | (((unsigned)r32 >> 2) & 3);
  const unsigned kaddr0 = LDS_RING + w * RING_W + r32 * 256 + (((unsigned)hi ^ fk) << 4);
  const unsigned tq = (lane & 15) >> 2, tp = lane & 3, tblk = (lane >> 4) & 1;
  const unsigned vaddr0 = LDS_RING + w * RING_W + (4 * hi + tq) * 256 + (tq << 6) + ((2 * tblk + (tp >> 1)) << 4) + ((tp & 1) << 3);
  unsigned cslot = 0;
  auto next_cslot = [&]() { cslot = (cslot == 2 * 8192) ? 0u : cslot + 8192; };

  unsigned cmask = helpmask | (1u << 16);
  while (cmask) {
    const int c = __builtin_ctz(cmask);
    cmask &= cmask - 1;
    const int cb = (c == 16) ? b : c;
    const int n = piece_pages(c), p0 = piece_p0(c);
    int Lc = lens[cb];
    Lc = (Lc <= 0 || Lc > SKV) ? SKV : Lc;
    const int lim = min(Lc, (p0 + n) * PAGE);
    const int ntile = (n + 7) >> 3;

    bf16x8 qf[8];
    VMWAIT(16);
#pragma unroll
    for (int s = 0; s < 8; ++s) qf[s] = *(LAS bf16x8*)(lds + ((kaddr0 + cslot) ^ (s << 5)));
    LGKM0();
    next_cslot();
    issue_next();

    f32x16 o[4];
#pragma unroll
    for (int i = 0; i < 4; ++i)
#pragma unroll
      for (int r = 0; r < 16; ++r) o[i][r] = 0.f;
    float m = -3.0e38f, l = 0.f;

    for (int t = 0; t < ntile; ++t) {
      bf16x8 kf[8];
      VMWAIT(16);
#pragma unroll
      for (int s = 0; s < 8; ++s) kf[s] = *(LAS bf16x8*)(lds + ((kaddr0 + cslot) ^ (s << 5)));
      LGKM0();
      next_cslot();
      issue_next();
      f32x16 sc;
#pragma unroll
      for (int r = 0; r < 16; ++r) sc[r] = 0.f;
#pragma unroll
      for (int s = 0; s < 8; ++s) sc = __builtin_amdgcn_mfma_f32_32x32x16_bf16(kf[s], qf[s], sc, 0, 0, 0);
      if (t == ntile - 1) {
        const int kb = (p0 + 8 * t + hi) * PAGE + 4 * w;
#pragma unroll
        for (int r = 0; r < 16; ++r) {
          const int key = kb + (r >> 2) * 2 * PAGE + (r & 3);
          sc[r] = (key < lim) ? sc[r] : -__builtin_inff();
        }
      }
      float mx = sc[0];
#pragma unroll
      for (int r = 1; r < 16; ++r) mx = fmaxf(mx, sc[r]);
      mx = swap_max(mx);
      const float mn = fmaxf(m, mx);
      const float alpha = __builtin_amdgcn_exp2f((m - mn) * CS);
      m = mn;
      const float nmc = -mn * CS;
      float ps = 0.f;
#pragma unroll
      for (int r = 0; r < 16; ++r) { sc[r] = __builtin_amdgcn_exp2f(fmaf(sc[r], CS, nmc)); ps += sc[r]; }
      l = fmaf(l, alpha, ps);
      bf16x8 pf[2];
#pragma unroll
      for (int s = 0; s < 2; ++s) {
        u32x4 u = {pack2(sc[8 * s], sc[8 * s + 1]), pack2(sc[8 * s + 2], sc[8 * s + 3]), pack2(sc[8 * s + 4], sc[8 * s + 5]), pack2(sc[8 * s + 6], sc[8 * s + 7])};
        pf[s] = __builtin_bit_cast(bf16x8, u);
      }
#pragma unroll
      for (int i = 0; i < 4; ++i)
#pragma unroll
        for (int r = 0; r < 16; ++r) o[i][r] *= alpha;

      s16x4 vf[4][2][2];
      VMWAIT(16);
#pragma unroll
      for (int i = 0; i < 4; ++i) {
        const unsigned a = (vaddr0 + cslot) ^ (i << 6);
#pragma unroll
        for (int s = 0; s < 2; ++s)
#pragma unroll
          for (int u = 0; u < 2; ++u)
            vf[i][s][u] = __builtin_amdgcn_ds_read_tr16_b64_v4i16((LAS s16x4*)(lds + a + 4096 * s + 2048 * u));
      }
      LGKM0();
      next_cslot();
      issue_next();
#pragma unroll
      for (int i = 0; i < 4; ++i)
#pragma unroll
        for (int s = 0; s < 2; ++s) {
          const bf16x8 va = __builtin_bit_cast(bf16x8, __builtin_shufflevector(vf[i][s][0], vf[i][s][1], 0, 1, 2, 3, 4, 5, 6, 7));
          o[i] = __builtin_amdgcn_mfma_f32_32x32x16_bf16(va, pf[s], o[i], 0, 0, 0);
        }
    }

    l = swap_sum(l);
    if (hi == 0) *(LAS f32x2*)(lds + LDS_ML + w * 256 + r32 * 8) = f32x2{m, l};
#pragma unroll
    for (int i = 0; i < 4; ++i) {
      if (i != w) {
        const unsigned dst = LDS_MRG + (i * 3 + (w > i ? w - 1 : w)) * 4096 + lane * 16;
#pragma unroll
        for (int j = 0; j < 4; ++j) *(LAS f32x4*)(lds + dst + j * 1024) = f32x4{o[i][4 * j], o[i][4 * j + 1], o[i][4 * j + 2], o[i][4 * j + 3]};
      }
    }
    f32x16 of = (w == 0) ? o[0] : (w == 1) ? o[1] : (w == 2) ? o[2] : o[3];
    BARRIER();
    f32x2 ml[4];
#pragma unroll
    for (int i = 0; i < 4; ++i) ml[i] = *(LAS f32x2*)(lds + LDS_ML + i * 256 + r32 * 8);
    float M = fmaxf(fmaxf(ml[0][0], ml[1][0]), fmaxf(ml[2][0], ml[3][0]));
    float wt[4], Ls = 0.f;
#pragma unroll
    for (int i = 0; i < 4; ++i) { wt[i] = __builtin_amdgcn_exp2f((ml[i][0] - M) * CS); Ls = fmaf(wt[i], ml[i][1], Ls); }
    {
      const float wo = __builtin_amdgcn_exp2f((m - M) * CS);
#pragma unroll
      for (int r = 0; r < 16; ++r) of[r] *= wo;
#pragma unroll
      for (int k = 0; k < 3; ++k) {
        const float wk = (k < w) ? wt[k] : wt[k + 1];
        const unsigned src = LDS_MRG + (w * 3 + k) * 4096 + lane * 16;
#pragma unroll
        for (int j = 0; j < 4; ++j) {
          const f32x4 x = *(LAS f32x4*)(lds + src + j * 1024);
#pragma unroll
          for (int e = 0; e < 4; ++e) of[4 * j + e] = fmaf(wk, x[e], of[4 * j + e]);
        }
      }
    }
    if (c != 16) {
      const unsigned slot = (unsigned)((h * NB + b) * NB + c) * SLOT_BYTES;
      GAS unsigned long long* epw = (GAS unsigned long long*)(ws + slot + SLOT_EPOCH);
      GAS unsigned long long* flw = (GAS unsigned long long*)(ws + slot + SLOT_FLAG);
      unsigned long long ep = 0;
      if (w == 0) ep = __hip_atomic_load(epw, __ATOMIC_RELAXED, __HIP_MEMORY_SCOPE_AGENT);
#pragma unroll
      for (int j = 0; j < 4; ++j) {
        u32x4 x = {__float_as_uint(of[4 * j]), __float_as_uint(of[4 * j + 1]), __float_as_uint(of[4 * j + 2]), __float_as_uint(of[4 * j + 3])};
        __builtin_amdgcn_raw_buffer_store_b128(x, rw, slot + w * 4096 + j * 1024 + lane * 16, 0, 16);
      }
      u32x2 y = {__float_as_uint(M), __float_as_uint(Ls)};
      __builtin_amdgcn_raw_buffer_store_b64(y, rw, slot + SLOT_ML + w * 512 + lane * 8, 0, 16);
      VMWAIT(0);
      BARRIER();
      if (threadIdx.x == 0) __hip_atomic_store(flw, ep + 1, __ATOMIC_RELAXED, __HIP_MEMORY_SCOPE_AGENT);
    } else {
      if (ehi > elo) {
        int dacc = 0;
        unsigned gm = 0;
#pragma unroll
        for (int i = 0; i < NB; ++i) {
          const int df = max(S - np[i], 0);
          if (min(dacc + df, ehi) > max(dacc, elo)) gm |= 1u << i;
          dacc += df;
        }
        while (gm) {
          const int hb = __builtin_ctz(gm);
          gm &= gm - 1;
          const unsigned slot = (unsigned)((h * NB + hb) * NB + b) * SLOT_BYTES;
          GAS unsigned long long* epw = (GAS unsigned long long*)(ws + slot + SLOT_EPOCH);
          GAS unsigned long long* flw = (GAS unsigned long long*)(ws + slot + SLOT_FLAG);
          if (w == 0) {
            const unsigned long long ep = __hip_atomic_load(epw, __ATOMIC_RELAXED, __HIP_MEMORY_SCOPE_AGENT);
            const unsigned ts = (unsigned)__builtin_amdgcn_s_memrealtime();
            while (__hip_atomic_load(flw, __ATOMIC_RELAXED, __HIP_MEMORY_SCOPE_AGENT) != ep + 1) {
              __builtin_amdgcn_s_sleep(2);
              if ((unsigned)__builtin_amdgcn_s_memrealtime() - ts > 20000000u) break;
            }
            if (lane == 0) __hip_atomic_store(epw, ep + 1, __ATOMIC_RELAXED, __HIP_MEMORY_SCOPE_AGENT);
          }
          BARRIER();
          u32x4 x[4];
#pragma unroll
          for (int j = 0; j < 4; ++j) x[j] = __builtin_amdgcn_raw_buffer_load_b128(rw, slot + w * 4096 + j * 1024 + lane * 16, 0, 16);
          const u32x2 y = __builtin_amdgcn_raw_buffer_load_b64(rw, slot + SLOT_ML + w * 512 + lane * 8, 0, 16);
          const float Mp = __uint_as_float(y[0]), Lp = __uint_as_float(y[1]);
          const float Mn = fmaxf(M, Mp);
          const float a0 = __builtin_amdgcn_exp2f((M - Mn) * CS), a1 = __builtin_amdgcn_exp2f((Mp - Mn) * CS);
          Ls = Ls * a0 + Lp * a1;
          M = Mn;
#pragma unroll
          for (int j = 0; j < 4; ++j)
#pragma unroll
            for (int e = 0; e < 4; ++e) of[4 * j + e] = of[4 * j + e] * a0 + __uint_as_float(x[j][e]) * a1;
        }
      }
      const float inv = 1.0f / Ls;
      unsigned pk[8];
#pragma unroll
      for (int j = 0; j < 8; ++j) pk[j] = pack2(of[2 * j] * inv, of[2 * j + 1] * inv);
      char* orow = (char*)outp + ((size_t)((b * HQ + 2 * h) * SQ + r32) * HD + 32 * w) * 2;
#pragma unroll
      for (int j = 0; j < 2; ++j) {
        auto s0 = __builtin_amdgcn_permlane32_swap(pk[4 * j], pk[4 * j + 2], false, false);
        auto s1 = __builtin_amdgcn_permlane32_swap(pk[4 * j + 1], pk[4 * j + 3], false, false);
        u32x4 val = {s0[0], s1[0], s0[1], s1[1]};
        *(u32x4*)(orow + j * 32 + hi * 16) = val;
      }
    }
  }
}
}

extern "C" void kernel_launch(void* const* d_in, const int* in_sizes, int n_in, void* d_out, int out_size, void* d_ws,
                              size_t ws_size, hipStream_t stream) {
  (void)n_in; (void)out_size; (void)ws_size;
  const unsigned nphys = (unsigned)(in_sizes[1] / (PAGE * HKV * HD));
  const unsigned kvbytes = nphys * PAGE_BYTES;
  (void)hipFuncSetAttribute((const void*)paged_attn, hipFuncAttributeMaxDynamicSharedMemorySize, LDS_TOTAL);
  hipLaunchKernelGGL(paged_attn, dim3(256), dim3(256), LDS_TOTAL, stream, d_in[0], d_in[1], d_in[2], (const int*)d_in[3],
                     (const int*)d_in[4], d_out, (char*)d_ws, kvbytes, nphys);
}
```

```cpp
#define PACE_X16 0
#define VPOL ""
#include <hip/hip_runtime.h>
#include <hip/hip_bf16.h>

#define BALANCE 1
#define PACE_LEAD 1
#define KPOL "nt"

#define STAMP(i) do {} while (0)
namespace {
constexpr int NB = 16, HQ = 32, HKV = 16, SQ = 16, HD = 128, PAGE = 16, NBPS = 256, SKV = NBPS * PAGE;
constexpr unsigned PAGE_BYTES = PAGE * HKV * HD * 2;
constexpr unsigned INVALID = 0x80000000u;
constexpr int MAXENT = 448;
constexpr unsigned RING_W = 3 * 8192;
constexpr unsigned LDS_RING = 0;
constexpr unsigned LDS_MRG = 4 * RING_W;
constexpr unsigned LDS_ML = LDS_MRG + 4 * 3 * 4096;
constexpr unsigned LDS_PL = LDS_ML + 4 * 32 * 8;
constexpr unsigned LDS_TOTAL = LDS_PL + 4 * MAXENT * 4;
static_assert(LDS_TOTAL <= 163840, "lds");
constexpr unsigned SLOT_BYTES = 20480, SLOT_ML = 16384, SLOT_FLAG = 18432, SLOT_EPOCH = 18560;
constexpr size_t WS_NEED = (size_t)HKV * NB * NB * SLOT_BYTES;

typedef __attribute__((ext_vector_type(8))) __bf16 bf16x8;
typedef __attribute__((ext_vector_type(4))) __bf16 bf16x4;
typedef __attribute__((ext_vector_type(2))) __bf16 bf16x2;
typedef __attribute__((ext_vector_type(4))) short s16x4;
typedef __attribute__((ext_vector_type(16))) float f32x16;
typedef __attribute__((ext_vector_type(4))) float f32x4;
typedef __attribute__((ext_vector_type(2))) float f32x2;
typedef __attribute__((ext_vector_type(4))) unsigned u32x4;
typedef __attribute__((ext_vector_type(2))) unsigned u32x2;
#define LAS __attribute__((address_space(3)))
#define GAS __attribute__((address_space(1)))

#define DMA8(POL, rs, m0v, v0, v1, v2, v3, v4, v5, v6, v7)                                            \
  asm volatile("s_mov_b32 m0, %0\n\ts_nop 4\n\tbuffer_load_dwordx4 %2, %1, 0 offen " POL " lds\n\t"   \
               "s_add_u32 m0, m0, 1024\n\ts_nop 0\n\tbuffer_load_dwordx4 %3, %1, 0 offen " POL " lds\n\t" \
               "s_add_u32 m0, m0, 1024\n\ts_nop 0\n\tbuffer_load_dwordx4 %4, %1, 0 offen " POL " lds\n\t" \
               "s_add_u32 m0, m0, 1024\n\ts_nop 0\n\tbuffer_load_dwordx4 %5, %1, 0 offen " POL " lds\n\t" \
               "s_add_u32 m0, m0, 1024\n\ts_nop 0\n\tbuffer_load_dwordx4 %6, %1, 0 offen " POL " lds\n\t" \
               "s_add_u32 m0, m0, 1024\n\ts_nop 0\n\tbuffer_load_dwordx4 %7, %1, 0 offen " POL " lds\n\t" \
               "s_add_u32 m0, m0, 1024\n\ts_nop 0\n\tbuffer_load_dwordx4 %8, %1, 0 offen " POL " lds\n\t" \
               "s_add_u32 m0, m0, 1024\n\ts_nop 0\n\tbuffer_load_dwordx4 %9, %1, 0 offen " POL " lds"     \
               :: "s"(m0v), "s"(rs), "v"(v0), "v"(v1), "v"(v2), "v"(v3), "v"(v4), "v"(v5), "v"(v6), "v"(v7) \
               : "memory", "scc")

__device__ __forceinline__ float swap_max(float x) {
  auto r = __builtin_amdgcn_permlane32_swap(__float_as_uint(x), __float_as_uint(x), false, false);
  return fmaxf(__uint_as_float(r[0]), __uint_as_float(r[1]));
}
__device__ __forceinline__ float swap_sum(float x) {
  auto r = __builtin_amdgcn_permlane32_swap(__float_as_uint(x), __float_as_uint(x), false, false);
  return __uint_as_float(r[0]) + __uint_as_float(r[1]);
}
__device__ __forceinline__ unsigned pack2(float a, float b) {
  bf16x2 v = {(__bf16)a, (__bf16)b};
  return __builtin_bit_cast(unsigned, v);
}
#define LGKM0() asm volatile("s_waitcnt lgkmcnt(0)" ::: "memory")
#define VMWAIT(n) asm volatile("s_waitcnt vmcnt(" #n ")" ::: "memory")
#define BARRIER() do { asm volatile("s_waitcnt lgkmcnt(0)" ::: "memory"); __builtin_amdgcn_s_barrier(); asm volatile("" ::: "memory"); } while (0)

__global__ void __launch_bounds__(256, 1)
paged_attn(const void* __restrict__ qp, const void* __restrict__ kp, const void* __restrict__ vp, const int* __restrict__ bt,
           const int* __restrict__ lens, void* __restrict__ outp, char* __restrict__ ws, unsigned kvbytes, unsigned nphys) {
  extern __shared__ __attribute__((aligned(16))) char smem[];
  LAS char* lds = (LAS char*)smem;
  const unsigned t0 = (unsigned)__builtin_amdgcn_s_memrealtime();
  const int lane = threadIdx.x & 63;
  const int w = __builtin_amdgcn_readfirstlane(threadIdx.x >> 6);
  const int g = blockIdx.x;
  const int b = 2 * (g & 7) + ((g >> 3) & 1), h = g >> 4;
  constexpr float CS = 0.08838834764831845f * 1.4426950408889634f;

  int np[NB], tot = 0;
#pragma unroll
  for (int i = 0; i < NB; ++i) {
    int L = lens[i];
    L = (L <= 0 || L > SKV) ? SKV : L;
    np[i] = (L + PAGE - 1) / PAGE;
    tot += np[i];
  }
  const int S = BALANCE ? (tot + NB - 1) / NB : NBPS;
  int dlo = 0, elo = 0, npb = 0;
  {
    int dacc = 0, eacc = 0;
#pragma unroll
    for (int i = 0; i < NB; ++i) {
      if (i == b) { dlo = dacc; elo = eacc; npb = np[i]; }
      dacc += max(S - np[i], 0);
      eacc += max(np[i] - S, 0);
    }
  }
  const int dhi = dlo + max(S - npb, 0);
  const int ehi = elo + max(npb - S, 0);
  const int own = min(npb, S);

  unsigned helpmask = 0;
  int nent;
  {
    int ebatch[MAXENT / 64], epage[MAXENT / 64];
#pragma unroll
    for (int r = 0; r < MAXENT / 64; ++r) { ebatch[r] = 0; epage[r] = -1; }
    int pos = 0, eacc = 0;
#pragma unroll
    for (int c = 0; c < NB; ++c) {
      const int ex = max(np[c] - S, 0);
      const int lo = max(eacc, dlo), hi = min(eacc + ex, dhi);
      if (hi > lo) {
        helpmask |= 1u << c;
        const int p0 = S + (lo - eacc), n = hi - lo;
#pragma unroll
        for (int r = 0; r < MAXENT / 64; ++r) {
          const int k = lane + 64 * r - pos;
          if (k >= 0 && k < n) { ebatch[r] = c; epage[r] = p0 + k; }
        }
        pos += (n + 7) & ~7;
      }
      eacc += ex;
    }
#pragma unroll
    for (int r = 0; r < MAXENT / 64; ++r) {
      const int k = lane + 64 * r - pos;
      if (k >= 0 && k < own) { ebatch[r] = b; epage[r] = k; }
    }
    nent = pos + ((own + 7) & ~7);
    LAS unsigned* pl = (LAS unsigned*)(lds + LDS_PL + w * MAXENT * 4);
    unsigned ph[MAXENT / 64];
#pragma unroll
    for (int r = 0; r < MAXENT / 64; ++r) ph[r] = (unsigned)bt[ebatch[r] * NBPS + max(epage[r], 0)];
#pragma unroll
    for (int r = 0; r < MAXENT / 64; ++r)
      pl[lane + 64 * r] = (epage[r] >= 0 && ph[r] < nphys) ? ph[r] * PAGE_BYTES + h * (HD * 2) : INVALID;
  }
  LGKM0();

  const __amdgpu_buffer_rsrc_t rk = __builtin_amdgcn_make_buffer_rsrc((void*)kp, 0, (int)kvbytes, 0x00020000);
  const __amdgpu_buffer_rsrc_t rv = __builtin_amdgcn_make_buffer_rsrc((void*)vp, 0, (int)kvbytes, 0x00020000);
  const __amdgpu_buffer_rsrc_t rq = __builtin_amdgcn_make_buffer_rsrc((void*)qp, 0, NB * HQ * SQ * HD * 2, 0x00020000);
  const __amdgpu_buffer_rsrc_t rw = __builtin_amdgcn_make_buffer_rsrc((void*)ws, 0, (int)WS_NEED, 0x00020000);

  const unsigned rq4 = lane >> 4;
  const unsigned chs = ((unsigned)(lane & 15) ^ (rq4 << 2)) << 4;
  const unsigned vo_kv = (4 * w + rq4) * 4096 + chs;
  const unsigned vo_q = rq4 * 256 + chs;
  const unsigned ring = (unsigned)(size_t)(lds) + LDS_RING + w * RING_W;
  const unsigned plbase = LDS_PL + w * MAXENT * 4;

  unsigned imask = helpmask | (1u << 16);
  int ient = 0;
  int iend = 0;
  int iphase = 0;
  int ieacc = 0, ic = 0;
  int due = -PACE_LEAD * 4 * PACE_X16;
  int ileft = 0;
  unsigned islot = 0;
  u32x4 ea = {INVALID, INVALID, INVALID, INVALID}, eb = ea;
  (void)ieacc; (void)ic;

  auto piece_pages = [&](int c) -> int {
    if (c == 16) return own;
    int eacc = 0, n = 0;
#pragma unroll
    for (int i = 0; i < NB; ++i) {
      const int ex = max(np[i] - S, 0);
      if (i == c) n = min(eacc + ex, dhi) - max(eacc, dlo);
      eacc += ex;
    }
    return n;
  };
  auto piece_p0 = [&](int c) -> int {
    if (c == 16) return 0;
    int eacc = 0, p = 0;
#pragma unroll
    for (int i = 0; i < NB; ++i) {
      const int ex = max(np[i] - S, 0);
      if (i == c) p = S + max(eacc, dlo) - eacc;
      eacc += ex;
    }
    return p;
  };

  auto issue_next = [&]() {
    const unsigned m0v = ring + islot;
    islot = (islot == 2 * 8192) ? 0u : islot + 8192;
    if (iphase == 0) {
      unsigned qoff = INVALID;
      if (imask) {
        const int c = __builtin_ctz(imask);
        imask &= imask - 1;
        const int n = piece_pages(c);
        ileft = n;
        iend = ient + ((n + 7) & ~7);
        const int cb = (c == 16) ? b : c;
        if (lens[cb] > 0) qoff = (unsigned)((cb * HQ + 2 * h) * SQ * HD * 2);
        iphase = 1;
      }
      const unsigned q0 = qoff + vo_q;
      DMA8("", rq, m0v, q0, (q0 ^ 16) + 1024, (q0 ^ 32) + 2048, (q0 ^ 48) + 3072, q0 + 4096, (q0 ^ 16) + 5120, (q0 ^ 32) + 6144, (q0 ^ 48) + 7168);
    } else {
      if (PACE_X16 > 0) {
        while ((int)(((unsigned)__builtin_amdgcn_s_memrealtime() - t0) * 16u) < due) __builtin_amdgcn_s_sleep(1);
        due += min(ileft, 8) * (PACE_X16 / 2);
      }
      if (iphase == 1) {
        ea = *(LAS u32x4*)(lds + plbase + ient * 4);
        eb = *(LAS u32x4*)(lds + plbase + ient * 4 + 16);
        DMA8(KPOL, rk, m0v, ea[0] + vo_kv, ea[1] + (vo_kv ^ 16), ea[2] + (vo_kv ^ 32), ea[3] + (vo_kv ^ 48),
             eb[0] + vo_kv, eb[1] + (vo_kv ^ 16), eb[2] + (vo_kv ^ 32), eb[3] + (vo_kv ^ 48));
        iphase = 2;
      } else {
        DMA8(VPOL, rv, m0v, ea[0] + vo_kv, ea[1] + vo_kv, ea[2] + vo_kv, ea[3] + vo_kv,
             eb[0] + vo_kv, eb[1] + vo_kv, eb[2] + vo_kv, eb[3] + vo_kv);
        ient += 8;
        ileft -= 8;
        iphase = (ient == iend) ? 0 : 1;
      }
    }
  };

  STAMP(1);
  issue_next(); issue_next(); issue_next();

  const int r32 = lane & 31, hi = lane >> 5;
  const unsigned fk = (((unsigned)r32 & 3) << 2) | (((unsigned)r32 >> 2) & 3);
  const unsigned kaddr0 = LDS_RING + w * RING_W + r32 * 256 + (((unsigned)hi ^ fk) << 4);
  const unsigned tq = (lane & 15) >> 2, tp = lane & 3, tblk = (lane >> 4) & 1;
  const unsigned vaddr0 = LDS_RING + w * RING_W + (4 * hi + tq) * 256 + (tq << 6) + ((2 * tblk + (tp >> 1)) << 4) + ((tp & 1) << 3);
  unsigned cslot = 0;
  auto next_cslot = [&]() { cslot = (cslot == 2 * 8192) ? 0u : cslot + 8192; };

  unsigned cmask = helpmask | (1u << 16);
  while (cmask) {
    const int c = __builtin_ctz(cmask);
    cmask &= cmask - 1;
    const int cb = (c == 16) ? b : c;
    const int n = piece_pages(c), p0 = piece_p0(c);
    int Lc = lens[cb];
    Lc = (Lc <= 0 || Lc > SKV) ? SKV : Lc;
    const int lim = min(Lc, (p0 + n) * PAGE);
    const int ntile = (n + 7) >> 3;

    bf16x8 qf[8];
    VMWAIT(16);
#pragma unroll
    for (int s = 0; s < 8; ++s) qf[s] = *(LAS bf16x8*)(lds + ((kaddr0 + cslot) ^ (s << 5)));
    LGKM0();
    next_cslot();
    issue_next();

    if (cmask == 0) STAMP(2);
    f32x16 o[4];
#pragma unroll
    for (int i = 0; i < 4; ++i)
#pragma unroll
      for (int r = 0; r < 16; ++r) o[i][r] = 0.f;
    float m = -3.0e38f, l = 0.f;

    for (int t = 0; t < ntile; ++t) {
      bf16x8 kf[8];
      VMWAIT(16);
#pragma unroll
      for (int s = 0; s < 8; ++s) kf[s] = *(LAS bf16x8*)(lds + ((kaddr0 + cslot) ^ (s << 5)));
      LGKM0();
      next_cslot();
      issue_next();
      f32x16 sc;
#pragma unroll
      for (int r = 0; r < 16; ++r) sc[r] = 0.f;
#pragma unroll
      for (int s = 0; s < 8; ++s) sc = __builtin_amdgcn_mfma_f32_32x32x16_bf16(kf[s], qf[s], sc, 0, 0, 0);
      if (t == ntile - 1) {
        const int kb = (p0 + 8 * t + hi) * PAGE + 4 * w;
#pragma unroll
        for (int r = 0; r < 16; ++r) {
          const int key = kb + (r >> 2) * 2 * PAGE + (r & 3);
          sc[r] = (key < lim) ? sc[r] : -__builtin_inff();
        }
      }
      float mx = sc[0];
#pragma unroll
      for (int r = 1; r < 16; ++r) mx = fmaxf(mx, sc[r]);
      mx = swap_max(mx);
      const float mn = fmaxf(m, mx);
      const float alpha = __builtin_amdgcn_exp2f((m - mn) * CS);
      m = mn;
      const float nmc = -mn * CS;
      float ps = 0.f;
#pragma unroll
      for (int r = 0; r < 16; ++r) { sc[r] = __builtin_amdgcn_exp2f(fmaf(sc[r], CS, nmc)); ps += sc[r]; }
      l = fmaf(l, alpha, ps);
      bf16x8 pf[2];
#pragma unroll
      for (int s = 0; s < 2; ++s) {
        u32x4 u = {pack2(sc[8 * s], sc[8 * s + 1]), pack2(sc[8 * s + 2], sc[8 * s + 3]), pack2(sc[8 * s + 4], sc[8 * s + 5]), pack2(sc[8 * s + 6], sc[8 * s + 7])};
        pf[s] = __builtin_bit_cast(bf16x8, u);
      }
#pragma unroll
      for (int i = 0; i < 4; ++i)
#pragma unroll
        for (int r = 0; r < 16; ++r) o[i][r] *= alpha;

      s16x4 vf[4][2][2];
      VMWAIT(16);
#pragma unroll
      for (int i = 0; i < 4; ++i) {
        const unsigned a = (vaddr0 + cslot) ^ (i << 6);
#pragma unroll
        for (int s = 0; s < 2; ++s)
#pragma unroll
          for (int u = 0; u < 2; ++u)
            vf[i][s][u] = __builtin_amdgcn_ds_read_tr16_b64_v4i16((LAS s16x4*)(lds + a + 4096 * s + 2048 * u));
      }
      LGKM0();
      next_cslot();
      issue_next();
#pragma unroll
      for (int i = 0; i < 4; ++i)
#pragma unroll
        for (int s = 0; s < 2; ++s) {
          const bf16x8 va = __builtin_bit_cast(bf16x8, __builtin_shufflevector(vf[i][s][0], vf[i][s][1], 0, 1, 2, 3, 4, 5, 6, 7));
          o[i] = __builtin_amdgcn_mfma_f32_32x32x16_bf16(va, pf[s], o[i], 0, 0, 0);
        }
    }

    if (cmask == 0) STAMP(3);
    l = swap_sum(l);
    if (hi == 0) *(LAS f32x2*)(lds + LDS_ML + w * 256 + r32 * 8) = f32x2{m, l};
#pragma unroll
    for (int i = 0; i < 4; ++i) {
      if (i != w) {
        const unsigned dst = LDS_MRG + (i * 3 + (w > i ? w - 1 : w)) * 4096 + lane * 16;
#pragma unroll
        for (int j = 0; j < 4; ++j) *(LAS f32x4*)(lds + dst + j * 1024) = f32x4{o[i][4 * j], o[i][4 * j + 1], o[i][4 * j + 2], o[i][4 * j + 3]};
      }
    }
    f32x16 of = (w == 0) ? o[0] : (w == 1) ? o[1] : (w == 2) ? o[2] : o[3];
    BARRIER();
    f32x2 ml[4];
#pragma unroll
    for (int i = 0; i < 4; ++i) ml[i] = *(LAS f32x2*)(lds + LDS_ML + i * 256 + r32 * 8);
    float M = fmaxf(fmaxf(ml[0][0], ml[1][0]), fmaxf(ml[2][0], ml[3][0]));
    float wt[4], Ls = 0.f;
#pragma unroll
    for (int i = 0; i < 4; ++i) { wt[i] = __builtin_amdgcn_exp2f((ml[i][0] - M) * CS); Ls = fmaf(wt[i], ml[i][1], Ls); }
    {
      const float wo = __builtin_amdgcn_exp2f((m - M) * CS);
#pragma unroll
      for (int r = 0; r < 16; ++r) of[r] *= wo;
#pragma unroll
      for (int k = 0; k < 3; ++k) {
        const float wk = (k < w) ? wt[k] : wt[k + 1];
        const unsigned src = LDS_MRG + (w * 3 + k) * 4096 + lane * 16;
#pragma unroll
        for (int j = 0; j < 4; ++j) {
          const f32x4 x = *(LAS f32x4*)(lds + src + j * 1024);
#pragma unroll
          for (int e = 0; e < 4; ++e) of[4 * j + e] = fmaf(wk, x[e], of[4 * j + e]);
        }
      }
    }
    if (c != 16) {
      const unsigned slot = (unsigned)((h * NB + b) * NB + c) * SLOT_BYTES;
      GAS unsigned long long* epw = (GAS unsigned long long*)(ws + slot + SLOT_EPOCH);
      GAS unsigned long long* flw = (GAS unsigned long long*)(ws + slot + SLOT_FLAG);
      unsigned long long ep = 0;
      if (w == 0) ep = __hip_atomic_load(epw, __ATOMIC_RELAXED, __HIP_MEMORY_SCOPE_AGENT);
#pragma unroll
      for (int j = 0; j < 4; ++j) {
        u32x4 x = {__float_as_uint(of[4 * j]), __float_as_uint(of[4 * j + 1]), __float_as_uint(of[4 * j + 2]), __float_as_uint(of[4 * j + 3])};
        __builtin_amdgcn_raw_buffer_store_b128(x, rw, slot + w * 4096 + j * 1024 + lane * 16, 0, 16);
      }
      u32x2 y = {__float_as_uint(M), __float_as_uint(Ls)};
      __builtin_amdgcn_raw_buffer_store_b64(y, rw, slot + SLOT_ML + w * 512 + lane * 8, 0, 16);
      VMWAIT(0);
      BARRIER();
      if (threadIdx.x == 0) __hip_atomic_store(flw, ep + 1, __ATOMIC_RELAXED, __HIP_MEMORY_SCOPE_AGENT);
    } else {
      STAMP(4);
      if (ehi > elo) {
        int dacc = 0;
        unsigned gm = 0;
#pragma unroll
        for (int i = 0; i < NB; ++i) {
          const int df = max(S - np[i], 0);
          if (min(dacc + df, ehi) > max(dacc, elo)) gm |= 1u << i;
          dacc += df;
        }
        while (gm) {
          const int hb = __builtin_ctz(gm);
          gm &= gm - 1;
          const unsigned slot = (unsigned)((h * NB + hb) * NB + b) * SLOT_BYTES;
          GAS unsigned long long* epw = (GAS unsigned long long*)(ws + slot + SLOT_EPOCH);
          GAS unsigned long long* flw = (GAS unsigned long long*)(ws + slot + SLOT_FLAG);
          if (w == 0) {
            const unsigned long long ep = __hip_atomic_load(epw, __ATOMIC_RELAXED, __HIP_MEMORY_SCOPE_AGENT);
            const unsigned ts = (unsigned)__builtin_amdgcn_s_memrealtime();
            while (__hip_atomic_load(flw, __ATOMIC_RELAXED, __HIP_MEMORY_SCOPE_AGENT) != ep + 1) {
              __builtin_amdgcn_s_sleep(2);
              if ((unsigned)__builtin_amdgcn_s_memrealtime() - ts > 20000000u) break;
            }
            if (lane == 0) __hip_atomic_store(epw, ep + 1, __ATOMIC_RELAXED, __HIP_MEMORY_SCOPE_AGENT);
          }
          BARRIER();
          u32x4 x[4];
#pragma unroll
          for (int j = 0; j < 4; ++j) x[j] = __builtin_amdgcn_raw_buffer_load_b128(rw, slot + w * 4096 + j * 1024 + lane * 16, 0, 16);
          const u32x2 y = __builtin_amdgcn_raw_buffer_load_b64(rw, slot + SLOT_ML + w * 512 + lane * 8, 0, 16);
          const float Mp = __uint_as_float(y[0]), Lp = __uint_as_float(y[1]);
          const float Mn = fmaxf(M, Mp);
          const float a0 = __builtin_amdgcn_exp2f((M - Mn) * CS), a1 = __builtin_amdgcn_exp2f((Mp - Mn) * CS);
          Ls = Ls * a0 + Lp * a1;
          M = Mn;
#pragma unroll
          for (int j = 0; j < 4; ++j)
#pragma unroll
            for (int e = 0; e < 4; ++e) of[4 * j + e] = of[4 * j + e] * a0 + __uint_as_float(x[j][e]) * a1;
        }
      }
      const float inv = 1.0f / Ls;
      unsigned pk[8];
#pragma unroll
      for (int j = 0; j < 8; ++j) pk[j] = pack2(of[2 * j] * inv, of[2 * j + 1] * inv);
      char* orow = (char*)outp + ((size_t)((b * HQ + 2 * h) * SQ + r32) * HD + 32 * w) * 2;
#pragma unroll
      for (int j = 0; j < 2; ++j) {
        auto s0 = __builtin_amdgcn_permlane32_swap(pk[4 * j], pk[4 * j + 2], false, false);
        auto s1 = __builtin_amdgcn_permlane32_swap(pk[4 * j + 1], pk[4 * j + 3], false, false);
        u32x4 val = {s0[0], s1[0], s0[1], s1[1]};
        *(u32x4*)(orow + j * 32 + hi * 16) = val;
      }
    }
  }
}
}

extern "C" void kernel_launch(void* const* d_in, const int* in_sizes, int n_in, void* d_out, int out_size, void* d_ws,
                              size_t ws_size, hipStream_t stream) {
  (void)n_in; (void)out_size; (void)ws_size;
  const unsigned nphys = (unsigned)(in_sizes[1] / (PAGE * HKV * HD));
  const unsigned kvbytes = nphys * PAGE_BYTES;
  (void)hipFuncSetAttribute((const void*)paged_attn, hipFuncAttributeMaxDynamicSharedMemorySize, LDS_TOTAL);
  hipLaunchKernelGGL(paged_attn, dim3(256), dim3(256), LDS_TOTAL, stream, d_in[0], d_in[1], d_in[2], (const int*)d_in[3],
                     (const int*)d_in[4], d_out, (char*)d_ws, kvbytes, nphys);
}
```

```cpp
#define PACE_X16 470
#include <hip/hip_runtime.h>
#include <hip/hip_bf16.h>

#define BALANCE 1
#define LEAD_X16 (120 * 16)
#define VLAG_X16 (70 * 16)
#define LSLEEP ""
#define CSLEEP 1
#define MAPPING 1

#define STAMP(i) do {} while (0)

namespace {
constexpr int NB = 16, HQ = 32, HKV = 16, SQ = 16, HD = 128, PAGE = 16, NBPS = 256, SKV = NBPS * PAGE;
constexpr unsigned PAGE_BYTES = PAGE * HKV * HD * 2;
constexpr unsigned INVALID = 0x80000000u;
constexpr int NLR = 5;
constexpr unsigned LDS_RING = 0;
constexpr unsigned LDS_ACC = 131072;
constexpr unsigned LDS_CTL = LDS_ACC + 16384 + 512;
constexpr unsigned CTL_LAND = 0;
constexpr unsigned CTL_FREE = 16;
constexpr unsigned CTL_CHAIN = 48;
constexpr unsigned LDS_TOTAL = LDS_CTL + 64;
static_assert(LDS_TOTAL <= 163840, "lds");
constexpr unsigned SLOT_BYTES = 20480, SLOT_ML = 16384, SLOT_FLAG = 16896, SLOT_EPOCH = 17024;
constexpr size_t WS_NEED = (size_t)HKV * NB * NB * SLOT_BYTES;

typedef __attribute__((ext_vector_type(8))) __bf16 bf16x8;
typedef __attribute__((ext_vector_type(2))) __bf16 bf16x2;
typedef __attribute__((ext_vector_type(4))) short s16x4;
typedef __attribute__((ext_vector_type(16))) float f32x16;
typedef __attribute__((ext_vector_type(4))) float f32x4;
typedef __attribute__((ext_vector_type(2))) float f32x2;
typedef __attribute__((ext_vector_type(4))) unsigned u32x4;
typedef __attribute__((ext_vector_type(2))) unsigned u32x2;
#define LAS __attribute__((address_space(3)))
#define GAS __attribute__((address_space(1)))

#define DMA4(POL, rs, m0v, v0, v1, v2, v3)                                                               \
  asm volatile("s_mov_b32 m0, %0\n\ts_nop 4\n\tbuffer_load_dwordx4 %2, %1, 0 offen " POL " lds\n\t"       \
               "s_add_u32 m0, m0, 1024\n\ts_nop 0\n\tbuffer_load_dwordx4 %3, %1, 0 offen " POL " lds\n\t" \
               "s_add_u32 m0, m0, 1024\n\ts_nop 0\n\tbuffer_load_dwordx4 %4, %1, 0 offen " POL " lds\n\t" \
               "s_add_u32 m0, m0, 1024\n\ts_nop 0\n\tbuffer_load_dwordx4 %5, %1, 0 offen " POL " lds"     \
               :: "s"(m0v), "s"(rs), "v"(v0), "v"(v1), "v"(v2), "v"(v3) : "memory", "scc")

__device__ __forceinline__ float swap_max(float x) {
  auto r = __builtin_amdgcn_permlane32_swap(__float_as_uint(x), __float_as_uint(x), false, false);
  return fmaxf(__uint_as_float(r[0]), __uint_as_float(r[1]));
}
__device__ __forceinline__ float swap_sum(float x) {
  auto r = __builtin_amdgcn_permlane32_swap(__float_as_uint(x), __float_as_uint(x), false, false);
  return __uint_as_float(r[0]) + __uint_as_float(r[1]);
}
__device__ __forceinline__ unsigned pack2(float a, float b) {
  bf16x2 v = {(__bf16)a, (__bf16)b};
  return __builtin_bit_cast(unsigned, v);
}
template <int CTRL> __device__ __forceinline__ int dpp_add(int x) {
  return x + __builtin_amdgcn_update_dpp(0, x, CTRL, 0xf, 0xf, true);
}
__device__ __forceinline__ unsigned now32() { return (unsigned)__builtin_amdgcn_s_memrealtime(); }
#define LGKM0() asm volatile("s_waitcnt lgkmcnt(0)" ::: "memory")
#define VMWAIT(n) asm volatile("s_waitcnt vmcnt(" #n ")" ::: "memory")
#define CBAR() asm volatile("" ::: "memory")
#define SPIN(cond) for (int sp_ = 0; (cond) && sp_ < (1 << 18); ++sp_)

__global__ void __launch_bounds__(384, 1)
paged_attn(const void* __restrict__ qp, const void* __restrict__ kp, const void* __restrict__ vp, const int* __restrict__ bt,
           const int* __restrict__ lens, void* __restrict__ outp, char* __restrict__ ws, unsigned kvbytes, unsigned nphys) {
  extern __shared__ __attribute__((aligned(16))) char smem[];
  LAS char* lds = (LAS char*)smem;
  const int lane = threadIdx.x & 63;
  const int wv = __builtin_amdgcn_readfirstlane(threadIdx.x >> 6);
  const int g = blockIdx.x;
  const int b = MAPPING == 1 ? 2 * (g & 7) + ((g >> 3) & 1) : (g >> 4);
  const int h = MAPPING == 1 ? (g >> 4) : 2 * (g & 7) + ((g >> 3) & 1);
  constexpr float CS = 0.08838834764831845f * 1.4426950408889634f;
  volatile LAS unsigned* ctl = (volatile LAS unsigned*)(lds + LDS_CTL);

  const int lraw = lens[lane & 15];
  if (wv < 2) {
    __builtin_amdgcn_s_setprio(3);
    const __amdgpu_buffer_rsrc_t rb = __builtin_amdgcn_make_buffer_rsrc((void*)bt, 0, NB * NBPS * 4, 0x00020000);
    const unsigned tb = (unsigned)(size_t)lds + LDS_ACC, vo = lane * 16;
#pragma unroll
    for (int i = 0; i < 4; ++i) DMA4("", rb, tb + i * 4096, vo + i * 4096, vo + i * 4096 + 1024, vo + i * 4096 + 2048, vo + i * 4096 + 3072);
  } else if (wv == 2) {
    if (lane < 16) ctl[lane] = 0u;
  }
  LGKM0();
  __builtin_amdgcn_s_barrier();
  CBAR();

  const int lcl = (lraw <= 0 || lraw > SKV) ? SKV : lraw;
  const int np = (lcl + PAGE - 1) / PAGE;
  int tot = np;
  tot = dpp_add<0x121>(tot); tot = dpp_add<0x122>(tot); tot = dpp_add<0x124>(tot); tot = dpp_add<0x128>(tot);
  const int S = BALANCE ? __builtin_amdgcn_readfirstlane((tot + NB - 1) / NB) : NBPS;
  const int ex = max(np - S, 0), df = max(S - np, 0);
  int pk = ex | (df << 16);
  const int pk0 = pk;
  pk = dpp_add<0x111>(pk); pk = dpp_add<0x112>(pk); pk = dpp_add<0x114>(pk); pk = dpp_add<0x118>(pk);
  pk -= pk0;
  const int E = pk & 0xffff, Dp = pk >> 16;
  const int dlo = __builtin_amdgcn_readlane(Dp, b), dhi = dlo + __builtin_amdgcn_readlane(df, b);
  const int elo = __builtin_amdgcn_readlane(E, b), ehi = elo + __builtin_amdgcn_readlane(ex, b);
  const int lo_c = max(E, dlo);
  int pn = max(min(E + ex, dhi) - lo_c, 0);
  int pp = S + lo_c - E;
  const unsigned helpmask = (unsigned)__builtin_amdgcn_ballot_w64(pn > 0) & 0xffffu;
  if ((lane & 15) == b) { pn = min(np, S); pp = 0; }
  const unsigned gathermask = (unsigned)__builtin_amdgcn_ballot_w64(min(Dp + df, ehi) > max(Dp, elo)) & 0xffffu;

  if (wv < 2) {
    int idx[NLR];
#pragma unroll
    for (int r = 0; r < NLR; ++r) idx[r] = -1;
    int nent = 0;
    for (unsigned mk = helpmask | 0x10000u; mk; mk &= mk - 1) {
      int c = __builtin_ctz(mk);
      c = (c == 16) ? b : c;
      const int n = __builtin_amdgcn_readlane(pn, c), base = c * NBPS + __builtin_amdgcn_readlane(pp, c) - nent;
#pragma unroll
      for (int r = 0; r < NLR; ++r) {
        const int e = lane + 64 * r;
        if ((unsigned)(e - nent) < (unsigned)n) idx[r] = base + e;
      }
      nent += (n + 1) & ~1;
    }
    VMWAIT(0);
    unsigned lst[NLR];
#pragma unroll
    for (int r = 0; r < NLR; ++r) lst[r] = *(LAS unsigned*)(lds + LDS_ACC + max(idx[r], 0) * 4);
#pragma unroll
    for (int r = 0; r < NLR; ++r) lst[r] = (idx[r] >= 0 && lst[r] < nphys) ? lst[r] * PAGE_BYTES + h * (HD * 2) : INVALID;

    const __amdgpu_buffer_rsrc_t rs = __builtin_amdgcn_make_buffer_rsrc(wv == 0 ? (void*)kp : (void*)vp, 0, (int)kvbytes, 0x00020000);
    const unsigned rq4 = lane >> 4;
    const unsigned chs = ((unsigned)(lane & 15) ^ (rq4 << 2)) << 4;
    const unsigned kx = (wv == 0) ? 16u : 0u;
    const unsigned vo0 = rq4 * 4096 + chs, vo1 = (4 + rq4) * 4096 + (chs ^ kx), vo2 = (8 + rq4) * 4096 + (chs ^ (2 * kx)),
                   vo3 = (12 + rq4) * 4096 + (chs ^ (3 * kx));
    const unsigned ring = (unsigned)(size_t)lds + LDS_RING + wv * 8192;
    const unsigned vland = (unsigned)(size_t)lds + LDS_CTL + CTL_LAND + wv * 4;
    const unsigned vfree = (unsigned)(size_t)lds + LDS_CTL + CTL_FREE + wv * 16 + (lane & 3) * 4;
    STAMP(1);
    const unsigned t1 = now32();
    int due = -LEAD_X16 + wv * VLAG_X16;
    unsigned va0, va1, va2, va3, vf, vt;
    asm volatile(
        "s_waitcnt vmcnt(0) lgkmcnt(0)\n\t"
        "s_mov_b32 s60, 0\n\t"
        "s_mov_b32 s61, 0\n\t"
        "s_mov_b32 s62, 0\n\t"
        "s_nop 4\n"
        ".LldT%=:\n\t"
        "s_memrealtime s[68:69]\n\t"
        "ds_read_b32 %[vf], %[vfree]\n\t"
        "s_getreg_b32 s64, hwreg(HW_REG_IB_STS)\n\t"
        "v_readlane_b32 s63, %[l0], s60\n\t"
        "s_and_b32 s65, s64, 15\n\t"
        "s_lshr_b32 s64, s64, 18\n\t"
        "s_and_b32 s64, s64, 48\n\t"
        "s_or_b32 s64, s64, s65\n\t"
        "s_lshl_b32 s65, s60, 2\n\t"
        "s_sub_u32 s64, s65, s64\n\t"
        "s_lshr_b32 s64, s64, 3\n\t"
        "s_cmp_gt_u32 s64, s61\n\t"
        "s_cbranch_scc1 .LldP%=\n"
        ".LldB%=:\n\t"
        "s_cmp_ge_i32 s60, %[nent]\n\t"
        "s_cbranch_scc1 .LldD%=\n\t"
        "v_add_u32 %[a0], s63, %[o0]\n\t"
        "v_add_u32 %[a1], s63, %[o1]\n\t"
        "v_add_u32 %[a2], s63, %[o2]\n\t"
        "v_add_u32 %[a3], s63, %[o3]\n\t"
        "s_bfe_u32 s64, s60, 0x20001\n\t"
        "s_lshl_b32 s65, s64, 15\n\t"
        "s_add_u32 s65, s65, %[ring]\n\t"
        "s_and_b32 s66, s60, 8\n\t"
        "s_lshl_b32 s66, s66, 11\n\t"
        "s_add_u32 s65, s65, s66\n\t"
        "s_and_b32 s66, s60, 1\n\t"
        "s_lshl_b32 s66, s66, 12\n\t"
        "s_add_u32 m0, s65, s66\n\t"
        "s_lshr_b32 s66, s60, 3\n\t"
        "s_cmp_eq_u32 s63, 0x80000000\n\t"
        "s_cselect_b32 s67, 0x80000000, %[due]\n\t"
        "s_cselect_b32 s65, 0, %[pace]\n\t"
        "s_waitcnt lgkmcnt(0)\n\t"
        "v_readlane_b32 s64, %[vf], s64\n\t"
        "s_sub_u32 s68, s68, %[t1]\n\t"
        "s_lshl_b32 s68, s68, 4\n\t"
        "s_cmp_lt_i32 s68, s67\n\t"
        "s_cbranch_scc1 .LldR%=\n\t"
        "s_add_u32 s64, s64, 1\n\t"
        "s_cmp_lt_u32 s64, s66\n\t"
        "s_cbranch_scc1 .LldR%=\n\t"
        "s_waitcnt vmcnt(56)\n\t"
        "buffer_load_dwordx4 %[a0], %[rs], 0 offen nt lds\n\t"
        "s_add_u32 m0, m0, 1024\n\t"
        "s_add_u32 %[due], %[due], s65\n\t"
        "buffer_load_dwordx4 %[a1], %[rs], 0 offen nt lds\n\t"
        "s_add_u32 m0, m0, 1024\n\t"
        "s_add_u32 s60, s60, 1\n\t"
        "buffer_load_dwordx4 %[a2], %[rs], 0 offen nt lds\n\t"
        "s_add_u32 m0, m0, 1024\n\t"
        "s_and_b32 s64, s60, 63\n\t"
        "buffer_load_dwordx4 %[a3], %[rs], 0 offen nt lds\n\t"
        "s_cbranch_scc1 .LldT%=\n\t"
        "v_mov_b32 %[l0], %[l1]\n\t"
        "v_mov_b32 %[l1], %[l2]\n\t"
        "v_mov_b32 %[l2], %[l3]\n\t"
        "v_mov_b32 %[l3], %[l4]\n\t"
        "s_branch .LldT%=\n"
        ".LldP%=:\n\t"
        "s_lshl_b32 s66, s64, 3\n\t"
        "s_sub_u32 s66, s65, s66\n\t"
        "s_min_u32 s66, s66, 64\n\t"
        "s_lshl_b32 s66, s66, 1\n\t"
        "s_add_u32 s66, s66, 12\n\t"
        "s_getpc_b64 s[70:71]\n\t"
        "s_add_u32 s70, s70, s66\n\t"
        "s_addc_u32 s71, s71, 0\n\t"
        "s_setpc_b64 s[70:71]\n\t"
        "s_waitcnt vmcnt(0)\n\ts_branch .LldQ%=\n\t"
        "s_waitcnt vmcnt(4)\n\ts_branch .LldQ%=\n\t"
        "s_waitcnt vmcnt(8)\n\ts_branch .LldQ%=\n\t"
        "s_waitcnt vmcnt(12)\n\ts_branch .LldQ%=\n\t"
        "s_waitcnt vmcnt(16)\n\ts_branch .LldQ%=\n\t"
        "s_waitcnt vmcnt(20)\n\ts_branch .LldQ%=\n\t"
        "s_waitcnt vmcnt(24)\n\ts_branch .LldQ%=\n\t"
        "s_waitcnt vmcnt(28)\n\ts_branch .LldQ%=\n\t"
        "s_waitcnt vmcnt(32)\n\ts_branch .LldQ%=\n\t"
        "s_waitcnt vmcnt(36)\n\ts_branch .LldQ%=\n\t"
        "s_waitcnt vmcnt(40)\n\ts_branch .LldQ%=\n\t"
        "s_waitcnt vmcnt(44)\n\ts_branch .LldQ%=\n\t"
        "s_waitcnt vmcnt(48)\n\ts_branch .LldQ%=\n\t"
        "s_waitcnt vmcnt(52)\n\ts_branch .LldQ%=\n\t"
        "s_waitcnt vmcnt(56)\n\ts_branch .LldQ%=\n\t"
        "s_waitcnt vmcnt(60)\n\ts_branch .LldQ%=\n\t"
        "s_waitcnt vmcnt(60)\n"
        ".LldQ%=:\n\t"
        "s_mov_b32 s61, s64\n\t"
        "v_mov_b32 %[vt], s64\n\t"
        "ds_write_b32 %[vland], %[vt]\n\t"
        "s_branch .LldB%=\n"
        ".LldD%=:\n\t"
        "s_waitcnt lgkmcnt(0)\n\t"
        "s_lshr_b32 s64, %[nent], 1\n\t"
        "s_cmp_ge_u32 s61, s64\n\t"
        "s_cbranch_scc1 .LldE%=\n"
        ".LldR%=:\n\t"
        LSLEEP
        "s_add_u32 s62, s62, 1\n\t"
        "s_cmp_lt_u32 s62, 0x400000\n\t"
        "s_cbranch_scc1 .LldT%=\n"
        ".LldE%=:\n\t"
        "s_waitcnt vmcnt(0) lgkmcnt(0)"
        : [a0] "=&v"(va0), [a1] "=&v"(va1), [a2] "=&v"(va2), [a3] "=&v"(va3), [vf] "=&v"(vf), [vt] "=&v"(vt),
          [l0] "+v"(lst[0]), [l1] "+v"(lst[1]), [l2] "+v"(lst[2]), [l3] "+v"(lst[3]), [due] "+s"(due)
        : [l4] "v"(lst[4]), [o0] "v"(vo0), [o1] "v"(vo1), [o2] "v"(vo2), [o3] "v"(vo3), [vland] "v"(vland), [vfree] "v"(vfree),
          [rs] "s"(rs), [nent] "s"(nent), [ring] "s"(ring), [t1] "s"(t1), [pace] "s"(PACE_X16)
        : "memory", "scc", "s60", "s61", "s62", "s63", "s64", "s65", "s66", "s67", "s68", "s69", "s70", "s71");
    STAMP(3);
    return;
  }

  const int w = wv - 2;
  const int r32 = lane & 31, hi = lane >> 5;
  const unsigned fk = (((unsigned)r32 & 3) << 2) | (((unsigned)r32 >> 2) & 3);
  const unsigned kaddr0 = LDS_RING + w * 32768 + r32 * 256 + (((unsigned)hi ^ fk) << 4);
  const unsigned tq = (lane & 15) >> 2, tp = lane & 3, tblk = (lane >> 4) & 1;
  const unsigned vaddr0 = LDS_RING + w * 32768 + 8192 + (4 * hi + tq) * 256 + (tq << 6) + ((2 * tblk + (tp >> 1)) << 4) + ((tp & 1) << 3);
  const __amdgpu_buffer_rsrc_t rq = __builtin_amdgcn_make_buffer_rsrc((void*)qp, 0, NB * HQ * SQ * HD * 2, 0x00020000);
  const __amdgpu_buffer_rsrc_t rw = __builtin_amdgcn_make_buffer_rsrc((void*)ws, 0, (int)WS_NEED, 0x00020000);

  int T0 = 0;
  unsigned chain0 = 0;
  for (unsigned mk = helpmask | 0x10000u; mk; mk &= mk - 1) {
    const int cc = __builtin_ctz(mk);
    const bool own = cc == 16;
    const int c = own ? b : cc;
    const int n = __builtin_amdgcn_readlane(pn, c), p0 = __builtin_amdgcn_readlane(pp, c);
    const int T1 = T0 + ((n + 1) >> 1);
    const int npart = min(4, T1 - T0);
    const int tfirst = T0 + ((w - T0) & 3);
    if (tfirst < T1) {
      const int lim = min(__builtin_amdgcn_readlane(lcl, c), (p0 + n) * PAGE);
      const int tlast = T1 - 1 - ((T1 - 1 - w) & 3);
      const int pos = tlast - (T1 - npart);
      bf16x8 qf[8];
      {
        const unsigned qo = ((__builtin_amdgcn_readlane(lraw, c) > 0) ? (unsigned)((c * HQ + 2 * h) * SQ * HD * 2) : INVALID) + r32 * 256 + hi * 16;
#pragma unroll
        for (int s = 0; s < 8; ++s) qf[s] = __builtin_bit_cast(bf16x8, __builtin_amdgcn_raw_buffer_load_b128(rq, qo + s * 32, 0, 0));
      }
      f32x16 o[4];
#pragma unroll
      for (int i = 0; i < 4; ++i)
#pragma unroll
        for (int r = 0; r < 16; ++r) o[i][r] = 0.f;
      float m = -3.0e38f, l = 0.f;
      unsigned gm = 0;
      if (own) {
        int j = 0;
        for (unsigned x = gathermask; x; x &= x - 1, ++j)
          if (j % npart == pos) gm |= x & (0u - x);
      }
      const int tgather = T0 + ((T1 - T0) >> 1);

      for (int t = tfirst; t <= tlast; t += 4) {
        const unsigned so = ((unsigned)t & 4u) << 12;
        SPIN(__builtin_amdgcn_readfirstlane(ctl[CTL_LAND / 4]) <= (unsigned)t) __builtin_amdgcn_s_sleep(CSLEEP);
        CBAR();
        if (own && t < 4) STAMP(1);
        bf16x8 kf[8];
#pragma unroll
        for (int s = 0; s < 8; ++s) kf[s] = *(LAS bf16x8*)(lds + ((kaddr0 + so) ^ (s << 5)));
        LGKM0();
        ctl[CTL_FREE / 4 + w] = ((unsigned)t >> 2) + 1;
        f32x16 sc;
#pragma unroll
        for (int r = 0; r < 16; ++r) sc[r] = 0.f;
#pragma unroll
        for (int s = 0; s < 8; ++s) sc = __builtin_amdgcn_mfma_f32_32x32x16_bf16(kf[s], qf[s], sc, 0, 0, 0);
        if (t == T1 - 1) {
          const int kb = (p0 + 2 * (t - T0)) * PAGE + 4 * hi;
#pragma unroll
          for (int r = 0; r < 16; ++r) sc[r] = (kb + 8 * (r >> 2) + (r & 3) < lim) ? sc[r] : -__builtin_inff();
        }
        float mx = sc[0];
#pragma unroll
        for (int r = 1; r < 16; ++r) mx = fmaxf(mx, sc[r]);
        mx = swap_max(mx);
        const float mn = fmaxf(m, mx);
        const float alpha = __builtin_amdgcn_exp2f((m - mn) * CS);
        m = mn;
        const float nmc = -mn * CS;
        float ps = 0.f;
#pragma unroll
        for (int r = 0; r < 16; ++r) { sc[r] = __builtin_amdgcn_exp2f(fmaf(sc[r], CS, nmc)); ps += sc[r]; }
        l = fmaf(l, alpha, ps);
        bf16x8 pf[2];
#pragma unroll
        for (int s = 0; s < 2; ++s) {
          u32x4 u = {pack2(sc[8 * s], sc[8 * s + 1]), pack2(sc[8 * s + 2], sc[8 * s + 3]), pack2(sc[8 * s + 4], sc[8 * s + 5]), pack2(sc[8 * s + 6], sc[8 * s + 7])};
          pf[s] = __builtin_bit_cast(bf16x8, u);
        }
#pragma unroll
        for (int i = 0; i < 4; ++i)
#pragma unroll
          for (int r = 0; r < 16; ++r) o[i][r] *= alpha;

        SPIN(__builtin_amdgcn_readfirstlane(ctl[CTL_LAND / 4 + 1]) <= (unsigned)t) __builtin_amdgcn_s_sleep(CSLEEP);
        CBAR();
        s16x4 vf[4][2][2];
#pragma unroll
        for (int i = 0; i < 4; ++i) {
          const unsigned a = (vaddr0 + so) ^ (i << 6);
#pragma unroll
          for (int s = 0; s < 2; ++s)
#pragma unroll
            for (int u = 0; u < 2; ++u)
              vf[i][s][u] = __builtin_amdgcn_ds_read_tr16_b64_v4i16((LAS s16x4*)(lds + a + 4096 * s + 2048 * u));
        }
        LGKM0();
        ctl[CTL_FREE / 4 + 4 + w] = ((unsigned)t >> 2) + 1;
#pragma unroll
        for (int i = 0; i < 4; ++i)
#pragma unroll
          for (int s = 0; s < 2; ++s) {
            const bf16x8 va = __builtin_bit_cast(bf16x8, __builtin_shufflevector(vf[i][s][0], vf[i][s][1], 0, 1, 2, 3, 4, 5, 6, 7));
            o[i] = __builtin_amdgcn_mfma_f32_32x32x16_bf16(va, pf[s], o[i], 0, 0, 0);
          }

        while (gm != 0 && (t >= tgather || t == tlast)) {
          const int hb = __builtin_ctz(gm);
          const unsigned slot = (unsigned)((h * NB + hb) * NB + b) * SLOT_BYTES;
          GAS unsigned long long* epw = (GAS unsigned long long*)(ws + slot + SLOT_EPOCH);
          GAS unsigned long long* flw = (GAS unsigned long long*)(ws + slot + SLOT_FLAG);
          const unsigned long long ep = __hip_atomic_load(epw, __ATOMIC_RELAXED, __HIP_MEMORY_SCOPE_AGENT);
          bool ok = __hip_atomic_load(flw, __ATOMIC_RELAXED, __HIP_MEMORY_SCOPE_AGENT) == ep + 1;
          if (t == tlast) {
            const unsigned ts = now32();
            while (!__builtin_amdgcn_readfirstlane(ok)) {
              __builtin_amdgcn_s_sleep(2);
              ok = __hip_atomic_load(flw, __ATOMIC_RELAXED, __HIP_MEMORY_SCOPE_AGENT) == ep + 1;
              if (now32() - ts > 5000000u) ok = true;
            }
          }
          if (!__builtin_amdgcn_readfirstlane(ok)) break;
          __builtin_amdgcn_fence(__ATOMIC_ACQUIRE, "agent");
          if (lane == 0) __hip_atomic_store(epw, ep + 1, __ATOMIC_RELAXED, __HIP_MEMORY_SCOPE_AGENT);
          gm &= gm - 1;
          const u32x2 y = __builtin_amdgcn_raw_buffer_load_b64(rw, slot + SLOT_ML + lane * 8, 0, 16);
          u32x4 x[16];
#pragma unroll
          for (int j = 0; j < 16; ++j) x[j] = __builtin_amdgcn_raw_buffer_load_b128(rw, slot + j * 1024 + lane * 16, 0, 16);
          const float mp = __uint_as_float(y[0]), lp = __uint_as_float(y[1]);
          const float mn2 = fmaxf(m, mp);
          const float a0 = __builtin_amdgcn_exp2f((m - mn2) * CS), a1 = __builtin_amdgcn_exp2f((mp - mn2) * CS);
          l = l * a0 + lp * a1;
          m = mn2;
#pragma unroll
          for (int j = 0; j < 16; ++j)
#pragma unroll
            for (int e = 0; e < 4; ++e) o[j >> 2][4 * (j & 3) + e] = o[j >> 2][4 * (j & 3) + e] * a0 + __uint_as_float(x[j][e]) * a1;
          if (t != tlast) break;
        }
      }
      if (own) STAMP(2);

      SPIN(__builtin_amdgcn_readfirstlane(ctl[CTL_CHAIN / 4]) != chain0 + pos) __builtin_amdgcn_s_sleep(CSLEEP);
      CBAR();
      if (own) STAMP(3);
      if (pos > 0) {
        const f32x2 y = *(LAS f32x2*)(lds + LDS_ACC + 16384 + lane * 8);
        const float mn2 = fmaxf(m, y[0]);
        const float a0 = __builtin_amdgcn_exp2f((m - mn2) * CS), a1 = __builtin_amdgcn_exp2f((y[0] - mn2) * CS);
        l = l * a0 + y[1] * a1;
        m = mn2;
#pragma unroll
        for (int j = 0; j < 16; ++j) {
          const f32x4 x = *(LAS f32x4*)(lds + LDS_ACC + j * 1024 + lane * 16);
#pragma unroll
          for (int e = 0; e < 4; ++e) o[j >> 2][4 * (j & 3) + e] = o[j >> 2][4 * (j & 3) + e] * a0 + x[e] * a1;
        }
      }
      if (pos < npart - 1) {
        *(LAS f32x2*)(lds + LDS_ACC + 16384 + lane * 8) = f32x2{m, l};
#pragma unroll
        for (int j = 0; j < 16; ++j)
          *(LAS f32x4*)(lds + LDS_ACC + j * 1024 + lane * 16) =
              f32x4{o[j >> 2][4 * (j & 3)], o[j >> 2][4 * (j & 3) + 1], o[j >> 2][4 * (j & 3) + 2], o[j >> 2][4 * (j & 3) + 3]};
      }
      LGKM0();
      ctl[CTL_CHAIN / 4] = chain0 + pos + 1;
      if (pos == npart - 1) {
        if (!own) {
          const unsigned slot = (unsigned)((h * NB + b) * NB + c) * SLOT_BYTES;
          GAS unsigned long long* epw = (GAS unsigned long long*)(ws + slot + SLOT_EPOCH);
          GAS unsigned long long* flw = (GAS unsigned long long*)(ws + slot + SLOT_FLAG);
          const unsigned long long ep = __hip_atomic_load(epw, __ATOMIC_RELAXED, __HIP_MEMORY_SCOPE_AGENT);
#pragma unroll
          for (int j = 0; j < 16; ++j) {
            u32x4 x = {__float_as_uint(o[j >> 2][4 * (j & 3)]), __float_as_uint(o[j >> 2][4 * (j & 3) + 1]),
                       __float_as_uint(o[j >> 2][4 * (j & 3) + 2]), __float_as_uint(o[j >> 2][4 * (j & 3) + 3])};
            __builtin_amdgcn_raw_buffer_store_b128(x, rw, slot + j * 1024 + lane * 16, 0, 16);
          }
          u32x2 y = {__float_as_uint(m), __float_as_uint(l)};
          __builtin_amdgcn_raw_buffer_store_b64(y, rw, slot + SLOT_ML + lane * 8, 0, 16);
          VMWAIT(0);
          if (lane == 0) __hip_atomic_store(flw, ep + 1, __ATOMIC_RELAXED, __HIP_MEMORY_SCOPE_AGENT);
        } else {
          STAMP(4);
          const float inv = 1.0f / swap_sum(l);
          char* orow = (char*)outp + (size_t)((b * HQ + 2 * h) * SQ + r32) * HD * 2 + hi * 16;
#pragma unroll
          for (int i = 0; i < 4; ++i)
#pragma unroll
            for (int j = 0; j < 2; ++j) {
              const unsigned a0 = pack2(o[i][8 * j] * inv, o[i][8 * j + 1] * inv), a1 = pack2(o[i][8 * j + 2] * inv, o[i][8 * j + 3] * inv);
              const unsigned b0 = pack2(o[i][8 * j + 4] * inv, o[i][8 * j + 5] * inv), b1 = pack2(o[i][8 * j + 6] * inv, o[i][8 * j + 7] * inv);
              auto s0 = __builtin_amdgcn_permlane32_swap(a0, b0, false, false);
              auto s1 = __builtin_amdgcn_permlane32_swap(a1, b1, false, false);
              u32x4 val = {s0[0], s1[0], s0[1], s1[1]};
              *(u32x4*)(orow + i * 64 + j * 32) = val;
            }
        }
      }
    }
    T0 = T1;
    chain0 += npart;
  }
}
}

extern "C" void kernel_launch(void* const* d_in, const int* in_sizes, int n_in, void* d_out, int out_size, void* d_ws,
                              size_t ws_size, hipStream_t stream) {
  (void)n_in; (void)out_size; (void)ws_size;
  const unsigned nphys = (unsigned)(in_sizes[1] / (PAGE * HKV * HD));
  const unsigned kvbytes = nphys * PAGE_BYTES;
  (void)hipFuncSetAttribute((const void*)paged_attn, hipFuncAttributeMaxDynamicSharedMemorySize, LDS_TOTAL);
  hipLaunchKernelGGL(paged_attn, dim3(256), dim3(384), LDS_TOTAL, stream, d_in[0], d_in[1], d_in[2], (const int*)d_in[3],
                     (const int*)d_in[4], d_out, (char*)d_ws, kvbytes, nphys);
}
```

```cpp
#define PACE_X16 455
#define WINDOW 20
#define SPLIT_X64 72
#include <hip/hip_runtime.h>
#include <hip/hip_bf16.h>

#define BALANCE 1
#define LEAD_X16 0
#define VLAG_X16 (70 * 16)
#define LSLEEP ""
#define CSLEEP 1
#define ACQ_SCOPE "agent"
#define NSTORE 16
#define NLOAD 16
#define STOREAUX 16
#define LOADAUX 16
#define POLLSLEEP 16
#define GDELAY 0
#define STSLEEP 0
#define MAPPING 0
#define PUB_SYNC 1
#define PUB_MARGIN 450

#define STAMP(i) do {} while (0)

namespace {
constexpr int NB = 16, HQ = 32, HKV = 16, SQ = 16, HD = 128, PAGE = 16, NBPS = 256, SKV = NBPS * PAGE;
constexpr unsigned PAGE_BYTES = PAGE * HKV * HD * 2;
constexpr unsigned INVALID = 0x80000000u;
constexpr int NLR = 5;
constexpr unsigned LDS_RING = 0;
constexpr unsigned LDS_ACC = 131072;
constexpr unsigned LDS_CTL = LDS_ACC + 16384 + 512;
constexpr unsigned CTL_LAND = 0;
constexpr unsigned CTL_FREE = 16;
constexpr unsigned CTL_CHAIN = 48;
constexpr unsigned LDS_TOTAL = LDS_CTL + 64;
static_assert(LDS_TOTAL <= 163840, "lds");
constexpr unsigned SLOT_BYTES = 20480, SLOT_ML = 16384, SLOT_FLAG = 16896, SLOT_EPOCH = 17024;
constexpr size_t WS_NEED = (size_t)HKV * (NB / 2) * SLOT_BYTES;

typedef __attribute__((ext_vector_type(8))) __bf16 bf16x8;
typedef __attribute__((ext_vector_type(2))) __bf16 bf16x2;
typedef __attribute__((ext_vector_type(4))) short s16x4;
typedef __attribute__((ext_vector_type(16))) float f32x16;
typedef __attribute__((ext_vector_type(4))) float f32x4;
typedef __attribute__((ext_vector_type(2))) float f32x2;
typedef __attribute__((ext_vector_type(4))) unsigned u32x4;
typedef __attribute__((ext_vector_type(2))) unsigned u32x2;
typedef __attribute__((ext_vector_type(4))) int i32x4;
#define LAS __attribute__((address_space(3)))
#define GAS __attribute__((address_space(1)))

#define DMA4(POL, rs, m0v, v0, v1, v2, v3)                                                               \
  asm volatile("s_mov_b32 m0, %0\n\ts_nop 4\n\tbuffer_load_dwordx4 %2, %1, 0 offen " POL " lds\n\t"       \
               "s_add_u32 m0, m0, 1024\n\ts_nop 0\n\tbuffer_load_dwordx4 %3, %1, 0 offen " POL " lds\n\t" \
               "s_add_u32 m0, m0, 1024\n\ts_nop 0\n\tbuffer_load_dwordx4 %4, %1, 0 offen " POL " lds\n\t" \
               "s_add_u32 m0, m0, 1024\n\ts_nop 0\n\tbuffer_load_dwordx4 %5, %1, 0 offen " POL " lds"     \
               :: "s"(m0v), "s"(rs), "v"(v0), "v"(v1), "v"(v2), "v"(v3) : "memory", "scc")

__device__ __forceinline__ float swap_max(float x) {
  auto r = __builtin_amdgcn_permlane32_swap(__float_as_uint(x), __float_as_uint(x), false, false);
  return fmaxf(__uint_as_float(r[0]), __uint_as_float(r[1]));
}
__device__ __forceinline__ float swap_sum(float x) {
  auto r = __builtin_amdgcn_permlane32_swap(__float_as_uint(x), __float_as_uint(x), false, false);
  return __uint_as_float(r[0]) + __uint_as_float(r[1]);
}
__device__ __forceinline__ unsigned pack2(float a, float b) {
  bf16x2 v = {(__bf16)a, (__bf16)b};
  return __builtin_bit_cast(unsigned, v);
}
template <int CTRL> __device__ __forceinline__ int dpp_add(int x) {
  return x + __builtin_amdgcn_update_dpp(0, x, CTRL, 0xf, 0xf, true);
}
__device__ __forceinline__ unsigned now32() { return (unsigned)__builtin_amdgcn_s_memrealtime(); }
#define LGKM0() asm volatile("s_waitcnt lgkmcnt(0)" ::: "memory")
#define VMWAIT(n) asm volatile("s_waitcnt vmcnt(" #n ")" ::: "memory")
#define CBAR() asm volatile("" ::: "memory")
#define SPIN(cond) for (int sp_ = 0; (cond) && sp_ < (1 << 18); ++sp_)

__global__ void __launch_bounds__(448, 1)
paged_attn(const void* __restrict__ qp, const void* __restrict__ kp, const void* __restrict__ vp, const int* __restrict__ bt,
           const int* __restrict__ lens, void* __restrict__ outp, char* __restrict__ ws, unsigned kvbytes, unsigned nphys) {
  extern __shared__ __attribute__((aligned(16))) char smem[];
  LAS char* lds = (LAS char*)smem;
  const int lane = threadIdx.x & 63;
  const int wv = __builtin_amdgcn_readfirstlane(threadIdx.x >> 6);
  const int g = blockIdx.x;
  const int x = g & 7, h = MAPPING == 0 ? g >> 4 : MAPPING == 1 ? 2 * (g & 7) + ((g >> 3) & 1) : MAPPING == 2 ? ((g & 7) >> 1) * 4 + ((g >> 3) & 3) : (g & 1) * 8 + ((g >> 3) & 7);
  const bool shortside = (g >> 3) & 1;
  constexpr float CS = 0.08838834764831845f * 1.4426950408889634f;
  volatile LAS unsigned* ctl = (volatile LAS unsigned*)(lds + LDS_CTL);

  const int lraw = lens[lane & 15];
  const i32x4 lq = *(const i32x4*)(lens + (lane >> 4) * 4);
  if (wv < 2) {
    __builtin_amdgcn_s_setprio(3);
    const __amdgpu_buffer_rsrc_t rb = __builtin_amdgcn_make_buffer_rsrc((void*)bt, 0, NB * NBPS * 4, 0x00020000);
    const unsigned tb = (unsigned)(size_t)lds + LDS_ACC, vo = lane * 16;
#pragma unroll
    for (int i = 0; i < 4; ++i) DMA4("", rb, tb + i * 4096, vo + i * 4096, vo + i * 4096 + 1024, vo + i * 4096 + 2048, vo + i * 4096 + 3072);
  } else if (wv == 2) {
    if (lane < 16) ctl[lane] = 0u;
  }
  LGKM0();
  __builtin_amdgcn_s_barrier();
  CBAR();

  const int lcl = (lraw <= 0 || lraw > SKV) ? SKV : lraw;
  const int np = (lcl + PAGE - 1) / PAGE;
  int rank = 0;
  {
    const int key = lcl * 16 + (lane & 15);
#pragma unroll
    for (int j = 0; j < 4; ++j) {
      const int lj = (lq[j] <= 0 || lq[j] > SKV) ? SKV : lq[j];
      rank += (lj * 16 + (lane >> 4) * 4 + j > key) ? 1 : 0;
    }
    auto r1 = __builtin_amdgcn_permlane16_swap((unsigned)rank, (unsigned)rank, false, false);
    rank = (int)(r1[0] + r1[1]);
    auto r2 = __builtin_amdgcn_permlane32_swap((unsigned)rank, (unsigned)rank, false, false);
    rank = (int)(r2[0] + r2[1]);
  }
  int tot = np;
  tot = dpp_add<0x121>(tot); tot = dpp_add<0x122>(tot); tot = dpp_add<0x124>(tot); tot = dpp_add<0x128>(tot);
  tot = __builtin_amdgcn_readfirstlane(tot);
  const int bl = __builtin_ctz((unsigned)__builtin_amdgcn_ballot_w64(rank == x) | 0x8000u), bs = __builtin_ctz((unsigned)__builtin_amdgcn_ballot_w64(rank == 15 - x) | 0x8000u);
  const int nl = __builtin_amdgcn_readlane(np, bl), ns = __builtin_amdgcn_readlane(np, bs);
  const int mv = (BALANCE && nl * 16 * 64 > tot * SPLIT_X64) ? (nl - ns) >> 1 : 0;
  const int b = MAPPING == 0 ? (shortside ? bs : bl) : MAPPING == 1 ? (g >> 4) : MAPPING == 2 ? (g >> 5) * 2 + (g & 1) : (g >> 6) * 4 + ((g & 7) >> 1);
  const int nhelp = shortside ? mv : 0;
  const int nown = MAPPING ? __builtin_amdgcn_readlane(np, b) : shortside ? ns : nl - mv;
  const bool gather = !shortside && mv > 0;
  const int TH = (nhelp + 1) >> 1;
  const unsigned slot = (unsigned)(h * (NB / 2) + x) * SLOT_BYTES;

  if (wv < 2) {
    int idx[NLR];
    const int nent = 2 * TH + ((nown + 1) & ~1);
#pragma unroll
    for (int r = 0; r < NLR; ++r) {
      const int e = lane + 64 * r, k = e - 2 * TH;
      idx[r] = (e < nhelp) ? bl * NBPS + nl - mv + e : ((unsigned)k < (unsigned)nown ? b * NBPS + k : -1);
    }
    int pace;
    asm volatile("s_nop 1\n\tv_readfirstlane_b32 %0, %1" : "=s"(pace) : "v"((int)((float)(tot * PACE_X16) * __builtin_amdgcn_rcpf((float)(nhelp + nown)))));
    VMWAIT(0);
    unsigned lst[NLR];
#pragma unroll
    for (int r = 0; r < NLR; ++r) lst[r] = *(LAS unsigned*)(lds + LDS_ACC + max(idx[r], 0) * 4);
#pragma unroll
    for (int r = 0; r < NLR; ++r) lst[r] = (idx[r] >= 0 && lst[r] < nphys) ? lst[r] * PAGE_BYTES + h * (HD * 2) : INVALID;

    const __amdgpu_buffer_rsrc_t rs = __builtin_amdgcn_make_buffer_rsrc(wv == 0 ? (void*)kp : (void*)vp, 0, (int)kvbytes, 0x00020000);
    const unsigned rq4 = lane >> 4;
    const unsigned chs = ((unsigned)(lane & 15) ^ (rq4 << 2)) << 4;
    const unsigned kx = (wv == 0) ? 16u : 0u;
    const unsigned vo0 = rq4 * 4096 + chs, vo1 = (4 + rq4) * 4096 + (chs ^ kx), vo2 = (8 + rq4) * 4096 + (chs ^ (2 * kx)),
                   vo3 = (12 + rq4) * 4096 + (chs ^ (3 * kx));
    const unsigned ring = (unsigned)(size_t)lds + LDS_RING + wv * 8192;
    const unsigned vland = (unsigned)(size_t)lds + LDS_CTL + CTL_LAND + wv * 4;
    const unsigned vfree = (unsigned)(size_t)lds + LDS_CTL + CTL_FREE + wv * 16 + (lane & 3) * 4;
    STAMP(1);
    const unsigned t1 = now32();
    int due = (-LEAD_X16 + wv * VLAG_X16) * 16;
    unsigned va0, va1, va2, va3, vf, vt;
    asm volatile(
        "s_waitcnt vmcnt(0) lgkmcnt(0)\n\t"
        "s_mov_b32 s60, 0\n\t"
        "s_mov_b32 s61, 0\n\t"
        "s_mov_b32 s62, 0\n\t"
        "s_nop 4\n"
        ".LldT%=:\n\t"
        "s_memrealtime s[68:69]\n\t"
        "ds_read_b32 %[vf], %[vfree]\n\t"
        "s_getreg_b32 s64, hwreg(HW_REG_IB_STS)\n\t"
        "v_readlane_b32 s63, %[l0], s60\n\t"
        "s_and_b32 s65, s64, 15\n\t"
        "s_lshr_b32 s64, s64, 18\n\t"
        "s_and_b32 s64, s64, 48\n\t"
        "s_or_b32 s72, s64, s65\n\t"
        "s_lshl_b32 s65, s60, 2\n\t"
        "s_sub_u32 s64, s65, s72\n\t"
        "s_lshr_b32 s64, s64, 3\n\t"
        "s_cmp_gt_u32 s64, s61\n\t"
        "s_cbranch_scc1 .LldP%=\n"
        ".LldB%=:\n\t"
        "s_cmp_ge_i32 s60, %[nent]\n\t"
        "s_cbranch_scc1 .LldD%=\n\t"
        "v_add_u32 %[a0], s63, %[o0]\n\t"
        "v_add_u32 %[a1], s63, %[o1]\n\t"
        "v_add_u32 %[a2], s63, %[o2]\n\t"
        "v_add_u32 %[a3], s63, %[o3]\n\t"
        "s_bfe_u32 s64, s60, 0x20001\n\t"
        "s_lshl_b32 s65, s64, 15\n\t"
        "s_add_u32 s65, s65, %[ring]\n\t"
        "s_and_b32 s66, s60, 8\n\t"
        "s_lshl_b32 s66, s66, 11\n\t"
        "s_add_u32 s65, s65, s66\n\t"
        "s_and_b32 s66, s60, 1\n\t"
        "s_lshl_b32 s66, s66, 12\n\t"
        "s_add_u32 m0, s65, s66\n\t"
        "s_lshr_b32 s66, s60, 3\n\t"
        "s_cmp_eq_u32 s63, 0x80000000\n\t"
        "s_cselect_b32 s67, 0x80000000, %[due]\n\t"
        "s_cselect_b32 s65, 0, %[pace]\n\t"
        "s_waitcnt lgkmcnt(0)\n\t"
        "v_readlane_b32 s64, %[vf], s64\n\t"
        "s_sub_u32 s68, s68, %[t1]\n\t"
        "s_lshl_b32 s68, s68, 8\n\t"
        "s_cmp_lt_i32 s68, s67\n\t"
        "s_cbranch_scc1 .LldR%=\n\t"
        "s_add_u32 s64, s64, 1\n\t"
        "s_cmp_lt_u32 s64, s66\n\t"
        "s_cbranch_scc1 .LldR%=\n\t"
        "s_cmp_gt_u32 s72, %[win]\n\t"
        "s_cbranch_scc1 .LldR%=\n\t"
        "s_waitcnt vmcnt(56)\n\t"
        "buffer_load_dwordx4 %[a0], %[rs], 0 offen nt lds\n\t"
        "s_add_u32 m0, m0, 1024\n\t"
        "s_add_u32 %[due], %[due], s65\n\t"
        "buffer_load_dwordx4 %[a1], %[rs], 0 offen nt lds\n\t"
        "s_add_u32 m0, m0, 1024\n\t"
        "s_add_u32 s60, s60, 1\n\t"
        "buffer_load_dwordx4 %[a2], %[rs], 0 offen nt lds\n\t"
        "s_add_u32 m0, m0, 1024\n\t"
        "s_and_b32 s64, s60, 63\n\t"
        "buffer_load_dwordx4 %[a3], %[rs], 0 offen nt lds\n\t"
        "s_cbranch_scc1 .LldT%=\n\t"
        "v_mov_b32 %[l0], %[l1]\n\t"
        "v_mov_b32 %[l1], %[l2]\n\t"
        "v_mov_b32 %[l2], %[l3]\n\t"
        "v_mov_b32 %[l3], %[l4]\n\t"
        "s_branch .LldT%=\n"
        ".LldP%=:\n\t"
        "s_lshl_b32 s66, s64, 3\n\t"
        "s_sub_u32 s66, s65, s66\n\t"
        "s_min_u32 s66, s66, 64\n\t"
        "s_lshl_b32 s66, s66, 1\n\t"
        "s_add_u32 s66, s66, 12\n\t"
        "s_getpc_b64 s[70:71]\n\t"
        "s_add_u32 s70, s70, s66\n\t"
        "s_addc_u32 s71, s71, 0\n\t"
        "s_setpc_b64 s[70:71]\n\t"
        "s_waitcnt vmcnt(0)\n\ts_branch .LldQ%=\n\t"
        "s_waitcnt vmcnt(4)\n\ts_branch .LldQ%=\n\t"
        "s_waitcnt vmcnt(8)\n\ts_branch .LldQ%=\n\t"
        "s_waitcnt vmcnt(12)\n\ts_branch .LldQ%=\n\t"
        "s_waitcnt vmcnt(16)\n\ts_branch .LldQ%=\n\t"
        "s_waitcnt vmcnt(20)\n\ts_branch .LldQ%=\n\t"
        "s_waitcnt vmcnt(24)\n\ts_branch .LldQ%=\n\t"
        "s_waitcnt vmcnt(28)\n\ts_branch .LldQ%=\n\t"
        "s_waitcnt vmcnt(32)\n\ts_branch .LldQ%=\n\t"
        "s_waitcnt vmcnt(36)\n\ts_branch .LldQ%=\n\t"
        "s_waitcnt vmcnt(40)\n\ts_branch .LldQ%=\n\t"
        "s_waitcnt vmcnt(44)\n\ts_branch .LldQ%=\n\t"
        "s_waitcnt vmcnt(48)\n\ts_branch .LldQ%=\n\t"
        "s_waitcnt vmcnt(52)\n\ts_branch .LldQ%=\n\t"
        "s_waitcnt vmcnt(56)\n\ts_branch .LldQ%=\n\t"
        "s_waitcnt vmcnt(60)\n\ts_branch .LldQ%=\n\t"
        "s_waitcnt vmcnt(60)\n"
        ".LldQ%=:\n\t"
        "s_mov_b32 s61, s64\n\t"
        "v_mov_b32 %[vt], s64\n\t"
        "ds_write_b32 %[vland], %[vt]\n\t"
        "s_branch .LldB%=\n"
        ".LldD%=:\n\t"
        "s_waitcnt lgkmcnt(0)\n\t"
        "s_lshr_b32 s64, %[nent], 1\n\t"
        "s_cmp_ge_u32 s61, s64\n\t"
        "s_cbranch_scc1 .LldE%=\n"
        ".LldR%=:\n\t"
        LSLEEP
        "s_add_u32 s62, s62, 1\n\t"
        "s_cmp_lt_u32 s62, 0x400000\n\t"
        "s_cbranch_scc1 .LldT%=\n"
        ".LldE%=:\n\t"
        "s_waitcnt vmcnt(0) lgkmcnt(0)"
        : [a0] "=&v"(va0), [a1] "=&v"(va1), [a2] "=&v"(va2), [a3] "=&v"(va3), [vf] "=&v"(vf), [vt] "=&v"(vt),
          [l0] "+v"(lst[0]), [l1] "+v"(lst[1]), [l2] "+v"(lst[2]), [l3] "+v"(lst[3]), [due] "+s"(due)
        : [l4] "v"(lst[4]), [o0] "v"(vo0), [o1] "v"(vo1), [o2] "v"(vo2), [o3] "v"(vo3), [vland] "v"(vland), [vfree] "v"(vfree),
          [rs] "s"(rs), [nent] "s"(nent), [ring] "s"(ring), [t1] "s"(t1), [pace] "s"(pace), [win] "n"(WINDOW - 4)
        : "memory", "scc", "s60", "s61", "s62", "s63", "s64", "s65", "s66", "s67", "s68", "s69", "s70", "s71", "s72");
    STAMP(3);
    return;
  }

  const __amdgpu_buffer_rsrc_t rw = __builtin_amdgcn_make_buffer_rsrc((void*)ws, 0, (int)WS_NEED, 0x00020000);
  if (wv == 6) {
    GAS unsigned long long* epw = (GAS unsigned long long*)(ws + slot + SLOT_EPOCH);
    GAS unsigned long long* flw = (GAS unsigned long long*)(ws + slot + SLOT_FLAG);
    if (mv > 0) {
      const unsigned ts = now32();
      const unsigned long long ep = __hip_atomic_load(epw, __ATOMIC_RELAXED, __HIP_MEMORY_SCOPE_AGENT);
      const int n0 = __builtin_amdgcn_readlane(np, __builtin_ctzll(__builtin_amdgcn_ballot_w64(rank == 0)));
      const int n15 = __builtin_amdgcn_readlane(np, __builtin_ctzll(__builtin_amdgcn_ballot_w64(rank == 15)));
      const unsigned tpub = __builtin_amdgcn_readfirstlane((unsigned)((float)(tot * PACE_X16) * (1.0f / 256.0f) * (float)(n0 - n15) * __builtin_amdgcn_rcpf((float)(n0 + n15)))) + PUB_MARGIN;
      u32x4 xr[16];
      u32x2 yr;
      if (shortside) {
        const unsigned chain = min(4, TH);
        SPIN(__builtin_amdgcn_readfirstlane(ctl[CTL_CHAIN / 4]) != chain) __builtin_amdgcn_s_sleep(4);
        CBAR();
#pragma unroll
        for (int j = 0; j < 16; ++j) xr[j] = *(LAS u32x4*)(lds + LDS_ACC + j * 1024 + lane * 16);
        yr = *(LAS u32x2*)(lds + LDS_ACC + 16384 + lane * 8);
        LGKM0();
        ctl[CTL_CHAIN / 4] = chain + 1;
        STAMP(1);
        if (PUB_SYNC) while (now32() - ts < tpub) __builtin_amdgcn_s_sleep(2);
        STAMP(2);
#pragma unroll
        for (int j = 0; j < NSTORE; ++j) __builtin_amdgcn_raw_buffer_store_b128(xr[j], rw, slot + j * 1024 + lane * 16, 0, 16);
        __builtin_amdgcn_raw_buffer_store_b64(yr, rw, slot + SLOT_ML + lane * 8, 0, 16);
        VMWAIT(0);
        if (lane == 0) __hip_atomic_store(flw, ep + 1, __ATOMIC_RELAXED, __HIP_MEMORY_SCOPE_AGENT);
      } else {
        if (PUB_SYNC) while (now32() - ts < tpub + 200) __builtin_amdgcn_s_sleep(8);
        SPIN(__builtin_amdgcn_readfirstlane(__hip_atomic_load(flw, __ATOMIC_RELAXED, __HIP_MEMORY_SCOPE_AGENT) != ep + 1)) __builtin_amdgcn_s_sleep(POLLSLEEP);
        __builtin_amdgcn_fence(__ATOMIC_ACQUIRE, ACQ_SCOPE);
        if (lane == 0) __hip_atomic_store(epw, ep + 1, __ATOMIC_RELAXED, __HIP_MEMORY_SCOPE_AGENT);
        yr = __builtin_amdgcn_raw_buffer_load_b64(rw, slot + SLOT_ML + lane * 8, 0, 16);
#pragma unroll
        for (int j = 0; j < 16; ++j) xr[j] = __builtin_amdgcn_raw_buffer_load_b128(rw, slot + j * 1024 + lane * 16, 0, 16);
        SPIN(__builtin_amdgcn_readfirstlane(ctl[CTL_LAND / 4]) == 0 || __builtin_amdgcn_readfirstlane(ctl[CTL_LAND / 4 + 1]) == 0) __builtin_amdgcn_s_sleep(4);
        CBAR();
        *(LAS u32x2*)(lds + LDS_ACC + 16384 + lane * 8) = yr;
#pragma unroll
        for (int j = 0; j < 16; ++j) *(LAS u32x4*)(lds + LDS_ACC + j * 1024 + lane * 16) = xr[j];
        LGKM0();
        ctl[CTL_CHAIN / 4] = 1;
      }
    }
    STAMP(5);
    return;
  }

  const int w = wv - 2;
  const int r32 = lane & 31, hi = lane >> 5;
  const unsigned fk = (((unsigned)r32 & 3) << 2) | (((unsigned)r32 >> 2) & 3);
  const unsigned kaddr0 = LDS_RING + w * 32768 + r32 * 256 + (((unsigned)hi ^ fk) << 4);
  const unsigned tq = (lane & 15) >> 2, tp = lane & 3, tblk = (lane >> 4) & 1;
  const unsigned vaddr0 = LDS_RING + w * 32768 + 8192 + (4 * hi + tq) * 256 + (tq << 6) + ((2 * tblk + (tp >> 1)) << 4) + ((tp & 1) << 3);
  const __amdgpu_buffer_rsrc_t rq = __builtin_amdgcn_make_buffer_rsrc((void*)qp, 0, NB * HQ * SQ * HD * 2, 0x00020000);

  bf16x8 qf[8];
  auto load_q = [&](int c) {
    const unsigned qo = ((__builtin_amdgcn_readlane(lraw, c) > 0) ? (unsigned)((c * HQ + 2 * h) * SQ * HD * 2) : INVALID) + r32 * 256 + hi * 16;
#pragma unroll
    for (int s = 0; s < 8; ++s) qf[s] = __builtin_bit_cast(bf16x8, __builtin_amdgcn_raw_buffer_load_b128(rq, qo + s * 32, 0, 0));
  };
  load_q(nhelp ? bl : b);

  int T0 = 0;
  unsigned chain0 = gather ? 1 : 0;
#pragma unroll 1
  for (int pc = nhelp ? 0 : 1; pc < 2; ++pc) {
    const bool own = pc == 1;
    const int c = own ? b : bl;
    const int n = own ? nown : nhelp, p0 = own ? 0 : nl - mv;
    const int T1 = T0 + ((n + 1) >> 1);
    const int npart = min(4, T1 - T0);
    const int tfirst = T0 + ((w - T0) & 3);
    const int tlast = T1 - 1 - ((T1 - 1 - w) & 3);
    const int pos = tlast - (T1 - npart);
    const int lim = min(__builtin_amdgcn_readlane(lcl, c), (p0 + n) * PAGE);
    f32x16 o[4];
#pragma unroll
    for (int i = 0; i < 4; ++i)
#pragma unroll
      for (int r = 0; r < 16; ++r) o[i][r] = 0.f;
    float m = -3.0e38f, l = 0.f;

    for (int t = tfirst; t < T1; t += 4) {
      const unsigned so = ((unsigned)t & 4u) << 12;
      SPIN(__builtin_amdgcn_readfirstlane(ctl[CTL_LAND / 4]) <= (unsigned)t) __builtin_amdgcn_s_sleep(CSLEEP);
      CBAR();
      if (own && t < 4) STAMP(1);
      bf16x8 kf[8];
#pragma unroll
      for (int s = 0; s < 8; ++s) kf[s] = *(LAS bf16x8*)(lds + ((kaddr0 + so) ^ (s << 5)));
      asm volatile("s_waitcnt lgkmcnt(0)" : "+v"(kf[0]), "+v"(kf[1]), "+v"(kf[2]), "+v"(kf[3]), "+v"(kf[4]), "+v"(kf[5]), "+v"(kf[6]), "+v"(kf[7]) :: "memory");
      ctl[CTL_FREE / 4 + w] = ((unsigned)t >> 2) + 1;
      f32x16 sc;
#pragma unroll
      for (int r = 0; r < 16; ++r) sc[r] = 0.f;
#pragma unroll
      for (int s = 0; s < 8; ++s) sc = __builtin_amdgcn_mfma_f32_32x32x16_bf16(kf[s], qf[s], sc, 0, 0, 0);
      if (t == T1 - 1) {
        const int kb = (p0 + 2 * (t - T0)) * PAGE + 4 * hi;
#pragma unroll
        for (int r = 0; r < 16; ++r) sc[r] = (kb + 8 * (r >> 2) + (r & 3) < lim) ? sc[r] : -__builtin_inff();
      }
      float mx = sc[0];
#pragma unroll
      for (int r = 1; r < 16; ++r) mx = fmaxf(mx, sc[r]);
      mx = swap_max(mx);
      const float mn = fmaxf(m, mx);
      const float alpha = __builtin_amdgcn_exp2f((m - mn) * CS);
      m = mn;
      const float nmc = -mn * CS;
      float ps = 0.f;
#pragma unroll
      for (int r = 0; r < 16; ++r) { sc[r] = __builtin_amdgcn_exp2f(fmaf(sc[r], CS, nmc)); ps += sc[r]; }
      l = fmaf(l, alpha, ps);
      bf16x8 pf[2];
#pragma unroll
      for (int s = 0; s < 2; ++s) {
        u32x4 u = {pack2(sc[8 * s], sc[8 * s + 1]), pack2(sc[8 * s + 2], sc[8 * s + 3]), pack2(sc[8 * s + 4], sc[8 * s + 5]), pack2(sc[8 * s + 6], sc[8 * s + 7])};
        pf[s] = __builtin_bit_cast(bf16x8, u);
      }
      if (__builtin_amdgcn_ballot_w64(alpha != 1.0f)) {
#pragma unroll
        for (int i = 0; i < 4; ++i)
#pragma unroll
          for (int r = 0; r < 16; ++r) o[i][r] *= alpha;
      }
      asm volatile("" : "+v"(pf[0]), "+v"(pf[1]), "+v"(l), "+v"(o[0]), "+v"(o[1]), "+v"(o[2]), "+v"(o[3]));

      SPIN(__builtin_amdgcn_readfirstlane(ctl[CTL_LAND / 4 + 1]) <= (unsigned)t) __builtin_amdgcn_s_sleep(CSLEEP);
      CBAR();
      bf16x8 vf[4][2];
#pragma unroll
      for (int i = 0; i < 4; ++i) {
        const unsigned a = (vaddr0 + so) ^ (i << 6);
#pragma unroll
        for (int s = 0; s < 2; ++s) {
          const s16x4 lo = __builtin_amdgcn_ds_read_tr16_b64_v4i16((LAS s16x4*)(lds + a + 4096 * s));
          const s16x4 up = __builtin_amdgcn_ds_read_tr16_b64_v4i16((LAS s16x4*)(lds + a + 4096 * s + 2048));
          vf[i][s] = __builtin_bit_cast(bf16x8, __builtin_shufflevector(lo, up, 0, 1, 2, 3, 4, 5, 6, 7));
        }
      }
      asm volatile("s_waitcnt lgkmcnt(0)" : "+v"(vf[0][0]), "+v"(vf[0][1]), "+v"(vf[1][0]), "+v"(vf[1][1]), "+v"(vf[2][0]), "+v"(vf[2][1]), "+v"(vf[3][0]), "+v"(vf[3][1]) :: "memory");
      ctl[CTL_FREE / 4 + 4 + w] = ((unsigned)t >> 2) + 1;
#pragma unroll
      for (int s = 0; s < 2; ++s)
#pragma unroll
        for (int i = 0; i < 4; ++i) o[i] = __builtin_amdgcn_mfma_f32_32x32x16_bf16(vf[i][s], pf[s], o[i], 0, 0, 0);
    }
    if (own) STAMP(2);
    if (!own) load_q(b);
    if (tfirst < T1) {
      SPIN(__builtin_amdgcn_readfirstlane(ctl[CTL_CHAIN / 4]) != chain0 + pos) __builtin_amdgcn_s_sleep(CSLEEP);
      CBAR();
      if (own) STAMP(3);
      if (pos > 0 || gather) {
        const f32x2 y = *(LAS f32x2*)(lds + LDS_ACC + 16384 + lane * 8);
        f32x4 x[16];
#pragma unroll
        for (int j = 0; j < 16; ++j) x[j] = *(LAS f32x4*)(lds + LDS_ACC + j * 1024 + lane * 16);
        const float mn2 = fmaxf(m, y[0]);
        const float a0 = __builtin_amdgcn_exp2f((m - mn2) * CS), a1 = __builtin_amdgcn_exp2f((y[0] - mn2) * CS);
        l = l * a0 + y[1] * a1;
        m = mn2;
#pragma unroll
        for (int j = 0; j < 16; ++j)
#pragma unroll
          for (int e = 0; e < 4; ++e) o[j >> 2][4 * (j & 3) + e] = o[j >> 2][4 * (j & 3) + e] * a0 + x[j][e] * a1;
      }
      if (pos < npart - 1 || !own) {
        *(LAS f32x2*)(lds + LDS_ACC + 16384 + lane * 8) = f32x2{m, l};
#pragma unroll
        for (int j = 0; j < 16; ++j)
          *(LAS f32x4*)(lds + LDS_ACC + j * 1024 + lane * 16) =
              f32x4{o[j >> 2][4 * (j & 3)], o[j >> 2][4 * (j & 3) + 1], o[j >> 2][4 * (j & 3) + 2], o[j >> 2][4 * (j & 3) + 3]};
        LGKM0();
        ctl[CTL_CHAIN / 4] = chain0 + pos + 1;
      } else {
        STAMP(4);
        const float inv = 1.0f / swap_sum(l);
        char* orow = (char*)outp + (size_t)((b * HQ + 2 * h) * SQ + r32) * HD * 2 + hi * 16;
#pragma unroll
        for (int i = 0; i < 4; ++i)
#pragma unroll
          for (int j = 0; j < 2; ++j) {
            const unsigned a0 = pack2(o[i][8 * j] * inv, o[i][8 * j + 1] * inv), a1 = pack2(o[i][8 * j + 2] * inv, o[i][8 * j + 3] * inv);
            const unsigned b0 = pack2(o[i][8 * j + 4] * inv, o[i][8 * j + 5] * inv), b1 = pack2(o[i][8 * j + 6] * inv, o[i][8 * j + 7] * inv);
            auto s0 = __builtin_amdgcn_permlane32_swap(a0, b0, false, false);
            auto s1 = __builtin_amdgcn_permlane32_swap(a1, b1, false, false);
            u32x4 val = {s0[0], s1[0], s0[1], s1[1]};
            *(u32x4*)(orow + i * 64 + j * 32) = val;
          }
      }
    }
    T0 = T1;
    chain0 += npart + 1;
  }
}
}

extern "C" void kernel_launch(void* const* d_in, const int* in_sizes, int n_in, void* d_out, int out_size, void* d_ws,
                              size_t ws_size, hipStream_t stream) {
  (void)n_in; (void)out_size; (void)ws_size;
  const unsigned nphys = (unsigned)(in_sizes[1] / (PAGE * HKV * HD));
  const unsigned kvbytes = nphys * PAGE_BYTES;
  (void)hipFuncSetAttribute((const void*)paged_attn, hipFuncAttributeMaxDynamicSharedMemorySize, LDS_TOTAL);
  hipLaunchKernelGGL(paged_attn, dim3(256), dim3(448), LDS_TOTAL, stream, d_in[0], d_in[1], d_in[2], (const int*)d_in[3],
                     (const int*)d_in[4], d_out, (char*)d_ws, kvbytes, nphys);
}
```

```cpp
#define BALANCE 0
#define PACE_X16 455
#define WINDOW 24
#include <hip/hip_runtime.h>
#include <hip/hip_bf16.h>

#define LEAD_X16 0
#define VLAG_X16 (70 * 16)
#define LSLEEP ""
#define CSLEEP 1
#define ACQ_SCOPE "agent"
#define NSTORE 16
#define NLOAD 16
#define STOREAUX 16
#define LOADAUX 16
#define POLLSLEEP 16
#define GDELAY 0
#define STSLEEP 0
#define MAPPING 0
#define WPROP 0
#define WBONUS 0
#define SPLIT_X64 68
#define PUB_SYNC 1
#define PUB_MARGIN 450

#define STAMP(i) do {} while (0)

namespace {
constexpr int NB = 16, HQ = 32, HKV = 16, SQ = 16, HD = 128, PAGE = 16, NBPS = 256, SKV = NBPS * PAGE;
constexpr unsigned PAGE_BYTES = PAGE * HKV * HD * 2;
constexpr unsigned INVALID = 0x80000000u;
constexpr int NLR = 5;
constexpr unsigned LDS_RING = 0;
constexpr unsigned LDS_ACC = 131072;
constexpr unsigned LDS_CTL = LDS_ACC + 16384 + 512;
constexpr unsigned CTL_LAND = 0;
constexpr unsigned CTL_FREE = 16;
constexpr unsigned CTL_CHAIN = 48;
constexpr unsigned LDS_TOTAL = LDS_CTL + 64;
static_assert(LDS_TOTAL <= 163840, "lds");
constexpr unsigned SLOT_BYTES = 20480, SLOT_ML = 16384, SLOT_FLAG = 16896, SLOT_EPOCH = 17024;
constexpr size_t WS_NEED = (size_t)HKV * (NB / 2) * SLOT_BYTES;

typedef __attribute__((ext_vector_type(8))) __bf16 bf16x8;
typedef __attribute__((ext_vector_type(2))) __bf16 bf16x2;
typedef __attribute__((ext_vector_type(4))) short s16x4;
typedef __attribute__((ext_vector_type(16))) float f32x16;
typedef __attribute__((ext_vector_type(4))) float f32x4;
typedef __attribute__((ext_vector_type(2))) float f32x2;
typedef __attribute__((ext_vector_type(4))) unsigned u32x4;
typedef __attribute__((ext_vector_type(2))) unsigned u32x2;
typedef __attribute__((ext_vector_type(4))) int i32x4;
#define LAS __attribute__((address_space(3)))
#define GAS __attribute__((address_space(1)))

#define DMA4(POL, rs, m0v, v0, v1, v2, v3)                                                               \
  asm volatile("s_mov_b32 m0, %0\n\ts_nop 4\n\tbuffer_load_dwordx4 %2, %1, 0 offen " POL " lds\n\t"       \
               "s_add_u32 m0, m0, 1024\n\ts_nop 0\n\tbuffer_load_dwordx4 %3, %1, 0 offen " POL " lds\n\t" \
               "s_add_u32 m0, m0, 1024\n\ts_nop 0\n\tbuffer_load_dwordx4 %4, %1, 0 offen " POL " lds\n\t" \
               "s_add_u32 m0, m0, 1024\n\ts_nop 0\n\tbuffer_load_dwordx4 %5, %1, 0 offen " POL " lds"     \
               :: "s"(m0v), "s"(rs), "v"(v0), "v"(v1), "v"(v2), "v"(v3) : "memory", "scc")

__device__ __forceinline__ float swap_max(float x) {
  auto r = __builtin_amdgcn_permlane32_swap(__float_as_uint(x), __float_as_uint(x), false, false);
  return fmaxf(__uint_as_float(r[0]), __uint_as_float(r[1]));
}
__device__ __forceinline__ float swap_sum(float x) {
  auto r = __builtin_amdgcn_permlane32_swap(__float_as_uint(x), __float_as_uint(x), false, false);
  return __uint_as_float(r[0]) + __uint_as_float(r[1]);
}
__device__ __forceinline__ unsigned pack2(float a, float b) {
  bf16x2 v = {(__bf16)a, (__bf16)b};
  return __builtin_bit_cast(unsigned, v);
}
template <int CTRL> __device__ __forceinline__ int dpp_add(int x) {
  return x + __builtin_amdgcn_update_dpp(0, x, CTRL, 0xf, 0xf, true);
}
__device__ __forceinline__ unsigned now32() { return (unsigned)__builtin_amdgcn_s_memrealtime(); }
#define LGKM0() asm volatile("s_waitcnt lgkmcnt(0)" ::: "memory")
#define VMWAIT(n) asm volatile("s_waitcnt vmcnt(" #n ")" ::: "memory")
#define CBAR() asm volatile("" ::: "memory")
#define SPIN(cond) for (int sp_ = 0; (cond) && sp_ < (1 << 18); ++sp_)

__global__ void __launch_bounds__(448, 1)
paged_attn(const void* __restrict__ qp, const void* __restrict__ kp, const void* __restrict__ vp, const int* __restrict__ bt,
           const int* __restrict__ lens, void* __restrict__ outp, char* __restrict__ ws, unsigned kvbytes, unsigned nphys) {
  extern __shared__ __attribute__((aligned(16))) char smem[];
  LAS char* lds = (LAS char*)smem;
  const int lane = threadIdx.x & 63;
  const int wv = __builtin_amdgcn_readfirstlane(threadIdx.x >> 6);
  const int g = blockIdx.x;
  const int x = g & 7, h = MAPPING == 0 ? g >> 4 : MAPPING == 1 ? 2 * (g & 7) + ((g >> 3) & 1) : MAPPING == 2 ? ((g & 7) >> 1) * 4 + ((g >> 3) & 3) : (g & 1) * 8 + ((g >> 3) & 7);
  const bool shortside = (g >> 3) & 1;
  constexpr float CS = 0.08838834764831845f * 1.4426950408889634f;
  volatile LAS unsigned* ctl = (volatile LAS unsigned*)(lds + LDS_CTL);

  const int lraw = lens[lane & 15];
  const i32x4 lq = *(const i32x4*)(lens + (lane >> 4) * 4);
  if (wv < 2) {
    __builtin_amdgcn_s_setprio(3);
    const __amdgpu_buffer_rsrc_t rb = __builtin_amdgcn_make_buffer_rsrc((void*)bt, 0, NB * NBPS * 4, 0x00020000);
    const unsigned tb = (unsigned)(size_t)lds + LDS_ACC, vo = lane * 16;
#pragma unroll
    for (int i = 0; i < 4; ++i) DMA4("", rb, tb + i * 4096, vo + i * 4096, vo + i * 4096 + 1024, vo + i * 4096 + 2048, vo + i * 4096 + 3072);
  } else if (wv == 2) {
    if (lane < 16) ctl[lane] = 0u;
  }
  LGKM0();
  __builtin_amdgcn_s_barrier();
  CBAR();

  const int lcl = (lraw <= 0 || lraw > SKV) ? SKV : lraw;
  const int np = (lcl + PAGE - 1) / PAGE;
  int rank = 0;
  {
    const int key = lcl * 16 + (lane & 15);
#pragma unroll
    for (int j = 0; j < 4; ++j) {
      const int lj = (lq[j] <= 0 || lq[j] > SKV) ? SKV : lq[j];
      rank += (lj * 16 + (lane >> 4) * 4 + j > key) ? 1 : 0;
    }
    auto r1 = __builtin_amdgcn_permlane16_swap((unsigned)rank, (unsigned)rank, false, false);
    rank = (int)(r1[0] + r1[1]);
    auto r2 = __builtin_amdgcn_permlane32_swap((unsigned)rank, (unsigned)rank, false, false);
    rank = (int)(r2[0] + r2[1]);
  }
  int tot = np;
  tot = dpp_add<0x121>(tot); tot = dpp_add<0x122>(tot); tot = dpp_add<0x124>(tot); tot = dpp_add<0x128>(tot);
  tot = __builtin_amdgcn_readfirstlane(tot);
  const int bl = __builtin_ctz((unsigned)__builtin_amdgcn_ballot_w64(rank == x) | 0x8000u), bs = __builtin_ctz((unsigned)__builtin_amdgcn_ballot_w64(rank == 15 - x) | 0x8000u);
  const int nl = __builtin_amdgcn_readlane(np, bl), ns = __builtin_amdgcn_readlane(np, bs);
  const int mv = (BALANCE && nl * 16 * 64 > tot * SPLIT_X64) ? (nl - ns) >> 1 : 0;
  const int b = MAPPING == 0 ? (shortside ? bs : bl) : MAPPING == 1 ? (g >> 4) : MAPPING == 2 ? (g >> 5) * 2 + (g & 1) : (g >> 6) * 4 + ((g & 7) >> 1);
  const int nhelp = shortside ? mv : 0;
  const int nown = MAPPING ? __builtin_amdgcn_readlane(np, b) : shortside ? ns : nl - mv;
  const bool gather = !shortside && mv > 0;
  const int TH = (nhelp + 1) >> 1;
  const unsigned slot = (unsigned)(h * (NB / 2) + x) * SLOT_BYTES;

  if (wv < 2) {
    int idx[NLR];
    const int nent = 2 * TH + ((nown + 1) & ~1);
#pragma unroll
    for (int r = 0; r < NLR; ++r) {
      const int e = lane + 64 * r, k = e - 2 * TH;
      idx[r] = (e < nhelp) ? bl * NBPS + nl - mv + e : ((unsigned)k < (unsigned)nown ? b * NBPS + k : -1);
    }
    int pace;
    asm volatile("s_nop 1\n\tv_readfirstlane_b32 %0, %1" : "=s"(pace) : "v"((int)((float)(tot * PACE_X16) * __builtin_amdgcn_rcpf((float)(nhelp + nown)))));
    int win = WINDOW - 4;
    if (WPROP) asm volatile("s_nop 1\n\tv_readfirstlane_b32 %0, %1" : "=s"(win) : "v"(min(max((int)((float)((nhelp + nown) * WPROP) * __builtin_amdgcn_rcpf((float)tot) + 0.5f) - 4 + ((h & 3) == 1 ? WBONUS : 0), 4), 52)));
    VMWAIT(0);
    unsigned lst[NLR];
#pragma unroll
    for (int r = 0; r < NLR; ++r) lst[r] = *(LAS unsigned*)(lds + LDS_ACC + max(idx[r], 0) * 4);
#pragma unroll
    for (int r = 0; r < NLR; ++r) lst[r] = (idx[r] >= 0 && lst[r] < nphys) ? lst[r] * PAGE_BYTES + h * (HD * 2) : INVALID;

    const __amdgpu_buffer_rsrc_t rs = __builtin_amdgcn_make_buffer_rsrc(wv == 0 ? (void*)kp : (void*)vp, 0, (int)kvbytes, 0x00020000);
    const unsigned rq4 = lane >> 4;
    const unsigned chs = ((unsigned)(lane & 15) ^ (rq4 << 2)) << 4;
    const unsigned kx = (wv == 0) ? 16u : 0u;
    const unsigned vo0 = rq4 * 4096 + chs, vo1 = (4 + rq4) * 4096 + (chs ^ kx), vo2 = (8 + rq4) * 4096 + (chs ^ (2 * kx)),
                   vo3 = (12 + rq4) * 4096 + (chs ^ (3 * kx));
    const unsigned ring = (unsigned)(size_t)lds + LDS_RING + wv * 8192;
    const unsigned vland = (unsigned)(size_t)lds + LDS_CTL + CTL_LAND + wv * 4;
    const unsigned vfree = (unsigned)(size_t)lds + LDS_CTL + CTL_FREE + wv * 16 + (lane & 3) * 4;
    STAMP(1);
    const unsigned t1 = now32();
    int due = (-LEAD_X16 + wv * VLAG_X16) * 16;
    unsigned va0, va1, va2, va3, vf, vt;
    asm volatile(
        "s_waitcnt vmcnt(0) lgkmcnt(0)\n\t"
        "s_mov_b32 s60, 0\n\t"
        "s_mov_b32 s61, 0\n\t"
        "s_mov_b32 s62, 0\n\t"
        "s_nop 4\n"
        ".LldT%=:\n\t"
        "s_memrealtime s[68:69]\n\t"
        "ds_read_b32 %[vf], %[vfree]\n\t"
        "s_getreg_b32 s64, hwreg(HW_REG_IB_STS)\n\t"
        "v_readlane_b32 s63, %[l0], s60\n\t"
        "s_and_b32 s65, s64, 15\n\t"
        "s_lshr_b32 s64, s64, 18\n\t"
        "s_and_b32 s64, s64, 48\n\t"
        "s_or_b32 s72, s64, s65\n\t"
        "s_lshl_b32 s65, s60, 2\n\t"
        "s_sub_u32 s64, s65, s72\n\t"
        "s_lshr_b32 s64, s64, 3\n\t"
        "s_cmp_gt_u32 s64, s61\n\t"
        "s_cbranch_scc1 .LldP%=\n"
        ".LldB%=:\n\t"
        "s_cmp_ge_i32 s60, %[nent]\n\t"
        "s_cbranch_scc1 .LldD%=\n\t"
        "v_add_u32 %[a0], s63, %[o0]\n\t"
        "v_add_u32 %[a1], s63, %[o1]\n\t"
        "v_add_u32 %[a2], s63, %[o2]\n\t"
        "v_add_u32 %[a3], s63, %[o3]\n\t"
        "s_bfe_u32 s64, s60, 0x20001\n\t"
        "s_lshl_b32 s65, s64, 15\n\t"
        "s_add_u32 s65, s65, %[ring]\n\t"
        "s_and_b32 s66, s60, 8\n\t"
        "s_lshl_b32 s66, s66, 11\n\t"
        "s_add_u32 s65, s65, s66\n\t"
        "s_and_b32 s66, s60, 1\n\t"
        "s_lshl_b32 s66, s66, 12\n\t"
        "s_add_u32 m0, s65, s66\n\t"
        "s_lshr_b32 s66, s60, 3\n\t"
        "s_cmp_eq_u32 s63, 0x80000000\n\t"
        "s_cselect_b32 s67, 0x80000000, %[due]\n\t"
        "s_cselect_b32 s65, 0, %[pace]\n\t"
        "s_waitcnt lgkmcnt(0)\n\t"
        "v_readlane_b32 s64, %[vf], s64\n\t"
        "s_sub_u32 s68, s68, %[t1]\n\t"
        "s_lshl_b32 s68, s68, 8\n\t"
        "s_cmp_lt_i32 s68, s67\n\t"
        "s_cbranch_scc1 .LldR%=\n\t"
        "s_add_u32 s64, s64, 1\n\t"
        "s_cmp_lt_u32 s64, s66\n\t"
        "s_cbranch_scc1 .LldR%=\n\t"
        "s_cmp_gt_u32 s72, %[win]\n\t"
        "s_cbranch_scc1 .LldR%=\n\t"
        "s_waitcnt vmcnt(56)\n\t"
        "buffer_load_dwordx4 %[a0], %[rs], 0 offen nt lds\n\t"
        "s_add_u32 m0, m0, 1024\n\t"
        "s_add_u32 %[due], %[due], s65\n\t"
        "buffer_load_dwordx4 %[a1], %[rs], 0 offen nt lds\n\t"
        "s_add_u32 m0, m0, 1024\n\t"
        "s_add_u32 s60, s60, 1\n\t"
        "buffer_load_dwordx4 %[a2], %[rs], 0 offen nt lds\n\t"
        "s_add_u32 m0, m0, 1024\n\t"
        "s_and_b32 s64, s60, 63\n\t"
        "buffer_load_dwordx4 %[a3], %[rs], 0 offen nt lds\n\t"
        "s_cbranch_scc1 .LldT%=\n\t"
        "v_mov_b32 %[l0], %[l1]\n\t"
        "v_mov_b32 %[l1], %[l2]\n\t"
        "v_mov_b32 %[l2], %[l3]\n\t"
        "v_mov_b32 %[l3], %[l4]\n\t"
        "s_branch .LldT%=\n"
        ".LldP%=:\n\t"
        "s_lshl_b32 s66, s64, 3\n\t"
        "s_sub_u32 s66, s65, s66\n\t"
        "s_min_u32 s66, s66, 64\n\t"
        "s_lshl_b32 s66, s66, 1\n\t"
        "s_add_u32 s66, s66, 12\n\t"
        "s_getpc_b64 s[70:71]\n\t"
        "s_add_u32 s70, s70, s66\n\t"
        "s_addc_u32 s71, s71, 0\n\t"
        "s_setpc_b64 s[70:71]\n\t"
        "s_waitcnt vmcnt(0)\n\ts_branch .LldQ%=\n\t"
        "s_waitcnt vmcnt(4)\n\ts_branch .LldQ%=\n\t"
        "s_waitcnt vmcnt(8)\n\ts_branch .LldQ%=\n\t"
        "s_waitcnt vmcnt(12)\n\ts_branch .LldQ%=\n\t"
        "s_waitcnt vmcnt(16)\n\ts_branch .LldQ%=\n\t"
        "s_waitcnt vmcnt(20)\n\ts_branch .LldQ%=\n\t"
        "s_waitcnt vmcnt(24)\n\ts_branch .LldQ%=\n\t"
        "s_waitcnt vmcnt(28)\n\ts_branch .LldQ%=\n\t"
        "s_waitcnt vmcnt(32)\n\ts_branch .LldQ%=\n\t"
        "s_waitcnt vmcnt(36)\n\ts_branch .LldQ%=\n\t"
        "s_waitcnt vmcnt(40)\n\ts_branch .LldQ%=\n\t"
        "s_waitcnt vmcnt(44)\n\ts_branch .LldQ%=\n\t"
        "s_waitcnt vmcnt(48)\n\ts_branch .LldQ%=\n\t"
        "s_waitcnt vmcnt(52)\n\ts_branch .LldQ%=\n\t"
        "s_waitcnt vmcnt(56)\n\ts_branch .LldQ%=\n\t"
        "s_waitcnt vmcnt(60)\n\ts_branch .LldQ%=\n\t"
        "s_waitcnt vmcnt(60)\n"
        ".LldQ%=:\n\t"
        "s_mov_b32 s61, s64\n\t"
        "v_mov_b32 %[vt], s64\n\t"
        "ds_write_b32 %[vland], %[vt]\n\t"
        "s_branch .LldB%=\n"
        ".LldD%=:\n\t"
        "s_waitcnt lgkmcnt(0)\n\t"
        "s_lshr_b32 s64, %[nent], 1\n\t"
        "s_cmp_ge_u32 s61, s64\n\t"
        "s_cbranch_scc1 .LldE%=\n"
        ".LldR%=:\n\t"
        LSLEEP
        "s_add_u32 s62, s62, 1\n\t"
        "s_cmp_lt_u32 s62, 0x400000\n\t"
        "s_cbranch_scc1 .LldT%=\n"
        ".LldE%=:\n\t"
        "s_waitcnt vmcnt(0) lgkmcnt(0)"
        : [a0] "=&v"(va0), [a1] "=&v"(va1), [a2] "=&v"(va2), [a3] "=&v"(va3), [vf] "=&v"(vf), [vt] "=&v"(vt),
          [l0] "+v"(lst[0]), [l1] "+v"(lst[1]), [l2] "+v"(lst[2]), [l3] "+v"(lst[3]), [due] "+s"(due)
        : [l4] "v"(lst[4]), [o0] "v"(vo0), [o1] "v"(vo1), [o2] "v"(vo2), [o3] "v"(vo3), [vland] "v"(vland), [vfree] "v"(vfree),
          [rs] "s"(rs), [nent] "s"(nent), [ring] "s"(ring), [t1] "s"(t1), [pace] "s"(pace), [win] "s"(win)
        : "memory", "scc", "s60", "s61", "s62", "s63", "s64", "s65", "s66", "s67", "s68", "s69", "s70", "s71", "s72");
    STAMP(3);
    return;
  }

  const __amdgpu_buffer_rsrc_t rw = __builtin_amdgcn_make_buffer_rsrc((void*)ws, 0, (int)WS_NEED, 0x00020000);
  if (wv == 6) {
    GAS unsigned long long* epw = (GAS unsigned long long*)(ws + slot + SLOT_EPOCH);
    GAS unsigned long long* flw = (GAS unsigned long long*)(ws + slot + SLOT_FLAG);
    if (mv > 0) {
      const unsigned ts = now32();
      const unsigned long long ep = __hip_atomic_load(epw, __ATOMIC_RELAXED, __HIP_MEMORY_SCOPE_AGENT);
      const int n0 = __builtin_amdgcn_readlane(np, __builtin_ctzll(__builtin_amdgcn_ballot_w64(rank == 0)));
      const int n15 = __builtin_amdgcn_readlane(np, __builtin_ctzll(__builtin_amdgcn_ballot_w64(rank == 15)));
      const unsigned tpub = __builtin_amdgcn_readfirstlane((unsigned)((float)(tot * PACE_X16) * (1.0f / 256.0f) * (float)(n0 - n15) * __builtin_amdgcn_rcpf((float)(n0 + n15)))) + PUB_MARGIN;
      u32x4 xr[16];
      u32x2 yr;
      if (shortside) {
        const unsigned chain = min(4, TH);
        SPIN(__builtin_amdgcn_readfirstlane(ctl[CTL_CHAIN / 4]) != chain) __builtin_amdgcn_s_sleep(4);
        CBAR();
#pragma unroll
        for (int j = 0; j < 16; ++j) xr[j] = *(LAS u32x4*)(lds + LDS_ACC + j * 1024 + lane * 16);
        yr = *(LAS u32x2*)(lds + LDS_ACC + 16384 + lane * 8);
        LGKM0();
        ctl[CTL_CHAIN / 4] = chain + 1;
        STAMP(1);
        if (PUB_SYNC) while (now32() - ts < tpub) __builtin_amdgcn_s_sleep(2);
        STAMP(2);
#pragma unroll
        for (int j = 0; j < NSTORE; ++j) __builtin_amdgcn_raw_buffer_store_b128(xr[j], rw, slot + j * 1024 + lane * 16, 0, 16);
        __builtin_amdgcn_raw_buffer_store_b64(yr, rw, slot + SLOT_ML + lane * 8, 0, 16);
        VMWAIT(0);
        if (lane == 0) __hip_atomic_store(flw, ep + 1, __ATOMIC_RELAXED, __HIP_MEMORY_SCOPE_AGENT);
      } else {
        if (PUB_SYNC) while (now32() - ts < tpub + 200) __builtin_amdgcn_s_sleep(8);
        SPIN(__builtin_amdgcn_readfirstlane(__hip_atomic_load(flw, __ATOMIC_RELAXED, __HIP_MEMORY_SCOPE_AGENT) != ep + 1)) __builtin_amdgcn_s_sleep(POLLSLEEP);
        __builtin_amdgcn_fence(__ATOMIC_ACQUIRE, ACQ_SCOPE);
        if (lane == 0) __hip_atomic_store(epw, ep + 1, __ATOMIC_RELAXED, __HIP_MEMORY_SCOPE_AGENT);
        yr = __builtin_amdgcn_raw_buffer_load_b64(rw, slot + SLOT_ML + lane * 8, 0, 16);
#pragma unroll
        for (int j = 0; j < 16; ++j) xr[j] = __builtin_amdgcn_raw_buffer_load_b128(rw, slot + j * 1024 + lane * 16, 0, 16);
        SPIN(__builtin_amdgcn_readfirstlane(ctl[CTL_LAND / 4]) == 0 || __builtin_amdgcn_readfirstlane(ctl[CTL_LAND / 4 + 1]) == 0) __builtin_amdgcn_s_sleep(4);
        CBAR();
        *(LAS u32x2*)(lds + LDS_ACC + 16384 + lane * 8) = yr;
#pragma unroll
        for (int j = 0; j < 16; ++j) *(LAS u32x4*)(lds + LDS_ACC + j * 1024 + lane * 16) = xr[j];
        LGKM0();
        ctl[CTL_CHAIN / 4] = 1;
      }
    }
    STAMP(5);
    return;
  }

  const int w = wv - 2;
  const int r32 = lane & 31, hi = lane >> 5;
  const unsigned fk = (((unsigned)r32 & 3) << 2) | (((unsigned)r32 >> 2) & 3);
  const unsigned kaddr0 = LDS_RING + w * 32768 + r32 * 256 + (((unsigned)hi ^ fk) << 4);
  const unsigned tq = (lane & 15) >> 2, tp = lane & 3, tblk = (lane >> 4) & 1;
  const unsigned vaddr0 = LDS_RING + w * 32768 + 8192 + (4 * hi + tq) * 256 + (tq << 6) + ((2 * tblk + (tp >> 1)) << 4) + ((tp & 1) << 3);
  const __amdgpu_buffer_rsrc_t rq = __builtin_amdgcn_make_buffer_rsrc((void*)qp, 0, NB * HQ * SQ * HD * 2, 0x00020000);

  bf16x8 qf[8];
  auto load_q = [&](int c) {
    const unsigned qo = ((__builtin_amdgcn_readlane(lraw, c) > 0) ? (unsigned)((c * HQ + 2 * h) * SQ * HD * 2) : INVALID) + r32 * 256 + hi * 16;
#pragma unroll
    for (int s = 0; s < 8; ++s) qf[s] = __builtin_bit_cast(bf16x8, __builtin_amdgcn_raw_buffer_load_b128(rq, qo + s * 32, 0, 0));
  };
  load_q(nhelp ? bl : b);

  int T0 = 0;
  unsigned chain0 = gather ? 1 : 0;
#pragma unroll 1
  for (int pc = nhelp ? 0 : 1; pc < 2; ++pc) {
    const bool own = pc == 1;
    const int c = own ? b : bl;
    const int n = own ? nown : nhelp, p0 = own ? 0 : nl - mv;
    const int T1 = T0 + ((n + 1) >> 1);
    const int npart = min(4, T1 - T0);
    const int tfirst = T0 + ((w - T0) & 3);
    const int tlast = T1 - 1 - ((T1 - 1 - w) & 3);
    const int pos = tlast - (T1 - npart);
    const int lim = min(__builtin_amdgcn_readlane(lcl, c), (p0 + n) * PAGE);
    f32x16 o[4];
#pragma unroll
    for (int i = 0; i < 4; ++i)
#pragma unroll
      for (int r = 0; r < 16; ++r) o[i][r] = 0.f;
    float m = -3.0e38f, l = 0.f;

    for (int t = tfirst; t < T1; t += 4) {
      const unsigned so = ((unsigned)t & 4u) << 12;
      SPIN(__builtin_amdgcn_readfirstlane(ctl[CTL_LAND / 4]) <= (unsigned)t) __builtin_amdgcn_s_sleep(CSLEEP);
      CBAR();
      if (own && t < 4) STAMP(1);
      bf16x8 kf[8];
#pragma unroll
      for (int s = 0; s < 8; ++s) kf[s] = *(LAS bf16x8*)(lds + ((kaddr0 + so) ^ (s << 5)));
      asm volatile("s_waitcnt lgkmcnt(0)" : "+v"(kf[0]), "+v"(kf[1]), "+v"(kf[2]), "+v"(kf[3]), "+v"(kf[4]), "+v"(kf[5]), "+v"(kf[6]), "+v"(kf[7]) :: "memory");
      ctl[CTL_FREE / 4 + w] = ((unsigned)t >> 2) + 1;
      f32x16 sc;
#pragma unroll
      for (int r = 0; r < 16; ++r) sc[r] = 0.f;
#pragma unroll
      for (int s = 0; s < 8; ++s) sc = __builtin_amdgcn_mfma_f32_32x32x16_bf16(kf[s], qf[s], sc, 0, 0, 0);
      if (t == T1 - 1) {
        const int kb = (p0 + 2 * (t - T0)) * PAGE + 4 * hi;
#pragma unroll
        for (int r = 0; r < 16; ++r) sc[r] = (kb + 8 * (r >> 2) + (r & 3) < lim) ? sc[r] : -__builtin_inff();
      }
      float mx = sc[0];
#pragma unroll
      for (int r = 1; r < 16; ++r) mx = fmaxf(mx, sc[r]);
      mx = swap_max(mx);
      const float mn = fmaxf(m, mx);
      const float alpha = __builtin_amdgcn_exp2f((m - mn) * CS);
      m = mn;
      const float nmc = -mn * CS;
      float ps = 0.f;
#pragma unroll
      for (int r = 0; r < 16; ++r) { sc[r] = __builtin_amdgcn_exp2f(fmaf(sc[r], CS, nmc)); ps += sc[r]; }
      l = fmaf(l, alpha, ps);
      bf16x8 pf[2];
#pragma unroll
      for (int s = 0; s < 2; ++s) {
        u32x4 u = {pack2(sc[8 * s], sc[8 * s + 1]), pack2(sc[8 * s + 2], sc[8 * s + 3]), pack2(sc[8 * s + 4], sc[8 * s + 5]), pack2(sc[8 * s + 6], sc[8 * s + 7])};
        pf[s] = __builtin_bit_cast(bf16x8, u);
      }
      if (__builtin_amdgcn_ballot_w64(alpha != 1.0f)) {
#pragma unroll
        for (int i = 0; i < 4; ++i)
#pragma unroll
          for (int r = 0; r < 16; ++r) o[i][r] *= alpha;
      }
      asm volatile("" : "+v"(pf[0]), "+v"(pf[1]), "+v"(l), "+v"(o[0]), "+v"(o[1]), "+v"(o[2]), "+v"(o[3]));

      SPIN(__builtin_amdgcn_readfirstlane(ctl[CTL_LAND / 4 + 1]) <= (unsigned)t) __builtin_amdgcn_s_sleep(CSLEEP);
      CBAR();
      bf16x8 vf[4][2];
#pragma unroll
      for (int i = 0; i < 4; ++i) {
        const unsigned a = (vaddr0 + so) ^ (i << 6);
#pragma unroll
        for (int s = 0; s < 2; ++s) {
          const s16x4 lo = __builtin_amdgcn_ds_read_tr16_b64_v4i16((LAS s16x4*)(lds + a + 4096 * s));
          const s16x4 up = __builtin_amdgcn_ds_read_tr16_b64_v4i16((LAS s16x4*)(lds + a + 4096 * s + 2048));
          vf[i][s] = __builtin_bit_cast(bf16x8, __builtin_shufflevector(lo, up, 0, 1, 2, 3, 4, 5, 6, 7));
        }
      }
      asm volatile("s_waitcnt lgkmcnt(0)" : "+v"(vf[0][0]), "+v"(vf[0][1]), "+v"(vf[1][0]), "+v"(vf[1][1]), "+v"(vf[2][0]), "+v"(vf[2][1]), "+v"(vf[3][0]), "+v"(vf[3][1]) :: "memory");
      ctl[CTL_FREE / 4 + 4 + w] = ((unsigned)t >> 2) + 1;
#pragma unroll
      for (int s = 0; s < 2; ++s)
#pragma unroll
        for (int i = 0; i < 4; ++i) o[i] = __builtin_amdgcn_mfma_f32_32x32x16_bf16(vf[i][s], pf[s], o[i], 0, 0, 0);
    }
    if (own) STAMP(2);
    if (!own) load_q(b);
    if (tfirst < T1) {
      SPIN(__builtin_amdgcn_readfirstlane(ctl[CTL_CHAIN / 4]) != chain0 + pos) __builtin_amdgcn_s_sleep(CSLEEP);
      CBAR();
      if (own) STAMP(3);
      if (pos > 0 || gather) {
        const f32x2 y = *(LAS f32x2*)(lds + LDS_ACC + 16384 + lane * 8);
        f32x4 x[16];
#pragma unroll
        for (int j = 0; j < 16; ++j) x[j] = *(LAS f32x4*)(lds + LDS_ACC + j * 1024 + lane * 16);
        const float mn2 = fmaxf(m, y[0]);
        const float a0 = __builtin_amdgcn_exp2f((m - mn2) * CS), a1 = __builtin_amdgcn_exp2f((y[0] - mn2) * CS);
        l = l * a0 + y[1] * a1;
        m = mn2;
#pragma unroll
        for (int j = 0; j < 16; ++j)
#pragma unroll
          for (int e = 0; e < 4; ++e) o[j >> 2][4 * (j & 3) + e] = o[j >> 2][4 * (j & 3) + e] * a0 + x[j][e] * a1;
      }
      if (pos < npart - 1 || !own) {
        *(LAS f32x2*)(lds + LDS_ACC + 16384 + lane * 8) = f32x2{m, l};
#pragma unroll
        for (int j = 0; j < 16; ++j)
          *(LAS f32x4*)(lds + LDS_ACC + j * 1024 + lane * 16) =
              f32x4{o[j >> 2][4 * (j & 3)], o[j >> 2][4 * (j & 3) + 1], o[j >> 2][4 * (j & 3) + 2], o[j >> 2][4 * (j & 3) + 3]};
        LGKM0();
        ctl[CTL_CHAIN / 4] = chain0 + pos + 1;
      } else {
        STAMP(4);
        const float inv = 1.0f / swap_sum(l);
        char* orow = (char*)outp + (size_t)((b * HQ + 2 * h) * SQ + r32) * HD * 2 + hi * 16;
#pragma unroll
        for (int i = 0; i < 4; ++i)
#pragma unroll
          for (int j = 0; j < 2; ++j) {
            const unsigned a0 = pack2(o[i][8 * j] * inv, o[i][8 * j + 1] * inv), a1 = pack2(o[i][8 * j + 2] * inv, o[i][8 * j + 3] * inv);
            const unsigned b0 = pack2(o[i][8 * j + 4] * inv, o[i][8 * j + 5] * inv), b1 = pack2(o[i][8 * j + 6] * inv, o[i][8 * j + 7] * inv);
            auto s0 = __builtin_amdgcn_permlane32_swap(a0, b0, false, false);
            auto s1 = __builtin_amdgcn_permlane32_swap(a1, b1, false, false);
            u32x4 val = {s0[0], s1[0], s0[1], s1[1]};
            *(u32x4*)(orow + i * 64 + j * 32) = val;
          }
      }
    }
    T0 = T1;
    chain0 += npart + 1;
  }
}
}

extern "C" void kernel_launch(void* const* d_in, const int* in_sizes, int n_in, void* d_out, int out_size, void* d_ws,
                              size_t ws_size, hipStream_t stream) {
  (void)n_in; (void)out_size; (void)ws_size;
  const unsigned nphys = (unsigned)(in_sizes[1] / (PAGE * HKV * HD));
  const unsigned kvbytes = nphys * PAGE_BYTES;
  (void)hipFuncSetAttribute((const void*)paged_attn, hipFuncAttributeMaxDynamicSharedMemorySize, LDS_TOTAL);
  hipLaunchKernelGGL(paged_attn, dim3(256), dim3(448), LDS_TOTAL, stream, d_in[0], d_in[1], d_in[2], (const int*)d_in[3],
                     (const int*)d_in[4], d_out, (char*)d_ws, kvbytes, nphys);
}
```

```cpp
#define PACE_X16 456
#include <hip/hip_runtime.h>
#include <hip/hip_bf16.h>

#define GAP_TICKS 0
#define WINDOW 24
#define QWAVE 2
#define SOFT_PAGES 0
#define SOFT_TICKS 0
#define MAPPING 0
#define CSLEEP 1

#define STAMP(i) do {} while (0)

namespace {
constexpr int NB = 16, HQ = 32, HKV = 16, SQ = 16, HD = 128, PAGE = 16, NBPS = 256, SKV = NBPS * PAGE;
constexpr unsigned PAGE_BYTES = PAGE * HKV * HD * 2;
constexpr unsigned INVALID = 0x80000000u;
constexpr int NC = 6;
constexpr int KREG = 16, KLDS = 5;
static_assert(NC * (KREG + KLDS + 1) >= NBPS / 2, "P storage");
constexpr unsigned LDS_RING = 0;
constexpr unsigned LDS_WREG = 65536;
constexpr unsigned W_ALP = KLDS * 2048;
constexpr unsigned W_LAST = W_ALP + (KREG + KLDS + 1) * 128;
constexpr unsigned W_SIZE = W_LAST + 2048;
static_assert(W_LAST >= 3 * 4096 && W_SIZE % 256 == 0, "consumer region");
constexpr unsigned LDS_TBL = LDS_WREG + 2 * W_SIZE;
constexpr unsigned LDS_Q = LDS_WREG;
constexpr unsigned LDS_ML = LDS_WREG + NC * W_SIZE;
constexpr unsigned LDS_WT = LDS_ML + NC * 256;
constexpr unsigned LDS_CTL = LDS_WT + NC * 256;
constexpr unsigned CTL_LAND = 0;
constexpr unsigned CTL_QOK = 16;
constexpr unsigned CTL_FREE = 32;
constexpr unsigned CTL_MLOK = 64;
constexpr unsigned CTL_ODONE = 96;
constexpr unsigned CTL_WTOK = 128;
constexpr unsigned LDS_TOTAL = LDS_CTL + 160;
static_assert(LDS_TOTAL <= 163840, "lds");

typedef __attribute__((ext_vector_type(8))) __bf16 bf16x8;
typedef __attribute__((ext_vector_type(2))) __bf16 bf16x2;
typedef __attribute__((ext_vector_type(4))) short s16x4;
typedef __attribute__((ext_vector_type(16))) float f32x16;
typedef __attribute__((ext_vector_type(4))) float f32x4;
typedef __attribute__((ext_vector_type(2))) float f32x2;
typedef __attribute__((ext_vector_type(4))) unsigned u32x4;
typedef __attribute__((ext_vector_type(2))) unsigned u32x2;
typedef __attribute__((ext_vector_type(4))) int i32x4;
#define LAS __attribute__((address_space(3)))

__device__ __forceinline__ float swap_max(float x) {
  auto r = __builtin_amdgcn_permlane32_swap(__float_as_uint(x), __float_as_uint(x), false, false);
  return fmaxf(__uint_as_float(r[0]), __uint_as_float(r[1]));
}
__device__ __forceinline__ float swap_sum(float x) {
  auto r = __builtin_amdgcn_permlane32_swap(__float_as_uint(x), __float_as_uint(x), false, false);
  return __uint_as_float(r[0]) + __uint_as_float(r[1]);
}
__device__ __forceinline__ unsigned pack2(float a, float b) {
  bf16x2 v = {(__bf16)a, (__bf16)b};
  return __builtin_bit_cast(unsigned, v);
}
template <int CTRL> __device__ __forceinline__ int dpp_add(int x) { return x + __builtin_amdgcn_update_dpp(0, x, CTRL, 0xf, 0xf, true); }
__device__ __forceinline__ unsigned now32() { return (unsigned)__builtin_amdgcn_s_memrealtime(); }
__device__ __forceinline__ int to_sgpr(int v) {
  int r;
  asm volatile("s_nop 1\n\tv_readfirstlane_b32 %0, %1" : "=s"(r) : "v"(v));
  return r;
}
#define LGKM0() asm volatile("s_waitcnt lgkmcnt(0)" ::: "memory")
#define VMWAIT(n) asm volatile("s_waitcnt vmcnt(" #n ")" ::: "memory")
#define CBAR() asm volatile("" ::: "memory")
template <int SLEEP> __device__ __forceinline__ void wait_above(unsigned addr, unsigned u) {
  unsigned v, cnt;
  if (SLEEP)
    asm volatile("s_mov_b32 %1, 0x200000\n\ts_branch 2f\n"
                 "1:\n\ts_sleep 1\n"
                 "2:\n\tds_read_b32 %0, %2\n\ts_sub_u32 %1, %1, 1\n\ts_waitcnt lgkmcnt(0)\n\t"
                 "v_cmp_ge_u32 vcc, %3, %0\n\ts_cbranch_scc1 3f\n\ts_nop 1\n\ts_cbranch_vccnz 1b\n3:"
                 : "=&v"(v), "=&s"(cnt) : "v"(addr), "s"(u) : "memory", "scc", "vcc");
  else
    asm volatile("s_mov_b32 %1, 0x200000\n"
                 "1:\n\tds_read_b32 %0, %2\n\ts_sub_u32 %1, %1, 1\n\ts_waitcnt lgkmcnt(0)\n\t"
                 "v_cmp_ge_u32 vcc, %3, %0\n\ts_cbranch_scc1 3f\n\ts_nop 1\n\ts_cbranch_vccnz 1b\n3:"
                 : "=&v"(v), "=&s"(cnt) : "v"(addr), "s"(u) : "memory", "scc", "vcc");
}

__global__ void __launch_bounds__(512, 1) __attribute__((amdgpu_num_vgpr(64)))
paged_attn(const void* __restrict__ qp, const void* __restrict__ kp, const void* __restrict__ vp, const int* __restrict__ bt,
           const int* __restrict__ lens, void* __restrict__ outp, char* __restrict__ ws, unsigned kvbytes, unsigned nphys) {
  extern __shared__ __attribute__((aligned(16))) char smem[];
  LAS char* lds = (LAS char*)smem;
  const int lane = threadIdx.x & 63;
  const int wv = __builtin_amdgcn_readfirstlane(threadIdx.x >> 6);
  const int g = blockIdx.x;
  const int h = MAPPING == 0 ? g >> 4 : MAPPING == 1 ? 2 * (g & 7) + ((g >> 3) & 1) : ((g & 7) >> 1) * 4 + ((g >> 3) & 3);
  constexpr float CS = 0.08838834764831845f * 1.4426950408889634f;
  volatile LAS unsigned* ctl = (volatile LAS unsigned*)(lds + LDS_CTL);

  const int lraw = lens[lane & 15];
  const i32x4 lq = *(const i32x4*)(lens + (lane >> 4) * 4);
  if (wv < 4) {
    if (wv < 2) __builtin_amdgcn_s_setprio(3);
    const __amdgpu_buffer_rsrc_t rb = __builtin_amdgcn_make_buffer_rsrc((void*)bt, 0, NB * NBPS * 4, 0x00020000);
    const unsigned vo = lane * 16 + wv * 4096;
    asm volatile("s_mov_b32 m0, %0\n\ts_nop 4\n\tbuffer_load_dwordx4 %2, %1, 0 offen lds\n\t"
                 "buffer_load_dwordx4 %2, %1, 0 offen offset:1024 lds\n\t"
                 "buffer_load_dwordx4 %2, %1, 0 offen offset:2048 lds\n\t"
                 "buffer_load_dwordx4 %2, %1, 0 offen offset:3072 lds"
                 :: "s"((unsigned)(size_t)lds + LDS_TBL + wv * 4096), "s"(rb), "v"(vo) : "memory");
  } else if (wv == 4) {
    if (lane < 40) ctl[lane] = 0u;
  }
  if (wv >= 4) asm volatile("s_waitcnt lgkmcnt(0)\n\ts_barrier" ::: "memory");

  const int lcl = (lraw <= 0 || lraw > SKV) ? SKV : lraw;
  const int np = (lcl + PAGE - 1) / PAGE;
  int b;
  if (MAPPING == 0) {
    int rank = 0;
    const int key = lcl * 16 + (lane & 15);
#pragma unroll
    for (int j = 0; j < 4; ++j) {
      const int lj = (lq[j] <= 0 || lq[j] > SKV) ? SKV : lq[j];
      rank += (lj * 16 + (lane >> 4) * 4 + j > key) ? 1 : 0;
    }
    auto r1 = __builtin_amdgcn_permlane16_swap((unsigned)rank, (unsigned)rank, false, false);
    rank = (int)(r1[0] + r1[1]);
    auto r2 = __builtin_amdgcn_permlane32_swap((unsigned)rank, (unsigned)rank, false, false);
    rank = (int)(r2[0] + r2[1]);
    const int want = (g & 8) ? 15 - (g & 7) : (g & 7);
    b = __builtin_ctz((unsigned)__builtin_amdgcn_ballot_w64(rank == want) | 0x8000u);
  } else {
    b = MAPPING == 1 ? (g >> 4) : (g >> 5) * 2 + (g & 1);
  }
  const int nown = __builtin_amdgcn_readlane(np, b);
  const int NT = (nown + 1) >> 1;

  if (wv < 4) asm volatile("s_waitcnt vmcnt(0) lgkmcnt(0)\n\ts_barrier" ::: "memory");
  if (wv == QWAVE) {
    const __amdgpu_buffer_rsrc_t rq = __builtin_amdgcn_make_buffer_rsrc((void*)qp, 0, NB * HQ * SQ * HD * 2, 0x00020000);
    const unsigned q0 = ((__builtin_amdgcn_readlane(lraw, b) > 0) ? (unsigned)((b * HQ + 2 * h) * SQ * HD * 2) : INVALID) + (lane >> 4) * 256 +
                        (((unsigned)(lane & 15) ^ ((lane >> 4) << 2)) << 4);
    asm volatile("s_mov_b32 m0, %0\n\ts_nop 4\n\t"
                 "buffer_load_dwordx4 %2, %1, 0 offen lds\n\t"
                 "buffer_load_dwordx4 %3, %1, 0 offen offset:1024 lds\n\t"
                 "buffer_load_dwordx4 %4, %1, 0 offen offset:2048 lds\n\t"
                 "buffer_load_dwordx4 %5, %1, 0 offen offset:3072 lds\n\t"
                 "s_add_u32 m0, m0, 4096\n\ts_nop 0\n\t"
                 "buffer_load_dwordx4 %2, %1, %6 offen lds\n\t"
                 "buffer_load_dwordx4 %3, %1, %6 offen offset:1024 lds\n\t"
                 "buffer_load_dwordx4 %4, %1, %6 offen offset:2048 lds\n\t"
                 "buffer_load_dwordx4 %5, %1, %6 offen offset:3072 lds\n\t"
                 "s_waitcnt vmcnt(0)"
                 :: "s"((unsigned)(size_t)lds + LDS_Q), "s"(rq), "v"(q0), "v"(q0 ^ 16), "v"(q0 ^ 32), "v"(q0 ^ 48), "s"(4096) : "memory", "scc");
    ctl[CTL_QOK / 4] = 1;
  }

  if (wv < 2) {
    const int half = wv;
    unsigned l0 = *(LAS unsigned*)(lds + LDS_TBL + (b * NBPS + 2 * lane + half) * 4);
    unsigned l1 = *(LAS unsigned*)(lds + LDS_TBL + (b * NBPS + 2 * lane + half + 128) * 4);
    int tot = np;
    tot = dpp_add<0x121>(tot); tot = dpp_add<0x122>(tot); tot = dpp_add<0x124>(tot); tot = dpp_add<0x128>(tot);
    const int step = to_sgpr((int)((float)(tot * PACE_X16) * __builtin_amdgcn_rcpf((float)nown)));
    const int due0 = half * (step >> 1);
    const int due1 = due0 + to_sgpr(tot * PACE_X16) / 2 + (GAP_TICKS + SOFT_TICKS) * 256;
    const unsigned rq4 = lane >> 4;
    const unsigned vo0 = rq4 * 4096 + (((unsigned)(lane & 15) ^ (rq4 << 2)) << 4);
    unsigned vo1 = (vo0 + 16384) ^ 16, vo2 = (vo0 + 32768) ^ 32, vo3 = (vo0 + 49152) ^ 48;
    const unsigned ring = (unsigned)(size_t)lds + LDS_RING + half * 4096;
    const unsigned vland = (unsigned)(size_t)lds + LDS_CTL + CTL_LAND + half * 4;
    const unsigned vfree = (unsigned)(size_t)lds + LDS_CTL + CTL_FREE + (lane & 7) * 4;
    l0 = (2 * lane + half < nown && l0 < nphys) ? l0 * PAGE_BYTES + h * (HD * 2) : INVALID;
    l1 = (2 * lane + half + 128 < nown && l1 < nphys) ? l1 * PAGE_BYTES + h * (HD * 2) : INVALID;
    STAMP(1);
    const unsigned t1 = now32();
    unsigned va0, va1, va2, va3, vf, vt, lc;
    asm volatile(
        "s_waitcnt vmcnt(0) lgkmcnt(0)\n\t"
        "s_mov_b32 s60, 0\n\t"
        "s_mov_b32 s61, 0\n\t"
        "s_mov_b32 s62, 0\n\t"
        "s_mov_b32 s73, 0\n\t"
        "s_mov_b32 s80, %[softat]\n\t"
        "s_mov_b32 s74, %[due0]\n\t"
        "s_lshl_b32 s75, %[nt], 1\n\t"
        "s_mov_b64 s[76:77], %[kp]\n\t"
        "s_mov_b32 s78, %[nrec]\n\t"
        "s_mov_b32 s79, 0x20000\n\t"
        "s_and_b32 s77, s77, 0xffff\n\t"
        "v_mov_b32 %[lc], %[l0]\n\t"
        "s_nop 4\n"
        ".LldT%=:\n\t"
        "s_memrealtime s[68:69]\n\t"
        "ds_read_b32 %[vf], %[vfree]\n\t"
        "s_getreg_b32 s64, hwreg(HW_REG_IB_STS)\n\t"
        "v_readlane_b32 s63, %[lc], s73\n\t"
        "s_and_b32 s65, s64, 15\n\t"
        "s_lshr_b32 s64, s64, 18\n\t"
        "s_and_b32 s64, s64, 48\n\t"
        "s_or_b32 s72, s64, s65\n\t"
        "s_add_u32 s65, s72, 3\n\t"
        "s_lshr_b32 s65, s65, 2\n\t"
        "s_sub_u32 s64, s60, s65\n\t"
        "s_cmp_gt_i32 s64, s61\n\t"
        "s_cbranch_scc1 .LldP%=\n"
        ".LldB%=:\n\t"
        "s_cmp_ge_i32 s60, s75\n\t"
        "s_cbranch_scc1 .LldD%=\n\t"
        "v_add_u32 %[a0], s63, %[o0]\n\t"
        "v_add_u32 %[a1], s63, %[o1]\n\t"
        "v_add_u32 %[a2], s63, %[o2]\n\t"
        "v_add_u32 %[a3], s63, %[o3]\n\t"
        "s_and_b32 s64, s60, 7\n\t"
        "s_lshl_b32 s65, s64, 13\n\t"
        "s_add_u32 m0, s65, %[ring]\n\t"
        "s_lshr_b32 s66, s60, 3\n\t"
        "s_cmp_eq_u32 s63, 0x80000000\n\t"
        "s_cselect_b32 s67, 0x80000000, s74\n\t"
        "s_waitcnt lgkmcnt(0)\n\t"
        "v_readlane_b32 s64, %[vf], s64\n\t"
        "s_sub_u32 s68, s68, %[t1]\n\t"
        "s_lshl_b32 s68, s68, 8\n\t"
        "s_cmp_lt_i32 s68, s67\n\t"
        "s_cbranch_scc1 .LldR%=\n\t"
        "s_cmp_lt_u32 s64, s66\n\t"
        "s_cbranch_scc1 .LldR%=\n\t"
        "s_cmp_gt_u32 s72, %[win]\n\t"
        "s_cbranch_scc1 .LldR%=\n\t"
        "s_waitcnt vmcnt(56)\n\t"
        "buffer_load_dwordx4 %[a0], s[76:79], 0 offen nt lds\n\t"
        "s_add_u32 m0, m0, 1024\n\t"
        "s_add_u32 s74, s74, %[step]\n\t"
        "buffer_load_dwordx4 %[a1], s[76:79], 0 offen nt lds\n\t"
        "s_add_u32 m0, m0, 1024\n\t"
        "s_add_u32 s60, s60, 1\n\t"
        "buffer_load_dwordx4 %[a2], s[76:79], 0 offen nt lds\n\t"
        "s_add_u32 m0, m0, 1024\n\t"
        "s_add_u32 s73, s73, 1\n\t"
        "buffer_load_dwordx4 %[a3], s[76:79], 0 offen nt lds\n\t"
        "s_cmp_eq_u32 s60, %[nt]\n\t"
        "s_cbranch_scc1 .LldV%=\n\t"
        "s_cmp_lg_u32 s73, 64\n\t"
        "s_cbranch_scc1 .LldT%=\n\t"
        "v_mov_b32 %[lc], %[l1]\n\t"
        "s_branch .LldT%=\n"
        ".LldV%=:\n\t"
        "s_mov_b64 s[76:77], %[vp]\n\t"
        "s_and_b32 s77, s77, 0xffff\n\t"
        "s_mov_b32 s73, 0\n\t"
        "s_mov_b32 s74, %[due1]\n\t"
        "v_mov_b32 %[lc], %[l0]\n\t"
        "v_xor_b32 %[o1], 16, %[o1]\n\t"
        "v_xor_b32 %[o2], 32, %[o2]\n\t"
        "v_xor_b32 %[o3], 48, %[o3]\n\t"
        "s_branch .LldT%=\n"
        ".LldP%=:\n\t"
        "s_min_u32 s65, s65, 15\n\t"
        "s_lshl_b32 s66, s65, 3\n\t"
        "s_add_u32 s66, s66, 12\n\t"
        "s_getpc_b64 s[70:71]\n\t"
        "s_add_u32 s70, s70, s66\n\t"
        "s_addc_u32 s71, s71, 0\n\t"
        "s_setpc_b64 s[70:71]\n\t"
        "s_waitcnt vmcnt(0)\n\ts_branch .LldQ%=\n\t"
        "s_waitcnt vmcnt(4)\n\ts_branch .LldQ%=\n\t"
        "s_waitcnt vmcnt(8)\n\ts_branch .LldQ%=\n\t"
        "s_waitcnt vmcnt(12)\n\ts_branch .LldQ%=\n\t"
        "s_waitcnt vmcnt(16)\n\ts_branch .LldQ%=\n\t"
        "s_waitcnt vmcnt(20)\n\ts_branch .LldQ%=\n\t"
        "s_waitcnt vmcnt(24)\n\ts_branch .LldQ%=\n\t"
        "s_waitcnt vmcnt(28)\n\ts_branch .LldQ%=\n\t"
        "s_waitcnt vmcnt(32)\n\ts_branch .LldQ%=\n\t"
        "s_waitcnt vmcnt(36)\n\ts_branch .LldQ%=\n\t"
        "s_waitcnt vmcnt(40)\n\ts_branch .LldQ%=\n\t"
        "s_waitcnt vmcnt(44)\n\ts_branch .LldQ%=\n\t"
        "s_waitcnt vmcnt(48)\n\ts_branch .LldQ%=\n\t"
        "s_waitcnt vmcnt(52)\n\ts_branch .LldQ%=\n\t"
        "s_waitcnt vmcnt(56)\n\ts_branch .LldQ%=\n\t"
        "s_waitcnt vmcnt(60)\n"
        ".LldQ%=:\n\t"
        "s_mov_b32 s61, s64\n\t"
        "v_mov_b32 %[vt], s64\n\t"
        "ds_write_b32 %[vland], %[vt]\n\t"
        "s_branch .LldB%=\n"
        ".LldD%=:\n\t"
        "s_waitcnt lgkmcnt(0)\n\t"
        "s_cmp_ge_i32 s61, s75\n\t"
        "s_cbranch_scc1 .LldE%=\n"
        ".LldR%=:\n\t"
        "s_add_u32 s62, s62, 1\n\t"
        "s_cmp_lt_u32 s62, 0x400000\n\t"
        "s_cbranch_scc1 .LldT%=\n"
        ".LldE%=:\n\t"
        "s_waitcnt vmcnt(0) lgkmcnt(0)"
        : [a0] "=&v"(va0), [a1] "=&v"(va1), [a2] "=&v"(va2), [a3] "=&v"(va3), [vf] "=&v"(vf), [vt] "=&v"(vt), [lc] "=&v"(lc),
          [o1] "+v"(vo1), [o2] "+v"(vo2), [o3] "+v"(vo3)
        : [l0] "v"(l0), [l1] "v"(l1), [o0] "v"(vo0), [vland] "v"(vland), [vfree] "v"(vfree),
          [kp] "s"(kp), [vp] "s"(vp), [nrec] "s"(kvbytes), [nt] "s"(NT), [ring] "s"(ring), [t1] "s"(t1), [step] "s"(step),
          [due0] "s"(due0), [due1] "s"(due1), [win] "n"(WINDOW - 4), [soft] "n"(SOFT_TICKS * 256), [softat] "n"(SOFT_PAGES * 256)
        : "memory", "scc", "s60", "s61", "s62", "s63", "s64", "s65", "s66", "s67", "s68", "s69", "s70", "s71", "s72", "s73", "s74", "s75",
          "s76", "s77", "s78", "s79", "s80");
    STAMP(3);
    return;
  }

  const int w = wv - 2;
  const int r32 = lane & 31, hi = lane >> 5;
  const int nmine = (NT > w) ? (NT - w + NC - 1) / NC : 0;
  const unsigned lbase = (unsigned)(size_t)lds;
  const unsigned aland = lbase + LDS_CTL + CTL_LAND + (lane & 1) * 4;
  const unsigned palp = LDS_WREG + w * W_SIZE + W_ALP + r32 * 4;
  const unsigned ppst = LDS_WREG + w * W_SIZE + lane * 16;
  asm volatile("" ::: "v128", "v255");
  unsigned resc = 0;
  float m = -3.0e38f, mprev = -3.0e38f, l = 0.f;

  {
    const unsigned fk = (((unsigned)r32 & 3) << 2) | (((unsigned)r32 >> 2) & 3);
    const unsigned kaddr0 = LDS_RING + r32 * 256 + (((unsigned)hi ^ fk) << 4);
    bf16x8 qf[8];
    wait_above<1>(lbase + LDS_CTL + CTL_QOK, 0);
#pragma unroll
    for (int s = 0; s < 8; ++s) qf[s] = *(LAS bf16x8*)(lds + ((kaddr0 + LDS_Q - LDS_RING) ^ (s << 5)));
    const int lim = __builtin_amdgcn_readlane(lcl, b);
#pragma unroll 1
    for (int k = 0; k < nmine; ++k) {
      const unsigned t = w + NC * k;
      wait_above<1>(aland, t);
      if (k == 0) STAMP(1);
      bf16x8 kf[8];
#pragma unroll
      for (int s = 0; s < 8; ++s) kf[s] = *(LAS bf16x8*)(lds + ((kaddr0 + ((t & 7) << 13)) ^ (s << 5)));
      asm volatile("s_waitcnt lgkmcnt(0)" : "+v"(kf[0]), "+v"(kf[1]), "+v"(kf[2]), "+v"(kf[3]), "+v"(kf[4]), "+v"(kf[5]), "+v"(kf[6]), "+v"(kf[7]) :: "memory");
      ctl[CTL_FREE / 4 + (t & 7)] = (t >> 3) + 1;
      f32x16 sc;
#pragma unroll
      for (int r = 0; r < 16; ++r) sc[r] = 0.f;
#pragma unroll
      for (int s = 0; s < 8; ++s) sc = __builtin_amdgcn_mfma_f32_32x32x16_bf16(kf[s], qf[s], sc, 0, 0, 0);
      if ((int)t == NT - 1) {
        const int kb = 2 * t * PAGE + 4 * hi;
#pragma unroll
        for (int r = 0; r < 16; ++r) sc[r] = (kb + 8 * (r >> 2) + (r & 3) < lim) ? sc[r] : -__builtin_inff();
      }
      float mx = sc[0];
#pragma unroll
      for (int r = 1; r < 16; ++r) mx = fmaxf(mx, sc[r]);
      mx = swap_max(mx);
      const float mn = fmaxf(m, mx);
      const float alpha = __builtin_amdgcn_exp2f((m - mn) * CS);
      mprev = m;
      m = mn;
      const float nmc = -mn * CS;
      float ps = 0.f;
#pragma unroll
      for (int r = 0; r < 16; ++r) { sc[r] = __builtin_amdgcn_exp2f(fmaf(sc[r], CS, nmc)); ps += sc[r]; }
      l = fmaf(l, alpha, ps);
      u32x4 pf[2];
#pragma unroll
      for (int s = 0; s < 2; ++s)
        pf[s] = u32x4{pack2(sc[8 * s], sc[8 * s + 1]), pack2(sc[8 * s + 2], sc[8 * s + 3]), pack2(sc[8 * s + 4], sc[8 * s + 5]), pack2(sc[8 * s + 6], sc[8 * s + 7])};
      if (__builtin_amdgcn_ballot_w64(alpha != 1.0f)) resc |= 1u << k;
      *(LAS float*)(lds + palp + k * 128) = alpha;
      if (k >= KREG || k == nmine - 1) {
        const unsigned dst = ppst + ((k == nmine - 1) ? W_LAST : (k - KREG) * 2048);
        *(LAS u32x4*)(lds + dst) = pf[0];
        *(LAS u32x4*)(lds + dst + 1024) = pf[1];
      } else {
        asm volatile("s_set_gpr_idx_on %8, gpr_idx(DST)\n\t"
                     "v_mov_b32 v128, %0\n\tv_mov_b32 v129, %1\n\tv_mov_b32 v130, %2\n\tv_mov_b32 v131, %3\n\t"
                     "v_mov_b32 v132, %4\n\tv_mov_b32 v133, %5\n\tv_mov_b32 v134, %6\n\tv_mov_b32 v135, %7\n\t"
                     "s_set_gpr_idx_off"
                     :: "v"(pf[0][0]), "v"(pf[0][1]), "v"(pf[0][2]), "v"(pf[0][3]), "v"(pf[1][0]), "v"(pf[1][1]), "v"(pf[1][2]), "v"(pf[1][3]), "s"(8 * k));
      }
    }
  }
  l = swap_sum(l);
  *(LAS f32x2*)(lds + LDS_ML + w * 256 + r32 * 8) = f32x2{m, l};
  LGKM0();
  ctl[CTL_MLOK / 4 + w] = 1;
  STAMP(2);

  const unsigned tq = (lane & 15) >> 2, tp = lane & 3, tblk = (lane >> 4) & 1;
  const unsigned vaddr0 = LDS_RING + (4 * hi + tq) * 256 + (tq << 6) + ((2 * tblk + (tp >> 1)) << 4) + ((tp & 1) << 3);
  const int e0 = (NT + NC - 1) % NC;
  const int pos = NC - 1 - (e0 - w + NC) % NC;
  f32x16 o[4];
#pragma unroll
  for (int i = 0; i < 4; ++i)
#pragma unroll
    for (int r = 0; r < 16; ++r) o[i][r] = 0.f;
  auto weights = [&]() {
    wait_above<1>(lbase + LDS_CTL + CTL_MLOK + min(lane, NC - 1) * 4, 0);
    unsigned a = LDS_ML + r32 * 8;
    asm volatile("" : "+v"(a) :: "memory");
    float M = -3.0e38f, L = 0.f;
    f32x2 ml[NC];
#pragma unroll
    for (int i = 0; i < NC; ++i) { ml[i] = *(LAS f32x2*)(lds + a + i * 256); M = fmaxf(M, ml[i][0]); }
#pragma unroll
    for (int i = 0; i < NC; ++i) L = fmaf(__builtin_amdgcn_exp2f((ml[i][0] - M) * CS), ml[i][1], L);
    const float inv = 1.0f / L;
    *(LAS f32x2*)(lds + a + (LDS_WT - LDS_ML) + w * 256) = f32x2{__builtin_amdgcn_exp2f((mprev - M) * CS) * inv, __builtin_amdgcn_exp2f((m - M) * CS) * inv};
    LGKM0();
    ctl[CTL_WTOK / 4 + w] = 1;
  };
  resc &= ~1u;
  if (nmine <= 2) weights();
#pragma unroll 1
  for (int k = 0; k < nmine - 1; ++k) {
    if (k == 1) weights();
    if ((resc >> k) & 1) {
      const float alpha = *(LAS float*)(lds + palp + k * 128);
#pragma unroll
      for (int i = 0; i < 4; ++i)
#pragma unroll
        for (int r = 0; r < 16; ++r) o[i][r] *= alpha;
    }
    const unsigned u = NT + w + NC * k;
    u32x4 pf[2];
    if (k >= KREG) {
      pf[0] = *(LAS u32x4*)(lds + ppst + (k - KREG) * 2048);
      pf[1] = *(LAS u32x4*)(lds + ppst + (k - KREG) * 2048 + 1024);
    } else {
      asm volatile("s_set_gpr_idx_on %8, gpr_idx(SRC0)\n\t"
                   "v_mov_b32 %0, v128\n\tv_mov_b32 %1, v129\n\tv_mov_b32 %2, v130\n\tv_mov_b32 %3, v131\n\t"
                   "v_mov_b32 %4, v132\n\tv_mov_b32 %5, v133\n\tv_mov_b32 %6, v134\n\tv_mov_b32 %7, v135\n\t"
                   "s_set_gpr_idx_off\n\ts_nop 1"
                   : "=v"(pf[0][0]), "=v"(pf[0][1]), "=v"(pf[0][2]), "=v"(pf[0][3]), "=v"(pf[1][0]), "=v"(pf[1][1]), "=v"(pf[1][2]), "=v"(pf[1][3])
                   : "s"(8 * k));
    }
    asm volatile("" : "+v"(pf[0]), "+v"(pf[1]), "+v"(o[0]), "+v"(o[1]), "+v"(o[2]), "+v"(o[3]));
    wait_above<1>(aland, u);
    bf16x8 vf[4][2];
#pragma unroll
    for (int i = 0; i < 4; ++i) {
      const unsigned a = (vaddr0 + ((u & 7) << 13)) ^ (i << 6);
#pragma unroll
      for (int s = 0; s < 2; ++s) {
        const s16x4 lo = __builtin_amdgcn_ds_read_tr16_b64_v4i16((LAS s16x4*)(lds + a + 4096 * s));
        const s16x4 up = __builtin_amdgcn_ds_read_tr16_b64_v4i16((LAS s16x4*)(lds + a + 4096 * s + 2048));
        vf[i][s] = __builtin_bit_cast(bf16x8, __builtin_shufflevector(lo, up, 0, 1, 2, 3, 4, 5, 6, 7));
      }
    }
    asm volatile("s_waitcnt lgkmcnt(0)" : "+v"(vf[0][0]), "+v"(vf[0][1]), "+v"(vf[1][0]), "+v"(vf[1][1]), "+v"(vf[2][0]), "+v"(vf[2][1]), "+v"(vf[3][0]), "+v"(vf[3][1]) :: "memory");
    ctl[CTL_FREE / 4 + (u & 7)] = (u >> 3) + 1;
#pragma unroll
    for (int s = 0; s < 2; ++s)
#pragma unroll
      for (int i = 0; i < 4; ++i) o[i] = __builtin_amdgcn_mfma_f32_32x32x16_bf16(vf[i][s], __builtin_bit_cast(bf16x8, pf[s]), o[i], 0, 0, 0);
  }
  STAMP(3);

  {
    unsigned l16 = lane * 16;
    asm volatile("" : "+v"(l16));
    if (nmine > 1) {
#pragma unroll
      for (int i = 0; i < 4; ++i) {
        if (i != pos) {
          const unsigned dst = (i == 3 && pos > 3) ? LDS_RING + ((2 * NT - 2 * NC + pos) & 7) * 8192 + l16
                                                   : LDS_WREG + w * W_SIZE + l16 + (i > pos ? i - 1 : i) * 4096;
#pragma unroll
          for (int j = 0; j < 4; ++j) *(LAS f32x4*)(lds + dst + j * 1024) = f32x4{o[i][4 * j], o[i][4 * j + 1], o[i][4 * j + 2], o[i][4 * j + 3]};
        }
      }
      LGKM0();
      ctl[CTL_ODONE / 4 + pos] = 1;
    }
    if (pos < 4) {
      f32x16 of = (pos == 0) ? o[0] : (pos == 1) ? o[1] : (pos == 2) ? o[2] : o[3];
      const unsigned r8 = (l16 >> 1) & 255, l4 = l16 >> 2;
      wait_above<1>(lbase + LDS_CTL + CTL_WTOK + min(l4, (NC - 1) * 4u), 0);
      auto wave_of = [&](int p) { const int sw = e0 + 1 + p; return (sw >= NC) ? sw - NC : sw; };
      auto src_o = [&](int p) -> unsigned {
        return l16 + ((pos == 3 && p > 3) ? LDS_RING + ((2 * NT - 2 * NC + p) & 7) * 8192 : LDS_WREG + wave_of(p) * W_SIZE + (pos > p ? pos - 1 : pos) * 4096);
      };
      auto fma_o = [&](unsigned src, float c) {
#pragma unroll
        for (int j = 0; j < 4; ++j) {
          const f32x4 x = *(LAS f32x4*)(lds + src + j * 1024);
#pragma unroll
          for (int e = 0; e < 4; ++e) of[4 * j + e] = fmaf(x[e], c, of[4 * j + e]);
        }
      };
      auto fma_t = [&](int p, bf16x8 p0, bf16x8 p1, float c) {
        const unsigned a = (vaddr0 + (((2 * NT - NC + p) & 7) << 13)) ^ (pos << 6);
        bf16x8 vf[2];
#pragma unroll
        for (int s = 0; s < 2; ++s) {
          const s16x4 lo = __builtin_amdgcn_ds_read_tr16_b64_v4i16((LAS s16x4*)(lds + a + 4096 * s));
          const s16x4 up = __builtin_amdgcn_ds_read_tr16_b64_v4i16((LAS s16x4*)(lds + a + 4096 * s + 2048));
          vf[s] = __builtin_bit_cast(bf16x8, __builtin_shufflevector(lo, up, 0, 1, 2, 3, 4, 5, 6, 7));
        }
        f32x16 x;
#pragma unroll
        for (int r = 0; r < 16; ++r) x[r] = 0.f;
        x = __builtin_amdgcn_mfma_f32_32x32x16_bf16(vf[0], p0, x, 0, 0, 0);
        x = __builtin_amdgcn_mfma_f32_32x32x16_bf16(vf[1], p1, x, 0, 0, 0);
#pragma unroll
        for (int r = 0; r < 16; ++r) of[r] = fmaf(x[r], c, of[r]);
      };
      {
        const float cw = *(LAS float*)(lds + LDS_WT + w * 256 + r8);
#pragma unroll
        for (int r = 0; r < 16; ++r) of[r] *= cw;
      }
      if (NT >= 2 * NC) {
        float co[5], ct[NC];
        bf16x8 pl[NC][2];
        auto ld_p = [&](int p) {
          pl[p][0] = *(LAS bf16x8*)(lds + LDS_WREG + wave_of(p) * W_SIZE + W_LAST + l16);
          pl[p][1] = *(LAS bf16x8*)(lds + LDS_WREG + wave_of(p) * W_SIZE + W_LAST + l16 + 1024);
        };
#define PIN_OF() asm volatile("" : "+v"(of))
#pragma unroll
        for (int j = 0; j < 5; ++j) co[j] = *(LAS float*)(lds + LDS_WT + wave_of(j + (j >= pos ? 1 : 0)) * 256 + r8);
#pragma unroll
        for (int p = 0; p < NC; ++p) ct[p] = *(LAS float*)(lds + LDS_WT + 4 + wave_of(p) * 256 + r8);
        ld_p(0); ld_p(1);
        PIN_OF();
        wait_above<0>(lbase + LDS_CTL + CTL_ODONE + (l4 & 12), 0);
#pragma unroll
        for (int j = 0; j < 3; ++j) fma_o(src_o(j + (j >= pos ? 1 : 0)), co[j]);
        PIN_OF();
        wait_above<0>(aland, 2 * NT - NC + 1);
        fma_t(0, pl[0][0], pl[0][1], ct[0]);
        fma_t(1, pl[1][0], pl[1][1], ct[1]);
        PIN_OF();
        ld_p(2); ld_p(3); ld_p(4); ld_p(5);
        wait_above<0>(lbase + LDS_CTL + CTL_ODONE + 16 + (l4 & 4), 0);
        fma_o(src_o(4), co[3]);
        fma_o(src_o(5), co[4]);
        PIN_OF();
        STAMP(4);
        wait_above<0>(aland, 2 * NT - NC + 3);
        fma_t(2, pl[2][0], pl[2][1], ct[2]);
        fma_t(3, pl[3][0], pl[3][1], ct[3]);
        PIN_OF();
        wait_above<0>(aland, 2 * NT - NC + 4);
        fma_t(4, pl[4][0], pl[4][1], ct[4]);
        PIN_OF();
        wait_above<0>(aland, 2 * NT - NC + 5);
        fma_t(5, pl[5][0], pl[5][1], ct[5]);
      } else {
#pragma unroll 1
        for (int p = max(NC - NT, 0); p < NC; ++p) {
          const f32x2 c = *(LAS f32x2*)(lds + LDS_WT + wave_of(p) * 256 + r8);
          if (p != pos && NT - 2 * NC + p >= 0) {
            wait_above<1>(lbase + LDS_CTL + CTL_ODONE + p * 4, 0);
            fma_o(src_o(p), c[0]);
          }
          wait_above<1>(aland, 2 * NT - NC + p);
          fma_t(p, *(LAS bf16x8*)(lds + LDS_WREG + wave_of(p) * W_SIZE + W_LAST + l16),
                *(LAS bf16x8*)(lds + LDS_WREG + wave_of(p) * W_SIZE + W_LAST + l16 + 1024), c[1]);
        }
      }
      char* orow = (char*)outp + (size_t)(b * HQ + 2 * h) * SQ * HD * 2 + pos * 64 + ((l16 & 511) << 4) + ((l16 >> 5) & 16);
#pragma unroll
      for (int j = 0; j < 2; ++j) {
        const unsigned a0 = pack2(of[8 * j], of[8 * j + 1]), a1 = pack2(of[8 * j + 2], of[8 * j + 3]);
        const unsigned b0 = pack2(of[8 * j + 4], of[8 * j + 5]), b1 = pack2(of[8 * j + 6], of[8 * j + 7]);
        auto s0 = __builtin_amdgcn_permlane32_swap(a0, b0, false, false);
        auto s1 = __builtin_amdgcn_permlane32_swap(a1, b1, false, false);
        u32x4 val = {s0[0], s1[0], s0[1], s1[1]};
        *(u32x4*)(orow + j * 32) = val;
      }
    }
  }
}
}

extern "C" void kernel_launch(void* const* d_in, const int* in_sizes, int n_in, void* d_out, int out_size, void* d_ws,
                              size_t ws_size, hipStream_t stream) {
  (void)n_in; (void)out_size; (void)ws_size;
  const unsigned nphys = (unsigned)(in_sizes[1] / (PAGE * HKV * HD));
  const unsigned kvbytes = nphys * PAGE_BYTES;
  (void)hipFuncSetAttribute((const void*)paged_attn, hipFuncAttributeMaxDynamicSharedMemorySize, LDS_TOTAL);
  hipLaunchKernelGGL(paged_attn, dim3(256), dim3(512), LDS_TOTAL, stream, d_in[0], d_in[1], d_in[2], (const int*)d_in[3],
                     (const int*)d_in[4], d_out, (char*)d_ws, kvbytes, nphys);
}
```

```cpp
#define PACE_X16 456
#define OUTAUX 2
#include <hip/hip_runtime.h>
#include <hip/hip_bf16.h>

#define GAP_TICKS 0
#define WINDOW 24
#define QWAVE 2
#define SOFT_PAGES 0
#define SOFT_TICKS 0
#define MAPPING 0
#define CSLEEP 1

#define STAMP(i) do {} while (0)

namespace {
constexpr int NB = 16, HQ = 32, HKV = 16, SQ = 16, HD = 128, PAGE = 16, NBPS = 256, SKV = NBPS * PAGE;
constexpr unsigned PAGE_BYTES = PAGE * HKV * HD * 2;
constexpr unsigned INVALID = 0x80000000u;
constexpr int NC = 6;
constexpr int KREG = 16, KLDS = 5;
static_assert(NC * (KREG + KLDS + 1) >= NBPS / 2, "P storage");
constexpr unsigned LDS_RING = 0;
constexpr unsigned LDS_WREG = 65536;
constexpr unsigned W_ALP = KLDS * 2048;
constexpr unsigned W_LAST = W_ALP + (KREG + KLDS + 1) * 128;
constexpr unsigned W_SIZE = W_LAST + 2048;
static_assert(W_LAST >= 3 * 4096 && W_SIZE % 256 == 0, "consumer region");
constexpr unsigned LDS_TBL = LDS_WREG + 2 * W_SIZE;
constexpr unsigned LDS_Q = LDS_WREG;
constexpr unsigned LDS_ML = LDS_WREG + NC * W_SIZE;
constexpr unsigned LDS_WT = LDS_ML + NC * 256;
constexpr unsigned LDS_CTL = LDS_WT + NC * 256;
constexpr unsigned CTL_LAND = 0;
constexpr unsigned CTL_QOK = 16;
constexpr unsigned CTL_FREE = 32;
constexpr unsigned CTL_MLOK = 64;
constexpr unsigned CTL_ODONE = 96;
constexpr unsigned CTL_WTOK = 128;
constexpr unsigned LDS_TOTAL = LDS_CTL + 160;
static_assert(LDS_TOTAL <= 163840, "lds");

typedef __attribute__((ext_vector_type(8))) __bf16 bf16x8;
typedef __attribute__((ext_vector_type(2))) __bf16 bf16x2;
typedef __attribute__((ext_vector_type(4))) short s16x4;
typedef __attribute__((ext_vector_type(16))) float f32x16;
typedef __attribute__((ext_vector_type(4))) float f32x4;
typedef __attribute__((ext_vector_type(2))) float f32x2;
typedef __attribute__((ext_vector_type(4))) unsigned u32x4;
typedef __attribute__((ext_vector_type(2))) unsigned u32x2;
typedef __attribute__((ext_vector_type(4))) int i32x4;
#define LAS __attribute__((address_space(3)))

__device__ __forceinline__ float swap_max(float x) {
  auto r = __builtin_amdgcn_permlane32_swap(__float_as_uint(x), __float_as_uint(x), false, false);
  return fmaxf(__uint_as_float(r[0]), __uint_as_float(r[1]));
}
__device__ __forceinline__ float swap_sum(float x) {
  auto r = __builtin_amdgcn_permlane32_swap(__float_as_uint(x), __float_as_uint(x), false, false);
  return __uint_as_float(r[0]) + __uint_as_float(r[1]);
}
__device__ __forceinline__ unsigned pack2(float a, float b) {
  bf16x2 v = {(__bf16)a, (__bf16)b};
  return __builtin_bit_cast(unsigned, v);
}
template <int CTRL> __device__ __forceinline__ int dpp_add(int x) { return x + __builtin_amdgcn_update_dpp(0, x, CTRL, 0xf, 0xf, true); }
__device__ __forceinline__ unsigned now32() { return (unsigned)__builtin_amdgcn_s_memrealtime(); }
__device__ __forceinline__ int to_sgpr(int v) {
  int r;
  asm volatile("s_nop 1\n\tv_readfirstlane_b32 %0, %1" : "=s"(r) : "v"(v));
  return r;
}
#define LGKM0() asm volatile("s_waitcnt lgkmcnt(0)" ::: "memory")
#define VMWAIT(n) asm volatile("s_waitcnt vmcnt(" #n ")" ::: "memory")
#define CBAR() asm volatile("" ::: "memory")
template <int SLEEP> __device__ __forceinline__ void wait_above(unsigned addr, unsigned u) {
  unsigned v, cnt;
  if (SLEEP)
    asm volatile("s_mov_b32 %1, 0x200000\n\ts_branch 2f\n"
                 "1:\n\ts_sleep 1\n"
                 "2:\n\tds_read_b32 %0, %2\n\ts_sub_u32 %1, %1, 1\n\ts_waitcnt lgkmcnt(0)\n\t"
                 "v_cmp_ge_u32 vcc, %3, %0\n\ts_cbranch_scc1 3f\n\ts_nop 1\n\ts_cbranch_vccnz 1b\n3:"
                 : "=&v"(v), "=&s"(cnt) : "v"(addr), "s"(u) : "memory", "scc", "vcc");
  else
    asm volatile("s_mov_b32 %1, 0x200000\n"
                 "1:\n\tds_read_b32 %0, %2\n\ts_sub_u32 %1, %1, 1\n\ts_waitcnt lgkmcnt(0)\n\t"
                 "v_cmp_ge_u32 vcc, %3, %0\n\ts_cbranch_scc1 3f\n\ts_nop 1\n\ts_cbranch_vccnz 1b\n3:"
                 : "=&v"(v), "=&s"(cnt) : "v"(addr), "s"(u) : "memory", "scc", "vcc");
}

__global__ void __launch_bounds__(512, 1) __attribute__((amdgpu_num_vgpr(64)))
paged_attn(const void* __restrict__ qp, const void* __restrict__ kp, const void* __restrict__ vp, const int* __restrict__ bt,
           const int* __restrict__ lens, void* __restrict__ outp, char* __restrict__ ws, unsigned kvbytes, unsigned nphys) {
  extern __shared__ __attribute__((aligned(16))) char smem[];
  LAS char* lds = (LAS char*)smem;
  const int lane = threadIdx.x & 63;
  const int wv = __builtin_amdgcn_readfirstlane(threadIdx.x >> 6);
  const int g = blockIdx.x;
  const int h = MAPPING == 0 ? g >> 4 : MAPPING == 1 ? 2 * (g & 7) + ((g >> 3) & 1) : ((g & 7) >> 1) * 4 + ((g >> 3) & 3);
  constexpr float CS = 0.08838834764831845f * 1.4426950408889634f;
  volatile LAS unsigned* ctl = (volatile LAS unsigned*)(lds + LDS_CTL);

  const int lraw = lens[lane & 15];
  const i32x4 lq = *(const i32x4*)(lens + (lane >> 4) * 4);
  if (wv < 4) {
    if (wv < 2) __builtin_amdgcn_s_setprio(3);
    const __amdgpu_buffer_rsrc_t rb = __builtin_amdgcn_make_buffer_rsrc((void*)bt, 0, NB * NBPS * 4, 0x00020000);
    const unsigned vo = lane * 16 + wv * 4096;
    asm volatile("s_mov_b32 m0, %0\n\ts_nop 4\n\tbuffer_load_dwordx4 %2, %1, 0 offen lds\n\t"
                 "buffer_load_dwordx4 %2, %1, 0 offen offset:1024 lds\n\t"
                 "buffer_load_dwordx4 %2, %1, 0 offen offset:2048 lds\n\t"
                 "buffer_load_dwordx4 %2, %1, 0 offen offset:3072 lds"
                 :: "s"((unsigned)(size_t)lds + LDS_TBL + wv * 4096), "s"(rb), "v"(vo) : "memory");
  } else if (wv == 4) {
    if (lane < 40) ctl[lane] = 0u;
  }
  if (wv >= 4) asm volatile("s_waitcnt lgkmcnt(0)\n\ts_barrier" ::: "memory");

  const int lcl = (lraw <= 0 || lraw > SKV) ? SKV : lraw;
  const int np = (lcl + PAGE - 1) / PAGE;
  int b;
  if (MAPPING == 0) {
    int rank = 0;
    const int key = lcl * 16 + (lane & 15);
#pragma unroll
    for (int j = 0; j < 4; ++j) {
      const int lj = (lq[j] <= 0 || lq[j] > SKV) ? SKV : lq[j];
      rank += (lj * 16 + (lane >> 4) * 4 + j > key) ? 1 : 0;
    }
    auto r1 = __builtin_amdgcn_permlane16_swap((unsigned)rank, (unsigned)rank, false, false);
    rank = (int)(r1[0] + r1[1]);
    auto r2 = __builtin_amdgcn_permlane32_swap((unsigned)rank, (unsigned)rank, false, false);
    rank = (int)(r2[0] + r2[1]);
    const int want = (g & 8) ? 15 - (g & 7) : (g & 7);
    b = __builtin_ctz((unsigned)__builtin_amdgcn_ballot_w64(rank == want) | 0x8000u);
  } else {
    b = MAPPING == 1 ? (g >> 4) : (g >> 5) * 2 + (g & 1);
  }
  const int nown = __builtin_amdgcn_readlane(np, b);
  const int NT = (nown + 1) >> 1;

  if (wv < 4) asm volatile("s_waitcnt vmcnt(0) lgkmcnt(0)\n\ts_barrier" ::: "memory");
  if (wv == QWAVE) {
    const __amdgpu_buffer_rsrc_t rq = __builtin_amdgcn_make_buffer_rsrc((void*)qp, 0, NB * HQ * SQ * HD * 2, 0x00020000);
    const unsigned q0 = ((__builtin_amdgcn_readlane(lraw, b) > 0) ? (unsigned)((b * HQ + 2 * h) * SQ * HD * 2) : INVALID) + (lane >> 4) * 256 +
                        (((unsigned)(lane & 15) ^ ((lane >> 4) << 2)) << 4);
    asm volatile("s_mov_b32 m0, %0\n\ts_nop 4\n\t"
                 "buffer_load_dwordx4 %2, %1, 0 offen lds\n\t"
                 "buffer_load_dwordx4 %3, %1, 0 offen offset:1024 lds\n\t"
                 "buffer_load_dwordx4 %4, %1, 0 offen offset:2048 lds\n\t"
                 "buffer_load_dwordx4 %5, %1, 0 offen offset:3072 lds\n\t"
                 "s_add_u32 m0, m0, 4096\n\ts_nop 0\n\t"
                 "buffer_load_dwordx4 %2, %1, %6 offen lds\n\t"
                 "buffer_load_dwordx4 %3, %1, %6 offen offset:1024 lds\n\t"
                 "buffer_load_dwordx4 %4, %1, %6 offen offset:2048 lds\n\t"
                 "buffer_load_dwordx4 %5, %1, %6 offen offset:3072 lds\n\t"
                 "s_waitcnt vmcnt(0)"
                 :: "s"((unsigned)(size_t)lds + LDS_Q), "s"(rq), "v"(q0), "v"(q0 ^ 16), "v"(q0 ^ 32), "v"(q0 ^ 48), "s"(4096) : "memory", "scc");
    ctl[CTL_QOK / 4] = 1;
  }

  if (wv < 2) {
    const int half = wv;
    unsigned l0 = *(LAS unsigned*)(lds + LDS_TBL + (b * NBPS + 2 * lane + half) * 4);
    unsigned l1 = *(LAS unsigned*)(lds + LDS_TBL + (b * NBPS + 2 * lane + half + 128) * 4);
    int tot = np;
    tot = dpp_add<0x121>(tot); tot = dpp_add<0x122>(tot); tot = dpp_add<0x124>(tot); tot = dpp_add<0x128>(tot);
    const int step = to_sgpr((int)((float)(tot * PACE_X16) * __builtin_amdgcn_rcpf((float)nown)));
    const int due0 = half * (step >> 1);
    const int due1 = due0 + to_sgpr(tot * PACE_X16) / 2 + (GAP_TICKS + SOFT_TICKS) * 256;
    const unsigned rq4 = lane >> 4;
    const unsigned vo0 = rq4 * 4096 + (((unsigned)(lane & 15) ^ (rq4 << 2)) << 4);
    unsigned vo1 = (vo0 + 16384) ^ 16, vo2 = (vo0 + 32768) ^ 32, vo3 = (vo0 + 49152) ^ 48;
    const unsigned ring = (unsigned)(size_t)lds + LDS_RING + half * 4096;
    const unsigned vland = (unsigned)(size_t)lds + LDS_CTL + CTL_LAND + half * 4;
    const unsigned vfree = (unsigned)(size_t)lds + LDS_CTL + CTL_FREE + (lane & 7) * 4;
    l0 = (2 * lane + half < nown && l0 < nphys) ? l0 * PAGE_BYTES + h * (HD * 2) : INVALID;
    l1 = (2 * lane + half + 128 < nown && l1 < nphys) ? l1 * PAGE_BYTES + h * (HD * 2) : INVALID;
    STAMP(1);
    const unsigned t1 = now32();
    unsigned va0, va1, va2, va3, vf, vt, lc;
    asm volatile(
        "s_waitcnt vmcnt(0) lgkmcnt(0)\n\t"
        "s_mov_b32 s60, 0\n\t"
        "s_mov_b32 s61, 0\n\t"
        "s_mov_b32 s62, 0\n\t"
        "s_mov_b32 s73, 0\n\t"
        "s_mov_b32 s80, %[softat]\n\t"
        "s_mov_b32 s74, %[due0]\n\t"
        "s_lshl_b32 s75, %[nt], 1\n\t"
        "s_mov_b64 s[76:77], %[kp]\n\t"
        "s_mov_b32 s78, %[nrec]\n\t"
        "s_mov_b32 s79, 0x20000\n\t"
        "s_and_b32 s77, s77, 0xffff\n\t"
        "v_mov_b32 %[lc], %[l0]\n\t"
        "s_nop 4\n"
        ".LldT%=:\n\t"
        "s_memrealtime s[68:69]\n\t"
        "ds_read_b32 %[vf], %[vfree]\n\t"
        "s_getreg_b32 s64, hwreg(HW_REG_IB_STS)\n\t"
        "v_readlane_b32 s63, %[lc], s73\n\t"
        "s_and_b32 s65, s64, 15\n\t"
        "s_lshr_b32 s64, s64, 18\n\t"
        "s_and_b32 s64, s64, 48\n\t"
        "s_or_b32 s72, s64, s65\n\t"
        "s_add_u32 s65, s72, 3\n\t"
        "s_lshr_b32 s65, s65, 2\n\t"
        "s_sub_u32 s64, s60, s65\n\t"
        "s_cmp_gt_i32 s64, s61\n\t"
        "s_cbranch_scc1 .LldP%=\n"
        ".LldB%=:\n\t"
        "s_cmp_ge_i32 s60, s75\n\t"
        "s_cbranch_scc1 .LldD%=\n\t"
        "v_add_u32 %[a0], s63, %[o0]\n\t"
        "v_add_u32 %[a1], s63, %[o1]\n\t"
        "v_add_u32 %[a2], s63, %[o2]\n\t"
        "v_add_u32 %[a3], s63, %[o3]\n\t"
        "s_and_b32 s64, s60, 7\n\t"
        "s_lshl_b32 s65, s64, 13\n\t"
        "s_add_u32 m0, s65, %[ring]\n\t"
        "s_lshr_b32 s66, s60, 3\n\t"
        "s_cmp_eq_u32 s63, 0x80000000\n\t"
        "s_cselect_b32 s67, 0x80000000, s74\n\t"
        "s_waitcnt lgkmcnt(0)\n\t"
        "v_readlane_b32 s64, %[vf], s64\n\t"
        "s_sub_u32 s68, s68, %[t1]\n\t"
        "s_lshl_b32 s68, s68, 8\n\t"
        "s_cmp_lt_i32 s68, s67\n\t"
        "s_cbranch_scc1 .LldR%=\n\t"
        "s_cmp_lt_u32 s64, s66\n\t"
        "s_cbranch_scc1 .LldR%=\n\t"
        "s_cmp_gt_u32 s72, %[win]\n\t"
        "s_cbranch_scc1 .LldR%=\n\t"
        "s_waitcnt vmcnt(56)\n\t"
        "buffer_load_dwordx4 %[a0], s[76:79], 0 offen nt lds\n\t"
        "s_add_u32 m0, m0, 1024\n\t"
        "s_add_u32 s74, s74, %[step]\n\t"
        "buffer_load_dwordx4 %[a1], s[76:79], 0 offen nt lds\n\t"
        "s_add_u32 m0, m0, 1024\n\t"
        "s_add_u32 s60, s60, 1\n\t"
        "buffer_load_dwordx4 %[a2], s[76:79], 0 offen nt lds\n\t"
        "s_add_u32 m0, m0, 1024\n\t"
        "s_add_u32 s73, s73, 1\n\t"
        "buffer_load_dwordx4 %[a3], s[76:79], 0 offen nt lds\n\t"
        "s_cmp_eq_u32 s60, %[nt]\n\t"
        "s_cbranch_scc1 .LldV%=\n\t"
        "s_cmp_lg_u32 s73, 64\n\t"
        "s_cbranch_scc1 .LldT%=\n\t"
        "v_mov_b32 %[lc], %[l1]\n\t"
        "s_branch .LldT%=\n"
        ".LldV%=:\n\t"
        "s_mov_b64 s[76:77], %[vp]\n\t"
        "s_and_b32 s77, s77, 0xffff\n\t"
        "s_mov_b32 s73, 0\n\t"
        "s_mov_b32 s74, %[due1]\n\t"
        "v_mov_b32 %[lc], %[l0]\n\t"
        "v_xor_b32 %[o1], 16, %[o1]\n\t"
        "v_xor_b32 %[o2], 32, %[o2]\n\t"
        "v_xor_b32 %[o3], 48, %[o3]\n\t"
        "s_branch .LldT%=\n"
        ".LldP%=:\n\t"
        "s_min_u32 s65, s65, 15\n\t"
        "s_lshl_b32 s66, s65, 3\n\t"
        "s_add_u32 s66, s66, 12\n\t"
        "s_getpc_b64 s[70:71]\n\t"
        "s_add_u32 s70, s70, s66\n\t"
        "s_addc_u32 s71, s71, 0\n\t"
        "s_setpc_b64 s[70:71]\n\t"
        "s_waitcnt vmcnt(0)\n\ts_branch .LldQ%=\n\t"
        "s_waitcnt vmcnt(4)\n\ts_branch .LldQ%=\n\t"
        "s_waitcnt vmcnt(8)\n\ts_branch .LldQ%=\n\t"
        "s_waitcnt vmcnt(12)\n\ts_branch .LldQ%=\n\t"
        "s_waitcnt vmcnt(16)\n\ts_branch .LldQ%=\n\t"
        "s_waitcnt vmcnt(20)\n\ts_branch .LldQ%=\n\t"
        "s_waitcnt vmcnt(24)\n\ts_branch .LldQ%=\n\t"
        "s_waitcnt vmcnt(28)\n\ts_branch .LldQ%=\n\t"
        "s_waitcnt vmcnt(32)\n\ts_branch .LldQ%=\n\t"
        "s_waitcnt vmcnt(36)\n\ts_branch .LldQ%=\n\t"
        "s_waitcnt vmcnt(40)\n\ts_branch .LldQ%=\n\t"
        "s_waitcnt vmcnt(44)\n\ts_branch .LldQ%=\n\t"
        "s_waitcnt vmcnt(48)\n\ts_branch .LldQ%=\n\t"
        "s_waitcnt vmcnt(52)\n\ts_branch .LldQ%=\n\t"
        "s_waitcnt vmcnt(56)\n\ts_branch .LldQ%=\n\t"
        "s_waitcnt vmcnt(60)\n"
        ".LldQ%=:\n\t"
        "s_mov_b32 s61, s64\n\t"
        "v_mov_b32 %[vt], s64\n\t"
        "ds_write_b32 %[vland], %[vt]\n\t"
        "s_branch .LldB%=\n"
        ".LldD%=:\n\t"
        "s_waitcnt lgkmcnt(0)\n\t"
        "s_cmp_ge_i32 s61, s75\n\t"
        "s_cbranch_scc1 .LldE%=\n"
        ".LldR%=:\n\t"
        "s_add_u32 s62, s62, 1\n\t"
        "s_cmp_lt_u32 s62, 0x400000\n\t"
        "s_cbranch_scc1 .LldT%=\n"
        ".LldE%=:\n\t"
        "s_waitcnt vmcnt(0) lgkmcnt(0)"
        : [a0] "=&v"(va0), [a1] "=&v"(va1), [a2] "=&v"(va2), [a3] "=&v"(va3), [vf] "=&v"(vf), [vt] "=&v"(vt), [lc] "=&v"(lc),
          [o1] "+v"(vo1), [o2] "+v"(vo2), [o3] "+v"(vo3)
        : [l0] "v"(l0), [l1] "v"(l1), [o0] "v"(vo0), [vland] "v"(vland), [vfree] "v"(vfree),
          [kp] "s"(kp), [vp] "s"(vp), [nrec] "s"(kvbytes), [nt] "s"(NT), [ring] "s"(ring), [t1] "s"(t1), [step] "s"(step),
          [due0] "s"(due0), [due1] "s"(due1), [win] "n"(WINDOW - 4), [soft] "n"(SOFT_TICKS * 256), [softat] "n"(SOFT_PAGES * 256)
        : "memory", "scc", "s60", "s61", "s62", "s63", "s64", "s65", "s66", "s67", "s68", "s69", "s70", "s71", "s72", "s73", "s74", "s75",
          "s76", "s77", "s78", "s79", "s80");
    STAMP(3);
    return;
  }

  const int w = wv - 2;
  const int r32 = lane & 31, hi = lane >> 5;
  const int nmine = (NT > w) ? (NT - w + NC - 1) / NC : 0;
  const unsigned lbase = (unsigned)(size_t)lds;
  const unsigned aland = lbase + LDS_CTL + CTL_LAND + (lane & 1) * 4;
  const unsigned palp = LDS_WREG + w * W_SIZE + W_ALP + r32 * 4;
  const unsigned ppst = LDS_WREG + w * W_SIZE + lane * 16;
  asm volatile("" ::: "v128", "v255");
  unsigned resc = 0;
  float m = -3.0e38f, mprev = -3.0e38f, l = 0.f;

  {
    const unsigned fk = (((unsigned)r32 & 3) << 2) | (((unsigned)r32 >> 2) & 3);
    const unsigned kaddr0 = LDS_RING + r32 * 256 + (((unsigned)hi ^ fk) << 4);
    bf16x8 qf[8];
    wait_above<1>(lbase + LDS_CTL + CTL_QOK, 0);
#pragma unroll
    for (int s = 0; s < 8; ++s) qf[s] = *(LAS bf16x8*)(lds + ((kaddr0 + LDS_Q - LDS_RING) ^ (s << 5)));
    const int lim = __builtin_amdgcn_readlane(lcl, b);
#pragma unroll 1
    for (int k = 0; k < nmine; ++k) {
      const unsigned t = w + NC * k;
      wait_above<1>(aland, t);
      if (k == 0) STAMP(1);
      bf16x8 kf[8];
#pragma unroll
      for (int s = 0; s < 8; ++s) kf[s] = *(LAS bf16x8*)(lds + ((kaddr0 + ((t & 7) << 13)) ^ (s << 5)));
      asm volatile("s_waitcnt lgkmcnt(0)" : "+v"(kf[0]), "+v"(kf[1]), "+v"(kf[2]), "+v"(kf[3]), "+v"(kf[4]), "+v"(kf[5]), "+v"(kf[6]), "+v"(kf[7]) :: "memory");
      ctl[CTL_FREE / 4 + (t & 7)] = (t >> 3) + 1;
      f32x16 sc;
#pragma unroll
      for (int r = 0; r < 16; ++r) sc[r] = 0.f;
#pragma unroll
      for (int s = 0; s < 8; ++s) sc = __builtin_amdgcn_mfma_f32_32x32x16_bf16(kf[s], qf[s], sc, 0, 0, 0);
      if ((int)t == NT - 1) {
        const int kb = 2 * t * PAGE + 4 * hi;
#pragma unroll
        for (int r = 0; r < 16; ++r) sc[r] = (kb + 8 * (r >> 2) + (r & 3) < lim) ? sc[r] : -__builtin_inff();
      }
      float mx = sc[0];
#pragma unroll
      for (int r = 1; r < 16; ++r) mx = fmaxf(mx, sc[r]);
      mx = swap_max(mx);
      const float mn = fmaxf(m, mx);
      const float alpha = __builtin_amdgcn_exp2f((m - mn) * CS);
      mprev = m;
      m = mn;
      const float nmc = -mn * CS;
      float ps = 0.f;
#pragma unroll
      for (int r = 0; r < 16; ++r) { sc[r] = __builtin_amdgcn_exp2f(fmaf(sc[r], CS, nmc)); ps += sc[r]; }
      l = fmaf(l, alpha, ps);
      u32x4 pf[2];
#pragma unroll
      for (int s = 0; s < 2; ++s)
        pf[s] = u32x4{pack2(sc[8 * s], sc[8 * s + 1]), pack2(sc[8 * s + 2], sc[8 * s + 3]), pack2(sc[8 * s + 4], sc[8 * s + 5]), pack2(sc[8 * s + 6], sc[8 * s + 7])};
      if (__builtin_amdgcn_ballot_w64(alpha != 1.0f)) resc |= 1u << k;
      *(LAS float*)(lds + palp + k * 128) = alpha;
      if (k >= KREG || k == nmine - 1) {
        const unsigned dst = ppst + ((k == nmine - 1) ? W_LAST : (k - KREG) * 2048);
        *(LAS u32x4*)(lds + dst) = pf[0];
        *(LAS u32x4*)(lds + dst + 1024) = pf[1];
      } else {
        asm volatile("s_set_gpr_idx_on %8, gpr_idx(DST)\n\t"
                     "v_mov_b32 v128, %0\n\tv_mov_b32 v129, %1\n\tv_mov_b32 v130, %2\n\tv_mov_b32 v131, %3\n\t"
                     "v_mov_b32 v132, %4\n\tv_mov_b32 v133, %5\n\tv_mov_b32 v134, %6\n\tv_mov_b32 v135, %7\n\t"
                     "s_set_gpr_idx_off"
                     :: "v"(pf[0][0]), "v"(pf[0][1]), "v"(pf[0][2]), "v"(pf[0][3]), "v"(pf[1][0]), "v"(pf[1][1]), "v"(pf[1][2]), "v"(pf[1][3]), "s"(8 * k));
      }
    }
  }
  l = swap_sum(l);
  *(LAS f32x2*)(lds + LDS_ML + w * 256 + r32 * 8) = f32x2{m, l};
  LGKM0();
  ctl[CTL_MLOK / 4 + w] = 1;
  STAMP(2);

  const unsigned tq = (lane & 15) >> 2, tp = lane & 3, tblk = (lane >> 4) & 1;
  const unsigned vaddr0 = LDS_RING + (4 * hi + tq) * 256 + (tq << 6) + ((2 * tblk + (tp >> 1)) << 4) + ((tp & 1) << 3);
  const int e0 = (NT + NC - 1) % NC;
  const int pos = NC - 1 - (e0 - w + NC) % NC;
  f32x16 o[4];
#pragma unroll
  for (int i = 0; i < 4; ++i)
#pragma unroll
    for (int r = 0; r < 16; ++r) o[i][r] = 0.f;
  auto weights = [&]() {
    wait_above<1>(lbase + LDS_CTL + CTL_MLOK + min(lane, NC - 1) * 4, 0);
    unsigned a = LDS_ML + r32 * 8;
    asm volatile("" : "+v"(a) :: "memory");
    float M = -3.0e38f, L = 0.f;
    f32x2 ml[NC];
#pragma unroll
    for (int i = 0; i < NC; ++i) { ml[i] = *(LAS f32x2*)(lds + a + i * 256); M = fmaxf(M, ml[i][0]); }
#pragma unroll
    for (int i = 0; i < NC; ++i) L = fmaf(__builtin_amdgcn_exp2f((ml[i][0] - M) * CS), ml[i][1], L);
    const float inv = 1.0f / L;
    *(LAS f32x2*)(lds + a + (LDS_WT - LDS_ML) + w * 256) = f32x2{__builtin_amdgcn_exp2f((mprev - M) * CS) * inv, __builtin_amdgcn_exp2f((m - M) * CS) * inv};
    LGKM0();
    ctl[CTL_WTOK / 4 + w] = 1;
  };
  resc &= ~1u;
  if (nmine <= 2) weights();
#pragma unroll 1
  for (int k = 0; k < nmine - 1; ++k) {
    if (k == 1) weights();
    if ((resc >> k) & 1) {
      const float alpha = *(LAS float*)(lds + palp + k * 128);
#pragma unroll
      for (int i = 0; i < 4; ++i)
#pragma unroll
        for (int r = 0; r < 16; ++r) o[i][r] *= alpha;
    }
    const unsigned u = NT + w + NC * k;
    u32x4 pf[2];
    if (k >= KREG) {
      pf[0] = *(LAS u32x4*)(lds + ppst + (k - KREG) * 2048);
      pf[1] = *(LAS u32x4*)(lds + ppst + (k - KREG) * 2048 + 1024);
    } else {
      asm volatile("s_set_gpr_idx_on %8, gpr_idx(SRC0)\n\t"
                   "v_mov_b32 %0, v128\n\tv_mov_b32 %1, v129\n\tv_mov_b32 %2, v130\n\tv_mov_b32 %3, v131\n\t"
                   "v_mov_b32 %4, v132\n\tv_mov_b32 %5, v133\n\tv_mov_b32 %6, v134\n\tv_mov_b32 %7, v135\n\t"
                   "s_set_gpr_idx_off\n\ts_nop 1"
                   : "=v"(pf[0][0]), "=v"(pf[0][1]), "=v"(pf[0][2]), "=v"(pf[0][3]), "=v"(pf[1][0]), "=v"(pf[1][1]), "=v"(pf[1][2]), "=v"(pf[1][3])
                   : "s"(8 * k));
    }
    asm volatile("" : "+v"(pf[0]), "+v"(pf[1]), "+v"(o[0]), "+v"(o[1]), "+v"(o[2]), "+v"(o[3]));
    wait_above<1>(aland, u);
    bf16x8 vf[4][2];
#pragma unroll
    for (int i = 0; i < 4; ++i) {
      const unsigned a = (vaddr0 + ((u & 7) << 13)) ^ (i << 6);
#pragma unroll
      for (int s = 0; s < 2; ++s) {
        const s16x4 lo = __builtin_amdgcn_ds_read_tr16_b64_v4i16((LAS s16x4*)(lds + a + 4096 * s));
        const s16x4 up = __builtin_amdgcn_ds_read_tr16_b64_v4i16((LAS s16x4*)(lds + a + 4096 * s + 2048));
        vf[i][s] = __builtin_bit_cast(bf16x8, __builtin_shufflevector(lo, up, 0, 1, 2, 3, 4, 5, 6, 7));
      }
    }
    asm volatile("s_waitcnt lgkmcnt(0)" : "+v"(vf[0][0]), "+v"(vf[0][1]), "+v"(vf[1][0]), "+v"(vf[1][1]), "+v"(vf[2][0]), "+v"(vf[2][1]), "+v"(vf[3][0]), "+v"(vf[3][1]) :: "memory");
    ctl[CTL_FREE / 4 + (u & 7)] = (u >> 3) + 1;
#pragma unroll
    for (int s = 0; s < 2; ++s)
#pragma unroll
      for (int i = 0; i < 4; ++i) o[i] = __builtin_amdgcn_mfma_f32_32x32x16_bf16(vf[i][s], __builtin_bit_cast(bf16x8, pf[s]), o[i], 0, 0, 0);
  }
  STAMP(3);

  {
    unsigned l16 = lane * 16;
    asm volatile("" : "+v"(l16));
    if (nmine > 1) {
#pragma unroll
      for (int i = 0; i < 4; ++i) {
        if (i != pos) {
          const unsigned dst = (i == 3 && pos > 3) ? LDS_RING + ((2 * NT - 2 * NC + pos) & 7) * 8192 + l16
                                                   : LDS_WREG + w * W_SIZE + l16 + (i > pos ? i - 1 : i) * 4096;
#pragma unroll
          for (int j = 0; j < 4; ++j) *(LAS f32x4*)(lds + dst + j * 1024) = f32x4{o[i][4 * j], o[i][4 * j + 1], o[i][4 * j + 2], o[i][4 * j + 3]};
        }
      }
      LGKM0();
      ctl[CTL_ODONE / 4 + pos] = 1;
    }
    if (pos < 4) {
      f32x16 of = (pos == 0) ? o[0] : (pos == 1) ? o[1] : (pos == 2) ? o[2] : o[3];
      const unsigned r8 = (l16 >> 1) & 255, l4 = l16 >> 2;
      wait_above<1>(lbase + LDS_CTL + CTL_WTOK + min(l4, (NC - 1) * 4u), 0);
      auto wave_of = [&](int p) { const int sw = e0 + 1 + p; return (sw >= NC) ? sw - NC : sw; };
      auto src_o = [&](int p) -> unsigned {
        return l16 + ((pos == 3 && p > 3) ? LDS_RING + ((2 * NT - 2 * NC + p) & 7) * 8192 : LDS_WREG + wave_of(p) * W_SIZE + (pos > p ? pos - 1 : pos) * 4096);
      };
      auto fma_o = [&](unsigned src, float c) {
#pragma unroll
        for (int j = 0; j < 4; ++j) {
          const f32x4 x = *(LAS f32x4*)(lds + src + j * 1024);
#pragma unroll
          for (int e = 0; e < 4; ++e) of[4 * j + e] = fmaf(x[e], c, of[4 * j + e]);
        }
      };
      auto fma_t = [&](int p, bf16x8 p0, bf16x8 p1, float c) {
        const unsigned a = (vaddr0 + (((2 * NT - NC + p) & 7) << 13)) ^ (pos << 6);
        bf16x8 vf[2];
#pragma unroll
        for (int s = 0; s < 2; ++s) {
          const s16x4 lo = __builtin_amdgcn_ds_read_tr16_b64_v4i16((LAS s16x4*)(lds + a + 4096 * s));
          const s16x4 up = __builtin_amdgcn_ds_read_tr16_b64_v4i16((LAS s16x4*)(lds + a + 4096 * s + 2048));
          vf[s] = __builtin_bit_cast(bf16x8, __builtin_shufflevector(lo, up, 0, 1, 2, 3, 4, 5, 6, 7));
        }
        f32x16 x;
#pragma unroll
        for (int r = 0; r < 16; ++r) x[r] = 0.f;
        x = __builtin_amdgcn_mfma_f32_32x32x16_bf16(vf[0], p0, x, 0, 0, 0);
        x = __builtin_amdgcn_mfma_f32_32x32x16_bf16(vf[1], p1, x, 0, 0, 0);
#pragma unroll
        for (int r = 0; r < 16; ++r) of[r] = fmaf(x[r], c, of[r]);
      };
      {
        const float cw = *(LAS float*)(lds + LDS_WT + w * 256 + r8);
#pragma unroll
        for (int r = 0; r < 16; ++r) of[r] *= cw;
      }
      if (NT >= 2 * NC) {
        float co[5], ct[NC];
        bf16x8 pl[NC][2];
        auto ld_p = [&](int p) {
          pl[p][0] = *(LAS bf16x8*)(lds + LDS_WREG + wave_of(p) * W_SIZE + W_LAST + l16);
          pl[p][1] = *(LAS bf16x8*)(lds + LDS_WREG + wave_of(p) * W_SIZE + W_LAST + l16 + 1024);
        };
#define PIN_OF() asm volatile("" : "+v"(of))
#pragma unroll
        for (int j = 0; j < 5; ++j) co[j] = *(LAS float*)(lds + LDS_WT + wave_of(j + (j >= pos ? 1 : 0)) * 256 + r8);
#pragma unroll
        for (int p = 0; p < NC; ++p) ct[p] = *(LAS float*)(lds + LDS_WT + 4 + wave_of(p) * 256 + r8);
        ld_p(0); ld_p(1);
        PIN_OF();
        wait_above<0>(lbase + LDS_CTL + CTL_ODONE + (l4 & 12), 0);
#pragma unroll
        for (int j = 0; j < 3; ++j) fma_o(src_o(j + (j >= pos ? 1 : 0)), co[j]);
        PIN_OF();
        wait_above<0>(aland, 2 * NT - NC + 1);
        fma_t(0, pl[0][0], pl[0][1], ct[0]);
        fma_t(1, pl[1][0], pl[1][1], ct[1]);
        PIN_OF();
        ld_p(2); ld_p(3); ld_p(4); ld_p(5);
        wait_above<0>(lbase + LDS_CTL + CTL_ODONE + 16 + (l4 & 4), 0);
        fma_o(src_o(4), co[3]);
        fma_o(src_o(5), co[4]);
        PIN_OF();
        STAMP(4);
        wait_above<0>(aland, 2 * NT - NC + 3);
        fma_t(2, pl[2][0], pl[2][1], ct[2]);
        fma_t(3, pl[3][0], pl[3][1], ct[3]);
        PIN_OF();
        wait_above<0>(aland, 2 * NT - NC + 4);
        fma_t(4, pl[4][0], pl[4][1], ct[4]);
        PIN_OF();
        wait_above<0>(aland, 2 * NT - NC + 5);
        fma_t(5, pl[5][0], pl[5][1], ct[5]);
      } else {
#pragma unroll 1
        for (int p = max(NC - NT, 0); p < NC; ++p) {
          const f32x2 c = *(LAS f32x2*)(lds + LDS_WT + wave_of(p) * 256 + r8);
          if (p != pos && NT - 2 * NC + p >= 0) {
            wait_above<1>(lbase + LDS_CTL + CTL_ODONE + p * 4, 0);
            fma_o(src_o(p), c[0]);
          }
          wait_above<1>(aland, 2 * NT - NC + p);
          fma_t(p, *(LAS bf16x8*)(lds + LDS_WREG + wave_of(p) * W_SIZE + W_LAST + l16),
                *(LAS bf16x8*)(lds + LDS_WREG + wave_of(p) * W_SIZE + W_LAST + l16 + 1024), c[1]);
        }
      }
      const __amdgpu_buffer_rsrc_t ro = __builtin_amdgcn_make_buffer_rsrc(outp, 0, NB * HQ * SQ * HD * 2, 0x00020000);
      const unsigned orow = (unsigned)(b * HQ + 2 * h) * SQ * HD * 2 + pos * 64 + ((l16 & 511) << 4) + ((l16 >> 5) & 16);
#pragma unroll
      for (int j = 0; j < 2; ++j) {
        const unsigned a0 = pack2(of[8 * j], of[8 * j + 1]), a1 = pack2(of[8 * j + 2], of[8 * j + 3]);
        const unsigned b0 = pack2(of[8 * j + 4], of[8 * j + 5]), b1 = pack2(of[8 * j + 6], of[8 * j + 7]);
        auto s0 = __builtin_amdgcn_permlane32_swap(a0, b0, false, false);
        auto s1 = __builtin_amdgcn_permlane32_swap(a1, b1, false, false);
        u32x4 val = {s0[0], s1[0], s0[1], s1[1]};
        __builtin_amdgcn_raw_buffer_store_b128(val, ro, orow + j * 32, 0, OUTAUX);
      }
    }
  }
}
}

extern "C" void kernel_launch(void* const* d_in, const int* in_sizes, int n_in, void* d_out, int out_size, void* d_ws,
                              size_t ws_size, hipStream_t stream) {
  (void)n_in; (void)out_size; (void)ws_size;
  const unsigned nphys = (unsigned)(in_sizes[1] / (PAGE * HKV * HD));
  const unsigned kvbytes = nphys * PAGE_BYTES;
  (void)hipFuncSetAttribute((const void*)paged_attn, hipFuncAttributeMaxDynamicSharedMemorySize, LDS_TOTAL);
  hipLaunchKernelGGL(paged_attn, dim3(256), dim3(512), LDS_TOTAL, stream, d_in[0], d_in[1], d_in[2], (const int*)d_in[3],
                     (const int*)d_in[4], d_out, (char*)d_ws, kvbytes, nphys);
}
```

```cpp
#define PROBE 2
#include <hip/hip_runtime.h>
#include <hip/hip_bf16.h>

#define PROBE_G 0
#define PACE_X16 442
#define GAP_TICKS 0
#define OUTAUX 0
#define HOFF 4
#define LSLEEP 0
#define PRESLEEP 0
#define PF_EVERY 2
#define PF_X64 76
#define PF_ALL 0
#define PF_LEAD 380
#define PF_POL ""
#define PF_SLOW 0
#define PF_RUN 4
#define TSLEEP 0
#define TWAIT 0
#define TPRIO 0
#define CSLEEP 1

#define STAMP(i) do {} while (0)

namespace {
constexpr int NB = 16, HQ = 32, HKV = 16, SQ = 16, HD = 128, PAGE = 16, NBPS = 256, SKV = NBPS * PAGE;
constexpr unsigned PAGE_BYTES = PAGE * HKV * HD * 2;
constexpr unsigned INVALID = 0x80000000u;
constexpr int NC = 6;
constexpr int KREG = 16, KLDS = 5;
static_assert(NC * (KREG + KLDS + 1) >= NBPS / 2, "P storage");
constexpr unsigned LDS_RING = 0;
constexpr unsigned LDS_WREG = 65536;
constexpr unsigned W_ALP = KLDS * 2048;
constexpr unsigned W_LAST = W_ALP + (KREG + KLDS + 1) * 128;
constexpr unsigned W_SIZE = W_LAST + 2048;
static_assert(W_LAST >= 3 * 4096 && W_SIZE % 256 == 0, "consumer region");
constexpr unsigned LDS_TBL = LDS_WREG + 2 * W_SIZE;
constexpr unsigned LDS_Q = LDS_WREG;
constexpr unsigned LDS_ML = LDS_WREG + NC * W_SIZE;
constexpr unsigned LDS_WT = LDS_ML + NC * 256;
constexpr unsigned LDS_CTL = LDS_WT + NC * 128;
constexpr unsigned CTL_LAND = 0;
constexpr unsigned CTL_QOK = 16;
constexpr unsigned CTL_FREE = 32;
constexpr unsigned CTL_MLOK = 64;
constexpr unsigned CTL_ODONE = 96;
constexpr unsigned CTL_WTOK = 128;
constexpr unsigned LDS_DUMMY = LDS_CTL + 256;
constexpr unsigned LDS_TOTAL = LDS_DUMMY + 256;
static_assert(LDS_TOTAL <= 163840, "lds");

typedef __attribute__((ext_vector_type(8))) __bf16 bf16x8;
typedef __attribute__((ext_vector_type(2))) __bf16 bf16x2;
typedef __attribute__((ext_vector_type(4))) short s16x4;
typedef __attribute__((ext_vector_type(16))) float f32x16;
typedef __attribute__((ext_vector_type(4))) float f32x4;
typedef __attribute__((ext_vector_type(2))) float f32x2;
typedef __attribute__((ext_vector_type(4))) unsigned u32x4;
typedef __attribute__((ext_vector_type(2))) unsigned u32x2;
typedef __attribute__((ext_vector_type(4))) int i32x4;
#define LAS __attribute__((address_space(3)))

__device__ __forceinline__ float swap_max(float x) {
  auto r = __builtin_amdgcn_permlane32_swap(__float_as_uint(x), __float_as_uint(x), false, false);
  return fmaxf(__uint_as_float(r[0]), __uint_as_float(r[1]));
}
__device__ __forceinline__ float swap_sum(float x) {
  auto r = __builtin_amdgcn_permlane32_swap(__float_as_uint(x), __float_as_uint(x), false, false);
  return __uint_as_float(r[0]) + __uint_as_float(r[1]);
}
__device__ __forceinline__ unsigned pack2(float a, float b) {
  bf16x2 v = {(__bf16)a, (__bf16)b};
  return __builtin_bit_cast(unsigned, v);
}
template <int CTRL> __device__ __forceinline__ int dpp_add(int x) { return x + __builtin_amdgcn_update_dpp(0, x, CTRL, 0xf, 0xf, true); }
__device__ __forceinline__ unsigned now32() { return (unsigned)__builtin_amdgcn_s_memrealtime(); }
__device__ __forceinline__ int to_sgpr(int v) {
  int r;
  asm volatile("s_nop 1\n\tv_readfirstlane_b32 %0, %1" : "=s"(r) : "v"(v));
  return r;
}
#define LGKM0() asm volatile("s_waitcnt lgkmcnt(0)" ::: "memory")
#define VMWAIT(n) asm volatile("s_waitcnt vmcnt(" #n ")" ::: "memory")
#define CBAR() asm volatile("" ::: "memory")
template <int SLEEP> __device__ __forceinline__ void wait_above(unsigned addr, unsigned u) {
  unsigned v, cnt;
  if (SLEEP)
    asm volatile("s_mov_b32 %1, 0x200000\n\ts_branch 2f\n"
                 "1:\n\ts_sleep %4\n"
                 "2:\n\tds_read_b32 %0, %2\n\ts_sub_u32 %1, %1, 1\n\ts_waitcnt lgkmcnt(0)\n\t"
                 "v_cmp_ge_u32 vcc, %3, %0\n\ts_cbranch_scc1 3f\n\ts_nop 1\n\ts_cbranch_vccnz 1b\n3:"
                 : "=&v"(v), "=&s"(cnt) : "v"(addr), "s"(u), "n"(SLEEP) : "memory", "scc", "vcc");
  else
    asm volatile("s_mov_b32 %1, 0x200000\n"
                 "1:\n\tds_read_b32 %0, %2\n\ts_sub_u32 %1, %1, 1\n\ts_waitcnt lgkmcnt(0)\n\t"
                 "v_cmp_ge_u32 vcc, %3, %0\n\ts_cbranch_scc1 3f\n\ts_nop 1\n\ts_cbranch_vccnz 1b\n3:"
                 : "=&v"(v), "=&s"(cnt) : "v"(addr), "s"(u) : "memory", "scc", "vcc");
}

__global__ void __launch_bounds__(512, 1) __attribute__((amdgpu_num_vgpr(64)))
paged_attn(const void* __restrict__ qp, const void* __restrict__ kp, const void* __restrict__ vp, const int* __restrict__ bt,
           const int* __restrict__ lens, void* __restrict__ outp, char* __restrict__ ws, unsigned kvbytes, unsigned nphys) {
  extern __shared__ __attribute__((aligned(16))) char smem[];
  LAS char* lds = (LAS char*)smem;
  const unsigned pr0 = now32();
  const unsigned long long pc0 = __builtin_amdgcn_s_memtime();
  const int lane0 = threadIdx.x & 63;
  const int wv = __builtin_amdgcn_readfirstlane(threadIdx.x >> 6);
  const int g = blockIdx.x;
  const int h = g >> 4;
  constexpr float CS = 0.08838834764831845f * 1.4426950408889634f;
  volatile LAS unsigned* ctl = (volatile LAS unsigned*)(lds + LDS_CTL);

  const unsigned want = (g & 8) ? 15 - (g & 7) : (g & 7);
  unsigned lm1, rank;
  auto ranking = [&](int lane, unsigned& lr, u32x4& lq) __attribute__((always_inline)) {
    asm volatile("" : "+v"(lr), "+v"(lq));
    lm1 = min(lr - 1u, (unsigned)SKV - 1u);
    const unsigned key = lm1 * 16 + (lane & 15);
    rank = 0;
#pragma unroll
    for (int j = 0; j < 4; ++j) rank += (min(lq[j] - 1u, (unsigned)SKV - 1u) * 16 + ((lane >> 4) * 4 + j) > key) ? 1u : 0u;
    auto r1 = __builtin_amdgcn_permlane16_swap(rank, rank, false, false);
    rank = r1[0] + r1[1];
    auto r2 = __builtin_amdgcn_permlane32_swap(rank, rank, false, false);
    rank = r2[0] + r2[1];
  };
  auto total = [&]() __attribute__((always_inline)) {
    int t = (lm1 >> 4) + 1;
    t = dpp_add<0x121>(t); t = dpp_add<0x122>(t); t = dpp_add<0x124>(t); t = dpp_add<0x128>(t);
    return t;
  };

  if (__builtin_expect(wv < 2, 1)) {
    const int half = wv, lane = lane0;
    unsigned lr = lens[lane & 15];
    u32x4 lq = *(const u32x4*)(lens + (lane >> 4) * 4);
    unsigned first = bt[(lane & 15) * NBPS + half];
    __builtin_amdgcn_s_setprio(3);
    const unsigned rq4 = lane >> 4;
    unsigned vo0 = rq4 * 4096 + (((unsigned)(lane & 15) ^ (rq4 << 2)) << 4) + h * (HD * 2);
    unsigned vo1 = (vo0 + 16384 - 1024) ^ 16, vo2 = (vo0 + 32768 - 2048) ^ 32, vo3 = (vo0 + 49152 - 3072) ^ 48;
    unsigned vland = (unsigned)(size_t)lds + LDS_CTL + CTL_LAND + half * 4;
    unsigned vfree = (unsigned)(size_t)lds + LDS_CTL + CTL_FREE + (lane & 7) * 4;
    const unsigned ring = (unsigned)(size_t)lds + LDS_RING + half * 4096;
    const __amdgpu_buffer_rsrc_t rk = __builtin_amdgcn_make_buffer_rsrc((void*)kp, 0, (int)kvbytes, 0x00020000);
    asm volatile("" : "+v"(vo0), "+v"(vo1), "+v"(vo2), "+v"(vo3), "+v"(vland), "+v"(vfree) : "s"(rk), "s"(vp), "s"(nphys), "s"(ring));
    ranking(lane, lr, lq);
    first = min(first, nphys - 1) * PAGE_BYTES;
    const int b = __builtin_ctz((unsigned)__builtin_amdgcn_ballot_w64(rank == want) | 0x8000u);
    unsigned long long tstart;
    asm volatile("s_waitcnt vmcnt(0)\n\t"
                 "s_mov_b32 m0, %2\n\t"
                 "s_memrealtime %0\n\t"
                 "s_nop 2\n\t"
                 "buffer_load_dwordx4 %4, %1, %3 offen nt lds\n\t"
                 "buffer_load_dwordx4 %5, %1, %3 offen offset:1024 nt lds\n\t"
                 "buffer_load_dwordx4 %6, %1, %3 offen offset:2048 nt lds\n\t"
                 "buffer_load_dwordx4 %7, %1, %3 offen offset:3072 nt lds\n\t"
                 "s_waitcnt lgkmcnt(0)\n\t"
                 "s_barrier"
                 : "=&s"(tstart) : "s"(rk), "s"(ring), "s"(__builtin_amdgcn_readlane(first, b)), "v"(vo0), "v"(vo1), "v"(vo2), "v"(vo3) : "memory");
    __builtin_amdgcn_sched_barrier(0);
    asm volatile("" : "+v"(lm1));
    const int nown = (__builtin_amdgcn_readlane(lm1, b) >> 4) + 1, NT = (nown + 1) >> 1;
    unsigned l0 = *(LAS unsigned*)(lds + LDS_TBL + (b * NBPS + min(2 * lane + half, nown - 1)) * 4);
    unsigned l1 = *(LAS unsigned*)(lds + LDS_TBL + (b * NBPS + min(2 * lane + half + 128, nown - 1)) * 4);
    const int step = to_sgpr((int)((float)(total() * PACE_X16) * __builtin_amdgcn_rcpf((float)(2 * NT))));
    l0 = min(l0, nphys - 1) * PAGE_BYTES;
    l1 = min(l1, nphys - 1) * PAGE_BYTES;
#define CNT(r)
    unsigned vf, vt, lc;
    asm volatile(
        "s_waitcnt lgkmcnt(0)\n\t"
        "s_memrealtime s[68:69]\n\t"
        "ds_read_b32 %[vf], %[vfree]\n\t"
        "s_mov_b32 s60, 1\n\t"
        "s_mov_b32 s61, 4\n\t"
        "s_mov_b32 s62, 0x400000\n\t"
        "s_mov_b32 s66, 0\n\t"
        "s_min_u32 s67, %[nt], 64\n\t"
        "s_mov_b32 s72, 0x2000\n\t"
        "s_mov_b32 s73, 1\n\t"
        "s_mov_b32 s74, %[due1]\n\t"
        "s_mov_b64 s[76:77], %[kp]\n\t"
        "s_mov_b32 s78, %[nrec]\n\t"
        "s_mov_b32 s79, 0x20000\n\t"
        "s_and_b32 s77, s77, 0xffff\n\t"
        "v_mov_b32 %[lc], %[l0]\n\t"
        "s_getpc_b64 s[70:71]\n"
        ".LldA%=:\n\t"
        "s_add_u32 s80, s70, .LldL%=-.LldA%=\n\t"
        "s_addc_u32 s71, s71, 0\n\t"
        "s_branch .LldF%=\n"
        ".LldR%=:\n\t"
        "s_sub_u32 s62, s62, 1\n\t"
        "s_cbranch_scc1 .LldE%=\n"
        ".LldT%=:\n\t"
        "s_getreg_b32 s64, hwreg(HW_REG_IB_STS)\n\t"
        "s_and_b32 s65, s64, 15\n\t"
        "s_bfe_u32 s64, s64, 0x20016\n\t"
        "s_lshl4_add_u32 s64, s64, s65\n\t"
        "s_add_u32 s64, s64, 3\n\t"
        "s_and_b32 s64, s64, 60\n\t"
        "s_sub_u32 s65, s61, s64\n\t"
        "s_cmp_lg_u32 s65, s66\n\t"
        "s_cbranch_scc1 .LldP%=\n"
        ".LldC%=:\n\t"
        "s_waitcnt lgkmcnt(0)\n\t"
        "v_cmp_eq_u32 vcc, s60, %[vf]\n\t"
        "s_lshl_b32 s64, s68, 8\n\t"
        "s_memrealtime s[68:69]\n\t"
        "ds_read_b32 %[vf], %[vfree]\n\t"
        CNT("s82")
        "s_sub_u32 s64, s64, s74\n\t"
        "s_cmp_lt_i32 s64, 0\n\t"
        "s_cbranch_scc1 .LldR%=\n\t"
        CNT("s83")
        "s_cbranch_vccz .LldR%=\n\t"
        CNT("s84")
        "buffer_load_dwordx4 %[o0], s[76:79], s63 offen nt lds\n\t"
        "s_add_u32 s74, s74, %[step]\n\t"
        "s_add_u32 s73, s73, 1\n\t"
        "buffer_load_dwordx4 %[o1], s[76:79], s63 offen offset:1024 nt lds\n\t"
        "s_add_u32 s60, s60, 1\n\t"
        "s_add_u32 s61, s61, 4\n\t"
        "buffer_load_dwordx4 %[o2], s[76:79], s63 offen offset:2048 nt lds\n\t"
        "s_add_u32 s72, s72, 0x2000\n\t"
        "s_and_b32 s72, s72, 0xe000\n\t"
        "buffer_load_dwordx4 %[o3], s[76:79], s63 offen offset:3072 nt lds\n"
        ".LldF%=:\n\t"
        "v_readlane_b32 s63, %[lc], s73\n\t"
        "s_add_u32 m0, s72, %[ring]\n\t"
        "s_cmp_lg_u32 s60, s67\n\t"
        "s_cbranch_scc1 .LldT%=\n\t"
        "s_cmp_eq_u32 s73, %[nt]\n\t"
        "s_cbranch_scc1 .LldV%=\n\t"
        "v_mov_b32 %[lc], %[l1]\n\t"
        "v_readfirstlane_b32 s63, %[l1]\n\t"
        "s_add_u32 s67, s60, %[nt]\n\t"
        "s_sub_u32 s67, s67, 64\n\t"
        "s_branch .LldT%=\n"
        ".LldV%=:\n\t"
        "s_cmp_lg_u32 s60, %[nt]\n\t"
        "s_cbranch_scc1 .LldD%=\n\t"
        "s_mov_b64 s[76:77], %[vp]\n\t"
        "s_and_b32 s77, s77, 0xffff\n\t"
        "s_mov_b32 s73, 0\n\t"
        "s_min_u32 s67, %[nt], 64\n\t"
        "s_add_u32 s67, s67, s60\n\t"
        "s_add_u32 s74, s74, %[gap]\n\t"
        "v_mov_b32 %[lc], %[l0]\n\t"
        "v_readfirstlane_b32 s63, %[l0]\n\t"
        "v_xor_b32 %[o1], 16, %[o1]\n\t"
        "v_xor_b32 %[o2], 32, %[o2]\n\t"
        "v_xor_b32 %[o3], 48, %[o3]\n\t"
        "s_branch .LldT%=\n"
        ".LldD%=:\n\t"
        "s_add_u32 s74, s74, 0x40000000\n\t"
        "s_mov_b32 s60, -1\n\t"
        "s_branch .LldT%=\n"
        ".LldP%=:\n\t"
        "s_min_u32 s64, s64, 32\n\t"
        "s_add_u32 s70, s80, s64\n\t"
        "v_mov_b32 %[vt], s65\n\t"
        "s_mov_b32 s66, s65\n\t"
        "s_setpc_b64 s[70:71]\n\t"
        ".p2align 6\n"
        ".LldL%=:\n\t"
        "s_waitcnt vmcnt(0)\n\t"
        "s_waitcnt vmcnt(4)\n\t"
        "s_waitcnt vmcnt(8)\n\t"
        "s_waitcnt vmcnt(12)\n\t"
        "s_waitcnt vmcnt(16)\n\t"
        "s_waitcnt vmcnt(20)\n\t"
        "s_waitcnt vmcnt(24)\n\t"
        "s_waitcnt vmcnt(28)\n\t"
        "s_waitcnt vmcnt(32)\n\t"
        "ds_write_b32 %[vland], %[vt]\n\t"
        "s_cmp_lg_u32 s65, %[fin]\n\t"
        "s_cbranch_scc1 .LldC%=\n"
        ".LldE%=:\n\t"
        "s_waitcnt vmcnt(0) lgkmcnt(0)\n\t"
        : [vf] "=&v"(vf), [vt] "=&v"(vt), [lc] "=&v"(lc), [o1] "+v"(vo1), [o2] "+v"(vo2), [o3] "+v"(vo3)
        : [l0] "v"(l0), [l1] "v"(l1), [o0] "v"(vo0), [vland] "v"(vland), [vfree] "v"(vfree),
          [kp] "s"(kp), [vp] "s"(vp), [nrec] "s"(kvbytes), [nt] "s"(NT), [ring] "s"(ring),
          [step] "s"(step), [due1] "s"(((unsigned)tstart << 8) + step + half * ((step * HOFF) >> 3)), [fin] "s"(8 * NT), [gap] "n"(GAP_TICKS * 256)
        : "memory", "scc", "vcc", "s60", "s61", "s62", "s63", "s64", "s65", "s66", "s67", "s68", "s69", "s70", "s71", "s72", "s73", "s74",
          "s76", "s77", "s78", "s79", "s80", "s82", "s83", "s84");
    STAMP(3);
    if (PROBE == 2) while (now32() - pr0 < 10000 + 10 * ((unsigned)tstart - pr0)) __builtin_amdgcn_s_sleep(2);
    asm volatile("s_endpgm" ::: "memory");
  }

  int lane = lane0;
  asm volatile("" : "+v"(lane));
  unsigned lraw = lens[lane & 15];
  u32x4 lq = *(const u32x4*)(lens + (lane >> 4) * 4);
  if (wv < 6) {
    const __amdgpu_buffer_rsrc_t rb = __builtin_amdgcn_make_buffer_rsrc((void*)bt, 0, NB * NBPS * 4, 0x00020000);
    asm volatile("s_mov_b32 m0, %0\n\ts_nop 4\n\tbuffer_load_dwordx4 %2, %1, 0 offen lds\n\t"
                 "buffer_load_dwordx4 %2, %1, 0 offen offset:1024 lds\n\t"
                 "buffer_load_dwordx4 %2, %1, 0 offen offset:2048 lds\n\t"
                 "buffer_load_dwordx4 %2, %1, 0 offen offset:3072 lds\n\t"
                 "s_waitcnt vmcnt(0)"
                 :: "s"((unsigned)(size_t)lds + LDS_TBL + (wv - 2) * 4096), "s"(rb), "v"(lane * 16 + (wv - 2) * 4096) : "memory");
  } else if (wv == 6) {
    if (lane < 40) ctl[lane] = ((lane >> 3) == CTL_FREE / 32) ? (lane & 7) : 0u;
  }
  if (wv != 7) asm volatile("s_waitcnt lgkmcnt(0)\n\ts_barrier" ::: "memory");
  ranking(lane, lraw, lq);
  const int b = __builtin_ctz((unsigned)__builtin_amdgcn_ballot_w64(rank == want) | 0x8000u);
  if (wv == 7) {
    const __amdgpu_buffer_rsrc_t rq = __builtin_amdgcn_make_buffer_rsrc((void*)qp, 0, NB * HQ * SQ * HD * 2, 0x00020000);
    const unsigned q0 = (((int)__builtin_amdgcn_readlane(lraw, b) > 0) ? (unsigned)((b * HQ + 2 * h) * SQ * HD * 2) : INVALID) + (lane >> 4) * 256 +
                        (((unsigned)(lane & 15) ^ ((lane >> 4) << 2)) << 4);
    asm volatile("s_mov_b32 m0, %0\n\ts_nop 4\n\t"
                 "buffer_load_dwordx4 %2, %1, 0 offen lds\n\t"
                 "buffer_load_dwordx4 %3, %1, 0 offen offset:1024 lds\n\t"
                 "buffer_load_dwordx4 %4, %1, 0 offen offset:2048 lds\n\t"
                 "buffer_load_dwordx4 %5, %1, 0 offen offset:3072 lds\n\t"
                 "s_add_u32 m0, m0, 4096\n\ts_nop 0\n\t"
                 "buffer_load_dwordx4 %2, %1, %6 offen lds\n\t"
                 "buffer_load_dwordx4 %3, %1, %6 offen offset:1024 lds\n\t"
                 "buffer_load_dwordx4 %4, %1, %6 offen offset:2048 lds\n\t"
                 "buffer_load_dwordx4 %5, %1, %6 offen offset:3072 lds\n\t"
                 "s_barrier\n\t"
                 "s_waitcnt vmcnt(0)"
                 :: "s"((unsigned)(size_t)lds + LDS_Q), "s"(rq), "v"(q0), "v"(q0 ^ 16), "v"(q0 ^ 32), "v"(q0 ^ 48), "s"(4096) : "memory", "scc");
    ctl[CTL_QOK / 4] = 1;
  }
  const int bp = __builtin_ctz((unsigned)__builtin_amdgcn_ballot_w64(rank == 15 - want) | 0x8000u);
  const int lim = __builtin_amdgcn_readlane(lm1, b) + 1;
  const int nown = (lim + PAGE - 1) / PAGE, NT = (nown + 1) >> 1;
  const int tot = total();
  const int w = wv - 2;
  const int r32 = lane & 31, hi = lane >> 5;
  const int nmine = (NT > w) ? (NT - w + NC - 1) / NC : 0;
  const unsigned lbase = (unsigned)(size_t)lds;
  const unsigned aland = lbase + LDS_CTL + CTL_LAND + (lane & 1) * 4;
  const int npl = (__builtin_amdgcn_readlane(lm1, bp) >> 4) + 1, NTL = (npl + 1) >> 1;
  const bool pf_on = PF_EVERY && npl * 16 * 64 > __builtin_amdgcn_readfirstlane(tot) * PF_X64 && npl > nown && (PF_ALL || (h & 3) == 1);
  const unsigned pf_lead = to_sgpr((int)((float)(2 * NTL * PF_LEAD) * 256.0f * __builtin_amdgcn_rcpf((float)(tot * PACE_X16))));
  const unsigned pf_ratio = to_sgpr((int)((float)NTL * 65536.0f * __builtin_amdgcn_rcpf((float)NT)));
  auto prefetch = [&](unsigned op) {
    const unsigned i = op / (PF_EVERY ? PF_EVERY : 1);
    unsigned tl = (((i | (PF_RUN - 1)) * PF_EVERY * pf_ratio) >> 16) + pf_lead - (~i & (PF_RUN - 1));
    if (tl >= 2u * NTL) return;
    const bool isv = tl >= (unsigned)NTL;
    tl -= isv ? NTL : 0;
    const unsigned p0 = min((unsigned)bt[bp * NBPS + 2 * tl], nphys - 1), p1 = min((unsigned)bt[bp * NBPS + min(2 * tl + 1, (unsigned)npl - 1)], nphys - 1);
    const __amdgpu_buffer_rsrc_t rp = __builtin_amdgcn_make_buffer_rsrc(isv ? (void*)vp : (void*)kp, 0, (int)kvbytes, 0x00020000);
    unsigned ln = lane;
    asm volatile("" : "+v"(ln));
    const unsigned vo = ((ln & 32) ? p1 : p0) * PAGE_BYTES + h * (HD * 2) + ((ln >> 1) & 15) * 4096 + (PF_SLOW == 1 ? 128 : PF_SLOW == 2 ? 0 : (ln & 1) * 128);
    asm volatile("s_mov_b32 m0, %0\n\ts_nop 4\n\tbuffer_load_dword %2, %1, 0 offen " PF_POL " lds" :: "s"(lbase + LDS_DUMMY), "s"(rp), "v"(vo) : "memory");
  };
  const int pos = (w + NC * 22 - NT) % NC;
  const unsigned palp = LDS_WREG + pos * W_SIZE + W_ALP + r32 * 4;
  const unsigned ppst = LDS_WREG + pos * W_SIZE + lane * 16;
  asm volatile("" ::: "v128", "v255");
  unsigned resc = 0;
  float m = -3.0e38f, mprev = -3.0e38f, l = 0.f;

  {
    const unsigned fk = (((unsigned)r32 & 3) << 2) | (((unsigned)r32 >> 2) & 3);
    const unsigned kaddr0 = LDS_RING + r32 * 256 + (((unsigned)hi ^ fk) << 4);
    bf16x8 qf[8];
    wait_above<1>(lbase + LDS_CTL + CTL_QOK, 0);
#pragma unroll
    for (int s = 0; s < 8; ++s) qf[s] = *(LAS bf16x8*)(lds + ((kaddr0 + LDS_Q - LDS_RING) ^ (s << 5)));
#pragma unroll 1
    for (int k = 0; k < nmine; ++k) {
      const unsigned t = w + NC * k;
      if (PRESLEEP && k > 0) __builtin_amdgcn_s_sleep(PRESLEEP);
      wait_above<CSLEEP>(aland, 4 * t);
      if (k == 0) STAMP(1);
      bf16x8 kf[8];
#pragma unroll
      for (int s = 0; s < 8; ++s) kf[s] = *(LAS bf16x8*)(lds + ((kaddr0 + ((t & 7) << 13)) ^ (s << 5)));
      asm volatile("s_waitcnt lgkmcnt(0)" : "+v"(kf[0]), "+v"(kf[1]), "+v"(kf[2]), "+v"(kf[3]), "+v"(kf[4]), "+v"(kf[5]), "+v"(kf[6]), "+v"(kf[7]) :: "memory");
      ctl[CTL_FREE / 4 + (t & 7)] = t + 8;
      if (pf_on && t % (PF_EVERY ? PF_EVERY : 1) == 0) prefetch(t);
      f32x16 sc;
#pragma unroll
      for (int r = 0; r < 16; ++r) sc[r] = 0.f;
#pragma unroll
      for (int s = 0; s < 8; ++s) sc = __builtin_amdgcn_mfma_f32_32x32x16_bf16(kf[s], qf[s], sc, 0, 0, 0);
      if ((int)t == NT - 1) {
        const int kb = 2 * t * PAGE + 4 * hi;
#pragma unroll
        for (int r = 0; r < 16; ++r) sc[r] = (kb + 8 * (r >> 2) + (r & 3) < lim) ? sc[r] : -__builtin_inff();
      }
      float mx = sc[0];
#pragma unroll
      for (int r = 1; r < 16; ++r) mx = fmaxf(mx, sc[r]);
      mx = swap_max(mx);
      const float mn = fmaxf(m, mx);
      const float alpha = __builtin_amdgcn_exp2f((m - mn) * CS);
      mprev = m;
      m = mn;
      const float nmc = -mn * CS;
      float ps = 0.f;
#pragma unroll
      for (int r = 0; r < 16; ++r) { sc[r] = __builtin_amdgcn_exp2f(fmaf(sc[r], CS, nmc)); ps += sc[r]; }
      l = fmaf(l, alpha, ps);
      u32x4 pf[2];
#pragma unroll
      for (int s = 0; s < 2; ++s)
        pf[s] = u32x4{pack2(sc[8 * s], sc[8 * s + 1]), pack2(sc[8 * s + 2], sc[8 * s + 3]), pack2(sc[8 * s + 4], sc[8 * s + 5]), pack2(sc[8 * s + 6], sc[8 * s + 7])};
      if (__builtin_amdgcn_ballot_w64(alpha != 1.0f)) resc |= 1u << k;
      *(LAS float*)(lds + palp + k * 128) = alpha;
      if (k >= KREG || k == nmine - 1) {
        const unsigned dst = ppst + ((k == nmine - 1) ? W_LAST : (k - KREG) * 2048);
        *(LAS u32x4*)(lds + dst) = pf[0];
        *(LAS u32x4*)(lds + dst + 1024) = pf[1];
      } else {
        asm volatile("s_set_gpr_idx_on %8, gpr_idx(DST)\n\t"
                     "v_mov_b32 v128, %0\n\tv_mov_b32 v129, %1\n\tv_mov_b32 v130, %2\n\tv_mov_b32 v131, %3\n\t"
                     "v_mov_b32 v132, %4\n\tv_mov_b32 v133, %5\n\tv_mov_b32 v134, %6\n\tv_mov_b32 v135, %7\n\t"
                     "s_set_gpr_idx_off"
                     :: "v"(pf[0][0]), "v"(pf[0][1]), "v"(pf[0][2]), "v"(pf[0][3]), "v"(pf[1][0]), "v"(pf[1][1]), "v"(pf[1][2]), "v"(pf[1][3]), "s"(8 * k));
      }
    }
  }
  l = swap_sum(l);
  *(LAS f32x2*)(lds + LDS_ML + w * 256 + r32 * 8) = f32x2{m, l};
  LGKM0();
  ctl[CTL_MLOK / 4 + w] = 1;
  STAMP(2);

  const unsigned tq = (lane & 15) >> 2, tp = lane & 3, tblk = (lane >> 4) & 1;
  const unsigned vaddr0 = LDS_RING + (4 * hi + tq) * 256 + ((tq ^ (pos & 3)) << 6) + ((2 * tblk + (tp >> 1)) << 4) + ((tp & 1) << 3);
  f32x16 o[4];
#pragma unroll
  for (int i = 0; i < 4; ++i)
#pragma unroll
    for (int r = 0; r < 16; ++r) o[i][r] = 0.f;
  auto weights = [&]() {
    wait_above<1>(lbase + LDS_CTL + CTL_MLOK + min(lane, NC - 1) * 4, 0);
    unsigned a = LDS_ML + r32 * 8;
    asm volatile("" : "+v"(a) :: "memory");
    float M = -3.0e38f, L = 0.f;
    f32x2 ml[NC];
#pragma unroll
    for (int i = 0; i < NC; ++i) { ml[i] = *(LAS f32x2*)(lds + a + i * 256); M = fmaxf(M, ml[i][0]); }
#pragma unroll
    for (int i = 0; i < NC; ++i) L = fmaf(__builtin_amdgcn_exp2f((ml[i][0] - M) * CS), ml[i][1], L);
    const float inv = 1.0f / L;
    *(LAS float*)(lds + (a >> 1) + (LDS_WT - LDS_ML / 2) + pos * 128) = __builtin_amdgcn_exp2f((mprev - M) * CS) * inv;
    const float ct = __builtin_amdgcn_exp2f((m - M) * CS) * inv;
#pragma unroll
    for (int s = 0; s < 2; ++s) {
      u32x4 x = *(LAS u32x4*)(lds + ppst + W_LAST + s * 1024);
#pragma unroll
      for (int e = 0; e < 4; ++e) x[e] = pack2(__uint_as_float(x[e] << 16) * ct, __uint_as_float(x[e] & 0xffff0000u) * ct);
      *(LAS u32x4*)(lds + ppst + W_LAST + s * 1024) = x;
    }
    LGKM0();
    ctl[CTL_WTOK / 4 + w] = 1;
  };
  resc &= ~1u;
  if (nmine <= 2) weights();
#pragma unroll 1
  for (int k = 0; k < nmine - 1; ++k) {
    if (k == 1) weights();
    if ((resc >> k) & 1) {
      const float alpha = *(LAS float*)(lds + palp + k * 128);
#pragma unroll
      for (int i = 0; i < 4; ++i)
#pragma unroll
        for (int r = 0; r < 16; ++r) o[i][r] *= alpha;
    }
    const unsigned u = NT + w + NC * k;
    u32x4 pf[2];
    if (k >= KREG) {
      pf[0] = *(LAS u32x4*)(lds + ppst + (k - KREG) * 2048);
      pf[1] = *(LAS u32x4*)(lds + ppst + (k - KREG) * 2048 + 1024);
    } else {
      asm volatile("s_set_gpr_idx_on %8, gpr_idx(SRC0)\n\t"
                   "v_mov_b32 %0, v128\n\tv_mov_b32 %1, v129\n\tv_mov_b32 %2, v130\n\tv_mov_b32 %3, v131\n\t"
                   "v_mov_b32 %4, v132\n\tv_mov_b32 %5, v133\n\tv_mov_b32 %6, v134\n\tv_mov_b32 %7, v135\n\t"
                   "s_set_gpr_idx_off\n\ts_nop 1"
                   : "=v"(pf[0][0]), "=v"(pf[0][1]), "=v"(pf[0][2]), "=v"(pf[0][3]), "=v"(pf[1][0]), "=v"(pf[1][1]), "=v"(pf[1][2]), "=v"(pf[1][3])
                   : "s"(8 * k));
    }
    asm volatile("" : "+v"(pf[0]), "+v"(pf[1]), "+v"(o[0]), "+v"(o[1]), "+v"(o[2]), "+v"(o[3]));
    if (PRESLEEP && k > 1) __builtin_amdgcn_s_sleep(2 * PRESLEEP);
    wait_above<CSLEEP>(aland, 4 * u);
    if (TPRIO && k == nmine - 2) __builtin_amdgcn_s_setprio(TPRIO);
    bf16x8 vf[4][2];
#pragma unroll
    for (int i = 0; i < 4; ++i) {
      const unsigned a = (vaddr0 + ((u & 7) << 13)) ^ (i << 6);
#pragma unroll
      for (int s = 0; s < 2; ++s) {
        const s16x4 lo = __builtin_amdgcn_ds_read_tr16_b64_v4i16((LAS s16x4*)(lds + a + 4096 * s));
        const s16x4 up = __builtin_amdgcn_ds_read_tr16_b64_v4i16((LAS s16x4*)(lds + a + 4096 * s + 2048));
        vf[i][s] = __builtin_bit_cast(bf16x8, __builtin_shufflevector(lo, up, 0, 1, 2, 3, 4, 5, 6, 7));
      }
    }
    asm volatile("s_waitcnt lgkmcnt(0)" : "+v"(vf[0][0]), "+v"(vf[0][1]), "+v"(vf[1][0]), "+v"(vf[1][1]), "+v"(vf[2][0]), "+v"(vf[2][1]), "+v"(vf[3][0]), "+v"(vf[3][1]) :: "memory");
    ctl[CTL_FREE / 4 + (u & 7)] = u + 8;
    if (pf_on && u % (PF_EVERY ? PF_EVERY : 1) == 0) prefetch(u);
#pragma unroll
    for (int s = 0; s < 2; ++s)
#pragma unroll
      for (int i = 0; i < 4; ++i) o[i] = __builtin_amdgcn_mfma_f32_32x32x16_bf16(vf[i][s], __builtin_bit_cast(bf16x8, pf[s]), o[i], 0, 0, 0);
  }
  STAMP(3);

  {
    unsigned l16 = lane * 16;
    asm volatile("" : "+v"(l16));
    if (nmine > 1) {
      auto put = [&](unsigned dst, const f32x16& x) {
#pragma unroll
        for (int j = 0; j < 4; ++j) *(LAS f32x4*)(lds + dst + j * 1024) = f32x4{x[4 * j], x[4 * j + 1], x[4 * j + 2], x[4 * j + 3]};
      };
      if (pos > 3) put(LDS_RING + ((2 * NT - 2 * NC + pos) & 7) * 8192 + l16, o[0]);
#pragma unroll
      for (int i = 1; i < 4; ++i) put(LDS_WREG + pos * W_SIZE + (i - 1) * 4096 + l16, o[i]);
      if (TWAIT) LGKM0(); else CBAR();
      ctl[CTL_ODONE / 4 + pos] = 1;
    }
    if (pos < 4) {
      f32x16 of = o[0];
      const unsigned l4 = l16 >> 2, awt = LDS_WT + (l4 & 127);
      wait_above<1>(lbase + LDS_CTL + CTL_WTOK + min(l4, (NC - 1) * 4u), 0);
      auto src_o = [&](int p) -> unsigned {
        const int i = pos ^ (p & 3);
        return l16 + (i == 0 ? LDS_RING + ((2 * NT - 2 * NC + p) & 7) * 8192 : LDS_WREG + p * W_SIZE + (i - 1) * 4096);
      };
      auto fma_o = [&](unsigned src, float c) {
#pragma unroll
        for (int j = 0; j < 4; ++j) {
          const f32x4 x = *(LAS f32x4*)(lds + src + j * 1024);
#pragma unroll
          for (int e = 0; e < 4; ++e) of[4 * j + e] = fmaf(x[e], c, of[4 * j + e]);
        }
      };
      auto mma_t = [&](int p, bf16x8 p0, bf16x8 p1) {
        const unsigned a = vaddr0 + (((2 * NT - NC + p) & 7) << 13);
        bf16x8 vf[2];
#pragma unroll
        for (int s = 0; s < 2; ++s) {
          const s16x4 lo = __builtin_amdgcn_ds_read_tr16_b64_v4i16((LAS s16x4*)(lds + a + 4096 * s));
          const s16x4 up = __builtin_amdgcn_ds_read_tr16_b64_v4i16((LAS s16x4*)(lds + a + 4096 * s + 2048));
          vf[s] = __builtin_bit_cast(bf16x8, __builtin_shufflevector(lo, up, 0, 1, 2, 3, 4, 5, 6, 7));
        }
        of = __builtin_amdgcn_mfma_f32_32x32x16_bf16(vf[0], p0, of, 0, 0, 0);
        of = __builtin_amdgcn_mfma_f32_32x32x16_bf16(vf[1], p1, of, 0, 0, 0);
      };
      {
        const float cw = *(LAS float*)(lds + awt + pos * 128);
#pragma unroll
        for (int r = 0; r < 16; ++r) of[r] *= cw;
      }
      if (NT >= 2 * NC) {
        float co[5];
        bf16x8 pl[NC][2];
        auto ld_p = [&](int p) {
          pl[p][0] = *(LAS bf16x8*)(lds + LDS_WREG + p * W_SIZE + W_LAST + l16);
          pl[p][1] = *(LAS bf16x8*)(lds + LDS_WREG + p * W_SIZE + W_LAST + l16 + 1024);
        };
#define PIN_OF() asm volatile("" : "+v"(of))
#pragma unroll
        for (int i = 1; i < 4; ++i) co[i - 1] = *(LAS float*)(lds + awt + (pos ^ i) * 128);
        co[3] = *(LAS float*)(lds + awt + 4 * 128);
        co[4] = *(LAS float*)(lds + awt + 5 * 128);
        ld_p(0); ld_p(1); ld_p(2);
        PIN_OF();
        wait_above<TSLEEP>(lbase + LDS_CTL + CTL_ODONE + (l4 & 12), 0);
#pragma unroll
        for (int i = 1; i < 4; ++i) fma_o(LDS_WREG + (pos ^ i) * W_SIZE + (i - 1) * 4096 + l16, co[i - 1]);
        PIN_OF();
        ld_p(3); ld_p(4); ld_p(5);
        wait_above<TSLEEP>(aland, 4 * (2 * NT - NC + 2));
        mma_t(0, pl[0][0], pl[0][1]);
        mma_t(1, pl[1][0], pl[1][1]);
        mma_t(2, pl[2][0], pl[2][1]);
        STAMP(4);
        wait_above<TSLEEP>(lbase + LDS_CTL + CTL_ODONE + 16 + (l4 & 4), 0);
        fma_o(src_o(4), co[3]);
        fma_o(src_o(5), co[4]);
        PIN_OF();
        wait_above<0>(aland, 4 * (2 * NT - NC + 4));
        mma_t(3, pl[3][0], pl[3][1]);
        mma_t(4, pl[4][0], pl[4][1]);
        PIN_OF();
        wait_above<0>(aland, 4 * (2 * NT - NC + 5));
        mma_t(5, pl[5][0], pl[5][1]);
      } else {
#pragma unroll 1
        for (int p = max(NC - NT, 0); p < NC; ++p) {
          if (p != pos && NT - 2 * NC + p >= 0) {
            wait_above<1>(lbase + LDS_CTL + CTL_ODONE + p * 4, 0);
            fma_o(src_o(p), *(LAS float*)(lds + awt + p * 128));
          }
          wait_above<1>(aland, 4 * (2 * NT - NC + p));
          mma_t(p, *(LAS bf16x8*)(lds + LDS_WREG + p * W_SIZE + W_LAST + l16), *(LAS bf16x8*)(lds + LDS_WREG + p * W_SIZE + W_LAST + l16 + 1024));
        }
      }
      const __amdgpu_buffer_rsrc_t ro = __builtin_amdgcn_make_buffer_rsrc(outp, 0, NB * HQ * SQ * HD * 2, 0x00020000);
      const unsigned orow = (unsigned)(b * HQ + 2 * h) * SQ * HD * 2 + pos * 64 + ((l16 & 511) << 4) + ((l16 >> 5) & 16);
#pragma unroll
      for (int j = 0; j < 2; ++j) {
        const unsigned a0 = pack2(of[8 * j], of[8 * j + 1]), a1 = pack2(of[8 * j + 2], of[8 * j + 3]);
        const unsigned b0 = pack2(of[8 * j + 4], of[8 * j + 5]), b1 = pack2(of[8 * j + 6], of[8 * j + 7]);
        auto s0 = __builtin_amdgcn_permlane32_swap(a0, b0, false, false);
        auto s1 = __builtin_amdgcn_permlane32_swap(a1, b1, false, false);
        u32x4 val = {s0[0], s1[0], s0[1], s1[1]};
        __builtin_amdgcn_raw_buffer_store_b128(val, ro, orow + j * 32, 0, OUTAUX);
      }
    }
  }
  {
    const unsigned el = now32() - pr0;
    unsigned tgt = 0;
    if (PROBE == 1) { const float f = (float)(unsigned)(__builtin_amdgcn_s_memtime() - pc0) / (float)el; tgt = 10000 + (unsigned)fmaxf((24.0f - f) * 1000.0f, 0.f); }
    if (PROBE == 3 && g == PROBE_G) tgt = 10000 + 5 * (el - 5000);
    tgt = __builtin_amdgcn_readfirstlane(min(tgt, 40000u));
    while (now32() - pr0 < tgt) __builtin_amdgcn_s_sleep(2);
  }
}
}

extern "C" void kernel_launch(void* const* d_in, const int* in_sizes, int n_in, void* d_out, int out_size, void* d_ws,
                              size_t ws_size, hipStream_t stream) {
  (void)n_in; (void)out_size; (void)ws_size;
  const unsigned nphys = (unsigned)(in_sizes[1] / (PAGE * HKV * HD));
  const unsigned kvbytes = nphys * PAGE_BYTES;
  (void)hipFuncSetAttribute((const void*)paged_attn, hipFuncAttributeMaxDynamicSharedMemorySize, LDS_TOTAL);
  hipLaunchKernelGGL(paged_attn, dim3(256), dim3(512), LDS_TOTAL, stream, d_in[0], d_in[1], d_in[2], (const int*)d_in[3],
                     (const int*)d_in[4], d_out, (char*)d_ws, kvbytes, nphys);
}
```

```cpp
#define PACE_X16 442
#include <hip/hip_runtime.h>
#include <hip/hip_bf16.h>

#define PROBE_G 0
#define QPOL "nt"
#define PF_2ND
#define GAP_TICKS 0
#define OUTAUX 0
#define HOFF 4
#define LSLEEP 0
#define PRESLEEP 0
#define PF_EVERY 2
#define PF_X64 76
#define PF_ALL 0
#define PF_LEAD 380
#define PF_POL ""
#define PF_SLOW 0
#define PF_RUN 4
#define TSLEEP 0
#define TWAIT 0
#define TPRIO 0
#define CSLEEP 1

#define STAMP(i) do {} while (0)

namespace {
constexpr int NB = 16, HQ = 32, HKV = 16, SQ = 16, HD = 128, PAGE = 16, NBPS = 256, SKV = NBPS * PAGE;
constexpr unsigned PAGE_BYTES = PAGE * HKV * HD * 2;
constexpr unsigned INVALID = 0x80000000u;
constexpr int NC = 6;
constexpr int KREG = 16, KLDS = 5;
static_assert(NC * (KREG + KLDS + 1) >= NBPS / 2, "P storage");
constexpr unsigned LDS_RING = 0;
constexpr unsigned LDS_WREG = 65536;
constexpr unsigned W_ALP = KLDS * 2048;
constexpr unsigned W_LAST = W_ALP + (KREG + KLDS + 1) * 128;
constexpr unsigned W_SIZE = W_LAST + 2048;
static_assert(W_LAST >= 3 * 4096 && W_SIZE % 256 == 0, "consumer region");
constexpr unsigned LDS_TBL = LDS_WREG + 2 * W_SIZE;
constexpr unsigned LDS_Q = LDS_WREG;
constexpr unsigned LDS_ML = LDS_WREG + NC * W_SIZE;
constexpr unsigned LDS_WT = LDS_ML + NC * 256;
constexpr unsigned LDS_CTL = LDS_WT + NC * 128;
constexpr unsigned CTL_LAND = 0;
constexpr unsigned CTL_QOK = 16;
constexpr unsigned CTL_FREE = 32;
constexpr unsigned CTL_MLOK = 64;
constexpr unsigned CTL_ODONE = 96;
constexpr unsigned CTL_WTOK = 128;
constexpr unsigned LDS_DUMMY = LDS_CTL + 256;
constexpr unsigned LDS_TOTAL = LDS_DUMMY + 256;
static_assert(LDS_TOTAL <= 163840, "lds");

typedef __attribute__((ext_vector_type(8))) __bf16 bf16x8;
typedef __attribute__((ext_vector_type(2))) __bf16 bf16x2;
typedef __attribute__((ext_vector_type(4))) short s16x4;
typedef __attribute__((ext_vector_type(16))) float f32x16;
typedef __attribute__((ext_vector_type(4))) float f32x4;
typedef __attribute__((ext_vector_type(2))) float f32x2;
typedef __attribute__((ext_vector_type(4))) unsigned u32x4;
typedef __attribute__((ext_vector_type(2))) unsigned u32x2;
typedef __attribute__((ext_vector_type(4))) int i32x4;
#define LAS __attribute__((address_space(3)))

__device__ __forceinline__ float swap_max(float x) {
  auto r = __builtin_amdgcn_permlane32_swap(__float_as_uint(x), __float_as_uint(x), false, false);
  return fmaxf(__uint_as_float(r[0]), __uint_as_float(r[1]));
}
__device__ __forceinline__ float swap_sum(float x) {
  auto r = __builtin_amdgcn_permlane32_swap(__float_as_uint(x), __float_as_uint(x), false, false);
  return __uint_as_float(r[0]) + __uint_as_float(r[1]);
}
__device__ __forceinline__ unsigned pack2(float a, float b) {
  bf16x2 v = {(__bf16)a, (__bf16)b};
  return __builtin_bit_cast(unsigned, v);
}
template <int CTRL> __device__ __forceinline__ int dpp_add(int x) { return x + __builtin_amdgcn_update_dpp(0, x, CTRL, 0xf, 0xf, true); }
__device__ __forceinline__ unsigned now32() { return (unsigned)__builtin_amdgcn_s_memrealtime(); }
__device__ __forceinline__ int to_sgpr(int v) {
  int r;
  asm volatile("s_nop 1\n\tv_readfirstlane_b32 %0, %1" : "=s"(r) : "v"(v));
  return r;
}
#define LGKM0() asm volatile("s_waitcnt lgkmcnt(0)" ::: "memory")
#define VMWAIT(n) asm volatile("s_waitcnt vmcnt(" #n ")" ::: "memory")
#define CBAR() asm volatile("" ::: "memory")
template <int SLEEP> __device__ __forceinline__ void wait_above(unsigned addr, unsigned u) {
  unsigned v, cnt;
  if (SLEEP)
    asm volatile("s_mov_b32 %1, 0x200000\n\ts_branch 2f\n"
                 "1:\n\ts_sleep %4\n"
                 "2:\n\tds_read_b32 %0, %2\n\ts_sub_u32 %1, %1, 1\n\ts_waitcnt lgkmcnt(0)\n\t"
                 "v_cmp_ge_u32 vcc, %3, %0\n\ts_cbranch_scc1 3f\n\ts_nop 1\n\ts_cbranch_vccnz 1b\n3:"
                 : "=&v"(v), "=&s"(cnt) : "v"(addr), "s"(u), "n"(SLEEP) : "memory", "scc", "vcc");
  else
    asm volatile("s_mov_b32 %1, 0x200000\n"
                 "1:\n\tds_read_b32 %0, %2\n\ts_sub_u32 %1, %1, 1\n\ts_waitcnt lgkmcnt(0)\n\t"
                 "v_cmp_ge_u32 vcc, %3, %0\n\ts_cbranch_scc1 3f\n\ts_nop 1\n\ts_cbranch_vccnz 1b\n3:"
                 : "=&v"(v), "=&s"(cnt) : "v"(addr), "s"(u) : "memory", "scc", "vcc");
}

__global__ void __launch_bounds__(512, 1) __attribute__((amdgpu_num_vgpr(64)))
paged_attn(const void* __restrict__ qp, const void* __restrict__ kp, const void* __restrict__ vp, const int* __restrict__ bt,
           const int* __restrict__ lens, void* __restrict__ outp, char* __restrict__ ws, unsigned kvbytes, unsigned nphys) {
  extern __shared__ __attribute__((aligned(16))) char smem[];
  LAS char* lds = (LAS char*)smem;
  const int lane0 = threadIdx.x & 63;
  const int wv = __builtin_amdgcn_readfirstlane(threadIdx.x >> 6);
  const int g = blockIdx.x;
  const int h = g >> 4;
  constexpr float CS = 0.08838834764831845f * 1.4426950408889634f;
  volatile LAS unsigned* ctl = (volatile LAS unsigned*)(lds + LDS_CTL);

  const unsigned want = (g & 8) ? 15 - (g & 7) : (g & 7);
  unsigned lm1, rank;
  auto ranking = [&](int lane, unsigned& lr, u32x4& lq) __attribute__((always_inline)) {
    asm volatile("" : "+v"(lr), "+v"(lq));
    lm1 = min(lr - 1u, (unsigned)SKV - 1u);
    const unsigned key = lm1 * 16 + (lane & 15);
    rank = 0;
#pragma unroll
    for (int j = 0; j < 4; ++j) rank += (min(lq[j] - 1u, (unsigned)SKV - 1u) * 16 + ((lane >> 4) * 4 + j) > key) ? 1u : 0u;
    auto r1 = __builtin_amdgcn_permlane16_swap(rank, rank, false, false);
    rank = r1[0] + r1[1];
    auto r2 = __builtin_amdgcn_permlane32_swap(rank, rank, false, false);
    rank = r2[0] + r2[1];
  };
  auto total = [&]() __attribute__((always_inline)) {
    int t = (lm1 >> 4) + 1;
    t = dpp_add<0x121>(t); t = dpp_add<0x122>(t); t = dpp_add<0x124>(t); t = dpp_add<0x128>(t);
    return t;
  };

  if (__builtin_expect(wv < 2, 1)) {
    const int half = wv, lane = lane0;
    unsigned lr = lens[lane & 15];
    u32x4 lq = *(const u32x4*)(lens + (lane >> 4) * 4);
    unsigned first = bt[(lane & 15) * NBPS + half];
    __builtin_amdgcn_s_setprio(3);
    const unsigned rq4 = lane >> 4;
    unsigned vo0 = rq4 * 4096 + (((unsigned)(lane & 15) ^ (rq4 << 2)) << 4) + h * (HD * 2);
    unsigned vo1 = (vo0 + 16384 - 1024) ^ 16, vo2 = (vo0 + 32768 - 2048) ^ 32, vo3 = (vo0 + 49152 - 3072) ^ 48;
    unsigned vland = (unsigned)(size_t)lds + LDS_CTL + CTL_LAND + half * 4;
    unsigned vfree = (unsigned)(size_t)lds + LDS_CTL + CTL_FREE + (lane & 7) * 4;
    const unsigned ring = (unsigned)(size_t)lds + LDS_RING + half * 4096;
    const __amdgpu_buffer_rsrc_t rk = __builtin_amdgcn_make_buffer_rsrc((void*)kp, 0, (int)kvbytes, 0x00020000);
    asm volatile("" : "+v"(vo0), "+v"(vo1), "+v"(vo2), "+v"(vo3), "+v"(vland), "+v"(vfree) : "s"(rk), "s"(vp), "s"(nphys), "s"(ring));
    ranking(lane, lr, lq);
    first = min(first, nphys - 1) * PAGE_BYTES;
    const int b = __builtin_ctz((unsigned)__builtin_amdgcn_ballot_w64(rank == want) | 0x8000u);
    unsigned long long tstart;
    asm volatile("s_waitcnt vmcnt(0)\n\t"
                 "s_mov_b32 m0, %2\n\t"
                 "s_memrealtime %0\n\t"
                 "s_nop 2\n\t"
                 "buffer_load_dwordx4 %4, %1, %3 offen nt lds\n\t"
                 "buffer_load_dwordx4 %5, %1, %3 offen offset:1024 nt lds\n\t"
                 "buffer_load_dwordx4 %6, %1, %3 offen offset:2048 nt lds\n\t"
                 "buffer_load_dwordx4 %7, %1, %3 offen offset:3072 nt lds\n\t"
                 "s_waitcnt lgkmcnt(0)\n\t"
                 "s_barrier"
                 : "=&s"(tstart) : "s"(rk), "s"(ring), "s"(__builtin_amdgcn_readlane(first, b)), "v"(vo0), "v"(vo1), "v"(vo2), "v"(vo3) : "memory");
    __builtin_amdgcn_sched_barrier(0);
    asm volatile("" : "+v"(lm1));
    const int nown = (__builtin_amdgcn_readlane(lm1, b) >> 4) + 1, NT = (nown + 1) >> 1;
    unsigned l0 = *(LAS unsigned*)(lds + LDS_TBL + (b * NBPS + min(2 * lane + half, nown - 1)) * 4);
    unsigned l1 = *(LAS unsigned*)(lds + LDS_TBL + (b * NBPS + min(2 * lane + half + 128, nown - 1)) * 4);
    const int step = to_sgpr((int)((float)(total() * PACE_X16) * __builtin_amdgcn_rcpf((float)(2 * NT))));
    l0 = min(l0, nphys - 1) * PAGE_BYTES;
    l1 = min(l1, nphys - 1) * PAGE_BYTES;
#define CNT(r)
    unsigned vf, vt, lc;
    asm volatile(
        "s_waitcnt lgkmcnt(0)\n\t"
        "s_memrealtime s[68:69]\n\t"
        "ds_read_b32 %[vf], %[vfree]\n\t"
        "s_mov_b32 s60, 1\n\t"
        "s_mov_b32 s61, 4\n\t"
        "s_mov_b32 s62, 0x400000\n\t"
        "s_mov_b32 s66, 0\n\t"
        "s_min_u32 s67, %[nt], 64\n\t"
        "s_mov_b32 s72, 0x2000\n\t"
        "s_mov_b32 s73, 1\n\t"
        "s_mov_b32 s74, %[due1]\n\t"
        "s_mov_b64 s[76:77], %[kp]\n\t"
        "s_mov_b32 s78, %[nrec]\n\t"
        "s_mov_b32 s79, 0x20000\n\t"
        "s_and_b32 s77, s77, 0xffff\n\t"
        "v_mov_b32 %[lc], %[l0]\n\t"
        "s_getpc_b64 s[70:71]\n"
        ".LldA%=:\n\t"
        "s_add_u32 s80, s70, .LldL%=-.LldA%=\n\t"
        "s_addc_u32 s71, s71, 0\n\t"
        "s_branch .LldF%=\n"
        ".LldR%=:\n\t"
        "s_sub_u32 s62, s62, 1\n\t"
        "s_cbranch_scc1 .LldE%=\n"
        ".LldT%=:\n\t"
        "s_getreg_b32 s64, hwreg(HW_REG_IB_STS)\n\t"
        "s_and_b32 s65, s64, 15\n\t"
        "s_bfe_u32 s64, s64, 0x20016\n\t"
        "s_lshl4_add_u32 s64, s64, s65\n\t"
        "s_add_u32 s64, s64, 3\n\t"
        "s_and_b32 s64, s64, 60\n\t"
        "s_sub_u32 s65, s61, s64\n\t"
        "s_cmp_lg_u32 s65, s66\n\t"
        "s_cbranch_scc1 .LldP%=\n"
        ".LldC%=:\n\t"
        "s_waitcnt lgkmcnt(0)\n\t"
        "v_cmp_eq_u32 vcc, s60, %[vf]\n\t"
        "s_lshl_b32 s64, s68, 8\n\t"
        "s_memrealtime s[68:69]\n\t"
        "ds_read_b32 %[vf], %[vfree]\n\t"
        CNT("s82")
        "s_sub_u32 s64, s64, s74\n\t"
        "s_cmp_lt_i32 s64, 0\n\t"
        "s_cbranch_scc1 .LldR%=\n\t"
        CNT("s83")
        "s_cbranch_vccz .LldR%=\n\t"
        CNT("s84")
        "buffer_load_dwordx4 %[o0], s[76:79], s63 offen nt lds\n\t"
        "s_add_u32 s74, s74, %[step]\n\t"
        "s_add_u32 s73, s73, 1\n\t"
        "buffer_load_dwordx4 %[o1], s[76:79], s63 offen offset:1024 nt lds\n\t"
        "s_add_u32 s60, s60, 1\n\t"
        "s_add_u32 s61, s61, 4\n\t"
        "buffer_load_dwordx4 %[o2], s[76:79], s63 offen offset:2048 nt lds\n\t"
        "s_add_u32 s72, s72, 0x2000\n\t"
        "s_and_b32 s72, s72, 0xe000\n\t"
        "buffer_load_dwordx4 %[o3], s[76:79], s63 offen offset:3072 nt lds\n"
        ".LldF%=:\n\t"
        "v_readlane_b32 s63, %[lc], s73\n\t"
        "s_add_u32 m0, s72, %[ring]\n\t"
        "s_cmp_lg_u32 s60, s67\n\t"
        "s_cbranch_scc1 .LldT%=\n\t"
        "s_cmp_eq_u32 s73, %[nt]\n\t"
        "s_cbranch_scc1 .LldV%=\n\t"
        "v_mov_b32 %[lc], %[l1]\n\t"
        "v_readfirstlane_b32 s63, %[l1]\n\t"
        "s_add_u32 s67, s60, %[nt]\n\t"
        "s_sub_u32 s67, s67, 64\n\t"
        "s_branch .LldT%=\n"
        ".LldV%=:\n\t"
        "s_cmp_lg_u32 s60, %[nt]\n\t"
        "s_cbranch_scc1 .LldD%=\n\t"
        "s_mov_b64 s[76:77], %[vp]\n\t"
        "s_and_b32 s77, s77, 0xffff\n\t"
        "s_mov_b32 s73, 0\n\t"
        "s_min_u32 s67, %[nt], 64\n\t"
        "s_add_u32 s67, s67, s60\n\t"
        "s_add_u32 s74, s74, %[gap]\n\t"
        "v_mov_b32 %[lc], %[l0]\n\t"
        "v_readfirstlane_b32 s63, %[l0]\n\t"
        "v_xor_b32 %[o1], 16, %[o1]\n\t"
        "v_xor_b32 %[o2], 32, %[o2]\n\t"
        "v_xor_b32 %[o3], 48, %[o3]\n\t"
        "s_branch .LldT%=\n"
        ".LldD%=:\n\t"
        "s_add_u32 s74, s74, 0x40000000\n\t"
        "s_mov_b32 s60, -1\n\t"
        "s_branch .LldT%=\n"
        ".LldP%=:\n\t"
        "s_min_u32 s64, s64, 32\n\t"
        "s_add_u32 s70, s80, s64\n\t"
        "v_mov_b32 %[vt], s65\n\t"
        "s_mov_b32 s66, s65\n\t"
        "s_setpc_b64 s[70:71]\n\t"
        ".p2align 6\n"
        ".LldL%=:\n\t"
        "s_waitcnt vmcnt(0)\n\t"
        "s_waitcnt vmcnt(4)\n\t"
        "s_waitcnt vmcnt(8)\n\t"
        "s_waitcnt vmcnt(12)\n\t"
        "s_waitcnt vmcnt(16)\n\t"
        "s_waitcnt vmcnt(20)\n\t"
        "s_waitcnt vmcnt(24)\n\t"
        "s_waitcnt vmcnt(28)\n\t"
        "s_waitcnt vmcnt(32)\n\t"
        "ds_write_b32 %[vland], %[vt]\n\t"
        "s_cmp_lg_u32 s65, %[fin]\n\t"
        "s_cbranch_scc1 .LldC%=\n"
        ".LldE%=:\n\t"
        "s_waitcnt vmcnt(0) lgkmcnt(0)\n\t"
        : [vf] "=&v"(vf), [vt] "=&v"(vt), [lc] "=&v"(lc), [o1] "+v"(vo1), [o2] "+v"(vo2), [o3] "+v"(vo3)
        : [l0] "v"(l0), [l1] "v"(l1), [o0] "v"(vo0), [vland] "v"(vland), [vfree] "v"(vfree),
          [kp] "s"(kp), [vp] "s"(vp), [nrec] "s"(kvbytes), [nt] "s"(NT), [ring] "s"(ring),
          [step] "s"(step), [due1] "s"(((unsigned)tstart << 8) + step + half * ((step * HOFF) >> 3)), [fin] "s"(8 * NT), [gap] "n"(GAP_TICKS * 256)
        : "memory", "scc", "vcc", "s60", "s61", "s62", "s63", "s64", "s65", "s66", "s67", "s68", "s69", "s70", "s71", "s72", "s73", "s74",
          "s76", "s77", "s78", "s79", "s80", "s82", "s83", "s84");
    STAMP(3);
    asm volatile("s_endpgm" ::: "memory");
  }

  int lane = lane0;
  asm volatile("" : "+v"(lane));
  unsigned lraw = lens[lane & 15];
  u32x4 lq = *(const u32x4*)(lens + (lane >> 4) * 4);
  if (wv < 6) {
    const __amdgpu_buffer_rsrc_t rb = __builtin_amdgcn_make_buffer_rsrc((void*)bt, 0, NB * NBPS * 4, 0x00020000);
    asm volatile("s_mov_b32 m0, %0\n\ts_nop 4\n\tbuffer_load_dwordx4 %2, %1, 0 offen lds\n\t"
                 "buffer_load_dwordx4 %2, %1, 0 offen offset:1024 lds\n\t"
                 "buffer_load_dwordx4 %2, %1, 0 offen offset:2048 lds\n\t"
                 "buffer_load_dwordx4 %2, %1, 0 offen offset:3072 lds\n\t"
                 "s_waitcnt vmcnt(0)"
                 :: "s"((unsigned)(size_t)lds + LDS_TBL + (wv - 2) * 4096), "s"(rb), "v"(lane * 16 + (wv - 2) * 4096) : "memory");
  } else if (wv == 6) {
    if (lane < 40) ctl[lane] = ((lane >> 3) == CTL_FREE / 32) ? (lane & 7) : 0u;
  }
  if (wv != 7) asm volatile("s_waitcnt lgkmcnt(0)\n\ts_barrier" ::: "memory");
  ranking(lane, lraw, lq);
  const int b = __builtin_ctz((unsigned)__builtin_amdgcn_ballot_w64(rank == want) | 0x8000u);
  if (wv == 7) {
    const __amdgpu_buffer_rsrc_t rq = __builtin_amdgcn_make_buffer_rsrc((void*)qp, 0, NB * HQ * SQ * HD * 2, 0x00020000);
    const unsigned q0 = (((int)__builtin_amdgcn_readlane(lraw, b) > 0) ? (unsigned)((b * HQ + 2 * h) * SQ * HD * 2) : INVALID) + (lane >> 4) * 256 +
                        (((unsigned)(lane & 15) ^ ((lane >> 4) << 2)) << 4);
    asm volatile("s_mov_b32 m0, %0\n\ts_nop 4\n\t"
                 "buffer_load_dwordx4 %2, %1, 0 offen " QPOL " lds\n\t"
                 "buffer_load_dwordx4 %3, %1, 0 offen offset:1024 " QPOL " lds\n\t"
                 "buffer_load_dwordx4 %4, %1, 0 offen offset:2048 " QPOL " lds\n\t"
                 "buffer_load_dwordx4 %5, %1, 0 offen offset:3072 " QPOL " lds\n\t"
                 "s_add_u32 m0, m0, 4096\n\ts_nop 0\n\t"
                 "buffer_load_dwordx4 %2, %1, %6 offen " QPOL " lds\n\t"
                 "buffer_load_dwordx4 %3, %1, %6 offen offset:1024 " QPOL " lds\n\t"
                 "buffer_load_dwordx4 %4, %1, %6 offen offset:2048 " QPOL " lds\n\t"
                 "buffer_load_dwordx4 %5, %1, %6 offen offset:3072 " QPOL " lds\n\t"
                 "s_barrier\n\t"
                 "s_waitcnt vmcnt(0)"
                 :: "s"((unsigned)(size_t)lds + LDS_Q), "s"(rq), "v"(q0), "v"(q0 ^ 16), "v"(q0 ^ 32), "v"(q0 ^ 48), "s"(4096) : "memory", "scc");
    ctl[CTL_QOK / 4] = 1;
  }
  const int bp = __builtin_ctz((unsigned)__builtin_amdgcn_ballot_w64(rank == 15 - want) | 0x8000u);
  const int lim = __builtin_amdgcn_readlane(lm1, b) + 1;
  const int nown = (lim + PAGE - 1) / PAGE, NT = (nown + 1) >> 1;
  const int tot = total();
  const int w = wv - 2;
  const int r32 = lane & 31, hi = lane >> 5;
  const int nmine = (NT > w) ? (NT - w + NC - 1) / NC : 0;
  const unsigned lbase = (unsigned)(size_t)lds;
  const unsigned aland = lbase + LDS_CTL + CTL_LAND + (lane & 1) * 4;
  const int npl = (__builtin_amdgcn_readlane(lm1, bp) >> 4) + 1, NTL = (npl + 1) >> 1;
  const bool pf_on = PF_EVERY && npl * 16 * 64 > __builtin_amdgcn_readfirstlane(tot) * PF_X64 && npl > nown && (PF_ALL || (h & 3) == 1);
  const unsigned pf_lead = to_sgpr((int)((float)(2 * NTL * PF_LEAD) * 256.0f * __builtin_amdgcn_rcpf((float)(tot * PACE_X16))));
  const unsigned pf_ratio = to_sgpr((int)((float)NTL * 65536.0f * __builtin_amdgcn_rcpf((float)NT)));
  auto prefetch = [&](unsigned op) {
    const unsigned i = op / (PF_EVERY ? PF_EVERY : 1);
    unsigned tl = (((i | (PF_RUN - 1)) * PF_EVERY * pf_ratio) >> 16) + pf_lead - (~i & (PF_RUN - 1));
    if (tl >= 2u * NTL) return;
    const bool isv = tl >= (unsigned)NTL;
    tl -= isv ? NTL : 0;
    const unsigned p0 = min((unsigned)bt[bp * NBPS + 2 * tl], nphys - 1), p1 = min((unsigned)bt[bp * NBPS + min(2 * tl + 1, (unsigned)npl - 1)], nphys - 1);
    const __amdgpu_buffer_rsrc_t rp = __builtin_amdgcn_make_buffer_rsrc(isv ? (void*)vp : (void*)kp, 0, (int)kvbytes, 0x00020000);
    unsigned ln = lane;
    asm volatile("" : "+v"(ln));
    const unsigned vo = ((ln & 32) ? p1 : p0) * PAGE_BYTES + h * (HD * 2) + ((ln >> 1) & 15) * 4096 + (PF_SLOW == 1 ? 128 : PF_SLOW == 2 ? 0 : (ln & 1) * 128);
    asm volatile("s_mov_b32 m0, %0\n\ts_nop 4\n\tbuffer_load_dword %2, %1, 0 offen " PF_POL " lds" PF_2ND :: "s"(lbase + LDS_DUMMY), "s"(rp), "v"(vo) : "memory");
  };
  const int pos = (w + NC * 22 - NT) % NC;
  const unsigned palp = LDS_WREG + pos * W_SIZE + W_ALP + r32 * 4;
  const unsigned ppst = LDS_WREG + pos * W_SIZE + lane * 16;
  asm volatile("" ::: "v128", "v255");
  unsigned resc = 0;
  float m = -3.0e38f, mprev = -3.0e38f, l = 0.f;

  {
    const unsigned fk = (((unsigned)r32 & 3) << 2) | (((unsigned)r32 >> 2) & 3);
    const unsigned kaddr0 = LDS_RING + r32 * 256 + (((unsigned)hi ^ fk) << 4);
    bf16x8 qf[8];
    wait_above<1>(lbase + LDS_CTL + CTL_QOK, 0);
#pragma unroll
    for (int s = 0; s < 8; ++s) qf[s] = *(LAS bf16x8*)(lds + ((kaddr0 + LDS_Q - LDS_RING) ^ (s << 5)));
#pragma unroll 1
    for (int k = 0; k < nmine; ++k) {
      const unsigned t = w + NC * k;
      if (PRESLEEP && k > 0) __builtin_amdgcn_s_sleep(PRESLEEP);
      wait_above<CSLEEP>(aland, 4 * t);
      if (k == 0) STAMP(1);
      bf16x8 kf[8];
#pragma unroll
      for (int s = 0; s < 8; ++s) kf[s] = *(LAS bf16x8*)(lds + ((kaddr0 + ((t & 7) << 13)) ^ (s << 5)));
      asm volatile("s_waitcnt lgkmcnt(0)" : "+v"(kf[0]), "+v"(kf[1]), "+v"(kf[2]), "+v"(kf[3]), "+v"(kf[4]), "+v"(kf[5]), "+v"(kf[6]), "+v"(kf[7]) :: "memory");
      ctl[CTL_FREE / 4 + (t & 7)] = t + 8;
      if (pf_on && t % (PF_EVERY ? PF_EVERY : 1) == 0) prefetch(t);
      f32x16 sc;
#pragma unroll
      for (int r = 0; r < 16; ++r) sc[r] = 0.f;
#pragma unroll
      for (int s = 0; s < 8; ++s) sc = __builtin_amdgcn_mfma_f32_32x32x16_bf16(kf[s], qf[s], sc, 0, 0, 0);
      if ((int)t == NT - 1) {
        const int kb = 2 * t * PAGE + 4 * hi;
#pragma unroll
        for (int r = 0; r < 16; ++r) sc[r] = (kb + 8 * (r >> 2) + (r & 3) < lim) ? sc[r] : -__builtin_inff();
      }
      float mx = sc[0];
#pragma unroll
      for (int r = 1; r < 16; ++r) mx = fmaxf(mx, sc[r]);
      mx = swap_max(mx);
      const float mn = fmaxf(m, mx);
      const float alpha = __builtin_amdgcn_exp2f((m - mn) * CS);
      mprev = m;
      m = mn;
      const float nmc = -mn * CS;
      float ps = 0.f;
#pragma unroll
      for (int r = 0; r < 16; ++r) { sc[r] = __builtin_amdgcn_exp2f(fmaf(sc[r], CS, nmc)); ps += sc[r]; }
      l = fmaf(l, alpha, ps);
      u32x4 pf[2];
#pragma unroll
      for (int s = 0; s < 2; ++s)
        pf[s] = u32x4{pack2(sc[8 * s], sc[8 * s + 1]), pack2(sc[8 * s + 2], sc[8 * s + 3]), pack2(sc[8 * s + 4], sc[8 * s + 5]), pack2(sc[8 * s + 6], sc[8 * s + 7])};
      if (__builtin_amdgcn_ballot_w64(alpha != 1.0f)) resc |= 1u << k;
      *(LAS float*)(lds + palp + k * 128) = alpha;
      if (k >= KREG || k == nmine - 1) {
        const unsigned dst = ppst + ((k == nmine - 1) ? W_LAST : (k - KREG) * 2048);
        *(LAS u32x4*)(lds + dst) = pf[0];
        *(LAS u32x4*)(lds + dst + 1024) = pf[1];
      } else {
        asm volatile("s_set_gpr_idx_on %8, gpr_idx(DST)\n\t"
                     "v_mov_b32 v128, %0\n\tv_mov_b32 v129, %1\n\tv_mov_b32 v130, %2\n\tv_mov_b32 v131, %3\n\t"
                     "v_mov_b32 v132, %4\n\tv_mov_b32 v133, %5\n\tv_mov_b32 v134, %6\n\tv_mov_b32 v135, %7\n\t"
                     "s_set_gpr_idx_off"
                     :: "v"(pf[0][0]), "v"(pf[0][1]), "v"(pf[0][2]), "v"(pf[0][3]), "v"(pf[1][0]), "v"(pf[1][1]), "v"(pf[1][2]), "v"(pf[1][3]), "s"(8 * k));
      }
    }
  }
  l = swap_sum(l);
  *(LAS f32x2*)(lds + LDS_ML + w * 256 + r32 * 8) = f32x2{m, l};
  LGKM0();
  ctl[CTL_MLOK / 4 + w] = 1;
  STAMP(2);

  const unsigned tq = (lane & 15) >> 2, tp = lane & 3, tblk = (lane >> 4) & 1;
  const unsigned vaddr0 = LDS_RING + (4 * hi + tq) * 256 + ((tq ^ (pos & 3)) << 6) + ((2 * tblk + (tp >> 1)) << 4) + ((tp & 1) << 3);
  f32x16 o[4];
#pragma unroll
  for (int i = 0; i < 4; ++i)
#pragma unroll
    for (int r = 0; r < 16; ++r) o[i][r] = 0.f;
  auto weights = [&]() {
    wait_above<1>(lbase + LDS_CTL + CTL_MLOK + min(lane, NC - 1) * 4, 0);
    unsigned a = LDS_ML + r32 * 8;
    asm volatile("" : "+v"(a) :: "memory");
    float M = -3.0e38f, L = 0.f;
    f32x2 ml[NC];
#pragma unroll
    for (int i = 0; i < NC; ++i) { ml[i] = *(LAS f32x2*)(lds + a + i * 256); M = fmaxf(M, ml[i][0]); }
#pragma unroll
    for (int i = 0; i < NC; ++i) L = fmaf(__builtin_amdgcn_exp2f((ml[i][0] - M) * CS), ml[i][1], L);
    const float inv = 1.0f / L;
    *(LAS float*)(lds + (a >> 1) + (LDS_WT - LDS_ML / 2) + pos * 128) = __builtin_amdgcn_exp2f((mprev - M) * CS) * inv;
    const float ct = __builtin_amdgcn_exp2f((m - M) * CS) * inv;
#pragma unroll
    for (int s = 0; s < 2; ++s) {
      u32x4 x = *(LAS u32x4*)(lds + ppst + W_LAST + s * 1024);
#pragma unroll
      for (int e = 0; e < 4; ++e) x[e] = pack2(__uint_as_float(x[e] << 16) * ct, __uint_as_float(x[e] & 0xffff0000u) * ct);
      *(LAS u32x4*)(lds + ppst + W_LAST + s * 1024) = x;
    }
    LGKM0();
    ctl[CTL_WTOK / 4 + w] = 1;
  };
  resc &= ~1u;
  if (nmine <= 2) weights();
#pragma unroll 1
  for (int k = 0; k < nmine - 1; ++k) {
    if (k == 1) weights();
    if ((resc >> k) & 1) {
      const float alpha = *(LAS float*)(lds + palp + k * 128);
#pragma unroll
      for (int i = 0; i < 4; ++i)
#pragma unroll
        for (int r = 0; r < 16; ++r) o[i][r] *= alpha;
    }
    const unsigned u = NT + w + NC * k;
    u32x4 pf[2];
    if (k >= KREG) {
      pf[0] = *(LAS u32x4*)(lds + ppst + (k - KREG) * 2048);
      pf[1] = *(LAS u32x4*)(lds + ppst + (k - KREG) * 2048 + 1024);
    } else {
      asm volatile("s_set_gpr_idx_on %8, gpr_idx(SRC0)\n\t"
                   "v_mov_b32 %0, v128\n\tv_mov_b32 %1, v129\n\tv_mov_b32 %2, v130\n\tv_mov_b32 %3, v131\n\t"
                   "v_mov_b32 %4, v132\n\tv_mov_b32 %5, v133\n\tv_mov_b32 %6, v134\n\tv_mov_b32 %7, v135\n\t"
                   "s_set_gpr_idx_off\n\ts_nop 1"
                   : "=v"(pf[0][0]), "=v"(pf[0][1]), "=v"(pf[0][2]), "=v"(pf[0][3]), "=v"(pf[1][0]), "=v"(pf[1][1]), "=v"(pf[1][2]), "=v"(pf[1][3])
                   : "s"(8 * k));
    }
    asm volatile("" : "+v"(pf[0]), "+v"(pf[1]), "+v"(o[0]), "+v"(o[1]), "+v"(o[2]), "+v"(o[3]));
    if (PRESLEEP && k > 1) __builtin_amdgcn_s_sleep(2 * PRESLEEP);
    wait_above<CSLEEP>(aland, 4 * u);
    if (TPRIO && k == nmine - 2) __builtin_amdgcn_s_setprio(TPRIO);
    bf16x8 vf[4][2];
#pragma unroll
    for (int i = 0; i < 4; ++i) {
      const unsigned a = (vaddr0 + ((u & 7) << 13)) ^ (i << 6);
#pragma unroll
      for (int s = 0; s < 2; ++s) {
        const s16x4 lo = __builtin_amdgcn_ds_read_tr16_b64_v4i16((LAS s16x4*)(lds + a + 4096 * s));
        const s16x4 up = __builtin_amdgcn_ds_read_tr16_b64_v4i16((LAS s16x4*)(lds + a + 4096 * s + 2048));
        vf[i][s] = __builtin_bit_cast(bf16x8, __builtin_shufflevector(lo, up, 0, 1, 2, 3, 4, 5, 6, 7));
      }
    }
    asm volatile("s_waitcnt lgkmcnt(0)" : "+v"(vf[0][0]), "+v"(vf[0][1]), "+v"(vf[1][0]), "+v"(vf[1][1]), "+v"(vf[2][0]), "+v"(vf[2][1]), "+v"(vf[3][0]), "+v"(vf[3][1]) :: "memory");
    ctl[CTL_FREE / 4 + (u & 7)] = u + 8;
    if (pf_on && u % (PF_EVERY ? PF_EVERY : 1) == 0) prefetch(u);
#pragma unroll
    for (int s = 0; s < 2; ++s)
#pragma unroll
      for (int i = 0; i < 4; ++i) o[i] = __builtin_amdgcn_mfma_f32_32x32x16_bf16(vf[i][s], __builtin_bit_cast(bf16x8, pf[s]), o[i], 0, 0, 0);
  }
  STAMP(3);

  {
    unsigned l16 = lane * 16;
    asm volatile("" : "+v"(l16));
    if (nmine > 1) {
      auto put = [&](unsigned dst, const f32x16& x) {
#pragma unroll
        for (int j = 0; j < 4; ++j) *(LAS f32x4*)(lds + dst + j * 1024) = f32x4{x[4 * j], x[4 * j + 1], x[4 * j + 2], x[4 * j + 3]};
      };
      if (pos > 3) put(LDS_RING + ((2 * NT - 2 * NC + pos) & 7) * 8192 + l16, o[0]);
#pragma unroll
      for (int i = 1; i < 4; ++i) put(LDS_WREG + pos * W_SIZE + (i - 1) * 4096 + l16, o[i]);
      if (TWAIT) LGKM0(); else CBAR();
      ctl[CTL_ODONE / 4 + pos] = 1;
    }
    if (pos < 4) {
      f32x16 of = o[0];
      const unsigned l4 = l16 >> 2, awt = LDS_WT + (l4 & 127);
      wait_above<1>(lbase + LDS_CTL + CTL_WTOK + min(l4, (NC - 1) * 4u), 0);
      auto src_o = [&](int p) -> unsigned {
        const int i = pos ^ (p & 3);
        return l16 + (i == 0 ? LDS_RING + ((2 * NT - 2 * NC + p) & 7) * 8192 : LDS_WREG + p * W_SIZE + (i - 1) * 4096);
      };
      auto fma_o = [&](unsigned src, float c) {
#pragma unroll
        for (int j = 0; j < 4; ++j) {
          const f32x4 x = *(LAS f32x4*)(lds + src + j * 1024);
#pragma unroll
          for (int e = 0; e < 4; ++e) of[4 * j + e] = fmaf(x[e], c, of[4 * j + e]);
        }
      };
      auto mma_t = [&](int p, bf16x8 p0, bf16x8 p1) {
        const unsigned a = vaddr0 + (((2 * NT - NC + p) & 7) << 13);
        bf16x8 vf[2];
#pragma unroll
        for (int s = 0; s < 2; ++s) {
          const s16x4 lo = __builtin_amdgcn_ds_read_tr16_b64_v4i16((LAS s16x4*)(lds + a + 4096 * s));
          const s16x4 up = __builtin_amdgcn_ds_read_tr16_b64_v4i16((LAS s16x4*)(lds + a + 4096 * s + 2048));
          vf[s] = __builtin_bit_cast(bf16x8, __builtin_shufflevector(lo, up, 0, 1, 2, 3, 4, 5, 6, 7));
        }
        of = __builtin_amdgcn_mfma_f32_32x32x16_bf16(vf[0], p0, of, 0, 0, 0);
        of = __builtin_amdgcn_mfma_f32_32x32x16_bf16(vf[1], p1, of, 0, 0, 0);
      };
      {
        const float cw = *(LAS float*)(lds + awt + pos * 128);
#pragma unroll
        for (int r = 0; r < 16; ++r) of[r] *= cw;
      }
      if (NT >= 2 * NC) {
        float co[5];
        bf16x8 pl[NC][2];
        auto ld_p = [&](int p) {
          pl[p][0] = *(LAS bf16x8*)(lds + LDS_WREG + p * W_SIZE + W_LAST + l16);
          pl[p][1] = *(LAS bf16x8*)(lds + LDS_WREG + p * W_SIZE + W_LAST + l16 + 1024);
        };
#define PIN_OF() asm volatile("" : "+v"(of))
#pragma unroll
        for (int i = 1; i < 4; ++i) co[i - 1] = *(LAS float*)(lds + awt + (pos ^ i) * 128);
        co[3] = *(LAS float*)(lds + awt + 4 * 128);
        co[4] = *(LAS float*)(lds + awt + 5 * 128);
        ld_p(0); ld_p(1); ld_p(2);
        PIN_OF();
        wait_above<TSLEEP>(lbase + LDS_CTL + CTL_ODONE + (l4 & 12), 0);
#pragma unroll
        for (int i = 1; i < 4; ++i) fma_o(LDS_WREG + (pos ^ i) * W_SIZE + (i - 1) * 4096 + l16, co[i - 1]);
        PIN_OF();
        ld_p(3); ld_p(4); ld_p(5);
        wait_above<TSLEEP>(aland, 4 * (2 * NT - NC + 2));
        mma_t(0, pl[0][0], pl[0][1]);
        mma_t(1, pl[1][0], pl[1][1]);
        mma_t(2, pl[2][0], pl[2][1]);
        STAMP(4);
        wait_above<TSLEEP>(lbase + LDS_CTL + CTL_ODONE + 16 + (l4 & 4), 0);
        fma_o(src_o(4), co[3]);
        fma_o(src_o(5), co[4]);
        PIN_OF();
        wait_above<0>(aland, 4 * (2 * NT - NC + 4));
        mma_t(3, pl[3][0], pl[3][1]);
        mma_t(4, pl[4][0], pl[4][1]);
        PIN_OF();
        wait_above<0>(aland, 4 * (2 * NT - NC + 5));
        mma_t(5, pl[5][0], pl[5][1]);
      } else {
#pragma unroll 1
        for (int p = max(NC - NT, 0); p < NC; ++p) {
          if (p != pos && NT - 2 * NC + p >= 0) {
            wait_above<1>(lbase + LDS_CTL + CTL_ODONE + p * 4, 0);
            fma_o(src_o(p), *(LAS float*)(lds + awt + p * 128));
          }
          wait_above<1>(aland, 4 * (2 * NT - NC + p));
          mma_t(p, *(LAS bf16x8*)(lds + LDS_WREG + p * W_SIZE + W_LAST + l16), *(LAS bf16x8*)(lds + LDS_WREG + p * W_SIZE + W_LAST + l16 + 1024));
        }
      }
      const __amdgpu_buffer_rsrc_t ro = __builtin_amdgcn_make_buffer_rsrc(outp, 0, NB * HQ * SQ * HD * 2, 0x00020000);
      const unsigned orow = (unsigned)(b * HQ + 2 * h) * SQ * HD * 2 + pos * 64 + ((l16 & 511) << 4) + ((l16 >> 5) & 16);
#pragma unroll
      for (int j = 0; j < 2; ++j) {
        const unsigned a0 = pack2(of[8 * j], of[8 * j + 1]), a1 = pack2(of[8 * j + 2], of[8 * j + 3]);
        const unsigned b0 = pack2(of[8 * j + 4], of[8 * j + 5]), b1 = pack2(of[8 * j + 6], of[8 * j + 7]);
        auto s0 = __builtin_amdgcn_permlane32_swap(a0, b0, false, false);
        auto s1 = __builtin_amdgcn_permlane32_swap(a1, b1, false, false);
        u32x4 val = {s0[0], s1[0], s0[1], s1[1]};
        __builtin_amdgcn_raw_buffer_store_b128(val, ro, orow + j * 32, 0, OUTAUX);
      }
    }
  }
}
}

extern "C" void kernel_launch(void* const* d_in, const int* in_sizes, int n_in, void* d_out, int out_size, void* d_ws,
                              size_t ws_size, hipStream_t stream) {
  (void)n_in; (void)out_size; (void)ws_size;
  const unsigned nphys = (unsigned)(in_sizes[1] / (PAGE * HKV * HD));
  const unsigned kvbytes = nphys * PAGE_BYTES;
  (void)hipFuncSetAttribute((const void*)paged_attn, hipFuncAttributeMaxDynamicSharedMemorySize, LDS_TOTAL);
  hipLaunchKernelGGL(paged_attn, dim3(256), dim3(512), LDS_TOTAL, stream, d_in[0], d_in[1], d_in[2], (const int*)d_in[3],
                     (const int*)d_in[4], d_out, (char*)d_ws, kvbytes, nphys);
}
```

```cpp
#include <hip/hip_runtime.h>
#include <hip/hip_bf16.h>

#define PROBE_G 0
#define QPOL "nt"
#define PF_2ND
#define TLBW 5
#define TLBG 0x200000u
#define SOFT 0
#define COLD_AT 115
#define COLD_PAUSE 90
#define TSHORT 1
#define TDELAY 0
#define PACE_X16 442
#define GAP_TICKS 0
#define OUTAUX 0
#define HOFF 4
#define LSLEEP 0
#define PRESLEEP 0
#define PF_EVERY 2
#define PF_X64 76
#define PF_ALL 0
#define PF_LEAD 380
#define PF_POL ""
#define PF_SLOW 0
#define PF_RUN 4
#define TSLEEP 0
#define TWAIT 0
#define TPRIO 0
#define CSLEEP 1

#define STAMP(i) do {} while (0)

namespace {
constexpr int NB = 16, HQ = 32, HKV = 16, SQ = 16, HD = 128, PAGE = 16, NBPS = 256, SKV = NBPS * PAGE;
constexpr unsigned PAGE_BYTES = PAGE * HKV * HD * 2;
constexpr unsigned INVALID = 0x80000000u;
constexpr int NC = 6;
constexpr int KREG = 16, KLDS = 5;
static_assert(NC * (KREG + KLDS + 1) >= NBPS / 2, "P storage");
constexpr unsigned LDS_RING = 0;
constexpr unsigned LDS_WREG = 65536;
constexpr unsigned W_ALP = KLDS * 2048;
constexpr unsigned W_LAST = W_ALP + (KREG + KLDS + 1) * 128;
constexpr unsigned W_SIZE = W_LAST + 2048;
static_assert(W_LAST >= 3 * 4096 && W_SIZE % 256 == 0, "consumer region");
constexpr unsigned LDS_TBL = LDS_WREG + 2 * W_SIZE;
constexpr unsigned LDS_Q = LDS_WREG;
constexpr unsigned LDS_ML = LDS_WREG + NC * W_SIZE;
constexpr unsigned LDS_WT = LDS_ML + NC * 256;
constexpr unsigned LDS_CTL = LDS_WT + NC * 128;
constexpr unsigned CTL_LAND = 0;
constexpr unsigned CTL_QOK = 16;
constexpr unsigned CTL_FREE = 32;
constexpr unsigned CTL_MLOK = 64;
constexpr unsigned CTL_ODONE = 96;
constexpr unsigned CTL_WTOK = 128;
constexpr unsigned LDS_DUMMY = LDS_CTL + 256;
constexpr unsigned LDS_TOTAL = LDS_DUMMY + 256;
static_assert(LDS_TOTAL <= 163840, "lds");

typedef __attribute__((ext_vector_type(8))) __bf16 bf16x8;
typedef __attribute__((ext_vector_type(2))) __bf16 bf16x2;
typedef __attribute__((ext_vector_type(4))) short s16x4;
typedef __attribute__((ext_vector_type(16))) float f32x16;
typedef __attribute__((ext_vector_type(4))) float f32x4;
typedef __attribute__((ext_vector_type(2))) float f32x2;
typedef __attribute__((ext_vector_type(4))) unsigned u32x4;
typedef __attribute__((ext_vector_type(2))) unsigned u32x2;
typedef __attribute__((ext_vector_type(4))) int i32x4;
#define LAS __attribute__((address_space(3)))

__device__ __forceinline__ float swap_max(float x) {
  auto r = __builtin_amdgcn_permlane32_swap(__float_as_uint(x), __float_as_uint(x), false, false);
  return fmaxf(__uint_as_float(r[0]), __uint_as_float(r[1]));
}
__device__ __forceinline__ float swap_sum(float x) {
  auto r = __builtin_amdgcn_permlane32_swap(__float_as_uint(x), __float_as_uint(x), false, false);
  return __uint_as_float(r[0]) + __uint_as_float(r[1]);
}
__device__ __forceinline__ unsigned pack2(float a, float b) {
  bf16x2 v = {(__bf16)a, (__bf16)b};
  return __builtin_bit_cast(unsigned, v);
}
template <int CTRL> __device__ __forceinline__ int dpp_add(int x) { return x + __builtin_amdgcn_update_dpp(0, x, CTRL, 0xf, 0xf, true); }
__device__ __forceinline__ unsigned now32() { return (unsigned)__builtin_amdgcn_s_memrealtime(); }
__device__ __forceinline__ int to_sgpr(int v) {
  int r;
  asm volatile("s_nop 1\n\tv_readfirstlane_b32 %0, %1" : "=s"(r) : "v"(v));
  return r;
}
#define LGKM0() asm volatile("s_waitcnt lgkmcnt(0)" ::: "memory")
#define VMWAIT(n) asm volatile("s_waitcnt vmcnt(" #n ")" ::: "memory")
#define CBAR() asm volatile("" ::: "memory")
template <int SLEEP> __device__ __forceinline__ void wait_above(unsigned addr, unsigned u) {
  unsigned v, cnt;
  if (SLEEP)
    asm volatile("s_mov_b32 %1, 0x200000\n\ts_branch 2f\n"
                 "1:\n\ts_sleep %4\n"
                 "2:\n\tds_read_b32 %0, %2\n\ts_sub_u32 %1, %1, 1\n\ts_waitcnt lgkmcnt(0)\n\t"
                 "v_cmp_ge_u32 vcc, %3, %0\n\ts_cbranch_scc1 3f\n\ts_nop 1\n\ts_cbranch_vccnz 1b\n3:"
                 : "=&v"(v), "=&s"(cnt) : "v"(addr), "s"(u), "n"(SLEEP) : "memory", "scc", "vcc");
  else
    asm volatile("s_mov_b32 %1, 0x200000\n"
                 "1:\n\tds_read_b32 %0, %2\n\ts_sub_u32 %1, %1, 1\n\ts_waitcnt lgkmcnt(0)\n\t"
                 "v_cmp_ge_u32 vcc, %3, %0\n\ts_cbranch_scc1 3f\n\ts_nop 1\n\ts_cbranch_vccnz 1b\n3:"
                 : "=&v"(v), "=&s"(cnt) : "v"(addr), "s"(u) : "memory", "scc", "vcc");
}

__global__ void __launch_bounds__(512, 1) __attribute__((amdgpu_num_vgpr(64)))
paged_attn(const void* __restrict__ qp, const void* __restrict__ kp, const void* __restrict__ vp, const int* __restrict__ bt,
           const int* __restrict__ lens, void* __restrict__ outp, char* __restrict__ ws, unsigned kvbytes, unsigned nphys) {
  extern __shared__ __attribute__((aligned(16))) char smem[];
  LAS char* lds = (LAS char*)smem;
  const unsigned tw0 = now32();
  __builtin_amdgcn_sched_barrier(0);
  const int lane0 = threadIdx.x & 63;
  const int wv = __builtin_amdgcn_readfirstlane(threadIdx.x >> 6);
  const int g = blockIdx.x;
  const int h = g >> 4;
  constexpr float CS = 0.08838834764831845f * 1.4426950408889634f;
  volatile LAS unsigned* ctl = (volatile LAS unsigned*)(lds + LDS_CTL);

  const unsigned want = (g & 8) ? 15 - (g & 7) : (g & 7);
  unsigned lm1, rank;
  auto ranking = [&](int lane, unsigned& lr, u32x4& lq) __attribute__((always_inline)) {
    asm volatile("" : "+v"(lr), "+v"(lq));
    lm1 = min(lr - 1u, (unsigned)SKV - 1u);
    const unsigned key = lm1 * 16 + (lane & 15);
    rank = 0;
#pragma unroll
    for (int j = 0; j < 4; ++j) rank += (min(lq[j] - 1u, (unsigned)SKV - 1u) * 16 + ((lane >> 4) * 4 + j) > key) ? 1u : 0u;
    auto r1 = __builtin_amdgcn_permlane16_swap(rank, rank, false, false);
    rank = r1[0] + r1[1];
    auto r2 = __builtin_amdgcn_permlane32_swap(rank, rank, false, false);
    rank = r2[0] + r2[1];
  };
  auto total = [&]() __attribute__((always_inline)) {
    int t = (lm1 >> 4) + 1;
    t = dpp_add<0x121>(t); t = dpp_add<0x122>(t); t = dpp_add<0x124>(t); t = dpp_add<0x128>(t);
    return t;
  };

  if (__builtin_expect(wv < 2, 1)) {
    const int half = wv, lane = lane0;
    unsigned lr = lens[lane & 15];
    u32x4 lq = *(const u32x4*)(lens + (lane >> 4) * 4);
    unsigned first = bt[(lane & 15) * NBPS + half];
    __builtin_amdgcn_s_setprio(3);
    const unsigned rq4 = lane >> 4;
    unsigned vo0 = rq4 * 4096 + (((unsigned)(lane & 15) ^ (rq4 << 2)) << 4) + h * (HD * 2);
    unsigned vo1 = (vo0 + 16384 - 1024) ^ 16, vo2 = (vo0 + 32768 - 2048) ^ 32, vo3 = (vo0 + 49152 - 3072) ^ 48;
    unsigned vland = (unsigned)(size_t)lds + LDS_CTL + CTL_LAND + half * 4;
    unsigned vfree = (unsigned)(size_t)lds + LDS_CTL + CTL_FREE + (lane & 7) * 4;
    const unsigned ring = (unsigned)(size_t)lds + LDS_RING + half * 4096;
    const __amdgpu_buffer_rsrc_t rk = __builtin_amdgcn_make_buffer_rsrc((void*)kp, 0, (int)kvbytes, 0x00020000);
    asm volatile("" : "+v"(vo0), "+v"(vo1), "+v"(vo2), "+v"(vo3), "+v"(vland), "+v"(vfree) : "s"(rk), "s"(vp), "s"(nphys), "s"(ring));
    ranking(lane, lr, lq);
    first = min(first, nphys - 1) * PAGE_BYTES;
    const int b = __builtin_ctz((unsigned)__builtin_amdgcn_ballot_w64(rank == want) | 0x8000u);
    unsigned long long tstart;
    asm volatile("s_waitcnt vmcnt(0)\n\t"
                 "s_mov_b32 m0, %2\n\t"
                 "s_memrealtime %0\n\t"
                 "s_nop 2\n\t"
                 "buffer_load_dwordx4 %4, %1, %3 offen nt lds\n\t"
                 "buffer_load_dwordx4 %5, %1, %3 offen offset:1024 nt lds\n\t"
                 "buffer_load_dwordx4 %6, %1, %3 offen offset:2048 nt lds\n\t"
                 "buffer_load_dwordx4 %7, %1, %3 offen offset:3072 nt lds\n\t"
                 "s_waitcnt lgkmcnt(0)\n\t"
                 "s_barrier"
                 : "=&s"(tstart) : "s"(rk), "s"(ring), "s"(__builtin_amdgcn_readlane(first, b)), "v"(vo0), "v"(vo1), "v"(vo2), "v"(vo3) : "memory");
    __builtin_amdgcn_sched_barrier(0);
    asm volatile("" : "+v"(lm1));
    const int nown = (__builtin_amdgcn_readlane(lm1, b) >> 4) + 1, NT = (nown + 1) >> 1;
    unsigned l0 = *(LAS unsigned*)(lds + LDS_TBL + (b * NBPS + min(2 * lane + half, nown - 1)) * 4);
    unsigned l1 = *(LAS unsigned*)(lds + LDS_TBL + (b * NBPS + min(2 * lane + half + 128, nown - 1)) * 4);
    const int step = to_sgpr((int)((float)(total() * PACE_X16) * __builtin_amdgcn_rcpf((float)(2 * NT))));
    l0 = min(l0, nphys - 1) * PAGE_BYTES;
    l1 = min(l1, nphys - 1) * PAGE_BYTES;
#define CNT(r)
    unsigned vf, vt, lc;
    asm volatile(
        "s_waitcnt lgkmcnt(0)\n\t"
        "s_memrealtime s[68:69]\n\t"
        "ds_read_b32 %[vf], %[vfree]\n\t"
        "s_mov_b32 s60, 1\n\t"
        "s_mov_b32 s61, 4\n\t"
        "s_mov_b32 s62, 0x400000\n\t"
        "s_mov_b32 s66, 0\n\t"
        "s_min_u32 s67, %[nt], 64\n\t"
        "s_mov_b32 s72, 0x2000\n\t"
        "s_mov_b32 s73, 1\n\t"
        "s_mov_b32 s74, %[due1]\n\t"
        "s_mov_b64 s[76:77], %[kp]\n\t"
        "s_mov_b32 s78, %[nrec]\n\t"
        "s_mov_b32 s79, 0x20000\n\t"
        "s_and_b32 s77, s77, 0xffff\n\t"
        "v_mov_b32 %[lc], %[l0]\n\t"
        "s_getpc_b64 s[70:71]\n"
        ".LldA%=:\n\t"
        "s_add_u32 s80, s70, .LldL%=-.LldA%=\n\t"
        "s_addc_u32 s71, s71, 0\n\t"
        "s_branch .LldF%=\n"
        ".LldR%=:\n\t"
        "s_sub_u32 s62, s62, 1\n\t"
        "s_cbranch_scc1 .LldE%=\n"
        ".LldT%=:\n\t"
        "s_getreg_b32 s64, hwreg(HW_REG_IB_STS)\n\t"
        "s_and_b32 s65, s64, 15\n\t"
        "s_bfe_u32 s64, s64, 0x20016\n\t"
        "s_lshl4_add_u32 s64, s64, s65\n\t"
        "s_add_u32 s64, s64, 3\n\t"
        "s_and_b32 s64, s64, 60\n\t"
        "s_sub_u32 s65, s61, s64\n\t"
        "s_cmp_lg_u32 s65, s66\n\t"
        "s_cbranch_scc1 .LldP%=\n"
        ".LldC%=:\n\t"
        "s_waitcnt lgkmcnt(0)\n\t"
        "v_cmp_eq_u32 vcc, s60, %[vf]\n\t"
        "s_lshl_b32 s64, s68, 8\n\t"
        "s_memrealtime s[68:69]\n\t"
        "ds_read_b32 %[vf], %[vfree]\n\t"
        CNT("s82")
        "s_sub_u32 s64, s64, s74\n\t"
        "s_cmp_lt_i32 s64, 0\n\t"
        "s_cbranch_scc1 .LldR%=\n\t"
        CNT("s83")
        "s_cbranch_vccz .LldR%=\n\t"
        CNT("s84")
        "buffer_load_dwordx4 %[o0], s[76:79], s63 offen nt lds\n\t"
        "s_add_u32 s74, s74, %[step]\n\t"
        "s_add_u32 s73, s73, 1\n\t"
        "buffer_load_dwordx4 %[o1], s[76:79], s63 offen offset:1024 nt lds\n\t"
        "s_add_u32 s60, s60, 1\n\t"
        "s_add_u32 s61, s61, 4\n\t"
        "buffer_load_dwordx4 %[o2], s[76:79], s63 offen offset:2048 nt lds\n\t"
        "s_add_u32 s72, s72, 0x2000\n\t"
        "s_and_b32 s72, s72, 0xe000\n\t"
        "buffer_load_dwordx4 %[o3], s[76:79], s63 offen offset:3072 nt lds\n"
        ".LldF%=:\n\t"
        "v_readlane_b32 s63, %[lc], s73\n\t"
        "s_add_u32 m0, s72, %[ring]\n\t"
        "s_cmp_lg_u32 s60, s67\n\t"
        "s_cbranch_scc1 .LldT%=\n\t"
        "s_cmp_eq_u32 s73, %[nt]\n\t"
        "s_cbranch_scc1 .LldV%=\n\t"
        "v_mov_b32 %[lc], %[l1]\n\t"
        "v_readfirstlane_b32 s63, %[l1]\n\t"
        "s_add_u32 s67, s60, %[nt]\n\t"
        "s_sub_u32 s67, s67, 64\n\t"
        "s_branch .LldT%=\n"
        ".LldV%=:\n\t"
        "s_cmp_lg_u32 s60, %[nt]\n\t"
        "s_cbranch_scc1 .LldD%=\n\t"
        "s_mov_b64 s[76:77], %[vp]\n\t"
        "s_and_b32 s77, s77, 0xffff\n\t"
        "s_mov_b32 s73, 0\n\t"
        "s_min_u32 s67, %[nt], 64\n\t"
        "s_add_u32 s67, s67, s60\n\t"
        "s_add_u32 s74, s74, %[gap]\n\t"
        "v_mov_b32 %[lc], %[l0]\n\t"
        "v_readfirstlane_b32 s63, %[l0]\n\t"
        "v_xor_b32 %[o1], 16, %[o1]\n\t"
        "v_xor_b32 %[o2], 32, %[o2]\n\t"
        "v_xor_b32 %[o3], 48, %[o3]\n\t"
        "s_branch .LldT%=\n"
        ".LldD%=:\n\t"
        "s_add_u32 s74, s74, 0x40000000\n\t"
        "s_mov_b32 s60, -1\n\t"
        "s_branch .LldT%=\n"
        ".LldP%=:\n\t"
        "s_min_u32 s64, s64, 32\n\t"
        "s_add_u32 s70, s80, s64\n\t"
        "v_mov_b32 %[vt], s65\n\t"
        "s_mov_b32 s66, s65\n\t"
        "s_setpc_b64 s[70:71]\n\t"
        ".p2align 6\n"
        ".LldL%=:\n\t"
        "s_waitcnt vmcnt(0)\n\t"
        "s_waitcnt vmcnt(4)\n\t"
        "s_waitcnt vmcnt(8)\n\t"
        "s_waitcnt vmcnt(12)\n\t"
        "s_waitcnt vmcnt(16)\n\t"
        "s_waitcnt vmcnt(20)\n\t"
        "s_waitcnt vmcnt(24)\n\t"
        "s_waitcnt vmcnt(28)\n\t"
        "s_waitcnt vmcnt(32)\n\t"
        "ds_write_b32 %[vland], %[vt]\n\t"
        "s_cmp_lg_u32 s65, %[fin]\n\t"
        "s_cbranch_scc1 .LldC%=\n"
        ".LldE%=:\n\t"
        "s_waitcnt vmcnt(0) lgkmcnt(0)\n\t"
        : [vf] "=&v"(vf), [vt] "=&v"(vt), [lc] "=&v"(lc), [o1] "+v"(vo1), [o2] "+v"(vo2), [o3] "+v"(vo3)
        : [l0] "v"(l0), [l1] "v"(l1), [o0] "v"(vo0), [vland] "v"(vland), [vfree] "v"(vfree),
          [kp] "s"(kp), [vp] "s"(vp), [nrec] "s"(kvbytes), [nt] "s"(NT), [ring] "s"(ring),
          [step] "s"(step), [due1] "s"(((unsigned)tstart << 8) + SOFT * 256 + (((unsigned)tstart - tw0 > COLD_AT) ? COLD_PAUSE * 256 : 0) + step + half * ((step * HOFF) >> 3)), [fin] "s"(8 * NT), [gap] "n"(GAP_TICKS * 256)
        : "memory", "scc", "vcc", "s60", "s61", "s62", "s63", "s64", "s65", "s66", "s67", "s68", "s69", "s70", "s71", "s72", "s73", "s74",
          "s76", "s77", "s78", "s79", "s80", "s82", "s83", "s84");
    STAMP(3);
    asm volatile("s_endpgm" ::: "memory");
  }

  int lane = lane0;
  asm volatile("" : "+v"(lane));
  auto touch = [&](const void* base, unsigned bytes) __attribute__((always_inline)) {
    const __amdgpu_buffer_rsrc_t rt = __builtin_amdgcn_make_buffer_rsrc((void*)base, 0, (int)bytes, 0x00020000);
    const unsigned idx = TSHORT == 2 ? (g >> 5) + 8 * lane : TSHORT ? (g >> 4) + 16 * lane : (g >> 3) + 32 * lane;
    const unsigned vo = (idx < 0x80000000u / TLBG) ? idx * TLBG + ((idx * 30592u) & (TLBG - 128)) : INVALID;
    asm volatile("s_mov_b32 m0, %0\n\ts_nop 4\n\tbuffer_load_dword %2, %1, 0 offen nt lds" :: "s"((unsigned)(size_t)lds + LDS_DUMMY), "s"(rt), "v"(vo) : "memory");
  };
  const bool toucher = TSHORT == 2 ? (g & 24) == 8 : TSHORT ? (g & 8) != 0 : true;
  unsigned lraw = lens[lane & 15];
  u32x4 lq = *(const u32x4*)(lens + (lane >> 4) * 4);
  if (wv < 6) {
    const __amdgpu_buffer_rsrc_t rb = __builtin_amdgcn_make_buffer_rsrc((void*)bt, 0, NB * NBPS * 4, 0x00020000);
    asm volatile("s_mov_b32 m0, %0\n\ts_nop 4\n\tbuffer_load_dwordx4 %2, %1, 0 offen lds\n\t"
                 "buffer_load_dwordx4 %2, %1, 0 offen offset:1024 lds\n\t"
                 "buffer_load_dwordx4 %2, %1, 0 offen offset:2048 lds\n\t"
                 "buffer_load_dwordx4 %2, %1, 0 offen offset:3072 lds\n\t"
                 "s_waitcnt vmcnt(0)"
                 :: "s"((unsigned)(size_t)lds + LDS_TBL + (wv - 2) * 4096), "s"(rb), "v"(lane * 16 + (wv - 2) * 4096) : "memory");
  } else if (wv == 6) {
    if (TDELAY) __builtin_amdgcn_s_sleep(TDELAY);
    if (toucher) {
      if (TLBW & 1) touch(kp, kvbytes);
      if (TLBW & 2) touch(vp, kvbytes);
      if (TLBW & 8) touch(outp, NB * HQ * SQ * HD * 2);
    }
    if (lane < 40) ctl[lane] = ((lane >> 3) == CTL_FREE / 32) ? (lane & 7) : 0u;
  }
  if (wv != 7) asm volatile("s_waitcnt lgkmcnt(0)\n\ts_barrier" ::: "memory");
  ranking(lane, lraw, lq);
  const int b = __builtin_ctz((unsigned)__builtin_amdgcn_ballot_w64(rank == want) | 0x8000u);
  if (wv == 7) {
    const __amdgpu_buffer_rsrc_t rq = __builtin_amdgcn_make_buffer_rsrc((void*)qp, 0, NB * HQ * SQ * HD * 2, 0x00020000);
    const unsigned q0 = (((int)__builtin_amdgcn_readlane(lraw, b) > 0) ? (unsigned)((b * HQ + 2 * h) * SQ * HD * 2) : INVALID) + (lane >> 4) * 256 +
                        (((unsigned)(lane & 15) ^ ((lane >> 4) << 2)) << 4);
    asm volatile("s_mov_b32 m0, %0\n\ts_nop 4\n\t"
                 "buffer_load_dwordx4 %2, %1, 0 offen " QPOL " lds\n\t"
                 "buffer_load_dwordx4 %3, %1, 0 offen offset:1024 " QPOL " lds\n\t"
                 "buffer_load_dwordx4 %4, %1, 0 offen offset:2048 " QPOL " lds\n\t"
                 "buffer_load_dwordx4 %5, %1, 0 offen offset:3072 " QPOL " lds\n\t"
                 "s_add_u32 m0, m0, 4096\n\ts_nop 0\n\t"
                 "buffer_load_dwordx4 %2, %1, %6 offen " QPOL " lds\n\t"
                 "buffer_load_dwordx4 %3, %1, %6 offen offset:1024 " QPOL " lds\n\t"
                 "buffer_load_dwordx4 %4, %1, %6 offen offset:2048 " QPOL " lds\n\t"
                 "buffer_load_dwordx4 %5, %1, %6 offen offset:3072 " QPOL " lds\n\t"
                 "s_barrier\n\t"
                 "s_waitcnt vmcnt(0)"
                 :: "s"((unsigned)(size_t)lds + LDS_Q), "s"(rq), "v"(q0), "v"(q0 ^ 16), "v"(q0 ^ 32), "v"(q0 ^ 48), "s"(4096) : "memory", "scc");
    ctl[CTL_QOK / 4] = 1;
  }
  const int bp = __builtin_ctz((unsigned)__builtin_amdgcn_ballot_w64(rank == 15 - want) | 0x8000u);
  const int lim = __builtin_amdgcn_readlane(lm1, b) + 1;
  const int nown = (lim + PAGE - 1) / PAGE, NT = (nown + 1) >> 1;
  const int tot = total();
  const int w = wv - 2;
  const int r32 = lane & 31, hi = lane >> 5;
  const int nmine = (NT > w) ? (NT - w + NC - 1) / NC : 0;
  const unsigned lbase = (unsigned)(size_t)lds;
  const unsigned aland = lbase + LDS_CTL + CTL_LAND + (lane & 1) * 4;
  const int npl = (__builtin_amdgcn_readlane(lm1, bp) >> 4) + 1, NTL = (npl + 1) >> 1;
  const bool pf_on = PF_EVERY && npl * 16 * 64 > __builtin_amdgcn_readfirstlane(tot) * PF_X64 && npl > nown && (PF_ALL || (h & 3) == 1);
  const unsigned pf_lead = to_sgpr((int)((float)(2 * NTL * PF_LEAD) * 256.0f * __builtin_amdgcn_rcpf((float)(tot * PACE_X16))));
  const unsigned pf_ratio = to_sgpr((int)((float)NTL * 65536.0f * __builtin_amdgcn_rcpf((float)NT)));
  auto prefetch = [&](unsigned op) {
    const unsigned i = op / (PF_EVERY ? PF_EVERY : 1);
    unsigned tl = (((i | (PF_RUN - 1)) * PF_EVERY * pf_ratio) >> 16) + pf_lead - (~i & (PF_RUN - 1));
    if (tl >= 2u * NTL) return;
    const bool isv = tl >= (unsigned)NTL;
    tl -= isv ? NTL : 0;
    const unsigned p0 = min((unsigned)bt[bp * NBPS + 2 * tl], nphys - 1), p1 = min((unsigned)bt[bp * NBPS + min(2 * tl + 1, (unsigned)npl - 1)], nphys - 1);
    const __amdgpu_buffer_rsrc_t rp = __builtin_amdgcn_make_buffer_rsrc(isv ? (void*)vp : (void*)kp, 0, (int)kvbytes, 0x00020000);
    unsigned ln = lane;
    asm volatile("" : "+v"(ln));
    const unsigned vo = ((ln & 32) ? p1 : p0) * PAGE_BYTES + h * (HD * 2) + ((ln >> 1) & 15) * 4096 + (PF_SLOW == 1 ? 128 : PF_SLOW == 2 ? 0 : (ln & 1) * 128);
    asm volatile("s_mov_b32 m0, %0\n\ts_nop 4\n\tbuffer_load_dword %2, %1, 0 offen " PF_POL " lds" PF_2ND :: "s"(lbase + LDS_DUMMY), "s"(rp), "v"(vo) : "memory");
  };
  const int pos = (w + NC * 22 - NT) % NC;
  const unsigned palp = LDS_WREG + pos * W_SIZE + W_ALP + r32 * 4;
  const unsigned ppst = LDS_WREG + pos * W_SIZE + lane * 16;
  asm volatile("" ::: "v128", "v255");
  unsigned resc = 0;
  float m = -3.0e38f, mprev = -3.0e38f, l = 0.f;

  {
    const unsigned fk = (((unsigned)r32 & 3) << 2) | (((unsigned)r32 >> 2) & 3);
    const unsigned kaddr0 = LDS_RING + r32 * 256 + (((unsigned)hi ^ fk) << 4);
    bf16x8 qf[8];
    wait_above<1>(lbase + LDS_CTL + CTL_QOK, 0);
#pragma unroll
    for (int s = 0; s < 8; ++s) qf[s] = *(LAS bf16x8*)(lds + ((kaddr0 + LDS_Q - LDS_RING) ^ (s << 5)));
#pragma unroll 1
    for (int k = 0; k < nmine; ++k) {
      const unsigned t = w + NC * k;
      if (PRESLEEP && k > 0) __builtin_amdgcn_s_sleep(PRESLEEP);
      wait_above<CSLEEP>(aland, 4 * t);
      if (k == 0) STAMP(1);
      bf16x8 kf[8];
#pragma unroll
      for (int s = 0; s < 8; ++s) kf[s] = *(LAS bf16x8*)(lds + ((kaddr0 + ((t & 7) << 13)) ^ (s << 5)));
      asm volatile("s_waitcnt lgkmcnt(0)" : "+v"(kf[0]), "+v"(kf[1]), "+v"(kf[2]), "+v"(kf[3]), "+v"(kf[4]), "+v"(kf[5]), "+v"(kf[6]), "+v"(kf[7]) :: "memory");
      ctl[CTL_FREE / 4 + (t & 7)] = t + 8;
      if (pf_on && t % (PF_EVERY ? PF_EVERY : 1) == 0) prefetch(t);
      if ((TLBW & 4) && t == 16 && toucher) touch(vp, kvbytes);
      f32x16 sc;
#pragma unroll
      for (int r = 0; r < 16; ++r) sc[r] = 0.f;
#pragma unroll
      for (int s = 0; s < 8; ++s) sc = __builtin_amdgcn_mfma_f32_32x32x16_bf16(kf[s], qf[s], sc, 0, 0, 0);
      if ((int)t == NT - 1) {
        const int kb = 2 * t * PAGE + 4 * hi;
#pragma unroll
        for (int r = 0; r < 16; ++r) sc[r] = (kb + 8 * (r >> 2) + (r & 3) < lim) ? sc[r] : -__builtin_inff();
      }
      float mx = sc[0];
#pragma unroll
      for (int r = 1; r < 16; ++r) mx = fmaxf(mx, sc[r]);
      mx = swap_max(mx);
      const float mn = fmaxf(m, mx);
      const float alpha = __builtin_amdgcn_exp2f((m - mn) * CS);
      mprev = m;
      m = mn;
      const float nmc = -mn * CS;
      float ps = 0.f;
#pragma unroll
      for (int r = 0; r < 16; ++r) { sc[r] = __builtin_amdgcn_exp2f(fmaf(sc[r], CS, nmc)); ps += sc[r]; }
      l = fmaf(l, alpha, ps);
      u32x4 pf[2];
#pragma unroll
      for (int s = 0; s < 2; ++s)
        pf[s] = u32x4{pack2(sc[8 * s], sc[8 * s + 1]), pack2(sc[8 * s + 2], sc[8 * s + 3]), pack2(sc[8 * s + 4], sc[8 * s + 5]), pack2(sc[8 * s + 6], sc[8 * s + 7])};
      if (__builtin_amdgcn_ballot_w64(alpha != 1.0f)) resc |= 1u << k;
      *(LAS float*)(lds + palp + k * 128) = alpha;
      if (k >= KREG || k == nmine - 1) {
        const unsigned dst = ppst + ((k == nmine - 1) ? W_LAST : (k - KREG) * 2048);
        *(LAS u32x4*)(lds + dst) = pf[0];
        *(LAS u32x4*)(lds + dst + 1024) = pf[1];
      } else {
        asm volatile("s_set_gpr_idx_on %8, gpr_idx(DST)\n\t"
                     "v_mov_b32 v128, %0\n\tv_mov_b32 v129, %1\n\tv_mov_b32 v130, %2\n\tv_mov_b32 v131, %3\n\t"
                     "v_mov_b32 v132, %4\n\tv_mov_b32 v133, %5\n\tv_mov_b32 v134, %6\n\tv_mov_b32 v135, %7\n\t"
                     "s_set_gpr_idx_off"
                     :: "v"(pf[0][0]), "v"(pf[0][1]), "v"(pf[0][2]), "v"(pf[0][3]), "v"(pf[1][0]), "v"(pf[1][1]), "v"(pf[1][2]), "v"(pf[1][3]), "s"(8 * k));
      }
    }
  }
  l = swap_sum(l);
  *(LAS f32x2*)(lds + LDS_ML + w * 256 + r32 * 8) = f32x2{m, l};
  LGKM0();
  ctl[CTL_MLOK / 4 + w] = 1;
  STAMP(2);

  const unsigned tq = (lane & 15) >> 2, tp = lane & 3, tblk = (lane >> 4) & 1;
  const unsigned vaddr0 = LDS_RING + (4 * hi + tq) * 256 + ((tq ^ (pos & 3)) << 6) + ((2 * tblk + (tp >> 1)) << 4) + ((tp & 1) << 3);
  f32x16 o[4];
#pragma unroll
  for (int i = 0; i < 4; ++i)
#pragma unroll
    for (int r = 0; r < 16; ++r) o[i][r] = 0.f;
  auto weights = [&]() {
    wait_above<1>(lbase + LDS_CTL + CTL_MLOK + min(lane, NC - 1) * 4, 0);
    unsigned a = LDS_ML + r32 * 8;
    asm volatile("" : "+v"(a) :: "memory");
    float M = -3.0e38f, L = 0.f;
    f32x2 ml[NC];
#pragma unroll
    for (int i = 0; i < NC; ++i) { ml[i] = *(LAS f32x2*)(lds + a + i * 256); M = fmaxf(M, ml[i][0]); }
#pragma unroll
    for (int i = 0; i < NC; ++i) L = fmaf(__builtin_amdgcn_exp2f((ml[i][0] - M) * CS), ml[i][1], L);
    const float inv = 1.0f / L;
    *(LAS float*)(lds + (a >> 1) + (LDS_WT - LDS_ML / 2) + pos * 128) = __builtin_amdgcn_exp2f((mprev - M) * CS) * inv;
    const float ct = __builtin_amdgcn_exp2f((m - M) * CS) * inv;
#pragma unroll
    for (int s = 0; s < 2; ++s) {
      u32x4 x = *(LAS u32x4*)(lds + ppst + W_LAST + s * 1024);
#pragma unroll
      for (int e = 0; e < 4; ++e) x[e] = pack2(__uint_as_float(x[e] << 16) * ct, __uint_as_float(x[e] & 0xffff0000u) * ct);
      *(LAS u32x4*)(lds + ppst + W_LAST + s * 1024) = x;
    }
    LGKM0();
    ctl[CTL_WTOK / 4 + w] = 1;
  };
  resc &= ~1u;
  if (nmine <= 2) weights();
#pragma unroll 1
  for (int k = 0; k < nmine - 1; ++k) {
    if (k == 1) weights();
    if ((resc >> k) & 1) {
      const float alpha = *(LAS float*)(lds + palp + k * 128);
#pragma unroll
      for (int i = 0; i < 4; ++i)
#pragma unroll
        for (int r = 0; r < 16; ++r) o[i][r] *= alpha;
    }
    const unsigned u = NT + w + NC * k;
    u32x4 pf[2];
    if (k >= KREG) {
      pf[0] = *(LAS u32x4*)(lds + ppst + (k - KREG) * 2048);
      pf[1] = *(LAS u32x4*)(lds + ppst + (k - KREG) * 2048 + 1024);
    } else {
      asm volatile("s_set_gpr_idx_on %8, gpr_idx(SRC0)\n\t"
                   "v_mov_b32 %0, v128\n\tv_mov_b32 %1, v129\n\tv_mov_b32 %2, v130\n\tv_mov_b32 %3, v131\n\t"
                   "v_mov_b32 %4, v132\n\tv_mov_b32 %5, v133\n\tv_mov_b32 %6, v134\n\tv_mov_b32 %7, v135\n\t"
                   "s_set_gpr_idx_off\n\ts_nop 1"
                   : "=v"(pf[0][0]), "=v"(pf[0][1]), "=v"(pf[0][2]), "=v"(pf[0][3]), "=v"(pf[1][0]), "=v"(pf[1][1]), "=v"(pf[1][2]), "=v"(pf[1][3])
                   : "s"(8 * k));
    }
    asm volatile("" : "+v"(pf[0]), "+v"(pf[1]), "+v"(o[0]), "+v"(o[1]), "+v"(o[2]), "+v"(o[3]));
    if (PRESLEEP && k > 1) __builtin_amdgcn_s_sleep(2 * PRESLEEP);
    wait_above<CSLEEP>(aland, 4 * u);
    if (TPRIO && k == nmine - 2) __builtin_amdgcn_s_setprio(TPRIO);
    bf16x8 vf[4][2];
#pragma unroll
    for (int i = 0; i < 4; ++i) {
      const unsigned a = (vaddr0 + ((u & 7) << 13)) ^ (i << 6);
#pragma unroll
      for (int s = 0; s < 2; ++s) {
        const s16x4 lo = __builtin_amdgcn_ds_read_tr16_b64_v4i16((LAS s16x4*)(lds + a + 4096 * s));
        const s16x4 up = __builtin_amdgcn_ds_read_tr16_b64_v4i16((LAS s16x4*)(lds + a + 4096 * s + 2048));
        vf[i][s] = __builtin_bit_cast(bf16x8, __builtin_shufflevector(lo, up, 0, 1, 2, 3, 4, 5, 6, 7));
      }
    }
    asm volatile("s_waitcnt lgkmcnt(0)" : "+v"(vf[0][0]), "+v"(vf[0][1]), "+v"(vf[1][0]), "+v"(vf[1][1]), "+v"(vf[2][0]), "+v"(vf[2][1]), "+v"(vf[3][0]), "+v"(vf[3][1]) :: "memory");
    ctl[CTL_FREE / 4 + (u & 7)] = u + 8;
    if (pf_on && u % (PF_EVERY ? PF_EVERY : 1) == 0) prefetch(u);
#pragma unroll
    for (int s = 0; s < 2; ++s)
#pragma unroll
      for (int i = 0; i < 4; ++i) o[i] = __builtin_amdgcn_mfma_f32_32x32x16_bf16(vf[i][s], __builtin_bit_cast(bf16x8, pf[s]), o[i], 0, 0, 0);
  }
  STAMP(3);

  {
    unsigned l16 = lane * 16;
    asm volatile("" : "+v"(l16));
    if (nmine > 1) {
      auto put = [&](unsigned dst, const f32x16& x) {
#pragma unroll
        for (int j = 0; j < 4; ++j) *(LAS f32x4*)(lds + dst + j * 1024) = f32x4{x[4 * j], x[4 * j + 1], x[4 * j + 2], x[4 * j + 3]};
      };
      if (pos > 3) put(LDS_RING + ((2 * NT - 2 * NC + pos) & 7) * 8192 + l16, o[0]);
#pragma unroll
      for (int i = 1; i < 4; ++i) put(LDS_WREG + pos * W_SIZE + (i - 1) * 4096 + l16, o[i]);
      if (TWAIT) LGKM0(); else CBAR();
      ctl[CTL_ODONE / 4 + pos] = 1;
    }
    if (pos < 4) {
      f32x16 of = o[0];
      const unsigned l4 = l16 >> 2, awt = LDS_WT + (l4 & 127);
      wait_above<1>(lbase + LDS_CTL + CTL_WTOK + min(l4, (NC - 1) * 4u), 0);
      auto src_o = [&](int p) -> unsigned {
        const int i = pos ^ (p & 3);
        return l16 + (i == 0 ? LDS_RING + ((2 * NT - 2 * NC + p) & 7) * 8192 : LDS_WREG + p * W_SIZE + (i - 1) * 4096);
      };
      auto fma_o = [&](unsigned src, float c) {
#pragma unroll
        for (int j = 0; j < 4; ++j) {
          const f32x4 x = *(LAS f32x4*)(lds + src + j * 1024);
#pragma unroll
          for (int e = 0; e < 4; ++e) of[4 * j + e] = fmaf(x[e], c, of[4 * j + e]);
        }
      };
      auto mma_t = [&](int p, bf16x8 p0, bf16x8 p1) {
        const unsigned a = vaddr0 + (((2 * NT - NC + p) & 7) << 13);
        bf16x8 vf[2];
#pragma unroll
        for (int s = 0; s < 2; ++s) {
          const s16x4 lo = __builtin_amdgcn_ds_read_tr16_b64_v4i16((LAS s16x4*)(lds + a + 4096 * s));
          const s16x4 up = __builtin_amdgcn_ds_read_tr16_b64_v4i16((LAS s16x4*)(lds + a + 4096 * s + 2048));
          vf[s] = __builtin_bit_cast(bf16x8, __builtin_shufflevector(lo, up, 0, 1, 2, 3, 4, 5, 6, 7));
        }
        of = __builtin_amdgcn_mfma_f32_32x32x16_bf16(vf[0], p0, of, 0, 0, 0);
        of = __builtin_amdgcn_mfma_f32_32x32x16_bf16(vf[1], p1, of, 0, 0, 0);
      };
      {
        const float cw = *(LAS float*)(lds + awt + pos * 128);
#pragma unroll
        for (int r = 0; r < 16; ++r) of[r] *= cw;
      }
      if (NT >= 2 * NC) {
        float co[5];
        bf16x8 pl[NC][2];
        auto ld_p = [&](int p) {
          pl[p][0] = *(LAS bf16x8*)(lds + LDS_WREG + p * W_SIZE + W_LAST + l16);
          pl[p][1] = *(LAS bf16x8*)(lds + LDS_WREG + p * W_SIZE + W_LAST + l16 + 1024);
        };
#define PIN_OF() asm volatile("" : "+v"(of))
#pragma unroll
        for (int i = 1; i < 4; ++i) co[i - 1] = *(LAS float*)(lds + awt + (pos ^ i) * 128);
        co[3] = *(LAS float*)(lds + awt + 4 * 128);
        co[4] = *(LAS float*)(lds + awt + 5 * 128);
        ld_p(0); ld_p(1); ld_p(2);
        PIN_OF();
        wait_above<TSLEEP>(lbase + LDS_CTL + CTL_ODONE + (l4 & 12), 0);
#pragma unroll
        for (int i = 1; i < 4; ++i) fma_o(LDS_WREG + (pos ^ i) * W_SIZE + (i - 1) * 4096 + l16, co[i - 1]);
        PIN_OF();
        ld_p(3); ld_p(4); ld_p(5);
        wait_above<TSLEEP>(aland, 4 * (2 * NT - NC + 2));
        mma_t(0, pl[0][0], pl[0][1]);
        mma_t(1, pl[1][0], pl[1][1]);
        mma_t(2, pl[2][0], pl[2][1]);
        STAMP(4);
        wait_above<TSLEEP>(lbase + LDS_CTL + CTL_ODONE + 16 + (l4 & 4), 0);
        fma_o(src_o(4), co[3]);
        fma_o(src_o(5), co[4]);
        PIN_OF();
        wait_above<0>(aland, 4 * (2 * NT - NC + 4));
        mma_t(3, pl[3][0], pl[3][1]);
        mma_t(4, pl[4][0], pl[4][1]);
        PIN_OF();
        wait_above<0>(aland, 4 * (2 * NT - NC + 5));
        mma_t(5, pl[5][0], pl[5][1]);
      } else {
#pragma unroll 1
        for (int p = max(NC - NT, 0); p < NC; ++p) {
          if (p != pos && NT - 2 * NC + p >= 0) {
            wait_above<1>(lbase + LDS_CTL + CTL_ODONE + p * 4, 0);
            fma_o(src_o(p), *(LAS float*)(lds + awt + p * 128));
          }
          wait_above<1>(aland, 4 * (2 * NT - NC + p));
          mma_t(p, *(LAS bf16x8*)(lds + LDS_WREG + p * W_SIZE + W_LAST + l16), *(LAS bf16x8*)(lds + LDS_WREG + p * W_SIZE + W_LAST + l16 + 1024));
        }
      }
      const __amdgpu_buffer_rsrc_t ro = __builtin_amdgcn_make_buffer_rsrc(outp, 0, NB * HQ * SQ * HD * 2, 0x00020000);
      const unsigned orow = (unsigned)(b * HQ + 2 * h) * SQ * HD * 2 + pos * 64 + ((l16 & 511) << 4) + ((l16 >> 5) & 16);
#pragma unroll
      for (int j = 0; j < 2; ++j) {
        const unsigned a0 = pack2(of[8 * j], of[8 * j + 1]), a1 = pack2(of[8 * j + 2], of[8 * j + 3]);
        const unsigned b0 = pack2(of[8 * j + 4], of[8 * j + 5]), b1 = pack2(of[8 * j + 6], of[8 * j + 7]);
        auto s0 = __builtin_amdgcn_permlane32_swap(a0, b0, false, false);
        auto s1 = __builtin_amdgcn_permlane32_swap(a1, b1, false, false);
        u32x4 val = {s0[0], s1[0], s0[1], s1[1]};
        __builtin_amdgcn_raw_buffer_store_b128(val, ro, orow + j * 32, 0, OUTAUX);
      }
    }
  }
}
}

extern "C" void kernel_launch(void* const* d_in, const int* in_sizes, int n_in, void* d_out, int out_size, void* d_ws,
                              size_t ws_size, hipStream_t stream) {
  (void)n_in; (void)out_size; (void)ws_size;
  const unsigned nphys = (unsigned)(in_sizes[1] / (PAGE * HKV * HD));
  const unsigned kvbytes = nphys * PAGE_BYTES;
  (void)hipFuncSetAttribute((const void*)paged_attn, hipFuncAttributeMaxDynamicSharedMemorySize, LDS_TOTAL);
  hipLaunchKernelGGL(paged_attn, dim3(256), dim3(512), LDS_TOTAL, stream, d_in[0], d_in[1], d_in[2], (const int*)d_in[3],
                     (const int*)d_in[4], d_out, (char*)d_ws, kvbytes, nphys);
}
```

```cpp
#define SOFT 80
#define COLD_PAUSE 0
#define TLBW 3
#define TSHORT 0
#include <hip/hip_runtime.h>
#include <hip/hip_bf16.h>

#define PROBE_G 0
#define QPOL "nt"
#define PF_2ND
#define TLBG 0x200000u
#define COLD_AT 115
#define TDELAY 0
#define PACE_X16 442
#define GAP_TICKS 0
#define OUTAUX 0
#define HOFF 4
#define LSLEEP 0
#define PRESLEEP 0
#define PF_EVERY 2
#define PF_X64 76
#define PF_ALL 0
#define PF_LEAD 380
#define PF_POL ""
#define PF_SLOW 0
#define PF_RUN 4
#define TSLEEP 0
#define TWAIT 0
#define TPRIO 0
#define CSLEEP 1

#define STAMP(i) do {} while (0)

namespace {
constexpr int NB = 16, HQ = 32, HKV = 16, SQ = 16, HD = 128, PAGE = 16, NBPS = 256, SKV = NBPS * PAGE;
constexpr unsigned PAGE_BYTES = PAGE * HKV * HD * 2;
constexpr unsigned INVALID = 0x80000000u;
constexpr int NC = 6;
constexpr int KREG = 16, KLDS = 5;
static_assert(NC * (KREG + KLDS + 1) >= NBPS / 2, "P storage");
constexpr unsigned LDS_RING = 0;
constexpr unsigned LDS_WREG = 65536;
constexpr unsigned W_ALP = KLDS * 2048;
constexpr unsigned W_LAST = W_ALP + (KREG + KLDS + 1) * 128;
constexpr unsigned W_SIZE = W_LAST + 2048;
static_assert(W_LAST >= 3 * 4096 && W_SIZE % 256 == 0, "consumer region");
constexpr unsigned LDS_TBL = LDS_WREG + 2 * W_SIZE;
constexpr unsigned LDS_Q = LDS_WREG;
constexpr unsigned LDS_ML = LDS_WREG + NC * W_SIZE;
constexpr unsigned LDS_WT = LDS_ML + NC * 256;
constexpr unsigned LDS_CTL = LDS_WT + NC * 128;
constexpr unsigned CTL_LAND = 0;
constexpr unsigned CTL_QOK = 16;
constexpr unsigned CTL_FREE = 32;
constexpr unsigned CTL_MLOK = 64;
constexpr unsigned CTL_ODONE = 96;
constexpr unsigned CTL_WTOK = 128;
constexpr unsigned LDS_DUMMY = LDS_CTL + 256;
constexpr unsigned LDS_TOTAL = LDS_DUMMY + 256;
static_assert(LDS_TOTAL <= 163840, "lds");

typedef __attribute__((ext_vector_type(8))) __bf16 bf16x8;
typedef __attribute__((ext_vector_type(2))) __bf16 bf16x2;
typedef __attribute__((ext_vector_type(4))) short s16x4;
typedef __attribute__((ext_vector_type(16))) float f32x16;
typedef __attribute__((ext_vector_type(4))) float f32x4;
typedef __attribute__((ext_vector_type(2))) float f32x2;
typedef __attribute__((ext_vector_type(4))) unsigned u32x4;
typedef __attribute__((ext_vector_type(2))) unsigned u32x2;
typedef __attribute__((ext_vector_type(4))) int i32x4;
#define LAS __attribute__((address_space(3)))

__device__ __forceinline__ float swap_max(float x) {
  auto r = __builtin_amdgcn_permlane32_swap(__float_as_uint(x), __float_as_uint(x), false, false);
  return fmaxf(__uint_as_float(r[0]), __uint_as_float(r[1]));
}
__device__ __forceinline__ float swap_sum(float x) {
  auto r = __builtin_amdgcn_permlane32_swap(__float_as_uint(x), __float_as_uint(x), false, false);
  return __uint_as_float(r[0]) + __uint_as_float(r[1]);
}
__device__ __forceinline__ unsigned pack2(float a, float b) {
  bf16x2 v = {(__bf16)a, (__bf16)b};
  return __builtin_bit_cast(unsigned, v);
}
template <int CTRL> __device__ __forceinline__ int dpp_add(int x) { return x + __builtin_amdgcn_update_dpp(0, x, CTRL, 0xf, 0xf, true); }
__device__ __forceinline__ unsigned now32() { return (unsigned)__builtin_amdgcn_s_memrealtime(); }
__device__ __forceinline__ int to_sgpr(int v) {
  int r;
  asm volatile("s_nop 1\n\tv_readfirstlane_b32 %0, %1" : "=s"(r) : "v"(v));
  return r;
}
#define LGKM0() asm volatile("s_waitcnt lgkmcnt(0)" ::: "memory")
#define VMWAIT(n) asm volatile("s_waitcnt vmcnt(" #n ")" ::: "memory")
#define CBAR() asm volatile("" ::: "memory")
template <int SLEEP> __device__ __forceinline__ void wait_above(unsigned addr, unsigned u) {
  unsigned v, cnt;
  if (SLEEP)
    asm volatile("s_mov_b32 %1, 0x200000\n\ts_branch 2f\n"
                 "1:\n\ts_sleep %4\n"
                 "2:\n\tds_read_b32 %0, %2\n\ts_sub_u32 %1, %1, 1\n\ts_waitcnt lgkmcnt(0)\n\t"
                 "v_cmp_ge_u32 vcc, %3, %0\n\ts_cbranch_scc1 3f\n\ts_nop 1\n\ts_cbranch_vccnz 1b\n3:"
                 : "=&v"(v), "=&s"(cnt) : "v"(addr), "s"(u), "n"(SLEEP) : "memory", "scc", "vcc");
  else
    asm volatile("s_mov_b32 %1, 0x200000\n"
                 "1:\n\tds_read_b32 %0, %2\n\ts_sub_u32 %1, %1, 1\n\ts_waitcnt lgkmcnt(0)\n\t"
                 "v_cmp_ge_u32 vcc, %3, %0\n\ts_cbranch_scc1 3f\n\ts_nop 1\n\ts_cbranch_vccnz 1b\n3:"
                 : "=&v"(v), "=&s"(cnt) : "v"(addr), "s"(u) : "memory", "scc", "vcc");
}

__global__ void __launch_bounds__(512, 1) __attribute__((amdgpu_num_vgpr(64)))
paged_attn(const void* __restrict__ qp, const void* __restrict__ kp, const void* __restrict__ vp, const int* __restrict__ bt,
           const int* __restrict__ lens, void* __restrict__ outp, char* __restrict__ ws, unsigned kvbytes, unsigned nphys) {
  extern __shared__ __attribute__((aligned(16))) char smem[];
  LAS char* lds = (LAS char*)smem;
  const unsigned tw0 = now32();
  __builtin_amdgcn_sched_barrier(0);
  const int lane0 = threadIdx.x & 63;
  const int wv = __builtin_amdgcn_readfirstlane(threadIdx.x >> 6);
  const int g = blockIdx.x;
  const int h = g >> 4;
  constexpr float CS = 0.08838834764831845f * 1.4426950408889634f;
  volatile LAS unsigned* ctl = (volatile LAS unsigned*)(lds + LDS_CTL);

  const unsigned want = (g & 8) ? 15 - (g & 7) : (g & 7);
  unsigned lm1, rank;
  auto ranking = [&](int lane, unsigned& lr, u32x4& lq) __attribute__((always_inline)) {
    asm volatile("" : "+v"(lr), "+v"(lq));
    lm1 = min(lr - 1u, (unsigned)SKV - 1u);
    const unsigned key = lm1 * 16 + (lane & 15);
    rank = 0;
#pragma unroll
    for (int j = 0; j < 4; ++j) rank += (min(lq[j] - 1u, (unsigned)SKV - 1u) * 16 + ((lane >> 4) * 4 + j) > key) ? 1u : 0u;
    auto r1 = __builtin_amdgcn_permlane16_swap(rank, rank, false, false);
    rank = r1[0] + r1[1];
    auto r2 = __builtin_amdgcn_permlane32_swap(rank, rank, false, false);
    rank = r2[0] + r2[1];
  };
  auto total = [&]() __attribute__((always_inline)) {
    int t = (lm1 >> 4) + 1;
    t = dpp_add<0x121>(t); t = dpp_add<0x122>(t); t = dpp_add<0x124>(t); t = dpp_add<0x128>(t);
    return t;
  };

  if (__builtin_expect(wv < 2, 1)) {
    const int half = wv, lane = lane0;
    unsigned lr = lens[lane & 15];
    u32x4 lq = *(const u32x4*)(lens + (lane >> 4) * 4);
    unsigned first = bt[(lane & 15) * NBPS + half];
    __builtin_amdgcn_s_setprio(3);
    const unsigned rq4 = lane >> 4;
    unsigned vo0 = rq4 * 4096 + (((unsigned)(lane & 15) ^ (rq4 << 2)) << 4) + h * (HD * 2);
    unsigned vo1 = (vo0 + 16384 - 1024) ^ 16, vo2 = (vo0 + 32768 - 2048) ^ 32, vo3 = (vo0 + 49152 - 3072) ^ 48;
    unsigned vland = (unsigned)(size_t)lds + LDS_CTL + CTL_LAND + half * 4;
    unsigned vfree = (unsigned)(size_t)lds + LDS_CTL + CTL_FREE + (lane & 7) * 4;
    const unsigned ring = (unsigned)(size_t)lds + LDS_RING + half * 4096;
    const __amdgpu_buffer_rsrc_t rk = __builtin_amdgcn_make_buffer_rsrc((void*)kp, 0, (int)kvbytes, 0x00020000);
    asm volatile("" : "+v"(vo0), "+v"(vo1), "+v"(vo2), "+v"(vo3), "+v"(vland), "+v"(vfree) : "s"(rk), "s"(vp), "s"(nphys), "s"(ring));
    ranking(lane, lr, lq);
    first = min(first, nphys - 1) * PAGE_BYTES;
    const int b = __builtin_ctz((unsigned)__builtin_amdgcn_ballot_w64(rank == want) | 0x8000u);
    unsigned long long tstart;
    asm volatile("s_waitcnt vmcnt(0)\n\t"
                 "s_mov_b32 m0, %2\n\t"
                 "s_memrealtime %0\n\t"
                 "s_nop 2\n\t"
                 "buffer_load_dwordx4 %4, %1, %3 offen nt lds\n\t"
                 "buffer_load_dwordx4 %5, %1, %3 offen offset:1024 nt lds\n\t"
                 "buffer_load_dwordx4 %6, %1, %3 offen offset:2048 nt lds\n\t"
                 "buffer_load_dwordx4 %7, %1, %3 offen offset:3072 nt lds\n\t"
                 "s_waitcnt lgkmcnt(0)\n\t"
                 "s_barrier"
                 : "=&s"(tstart) : "s"(rk), "s"(ring), "s"(__builtin_amdgcn_readlane(first, b)), "v"(vo0), "v"(vo1), "v"(vo2), "v"(vo3) : "memory");
    __builtin_amdgcn_sched_barrier(0);
    asm volatile("" : "+v"(lm1));
    const int nown = (__builtin_amdgcn_readlane(lm1, b) >> 4) + 1, NT = (nown + 1) >> 1;
    unsigned l0 = *(LAS unsigned*)(lds + LDS_TBL + (b * NBPS + min(2 * lane + half, nown - 1)) * 4);
    unsigned l1 = *(LAS unsigned*)(lds + LDS_TBL + (b * NBPS + min(2 * lane + half + 128, nown - 1)) * 4);
    const int step = to_sgpr((int)((float)(total() * PACE_X16) * __builtin_amdgcn_rcpf((float)(2 * NT))));
    l0 = min(l0, nphys - 1) * PAGE_BYTES;
    l1 = min(l1, nphys - 1) * PAGE_BYTES;
#define CNT(r)
    unsigned vf, vt, lc;
    asm volatile(
        "s_waitcnt lgkmcnt(0)\n\t"
        "s_memrealtime s[68:69]\n\t"
        "ds_read_b32 %[vf], %[vfree]\n\t"
        "s_mov_b32 s60, 1\n\t"
        "s_mov_b32 s61, 4\n\t"
        "s_mov_b32 s62, 0x400000\n\t"
        "s_mov_b32 s66, 0\n\t"
        "s_min_u32 s67, %[nt], 64\n\t"
        "s_mov_b32 s72, 0x2000\n\t"
        "s_mov_b32 s73, 1\n\t"
        "s_mov_b32 s74, %[due1]\n\t"
        "s_mov_b64 s[76:77], %[kp]\n\t"
        "s_mov_b32 s78, %[nrec]\n\t"
        "s_mov_b32 s79, 0x20000\n\t"
        "s_and_b32 s77, s77, 0xffff\n\t"
        "v_mov_b32 %[lc], %[l0]\n\t"
        "s_getpc_b64 s[70:71]\n"
        ".LldA%=:\n\t"
        "s_add_u32 s80, s70, .LldL%=-.LldA%=\n\t"
        "s_addc_u32 s71, s71, 0\n\t"
        "s_branch .LldF%=\n"
        ".LldR%=:\n\t"
        "s_sub_u32 s62, s62, 1\n\t"
        "s_cbranch_scc1 .LldE%=\n"
        ".LldT%=:\n\t"
        "s_getreg_b32 s64, hwreg(HW_REG_IB_STS)\n\t"
        "s_and_b32 s65, s64, 15\n\t"
        "s_bfe_u32 s64, s64, 0x20016\n\t"
        "s_lshl4_add_u32 s64, s64, s65\n\t"
        "s_add_u32 s64, s64, 3\n\t"
        "s_and_b32 s64, s64, 60\n\t"
        "s_sub_u32 s65, s61, s64\n\t"
        "s_cmp_lg_u32 s65, s66\n\t"
        "s_cbranch_scc1 .LldP%=\n"
        ".LldC%=:\n\t"
        "s_waitcnt lgkmcnt(0)\n\t"
        "v_cmp_eq_u32 vcc, s60, %[vf]\n\t"
        "s_lshl_b32 s64, s68, 8\n\t"
        "s_memrealtime s[68:69]\n\t"
        "ds_read_b32 %[vf], %[vfree]\n\t"
        CNT("s82")
        "s_sub_u32 s64, s64, s74\n\t"
        "s_cmp_lt_i32 s64, 0\n\t"
        "s_cbranch_scc1 .LldR%=\n\t"
        CNT("s83")
        "s_cbranch_vccz .LldR%=\n\t"
        CNT("s84")
        "buffer_load_dwordx4 %[o0], s[76:79], s63 offen nt lds\n\t"
        "s_add_u32 s74, s74, %[step]\n\t"
        "s_add_u32 s73, s73, 1\n\t"
        "buffer_load_dwordx4 %[o1], s[76:79], s63 offen offset:1024 nt lds\n\t"
        "s_add_u32 s60, s60, 1\n\t"
        "s_add_u32 s61, s61, 4\n\t"
        "buffer_load_dwordx4 %[o2], s[76:79], s63 offen offset:2048 nt lds\n\t"
        "s_add_u32 s72, s72, 0x2000\n\t"
        "s_and_b32 s72, s72, 0xe000\n\t"
        "buffer_load_dwordx4 %[o3], s[76:79], s63 offen offset:3072 nt lds\n"
        ".LldF%=:\n\t"
        "v_readlane_b32 s63, %[lc], s73\n\t"
        "s_add_u32 m0, s72, %[ring]\n\t"
        "s_cmp_lg_u32 s60, s67\n\t"
        "s_cbranch_scc1 .LldT%=\n\t"
        "s_cmp_eq_u32 s73, %[nt]\n\t"
        "s_cbranch_scc1 .LldV%=\n\t"
        "v_mov_b32 %[lc], %[l1]\n\t"
        "v_readfirstlane_b32 s63, %[l1]\n\t"
        "s_add_u32 s67, s60, %[nt]\n\t"
        "s_sub_u32 s67, s67, 64\n\t"
        "s_branch .LldT%=\n"
        ".LldV%=:\n\t"
        "s_cmp_lg_u32 s60, %[nt]\n\t"
        "s_cbranch_scc1 .LldD%=\n\t"
        "s_mov_b64 s[76:77], %[vp]\n\t"
        "s_and_b32 s77, s77, 0xffff\n\t"
        "s_mov_b32 s73, 0\n\t"
        "s_min_u32 s67, %[nt], 64\n\t"
        "s_add_u32 s67, s67, s60\n\t"
        "s_add_u32 s74, s74, %[gap]\n\t"
        "v_mov_b32 %[lc], %[l0]\n\t"
        "v_readfirstlane_b32 s63, %[l0]\n\t"
        "v_xor_b32 %[o1], 16, %[o1]\n\t"
        "v_xor_b32 %[o2], 32, %[o2]\n\t"
        "v_xor_b32 %[o3], 48, %[o3]\n\t"
        "s_branch .LldT%=\n"
        ".LldD%=:\n\t"
        "s_add_u32 s74, s74, 0x40000000\n\t"
        "s_mov_b32 s60, -1\n\t"
        "s_branch .LldT%=\n"
        ".LldP%=:\n\t"
        "s_min_u32 s64, s64, 32\n\t"
        "s_add_u32 s70, s80, s64\n\t"
        "v_mov_b32 %[vt], s65\n\t"
        "s_mov_b32 s66, s65\n\t"
        "s_setpc_b64 s[70:71]\n\t"
        ".p2align 6\n"
        ".LldL%=:\n\t"
        "s_waitcnt vmcnt(0)\n\t"
        "s_waitcnt vmcnt(4)\n\t"
        "s_waitcnt vmcnt(8)\n\t"
        "s_waitcnt vmcnt(12)\n\t"
        "s_waitcnt vmcnt(16)\n\t"
        "s_waitcnt vmcnt(20)\n\t"
        "s_waitcnt vmcnt(24)\n\t"
        "s_waitcnt vmcnt(28)\n\t"
        "s_waitcnt vmcnt(32)\n\t"
        "ds_write_b32 %[vland], %[vt]\n\t"
        "s_cmp_lg_u32 s65, %[fin]\n\t"
        "s_cbranch_scc1 .LldC%=\n"
        ".LldE%=:\n\t"
        "s_waitcnt vmcnt(0) lgkmcnt(0)\n\t"
        : [vf] "=&v"(vf), [vt] "=&v"(vt), [lc] "=&v"(lc), [o1] "+v"(vo1), [o2] "+v"(vo2), [o3] "+v"(vo3)
        : [l0] "v"(l0), [l1] "v"(l1), [o0] "v"(vo0), [vland] "v"(vland), [vfree] "v"(vfree),
          [kp] "s"(kp), [vp] "s"(vp), [nrec] "s"(kvbytes), [nt] "s"(NT), [ring] "s"(ring),
          [step] "s"(step), [due1] "s"(((unsigned)tstart << 8) + SOFT * 256 + (((unsigned)tstart - tw0 > COLD_AT) ? COLD_PAUSE * 256 : 0) + step + half * ((step * HOFF) >> 3)), [fin] "s"(8 * NT), [gap] "n"(GAP_TICKS * 256)
        : "memory", "scc", "vcc", "s60", "s61", "s62", "s63", "s64", "s65", "s66", "s67", "s68", "s69", "s70", "s71", "s72", "s73", "s74",
          "s76", "s77", "s78", "s79", "s80", "s82", "s83", "s84");
    STAMP(3);
    asm volatile("s_endpgm" ::: "memory");
  }

  int lane = lane0;
  asm volatile("" : "+v"(lane));
  auto touch = [&](const void* base, unsigned bytes) __attribute__((always_inline)) {
    const __amdgpu_buffer_rsrc_t rt = __builtin_amdgcn_make_buffer_rsrc((void*)base, 0, (int)bytes, 0x00020000);
    const unsigned idx = TSHORT == 2 ? (g >> 5) + 8 * lane : TSHORT ? (g >> 4) + 16 * lane : (g >> 3) + 32 * lane;
    const unsigned vo = (idx < 0x80000000u / TLBG) ? idx * TLBG + ((idx * 30592u) & (TLBG - 128)) : INVALID;
    asm volatile("s_mov_b32 m0, %0\n\ts_nop 4\n\tbuffer_load_dword %2, %1, 0 offen nt lds" :: "s"((unsigned)(size_t)lds + LDS_DUMMY), "s"(rt), "v"(vo) : "memory");
  };
  const bool toucher = TSHORT == 2 ? (g & 24) == 8 : TSHORT ? (g & 8) != 0 : true;
  unsigned lraw = lens[lane & 15];
  u32x4 lq = *(const u32x4*)(lens + (lane >> 4) * 4);
  if (wv < 6) {
    const __amdgpu_buffer_rsrc_t rb = __builtin_amdgcn_make_buffer_rsrc((void*)bt, 0, NB * NBPS * 4, 0x00020000);
    asm volatile("s_mov_b32 m0, %0\n\ts_nop 4\n\tbuffer_load_dwordx4 %2, %1, 0 offen lds\n\t"
                 "buffer_load_dwordx4 %2, %1, 0 offen offset:1024 lds\n\t"
                 "buffer_load_dwordx4 %2, %1, 0 offen offset:2048 lds\n\t"
                 "buffer_load_dwordx4 %2, %1, 0 offen offset:3072 lds\n\t"
                 "s_waitcnt vmcnt(0)"
                 :: "s"((unsigned)(size_t)lds + LDS_TBL + (wv - 2) * 4096), "s"(rb), "v"(lane * 16 + (wv - 2) * 4096) : "memory");
  } else if (wv == 6) {
    if (TDELAY) __builtin_amdgcn_s_sleep(TDELAY);
    if (toucher) {
      if (TLBW & 1) touch(kp, kvbytes);
      if (TLBW & 2) touch(vp, kvbytes);
      if (TLBW & 8) touch(outp, NB * HQ * SQ * HD * 2);
    }
    if (lane < 40) ctl[lane] = ((lane >> 3) == CTL_FREE / 32) ? (lane & 7) : 0u;
  }
  if (wv != 7) asm volatile("s_waitcnt lgkmcnt(0)\n\ts_barrier" ::: "memory");
  ranking(lane, lraw, lq);
  const int b = __builtin_ctz((unsigned)__builtin_amdgcn_ballot_w64(rank == want) | 0x8000u);
  if (wv == 7) {
    const __amdgpu_buffer_rsrc_t rq = __builtin_amdgcn_make_buffer_rsrc((void*)qp, 0, NB * HQ * SQ * HD * 2, 0x00020000);
    const unsigned q0 = (((int)__builtin_amdgcn_readlane(lraw, b) > 0) ? (unsigned)((b * HQ + 2 * h) * SQ * HD * 2) : INVALID) + (lane >> 4) * 256 +
                        (((unsigned)(lane & 15) ^ ((lane >> 4) << 2)) << 4);
    asm volatile("s_mov_b32 m0, %0\n\ts_nop 4\n\t"
                 "buffer_load_dwordx4 %2, %1, 0 offen " QPOL " lds\n\t"
                 "buffer_load_dwordx4 %3, %1, 0 offen offset:1024 " QPOL " lds\n\t"
                 "buffer_load_dwordx4 %4, %1, 0 offen offset:2048 " QPOL " lds\n\t"
                 "buffer_load_dwordx4 %5, %1, 0 offen offset:3072 " QPOL " lds\n\t"
                 "s_add_u32 m0, m0, 4096\n\ts_nop 0\n\t"
                 "buffer_load_dwordx4 %2, %1, %6 offen " QPOL " lds\n\t"
                 "buffer_load_dwordx4 %3, %1, %6 offen offset:1024 " QPOL " lds\n\t"
                 "buffer_load_dwordx4 %4, %1, %6 offen offset:2048 " QPOL " lds\n\t"
                 "buffer_load_dwordx4 %5, %1, %6 offen offset:3072 " QPOL " lds\n\t"
                 "s_barrier\n\t"
                 "s_waitcnt vmcnt(0)"
                 :: "s"((unsigned)(size_t)lds + LDS_Q), "s"(rq), "v"(q0), "v"(q0 ^ 16), "v"(q0 ^ 32), "v"(q0 ^ 48), "s"(4096) : "memory", "scc");
    ctl[CTL_QOK / 4] = 1;
  }
  const int bp = __builtin_ctz((unsigned)__builtin_amdgcn_ballot_w64(rank == 15 - want) | 0x8000u);
  const int lim = __builtin_amdgcn_readlane(lm1, b) + 1;
  const int nown = (lim + PAGE - 1) / PAGE, NT = (nown + 1) >> 1;
  const int tot = total();
  const int w = wv - 2;
  const int r32 = lane & 31, hi = lane >> 5;
  const int nmine = (NT > w) ? (NT - w + NC - 1) / NC : 0;
  const unsigned lbase = (unsigned)(size_t)lds;
  const unsigned aland = lbase + LDS_CTL + CTL_LAND + (lane & 1) * 4;
  const int npl = (__builtin_amdgcn_readlane(lm1, bp) >> 4) + 1, NTL = (npl + 1) >> 1;
  const bool pf_on = PF_EVERY && npl * 16 * 64 > __builtin_amdgcn_readfirstlane(tot) * PF_X64 && npl > nown && (PF_ALL || (h & 3) == 1);
  const unsigned pf_lead = to_sgpr((int)((float)(2 * NTL * PF_LEAD) * 256.0f * __builtin_amdgcn_rcpf((float)(tot * PACE_X16))));
  const unsigned pf_ratio = to_sgpr((int)((float)NTL * 65536.0f * __builtin_amdgcn_rcpf((float)NT)));
  auto prefetch = [&](unsigned op) {
    const unsigned i = op / (PF_EVERY ? PF_EVERY : 1);
    unsigned tl = (((i | (PF_RUN - 1)) * PF_EVERY * pf_ratio) >> 16) + pf_lead - (~i & (PF_RUN - 1));
    if (tl >= 2u * NTL) return;
    const bool isv = tl >= (unsigned)NTL;
    tl -= isv ? NTL : 0;
    const unsigned p0 = min((unsigned)bt[bp * NBPS + 2 * tl], nphys - 1), p1 = min((unsigned)bt[bp * NBPS + min(2 * tl + 1, (unsigned)npl - 1)], nphys - 1);
    const __amdgpu_buffer_rsrc_t rp = __builtin_amdgcn_make_buffer_rsrc(isv ? (void*)vp : (void*)kp, 0, (int)kvbytes, 0x00020000);
    unsigned ln = lane;
    asm volatile("" : "+v"(ln));
    const unsigned vo = ((ln & 32) ? p1 : p0) * PAGE_BYTES + h * (HD * 2) + ((ln >> 1) & 15) * 4096 + (PF_SLOW == 1 ? 128 : PF_SLOW == 2 ? 0 : (ln & 1) * 128);
    asm volatile("s_mov_b32 m0, %0\n\ts_nop 4\n\tbuffer_load_dword %2, %1, 0 offen " PF_POL " lds" PF_2ND :: "s"(lbase + LDS_DUMMY), "s"(rp), "v"(vo) : "memory");
  };
  const int pos = (w + NC * 22 - NT) % NC;
  const unsigned palp = LDS_WREG + pos * W_SIZE + W_ALP + r32 * 4;
  const unsigned ppst = LDS_WREG + pos * W_SIZE + lane * 16;
  asm volatile("" ::: "v128", "v255");
  unsigned resc = 0;
  float m = -3.0e38f, mprev = -3.0e38f, l = 0.f;

  {
    const unsigned fk = (((unsigned)r32 & 3) << 2) | (((unsigned)r32 >> 2) & 3);
    const unsigned kaddr0 = LDS_RING + r32 * 256 + (((unsigned)hi ^ fk) << 4);
    bf16x8 qf[8];
    wait_above<1>(lbase + LDS_CTL + CTL_QOK, 0);
#pragma unroll
    for (int s = 0; s < 8; ++s) qf[s] = *(LAS bf16x8*)(lds + ((kaddr0 + LDS_Q - LDS_RING) ^ (s << 5)));
#pragma unroll 1
    for (int k = 0; k < nmine; ++k) {
      const unsigned t = w + NC * k;
      if (PRESLEEP && k > 0) __builtin_amdgcn_s_sleep(PRESLEEP);
      wait_above<CSLEEP>(aland, 4 * t);
      if (k == 0) STAMP(1);
      bf16x8 kf[8];
#pragma unroll
      for (int s = 0; s < 8; ++s) kf[s] = *(LAS bf16x8*)(lds + ((kaddr0 + ((t & 7) << 13)) ^ (s << 5)));
      asm volatile("s_waitcnt lgkmcnt(0)" : "+v"(kf[0]), "+v"(kf[1]), "+v"(kf[2]), "+v"(kf[3]), "+v"(kf[4]), "+v"(kf[5]), "+v"(kf[6]), "+v"(kf[7]) :: "memory");
      ctl[CTL_FREE / 4 + (t & 7)] = t + 8;
      if (pf_on && t % (PF_EVERY ? PF_EVERY : 1) == 0) prefetch(t);
      if ((TLBW & 4) && t == 16 && toucher) touch(vp, kvbytes);
      f32x16 sc;
#pragma unroll
      for (int r = 0; r < 16; ++r) sc[r] = 0.f;
#pragma unroll
      for (int s = 0; s < 8; ++s) sc = __builtin_amdgcn_mfma_f32_32x32x16_bf16(kf[s], qf[s], sc, 0, 0, 0);
      if ((int)t == NT - 1) {
        const int kb = 2 * t * PAGE + 4 * hi;
#pragma unroll
        for (int r = 0; r < 16; ++r) sc[r] = (kb + 8 * (r >> 2) + (r & 3) < lim) ? sc[r] : -__builtin_inff();
      }
      float mx = sc[0];
#pragma unroll
      for (int r = 1; r < 16; ++r) mx = fmaxf(mx, sc[r]);
      mx = swap_max(mx);
      const float mn = fmaxf(m, mx);
      const float alpha = __builtin_amdgcn_exp2f((m - mn) * CS);
      mprev = m;
      m = mn;
      const float nmc = -mn * CS;
      float ps = 0.f;
#pragma unroll
      for (int r = 0; r < 16; ++r) { sc[r] = __builtin_amdgcn_exp2f(fmaf(sc[r], CS, nmc)); ps += sc[r]; }
      l = fmaf(l, alpha, ps);
      u32x4 pf[2];
#pragma unroll
      for (int s = 0; s < 2; ++s)
        pf[s] = u32x4{pack2(sc[8 * s], sc[8 * s + 1]), pack2(sc[8 * s + 2], sc[8 * s + 3]), pack2(sc[8 * s + 4], sc[8 * s + 5]), pack2(sc[8 * s + 6], sc[8 * s + 7])};
      if (__builtin_amdgcn_ballot_w64(alpha != 1.0f)) resc |= 1u << k;
      *(LAS float*)(lds + palp + k * 128) = alpha;
      if (k >= KREG || k == nmine - 1) {
        const unsigned dst = ppst + ((k == nmine - 1) ? W_LAST : (k - KREG) * 2048);
        *(LAS u32x4*)(lds + dst) = pf[0];
        *(LAS u32x4*)(lds + dst + 1024) = pf[1];
      } else {
        asm volatile("s_set_gpr_idx_on %8, gpr_idx(DST)\n\t"
                     "v_mov_b32 v128, %0\n\tv_mov_b32 v129, %1\n\tv_mov_b32 v130, %2\n\tv_mov_b32 v131, %3\n\t"
                     "v_mov_b32 v132, %4\n\tv_mov_b32 v133, %5\n\tv_mov_b32 v134, %6\n\tv_mov_b32 v135, %7\n\t"
                     "s_set_gpr_idx_off"
                     :: "v"(pf[0][0]), "v"(pf[0][1]), "v"(pf[0][2]), "v"(pf[0][3]), "v"(pf[1][0]), "v"(pf[1][1]), "v"(pf[1][2]), "v"(pf[1][3]), "s"(8 * k));
      }
    }
  }
  l = swap_sum(l);
  *(LAS f32x2*)(lds + LDS_ML + w * 256 + r32 * 8) = f32x2{m, l};
  LGKM0();
  ctl[CTL_MLOK / 4 + w] = 1;
  STAMP(2);

  const unsigned tq = (lane & 15) >> 2, tp = lane & 3, tblk = (lane >> 4) & 1;
  const unsigned vaddr0 = LDS_RING + (4 * hi + tq) * 256 + ((tq ^ (pos & 3)) << 6) + ((2 * tblk + (tp >> 1)) << 4) + ((tp & 1) << 3);
  f32x16 o[4];
#pragma unroll
  for (int i = 0; i < 4; ++i)
#pragma unroll
    for (int r = 0; r < 16; ++r) o[i][r] = 0.f;
  auto weights = [&]() {
    wait_above<1>(lbase + LDS_CTL + CTL_MLOK + min(lane, NC - 1) * 4, 0);
    unsigned a = LDS_ML + r32 * 8;
    asm volatile("" : "+v"(a) :: "memory");
    float M = -3.0e38f, L = 0.f;
    f32x2 ml[NC];
#pragma unroll
    for (int i = 0; i < NC; ++i) { ml[i] = *(LAS f32x2*)(lds + a + i * 256); M = fmaxf(M, ml[i][0]); }
#pragma unroll
    for (int i = 0; i < NC; ++i) L = fmaf(__builtin_amdgcn_exp2f((ml[i][0] - M) * CS), ml[i][1], L);
    const float inv = 1.0f / L;
    *(LAS float*)(lds + (a >> 1) + (LDS_WT - LDS_ML / 2) + pos * 128) = __builtin_amdgcn_exp2f((mprev - M) * CS) * inv;
    const float ct = __builtin_amdgcn_exp2f((m - M) * CS) * inv;
#pragma unroll
    for (int s = 0; s < 2; ++s) {
      u32x4 x = *(LAS u32x4*)(lds + ppst + W_LAST + s * 1024);
#pragma unroll
      for (int e = 0; e < 4; ++e) x[e] = pack2(__uint_as_float(x[e] << 16) * ct, __uint_as_float(x[e] & 0xffff0000u) * ct);
      *(LAS u32x4*)(lds + ppst + W_LAST + s * 1024) = x;
    }
    LGKM0();
    ctl[CTL_WTOK / 4 + w] = 1;
  };
  resc &= ~1u;
  if (nmine <= 2) weights();
#pragma unroll 1
  for (int k = 0; k < nmine - 1; ++k) {
    if (k == 1) weights();
    if ((resc >> k) & 1) {
      const float alpha = *(LAS float*)(lds + palp + k * 128);
#pragma unroll
      for (int i = 0; i < 4; ++i)
#pragma unroll
        for (int r = 0; r < 16; ++r) o[i][r] *= alpha;
    }
    const unsigned u = NT + w + NC * k;
    u32x4 pf[2];
    if (k >= KREG) {
      pf[0] = *(LAS u32x4*)(lds + ppst + (k - KREG) * 2048);
      pf[1] = *(LAS u32x4*)(lds + ppst + (k - KREG) * 2048 + 1024);
    } else {
      asm volatile("s_set_gpr_idx_on %8, gpr_idx(SRC0)\n\t"
                   "v_mov_b32 %0, v128\n\tv_mov_b32 %1, v129\n\tv_mov_b32 %2, v130\n\tv_mov_b32 %3, v131\n\t"
                   "v_mov_b32 %4, v132\n\tv_mov_b32 %5, v133\n\tv_mov_b32 %6, v134\n\tv_mov_b32 %7, v135\n\t"
                   "s_set_gpr_idx_off\n\ts_nop 1"
                   : "=v"(pf[0][0]), "=v"(pf[0][1]), "=v"(pf[0][2]), "=v"(pf[0][3]), "=v"(pf[1][0]), "=v"(pf[1][1]), "=v"(pf[1][2]), "=v"(pf[1][3])
                   : "s"(8 * k));
    }
    asm volatile("" : "+v"(pf[0]), "+v"(pf[1]), "+v"(o[0]), "+v"(o[1]), "+v"(o[2]), "+v"(o[3]));
    if (PRESLEEP && k > 1) __builtin_amdgcn_s_sleep(2 * PRESLEEP);
    wait_above<CSLEEP>(aland, 4 * u);
    if (TPRIO && k == nmine - 2) __builtin_amdgcn_s_setprio(TPRIO);
    bf16x8 vf[4][2];
#pragma unroll
    for (int i = 0; i < 4; ++i) {
      const unsigned a = (vaddr0 + ((u & 7) << 13)) ^ (i << 6);
#pragma unroll
      for (int s = 0; s < 2; ++s) {
        const s16x4 lo = __builtin_amdgcn_ds_read_tr16_b64_v4i16((LAS s16x4*)(lds + a + 4096 * s));
        const s16x4 up = __builtin_amdgcn_ds_read_tr16_b64_v4i16((LAS s16x4*)(lds + a + 4096 * s + 2048));
        vf[i][s] = __builtin_bit_cast(bf16x8, __builtin_shufflevector(lo, up, 0, 1, 2, 3, 4, 5, 6, 7));
      }
    }
    asm volatile("s_waitcnt lgkmcnt(0)" : "+v"(vf[0][0]), "+v"(vf[0][1]), "+v"(vf[1][0]), "+v"(vf[1][1]), "+v"(vf[2][0]), "+v"(vf[2][1]), "+v"(vf[3][0]), "+v"(vf[3][1]) :: "memory");
    ctl[CTL_FREE / 4 + (u & 7)] = u + 8;
    if (pf_on && u % (PF_EVERY ? PF_EVERY : 1) == 0) prefetch(u);
#pragma unroll
    for (int s = 0; s < 2; ++s)
#pragma unroll
      for (int i = 0; i < 4; ++i) o[i] = __builtin_amdgcn_mfma_f32_32x32x16_bf16(vf[i][s], __builtin_bit_cast(bf16x8, pf[s]), o[i], 0, 0, 0);
  }
  STAMP(3);

  {
    unsigned l16 = lane * 16;
    asm volatile("" : "+v"(l16));
    if (nmine > 1) {
      auto put = [&](unsigned dst, const f32x16& x) {
#pragma unroll
        for (int j = 0; j < 4; ++j) *(LAS f32x4*)(lds + dst + j * 1024) = f32x4{x[4 * j], x[4 * j + 1], x[4 * j + 2], x[4 * j + 3]};
      };
      if (pos > 3) put(LDS_RING + ((2 * NT - 2 * NC + pos) & 7) * 8192 + l16, o[0]);
#pragma unroll
      for (int i = 1; i < 4; ++i) put(LDS_WREG + pos * W_SIZE + (i - 1) * 4096 + l16, o[i]);
      if (TWAIT) LGKM0(); else CBAR();
      ctl[CTL_ODONE / 4 + pos] = 1;
    }
    if (pos < 4) {
      f32x16 of = o[0];
      const unsigned l4 = l16 >> 2, awt = LDS_WT + (l4 & 127);
      wait_above<1>(lbase + LDS_CTL + CTL_WTOK + min(l4, (NC - 1) * 4u), 0);
      auto src_o = [&](int p) -> unsigned {
        const int i = pos ^ (p & 3);
        return l16 + (i == 0 ? LDS_RING + ((2 * NT - 2 * NC + p) & 7) * 8192 : LDS_WREG + p * W_SIZE + (i - 1) * 4096);
      };
      auto fma_o = [&](unsigned src, float c) {
#pragma unroll
        for (int j = 0; j < 4; ++j) {
          const f32x4 x = *(LAS f32x4*)(lds + src + j * 1024);
#pragma unroll
          for (int e = 0; e < 4; ++e) of[4 * j + e] = fmaf(x[e], c, of[4 * j + e]);
        }
      };
      auto mma_t = [&](int p, bf16x8 p0, bf16x8 p1) {
        const unsigned a = vaddr0 + (((2 * NT - NC + p) & 7) << 13);
        bf16x8 vf[2];
#pragma unroll
        for (int s = 0; s < 2; ++s) {
          const s16x4 lo = __builtin_amdgcn_ds_read_tr16_b64_v4i16((LAS s16x4*)(lds + a + 4096 * s));
          const s16x4 up = __builtin_amdgcn_ds_read_tr16_b64_v4i16((LAS s16x4*)(lds + a + 4096 * s + 2048));
          vf[s] = __builtin_bit_cast(bf16x8, __builtin_shufflevector(lo, up, 0, 1, 2, 3, 4, 5, 6, 7));
        }
        of = __builtin_amdgcn_mfma_f32_32x32x16_bf16(vf[0], p0, of, 0, 0, 0);
        of = __builtin_amdgcn_mfma_f32_32x32x16_bf16(vf[1], p1, of, 0, 0, 0);
      };
      {
        const float cw = *(LAS float*)(lds + awt + pos * 128);
#pragma unroll
        for (int r = 0; r < 16; ++r) of[r] *= cw;
      }
      if (NT >= 2 * NC) {
        float co[5];
        bf16x8 pl[NC][2];
        auto ld_p = [&](int p) {
          pl[p][0] = *(LAS bf16x8*)(lds + LDS_WREG + p * W_SIZE + W_LAST + l16);
          pl[p][1] = *(LAS bf16x8*)(lds + LDS_WREG + p * W_SIZE + W_LAST + l16 + 1024);
        };
#define PIN_OF() asm volatile("" : "+v"(of))
#pragma unroll
        for (int i = 1; i < 4; ++i) co[i - 1] = *(LAS float*)(lds + awt + (pos ^ i) * 128);
        co[3] = *(LAS float*)(lds + awt + 4 * 128);
        co[4] = *(LAS float*)(lds + awt + 5 * 128);
        ld_p(0); ld_p(1); ld_p(2);
        PIN_OF();
        wait_above<TSLEEP>(lbase + LDS_CTL + CTL_ODONE + (l4 & 12), 0);
#pragma unroll
        for (int i = 1; i < 4; ++i) fma_o(LDS_WREG + (pos ^ i) * W_SIZE + (i - 1) * 4096 + l16, co[i - 1]);
        PIN_OF();
        ld_p(3); ld_p(4); ld_p(5);
        wait_above<TSLEEP>(aland, 4 * (2 * NT - NC + 2));
        mma_t(0, pl[0][0], pl[0][1]);
        mma_t(1, pl[1][0], pl[1][1]);
        mma_t(2, pl[2][0], pl[2][1]);
        STAMP(4);
        wait_above<TSLEEP>(lbase + LDS_CTL + CTL_ODONE + 16 + (l4 & 4), 0);
        fma_o(src_o(4), co[3]);
        fma_o(src_o(5), co[4]);
        PIN_OF();
        wait_above<0>(aland, 4 * (2 * NT - NC + 4));
        mma_t(3, pl[3][0], pl[3][1]);
        mma_t(4, pl[4][0], pl[4][1]);
        PIN_OF();
        wait_above<0>(aland, 4 * (2 * NT - NC + 5));
        mma_t(5, pl[5][0], pl[5][1]);
      } else {
#pragma unroll 1
        for (int p = max(NC - NT, 0); p < NC; ++p) {
          if (p != pos && NT - 2 * NC + p >= 0) {
            wait_above<1>(lbase + LDS_CTL + CTL_ODONE + p * 4, 0);
            fma_o(src_o(p), *(LAS float*)(lds + awt + p * 128));
          }
          wait_above<1>(aland, 4 * (2 * NT - NC + p));
          mma_t(p, *(LAS bf16x8*)(lds + LDS_WREG + p * W_SIZE + W_LAST + l16), *(LAS bf16x8*)(lds + LDS_WREG + p * W_SIZE + W_LAST + l16 + 1024));
        }
      }
      const __amdgpu_buffer_rsrc_t ro = __builtin_amdgcn_make_buffer_rsrc(outp, 0, NB * HQ * SQ * HD * 2, 0x00020000);
      const unsigned orow = (unsigned)(b * HQ + 2 * h) * SQ * HD * 2 + pos * 64 + ((l16 & 511) << 4) + ((l16 >> 5) & 16);
#pragma unroll
      for (int j = 0; j < 2; ++j) {
        const unsigned a0 = pack2(of[8 * j], of[8 * j + 1]), a1 = pack2(of[8 * j + 2], of[8 * j + 3]);
        const unsigned b0 = pack2(of[8 * j + 4], of[8 * j + 5]), b1 = pack2(of[8 * j + 6], of[8 * j + 7]);
        auto s0 = __builtin_amdgcn_permlane32_swap(a0, b0, false, false);
        auto s1 = __builtin_amdgcn_permlane32_swap(a1, b1, false, false);
        u32x4 val = {s0[0], s1[0], s0[1], s1[1]};
        __builtin_amdgcn_raw_buffer_store_b128(val, ro, orow + j * 32, 0, OUTAUX);
      }
    }
  }
}
}

extern "C" void kernel_launch(void* const* d_in, const int* in_sizes, int n_in, void* d_out, int out_size, void* d_ws,
                              size_t ws_size, hipStream_t stream) {
  (void)n_in; (void)out_size; (void)ws_size;
  const unsigned nphys = (unsigned)(in_sizes[1] / (PAGE * HKV * HD));
  const unsigned kvbytes = nphys * PAGE_BYTES;
  (void)hipFuncSetAttribute((const void*)paged_attn, hipFuncAttributeMaxDynamicSharedMemorySize, LDS_TOTAL);
  hipLaunchKernelGGL(paged_attn, dim3(256), dim3(512), LDS_TOTAL, stream, d_in[0], d_in[1], d_in[2], (const int*)d_in[3],
                     (const int*)d_in[4], d_out, (char*)d_ws, kvbytes, nphys);
}
```

```cpp
#define PACE_X16 440
#define SOFT 50
#include <hip/hip_runtime.h>
#include <hip/hip_bf16.h>

#define PROBE_G 0
#define QPOL "nt"
#define PF_2ND
#define TLBW 5
#define TLBG 0x200000u
#define SOFT_AT 0
#define TSHORT 1
#define TDELAY 0
#define PF_BY 0
#define PF_END 2u
#define PF_X64B 72
#define PF_DENSE 1
#define PF_PER 4
#define PF_PSTEP 2
#define GAP_TICKS 0
#define OUTAUX 0
#define HOFF 4
#define LSLEEP 0
#define PRESLEEP 0
#define PF_EVERY 2
#define PF_X64 76
#define PF_ALL 0
#define PF_LEAD 380
#define PF_POL ""
#define PF_SLOW 0
#define PF_RUN 3
#define TSLEEP 0
#define TWAIT 1
#define TPRIO 0
#define CSLEEP 1

#define STAMP(i) do {} while (0)

namespace {
constexpr int NB = 16, HQ = 32, HKV = 16, SQ = 16, HD = 128, PAGE = 16, NBPS = 256, SKV = NBPS * PAGE;
constexpr unsigned PAGE_BYTES = PAGE * HKV * HD * 2;
constexpr unsigned INVALID = 0x80000000u;
constexpr int NC = 6;
constexpr int KREG = 16, KLDS = 5;
static_assert(NC * (KREG + KLDS + 1) >= NBPS / 2, "P storage");
constexpr unsigned LDS_RING = 0;
constexpr unsigned LDS_WREG = 65536;
constexpr unsigned W_ALP = KLDS * 2048;
constexpr unsigned W_LAST = W_ALP + (KREG + KLDS + 1) * 128;
constexpr unsigned W_SIZE = W_LAST + 2048;
static_assert(W_LAST >= 3 * 4096 && W_SIZE % 256 == 0, "consumer region");
constexpr unsigned LDS_TBL = LDS_WREG + 2 * W_SIZE;
constexpr unsigned LDS_Q = LDS_WREG;
constexpr unsigned LDS_ML = LDS_WREG + NC * W_SIZE;
constexpr unsigned LDS_WT = LDS_ML + NC * 256;
constexpr unsigned LDS_CTL = LDS_WT + NC * 128;
constexpr unsigned CTL_LAND = 0;
constexpr unsigned CTL_QOK = 16;
constexpr unsigned CTL_FREE = 32;
constexpr unsigned CTL_MLOK = 64;
constexpr unsigned CTL_ODONE = 96;
constexpr unsigned CTL_WTOK = 128;
constexpr unsigned LDS_DUMMY = LDS_CTL + 256;
constexpr unsigned LDS_TOTAL = LDS_DUMMY + 256;
static_assert(LDS_TOTAL <= 163840, "lds");

typedef __attribute__((ext_vector_type(8))) __bf16 bf16x8;
typedef __attribute__((ext_vector_type(2))) __bf16 bf16x2;
typedef __attribute__((ext_vector_type(4))) short s16x4;
typedef __attribute__((ext_vector_type(16))) float f32x16;
typedef __attribute__((ext_vector_type(4))) float f32x4;
typedef __attribute__((ext_vector_type(2))) float f32x2;
typedef __attribute__((ext_vector_type(4))) unsigned u32x4;
typedef __attribute__((ext_vector_type(2))) unsigned u32x2;
typedef __attribute__((ext_vector_type(4))) int i32x4;
#define LAS __attribute__((address_space(3)))

__device__ __forceinline__ float swap_max(float x) {
  auto r = __builtin_amdgcn_permlane32_swap(__float_as_uint(x), __float_as_uint(x), false, false);
  return fmaxf(__uint_as_float(r[0]), __uint_as_float(r[1]));
}
__device__ __forceinline__ float swap_sum(float x) {
  auto r = __builtin_amdgcn_permlane32_swap(__float_as_uint(x), __float_as_uint(x), false, false);
  return __uint_as_float(r[0]) + __uint_as_float(r[1]);
}
__device__ __forceinline__ unsigned pack2(float a, float b) {
  bf16x2 v = {(__bf16)a, (__bf16)b};
  return __builtin_bit_cast(unsigned, v);
}
template <int CTRL> __device__ __forceinline__ int dpp_add(int x) { return x + __builtin_amdgcn_update_dpp(0, x, CTRL, 0xf, 0xf, true); }
__device__ __forceinline__ unsigned now32() { return (unsigned)__builtin_amdgcn_s_memrealtime(); }
__device__ __forceinline__ int to_sgpr(int v) {
  int r;
  asm volatile("s_nop 1\n\tv_readfirstlane_b32 %0, %1" : "=s"(r) : "v"(v));
  return r;
}
#define LGKM0() asm volatile("s_waitcnt lgkmcnt(0)" ::: "memory")
#define VMWAIT(n) asm volatile("s_waitcnt vmcnt(" #n ")" ::: "memory")
#define CBAR() asm volatile("" ::: "memory")
template <int SLEEP> __device__ __forceinline__ void wait_above(unsigned addr, unsigned u) {
  unsigned v, cnt;
  if (SLEEP)
    asm volatile("s_mov_b32 %1, 0x200000\n\ts_branch 2f\n"
                 "1:\n\ts_sleep %4\n"
                 "2:\n\tds_read_b32 %0, %2\n\ts_sub_u32 %1, %1, 1\n\ts_waitcnt lgkmcnt(0)\n\t"
                 "v_cmp_ge_u32 vcc, %3, %0\n\ts_cbranch_scc1 3f\n\ts_nop 1\n\ts_cbranch_vccnz 1b\n3:"
                 : "=&v"(v), "=&s"(cnt) : "v"(addr), "s"(u), "n"(SLEEP) : "memory", "scc", "vcc");
  else
    asm volatile("s_mov_b32 %1, 0x200000\n"
                 "1:\n\tds_read_b32 %0, %2\n\ts_sub_u32 %1, %1, 1\n\ts_waitcnt lgkmcnt(0)\n\t"
                 "v_cmp_ge_u32 vcc, %3, %0\n\ts_cbranch_scc1 3f\n\ts_nop 1\n\ts_cbranch_vccnz 1b\n3:"
                 : "=&v"(v), "=&s"(cnt) : "v"(addr), "s"(u) : "memory", "scc", "vcc");
}

__global__ void __launch_bounds__(512, 1) __attribute__((amdgpu_num_vgpr(64)))
paged_attn(const void* __restrict__ qp, const void* __restrict__ kp, const void* __restrict__ vp, const int* __restrict__ bt,
           const int* __restrict__ lens, void* __restrict__ outp, char* __restrict__ ws, unsigned kvbytes, unsigned nphys) {
  extern __shared__ __attribute__((aligned(16))) char smem[];
  LAS char* lds = (LAS char*)smem;
  const int lane0 = threadIdx.x & 63;
  const int wv = __builtin_amdgcn_readfirstlane(threadIdx.x >> 6);
  const int g = blockIdx.x;
  const int h = g >> 4;
  constexpr float CS = 0.08838834764831845f * 1.4426950408889634f;
  volatile LAS unsigned* ctl = (volatile LAS unsigned*)(lds + LDS_CTL);

  const unsigned want = (g & 8) ? 15 - (g & 7) : (g & 7);
  unsigned lm1, rank;
  auto ranking = [&](int lane, unsigned& lr, u32x4& lq) __attribute__((always_inline)) {
    asm volatile("" : "+v"(lr), "+v"(lq));
    lm1 = min(lr - 1u, (unsigned)SKV - 1u);
    const unsigned key = lm1 * 16 + (lane & 15);
    rank = 0;
#pragma unroll
    for (int j = 0; j < 4; ++j) rank += (min(lq[j] - 1u, (unsigned)SKV - 1u) * 16 + ((lane >> 4) * 4 + j) > key) ? 1u : 0u;
    auto r1 = __builtin_amdgcn_permlane16_swap(rank, rank, false, false);
    rank = r1[0] + r1[1];
    auto r2 = __builtin_amdgcn_permlane32_swap(rank, rank, false, false);
    rank = r2[0] + r2[1];
  };
  auto total = [&]() __attribute__((always_inline)) {
    int t = lm1 + 1;
    t = dpp_add<0x121>(t); t = dpp_add<0x122>(t); t = dpp_add<0x124>(t); t = dpp_add<0x128>(t);
    return t;
  };

  if (__builtin_expect(wv < 2, 1)) {
    const int half = wv, lane = lane0;
    unsigned lr = lens[lane & 15];
    u32x4 lq = *(const u32x4*)(lens + (lane >> 4) * 4);
    unsigned first = bt[(lane & 15) * NBPS + half];
    __builtin_amdgcn_s_setprio(3);
    const unsigned rq4 = lane >> 4;
    unsigned vo0 = rq4 * 4096 + (((unsigned)(lane & 15) ^ (rq4 << 2)) << 4) + h * (HD * 2);
    unsigned vo1 = (vo0 + 16384 - 1024) ^ 16, vo2 = (vo0 + 32768 - 2048) ^ 32, vo3 = (vo0 + 49152 - 3072) ^ 48;
    unsigned vland = (unsigned)(size_t)lds + LDS_CTL + CTL_LAND + half * 4;
    unsigned vfree = (unsigned)(size_t)lds + LDS_CTL + CTL_FREE + (lane & 7) * 4;
    const unsigned ring = (unsigned)(size_t)lds + LDS_RING + half * 4096;
    const __amdgpu_buffer_rsrc_t rk = __builtin_amdgcn_make_buffer_rsrc((void*)kp, 0, (int)kvbytes, 0x00020000);
    asm volatile("" : "+v"(vo0), "+v"(vo1), "+v"(vo2), "+v"(vo3), "+v"(vland), "+v"(vfree) : "s"(rk), "s"(vp), "s"(nphys), "s"(ring));
    ranking(lane, lr, lq);
    first = min(first, nphys - 1) * PAGE_BYTES;
    const int b = __builtin_ctz((unsigned)__builtin_amdgcn_ballot_w64(rank == want) | 0x8000u);
    unsigned long long tstart;
    asm volatile("s_waitcnt vmcnt(0)\n\t"
                 "s_mov_b32 m0, %2\n\t"
                 "s_memrealtime %0\n\t"
                 "s_nop 2\n\t"
                 "buffer_load_dwordx4 %4, %1, %3 offen nt lds\n\t"
                 "buffer_load_dwordx4 %5, %1, %3 offen offset:1024 nt lds\n\t"
                 "buffer_load_dwordx4 %6, %1, %3 offen offset:2048 nt lds\n\t"
                 "buffer_load_dwordx4 %7, %1, %3 offen offset:3072 nt lds\n\t"
                 "s_waitcnt lgkmcnt(0)\n\t"
                 "s_barrier"
                 : "=&s"(tstart) : "s"(rk), "s"(ring), "s"(__builtin_amdgcn_readlane(first, b)), "v"(vo0), "v"(vo1), "v"(vo2), "v"(vo3) : "memory");
    __builtin_amdgcn_sched_barrier(0);
    asm volatile("" : "+v"(lm1));
    const int len = __builtin_amdgcn_readlane(lm1, b) + 1, nown = (len + PAGE - 1) / PAGE, NT = (nown + 1) >> 1;
    unsigned l0 = *(LAS unsigned*)(lds + LDS_TBL + (b * NBPS + min(2 * lane + half, nown - 1)) * 4);
    unsigned l1 = *(LAS unsigned*)(lds + LDS_TBL + (b * NBPS + min(2 * lane + half + 128, nown - 1)) * 4);
    const int T16 = total() * PACE_X16;
    const int step = to_sgpr((int)((float)T16 * __builtin_amdgcn_rcpf((float)max(len, 32))));
    const unsigned due0 = ((unsigned)tstart << 8) + half * ((step * HOFF) >> 3);
    const int ua = SOFT_AT ? to_sgpr(min(min(NT, 64), 1 + (int)((float)(SOFT_AT * 256) * __builtin_amdgcn_rcpf((float)step)))) : 1;
    l0 = min(l0, nphys - 1) * PAGE_BYTES;
    l1 = min(l1, nphys - 1) * PAGE_BYTES;
    const unsigned lastrec = (unsigned)__builtin_amdgcn_readlane(NT > 64 ? l1 : l0, (NT - 1) & 63) + (unsigned)to_sgpr(min(max(len - (2 * (NT - 1) + half) * PAGE, 0), PAGE)) * 4096u;
#define CNT(r)
    unsigned vf, vt, lc;
    asm volatile(
        "s_waitcnt lgkmcnt(0)\n\t"
        "s_memrealtime s[68:69]\n\t"
        "ds_read_b32 %[vf], %[vfree]\n\t"
        "s_mov_b32 s60, 1\n\t"
        "s_mov_b32 s61, 4\n\t"
        "s_mov_b32 s62, 0x400000\n\t"
        "s_mov_b32 s66, 0\n\t"
        "s_mov_b32 s67, %[ua]\n\t"
        "s_mov_b32 s72, 0x2000\n\t"
        "s_mov_b32 s73, 1\n\t"
        "s_mov_b32 s74, %[due1]\n\t"
        "s_mov_b64 s[76:77], %[kp]\n\t"
        "s_mov_b32 s78, %[nrec]\n\t"
        "s_mov_b32 s79, 0x20000\n\t"
        "s_and_b32 s77, s77, 0xffff\n\t"
        "v_mov_b32 %[lc], %[l0]\n\t"
        "s_getpc_b64 s[70:71]\n"
        ".LldA%=:\n\t"
        "s_add_u32 s80, s70, .LldL%=-.LldA%=\n\t"
        "s_addc_u32 s71, s71, 0\n\t"
        "s_branch .LldF%=\n"
        ".LldR%=:\n\t"
        "s_sub_u32 s62, s62, 1\n\t"
        "s_cbranch_scc1 .LldE%=\n"
        ".LldT%=:\n\t"
        "s_getreg_b32 s64, hwreg(HW_REG_IB_STS)\n\t"
        "s_and_b32 s65, s64, 15\n\t"
        "s_bfe_u32 s64, s64, 0x20016\n\t"
        "s_lshl4_add_u32 s64, s64, s65\n\t"
        "s_add_u32 s64, s64, 3\n\t"
        "s_and_b32 s64, s64, 60\n\t"
        "s_sub_u32 s65, s61, s64\n\t"
        "s_cmp_lg_u32 s65, s66\n\t"
        "s_cbranch_scc1 .LldP%=\n"
        ".LldC%=:\n\t"
        "s_waitcnt lgkmcnt(0)\n\t"
        "v_cmp_eq_u32 vcc, s60, %[vf]\n\t"
        "s_lshl_b32 s64, s68, 8\n\t"
        "s_memrealtime s[68:69]\n\t"
        "ds_read_b32 %[vf], %[vfree]\n\t"
        CNT("s82")
        "s_sub_u32 s64, s64, s74\n\t"
        "s_cmp_lt_i32 s64, 0\n\t"
        "s_cbranch_scc1 .LldR%=\n\t"
        CNT("s83")
        "s_cbranch_vccz .LldR%=\n\t"
        CNT("s84")
        "buffer_load_dwordx4 %[o0], s[76:79], s63 offen nt lds\n\t"
        "s_add_u32 s74, s74, %[step]\n\t"
        "s_add_u32 s73, s73, 1\n\t"
        "buffer_load_dwordx4 %[o1], s[76:79], s63 offen offset:1024 nt lds\n\t"
        "s_add_u32 s60, s60, 1\n\t"
        "s_add_u32 s61, s61, 4\n\t"
        "buffer_load_dwordx4 %[o2], s[76:79], s63 offen offset:2048 nt lds\n\t"
        "s_add_u32 s72, s72, 0x2000\n\t"
        "s_and_b32 s72, s72, 0xe000\n\t"
        "buffer_load_dwordx4 %[o3], s[76:79], s63 offen offset:3072 nt lds\n"
        ".LldF%=:\n\t"
        "v_readlane_b32 s63, %[lc], s73\n\t"
        "s_add_u32 m0, s72, %[ring]\n\t"
        "s_cmp_eq_u32 s73, %[ntm]\n\t"
        "s_cselect_b32 s78, %[last], %[nrec]\n\t"
        "s_cmp_lg_u32 s60, s67\n\t"
        "s_cbranch_scc1 .LldT%=\n\t"
        "s_cmp_lg_u32 s60, %[ua]\n\t"
        "s_cbranch_scc1 .LldG%=\n\t"
        "s_add_u32 s74, s74, %[soft]\n\t"
        "s_min_u32 s67, %[nt], 64\n\t"
        "s_cmp_lg_u32 s60, s67\n\t"
        "s_cbranch_scc1 .LldT%=\n"
        ".LldG%=:\n\t"
        "s_cmp_eq_u32 s73, %[nt]\n\t"
        "s_cbranch_scc1 .LldV%=\n\t"
        "v_mov_b32 %[lc], %[l1]\n\t"
        "v_readfirstlane_b32 s63, %[l1]\n\t"
        "s_add_u32 s67, s60, %[nt]\n\t"
        "s_sub_u32 s67, s67, 64\n\t"
        "s_branch .LldT%=\n"
        ".LldV%=:\n\t"
        "s_cmp_lg_u32 s60, %[nt]\n\t"
        "s_cbranch_scc1 .LldD%=\n\t"
        "s_mov_b64 s[76:77], %[vp]\n\t"
        "s_and_b32 s77, s77, 0xffff\n\t"
        "s_mov_b32 s73, 0\n\t"
        "s_min_u32 s67, %[nt], 64\n\t"
        "s_add_u32 s67, s67, s60\n\t"
        "s_mov_b32 s74, %[duev]\n\t"
        "s_cmp_eq_u32 %[ntm], 0\n\t"
        "s_cselect_b32 s78, %[last], %[nrec]\n\t"
        "v_mov_b32 %[lc], %[l0]\n\t"
        "v_readfirstlane_b32 s63, %[l0]\n\t"
        "v_xor_b32 %[o1], 16, %[o1]\n\t"
        "v_xor_b32 %[o2], 32, %[o2]\n\t"
        "v_xor_b32 %[o3], 48, %[o3]\n\t"
        "s_branch .LldT%=\n"
        ".LldD%=:\n\t"
        "s_add_u32 s74, s74, 0x40000000\n\t"
        "s_mov_b32 s60, -1\n\t"
        "s_branch .LldT%=\n"
        ".LldP%=:\n\t"
        "s_min_u32 s64, s64, 32\n\t"
        "s_add_u32 s70, s80, s64\n\t"
        "v_mov_b32 %[vt], s65\n\t"
        "s_mov_b32 s66, s65\n\t"
        "s_setpc_b64 s[70:71]\n\t"
        ".p2align 6\n"
        ".LldL%=:\n\t"
        "s_waitcnt vmcnt(0)\n\t"
        "s_waitcnt vmcnt(4)\n\t"
        "s_waitcnt vmcnt(8)\n\t"
        "s_waitcnt vmcnt(12)\n\t"
        "s_waitcnt vmcnt(16)\n\t"
        "s_waitcnt vmcnt(20)\n\t"
        "s_waitcnt vmcnt(24)\n\t"
        "s_waitcnt vmcnt(28)\n\t"
        "s_waitcnt vmcnt(32)\n\t"
        "ds_write_b32 %[vland], %[vt]\n\t"
        "s_cmp_lg_u32 s65, %[fin]\n\t"
        "s_cbranch_scc1 .LldC%=\n"
        ".LldE%=:\n\t"
        "s_waitcnt vmcnt(0) lgkmcnt(0)\n\t"
        : [vf] "=&v"(vf), [vt] "=&v"(vt), [lc] "=&v"(lc), [o1] "+v"(vo1), [o2] "+v"(vo2), [o3] "+v"(vo3)
        : [l0] "v"(l0), [l1] "v"(l1), [o0] "v"(vo0), [vland] "v"(vland), [vfree] "v"(vfree),
          [kp] "s"(kp), [vp] "s"(vp), [nrec] "s"(kvbytes), [nt] "s"(NT), [ring] "s"(ring),
          [step] "s"(step), [due1] "s"(due0 + step), [duev] "s"(due0 + (to_sgpr(T16) >> 5) + (SOFT + GAP_TICKS) * 256), [last] "s"(lastrec), [ntm] "s"(NT - 1), [fin] "s"(8 * NT), [ua] "s"(ua), [soft] "n"(SOFT * 256)
        : "memory", "scc", "vcc", "s60", "s61", "s62", "s63", "s64", "s65", "s66", "s67", "s68", "s69", "s70", "s71", "s72", "s73", "s74",
          "s76", "s77", "s78", "s79", "s80", "s82", "s83", "s84");
    STAMP(3);
    asm volatile("s_endpgm" ::: "memory");
  }

  int lane = lane0;
  asm volatile("" : "+v"(lane));
  auto touch = [&](const void* base, unsigned bytes) __attribute__((always_inline)) {
    const __amdgpu_buffer_rsrc_t rt = __builtin_amdgcn_make_buffer_rsrc((void*)base, 0, (int)bytes, 0x00020000);
    const unsigned idx = TSHORT == 2 ? (g >> 5) + 8 * lane : TSHORT ? (g >> 4) + 16 * lane : (g >> 3) + 32 * lane;
    const unsigned vo = (idx < 0x80000000u / TLBG) ? idx * TLBG + ((idx * 30592u) & (TLBG - 128)) : INVALID;
    asm volatile("s_mov_b32 m0, %0\n\ts_nop 4\n\tbuffer_load_dword %2, %1, 0 offen nt lds" :: "s"((unsigned)(size_t)lds + LDS_DUMMY), "s"(rt), "v"(vo) : "memory");
  };
  const bool toucher = TSHORT == 2 ? (g & 24) == 8 : TSHORT ? (g & 8) != 0 : true;
  unsigned lraw = lens[lane & 15];
  u32x4 lq = *(const u32x4*)(lens + (lane >> 4) * 4);
  if (wv < 6) {
    const __amdgpu_buffer_rsrc_t rb = __builtin_amdgcn_make_buffer_rsrc((void*)bt, 0, NB * NBPS * 4, 0x00020000);
    asm volatile("s_mov_b32 m0, %0\n\ts_nop 4\n\tbuffer_load_dwordx4 %2, %1, 0 offen lds\n\t"
                 "buffer_load_dwordx4 %2, %1, 0 offen offset:1024 lds\n\t"
                 "buffer_load_dwordx4 %2, %1, 0 offen offset:2048 lds\n\t"
                 "buffer_load_dwordx4 %2, %1, 0 offen offset:3072 lds\n\t"
                 "s_waitcnt vmcnt(0)"
                 :: "s"((unsigned)(size_t)lds + LDS_TBL + (wv - 2) * 4096), "s"(rb), "v"(lane * 16 + (wv - 2) * 4096) : "memory");
  } else if (wv == 6) {
    if (TDELAY) __builtin_amdgcn_s_sleep(TDELAY);
    if (toucher) {
      if (TLBW & 1) touch(kp, kvbytes);
      if (TLBW & 2) touch(vp, kvbytes);
      if (TLBW & 8) touch(outp, NB * HQ * SQ * HD * 2);
    }
    if (lane < 40) ctl[lane] = ((lane >> 3) == CTL_FREE / 32) ? (lane & 7) : 0u;
  }
  if (wv != 7) asm volatile("s_waitcnt lgkmcnt(0)\n\ts_barrier" ::: "memory");
  ranking(lane, lraw, lq);
  const int b = __builtin_ctz((unsigned)__builtin_amdgcn_ballot_w64(rank == want) | 0x8000u);
  if (wv == 7) {
    const __amdgpu_buffer_rsrc_t rq = __builtin_amdgcn_make_buffer_rsrc((void*)qp, 0, NB * HQ * SQ * HD * 2, 0x00020000);
    const unsigned q0 = (((int)__builtin_amdgcn_readlane(lraw, b) > 0) ? (unsigned)((b * HQ + 2 * h) * SQ * HD * 2) : INVALID) + (lane >> 4) * 256 +
                        (((unsigned)(lane & 15) ^ ((lane >> 4) << 2)) << 4);
    asm volatile("s_mov_b32 m0, %0\n\ts_nop 4\n\t"
                 "buffer_load_dwordx4 %2, %1, 0 offen " QPOL " lds\n\t"
                 "buffer_load_dwordx4 %3, %1, 0 offen offset:1024 " QPOL " lds\n\t"
                 "buffer_load_dwordx4 %4, %1, 0 offen offset:2048 " QPOL " lds\n\t"
                 "buffer_load_dwordx4 %5, %1, 0 offen offset:3072 " QPOL " lds\n\t"
                 "s_add_u32 m0, m0, 4096\n\ts_nop 0\n\t"
                 "buffer_load_dwordx4 %2, %1, %6 offen " QPOL " lds\n\t"
                 "buffer_load_dwordx4 %3, %1, %6 offen offset:1024 " QPOL " lds\n\t"
                 "buffer_load_dwordx4 %4, %1, %6 offen offset:2048 " QPOL " lds\n\t"
                 "buffer_load_dwordx4 %5, %1, %6 offen offset:3072 " QPOL " lds\n\t"
                 "s_barrier\n\t"
                 "s_waitcnt vmcnt(0)"
                 :: "s"((unsigned)(size_t)lds + LDS_Q), "s"(rq), "v"(q0), "v"(q0 ^ 16), "v"(q0 ^ 32), "v"(q0 ^ 48), "s"(4096) : "memory", "scc");
    ctl[CTL_QOK / 4] = 1;
  }
  const int bp = __builtin_ctz((unsigned)__builtin_amdgcn_ballot_w64(rank == 15 - want) | 0x8000u);
  const int lim = __builtin_amdgcn_readlane(lm1, b) + 1;
  const int nown = (lim + PAGE - 1) / PAGE, NT = (nown + 1) >> 1;
  const int tot = total();
  const int w = wv - 2;
  const int r32 = lane & 31, hi = lane >> 5;
  const int nmine = (NT > w) ? (NT - w + NC - 1) / NC : 0;
  const unsigned lbase = (unsigned)(size_t)lds;
  const unsigned aland = lbase + LDS_CTL + CTL_LAND + (lane & 1) * 4;
  const int npl = (__builtin_amdgcn_readlane(lm1, bp) >> 4) + 1, NTL = (npl + 1) >> 1;
  const int tots = __builtin_amdgcn_readfirstlane(tot);
  const bool pf_on = PF_EVERY && npl * 256 * 64 > tots * PF_X64B && npl > nown && (h & 3) == PF_BY;
  const unsigned pf_max = (npl * 256 * 64 > tots * PF_X64) ? 256u : PF_END;
  const unsigned pf_last = 2 * NT - to_sgpr((int)((float)(2 * NT * PF_LEAD) * 4096.0f * __builtin_amdgcn_rcpf((float)(tot * PACE_X16))));
  const unsigned pf_ratio = to_sgpr((int)((float)NTL * 65536.0f * __builtin_amdgcn_rcpf((float)NT)));
  const unsigned pf_per = PF_PER + (g & 7) * PF_PSTEP, pf_inv = (65536 + pf_per - 1) / pf_per;
  auto prefetch = [&](unsigned op) {
    const unsigned d = pf_last - op;
    if (d >= 1024u) return;
    const unsigned r = d - ((d * pf_inv) >> 16) * pf_per;
    if (r >= PF_RUN) return;
    unsigned tl = 2 * NTL - 1 - (((d - r) * pf_ratio) >> 16) - r;
    if (tl >= 2u * NTL) return;
    const bool isv = tl >= (unsigned)NTL;
    tl -= isv ? NTL : 0;
    const unsigned p0 = min((unsigned)bt[bp * NBPS + 2 * tl], nphys - 1), p1 = min((unsigned)bt[bp * NBPS + min(2 * tl + 1, (unsigned)npl - 1)], nphys - 1);
    const __amdgpu_buffer_rsrc_t rp = __builtin_amdgcn_make_buffer_rsrc(isv ? (void*)vp : (void*)kp, 0, (int)kvbytes, 0x00020000);
    unsigned ln = lane;
    asm volatile("" : "+v"(ln));
    const unsigned vo = ((ln & 32) ? p1 : p0) * PAGE_BYTES + ((h & 12) | 1) * (HD * 2) + ((ln >> 1) & 15) * 4096 + (PF_SLOW == 1 ? 128 : PF_SLOW == 2 ? 0 : (ln & 1) * 128);
    asm volatile("s_mov_b32 m0, %0\n\ts_nop 4\n\tbuffer_load_dword %2, %1, 0 offen " PF_POL " lds" PF_2ND :: "s"(lbase + LDS_DUMMY), "s"(rp), "v"(vo) : "memory");
  };
  const int pos = (w + NC * 22 - NT) % NC;
  const unsigned palp = LDS_WREG + pos * W_SIZE + W_ALP + r32 * 4;
  const unsigned ppst = LDS_WREG + pos * W_SIZE + lane * 16;
  asm volatile("" ::: "v128", "v255");
  unsigned resc = 0;
  float m = -3.0e38f, mprev = -3.0e38f, l = 0.f;

  {
    const unsigned fk = (((unsigned)r32 & 3) << 2) | (((unsigned)r32 >> 2) & 3);
    const unsigned kaddr0 = LDS_RING + r32 * 256 + (((unsigned)hi ^ fk) << 4);
    bf16x8 qf[8];
    wait_above<1>(lbase + LDS_CTL + CTL_QOK, 0);
#pragma unroll
    for (int s = 0; s < 8; ++s) qf[s] = *(LAS bf16x8*)(lds + ((kaddr0 + LDS_Q - LDS_RING) ^ (s << 5)));
#pragma unroll 1
    for (int k = 0; k < nmine; ++k) {
      const unsigned t = w + NC * k;
      if (PRESLEEP && k > 0) __builtin_amdgcn_s_sleep(PRESLEEP);
      wait_above<CSLEEP>(aland, 4 * t);
      if (k == 0) STAMP(1);
      bf16x8 kf[8];
#pragma unroll
      for (int s = 0; s < 8; ++s) kf[s] = *(LAS bf16x8*)(lds + ((kaddr0 + ((t & 7) << 13)) ^ (s << 5)));
      asm volatile("s_waitcnt lgkmcnt(0)" : "+v"(kf[0]), "+v"(kf[1]), "+v"(kf[2]), "+v"(kf[3]), "+v"(kf[4]), "+v"(kf[5]), "+v"(kf[6]), "+v"(kf[7]) :: "memory");
      ctl[CTL_FREE / 4 + (t & 7)] = t + 8;
      if (pf_on) prefetch(t);
      if ((TLBW & 4) && t == 16 && toucher) touch(vp, kvbytes);
      f32x16 sc;
#pragma unroll
      for (int r = 0; r < 16; ++r) sc[r] = 0.f;
#pragma unroll
      for (int s = 0; s < 8; ++s) sc = __builtin_amdgcn_mfma_f32_32x32x16_bf16(kf[s], qf[s], sc, 0, 0, 0);
      if ((int)t == NT - 1) {
        const int kb = 2 * t * PAGE + 4 * hi;
#pragma unroll
        for (int r = 0; r < 16; ++r) sc[r] = (kb + 8 * (r >> 2) + (r & 3) < lim) ? sc[r] : -__builtin_inff();
      }
      float mx = sc[0];
#pragma unroll
      for (int r = 1; r < 16; ++r) mx = fmaxf(mx, sc[r]);
      mx = swap_max(mx);
      const float mn = fmaxf(m, mx);
      const float alpha = __builtin_amdgcn_exp2f((m - mn) * CS);
      mprev = m;
      m = mn;
      const float nmc = -mn * CS;
      float ps = 0.f;
#pragma unroll
      for (int r = 0; r < 16; ++r) { sc[r] = __builtin_amdgcn_exp2f(fmaf(sc[r], CS, nmc)); ps += sc[r]; }
      l = fmaf(l, alpha, ps);
      u32x4 pf[2];
#pragma unroll
      for (int s = 0; s < 2; ++s)
        pf[s] = u32x4{pack2(sc[8 * s], sc[8 * s + 1]), pack2(sc[8 * s + 2], sc[8 * s + 3]), pack2(sc[8 * s + 4], sc[8 * s + 5]), pack2(sc[8 * s + 6], sc[8 * s + 7])};
      if (__builtin_amdgcn_ballot_w64(alpha != 1.0f)) resc |= 1u << k;
      *(LAS float*)(lds + palp + k * 128) = alpha;
      if (k >= KREG || k == nmine - 1) {
        const unsigned dst = ppst + ((k == nmine - 1) ? W_LAST : (k - KREG) * 2048);
        *(LAS u32x4*)(lds + dst) = pf[0];
        *(LAS u32x4*)(lds + dst + 1024) = pf[1];
      } else {
        asm volatile("s_set_gpr_idx_on %8, gpr_idx(DST)\n\t"
                     "v_mov_b32 v128, %0\n\tv_mov_b32 v129, %1\n\tv_mov_b32 v130, %2\n\tv_mov_b32 v131, %3\n\t"
                     "v_mov_b32 v132, %4\n\tv_mov_b32 v133, %5\n\tv_mov_b32 v134, %6\n\tv_mov_b32 v135, %7\n\t"
                     "s_set_gpr_idx_off"
                     :: "v"(pf[0][0]), "v"(pf[0][1]), "v"(pf[0][2]), "v"(pf[0][3]), "v"(pf[1][0]), "v"(pf[1][1]), "v"(pf[1][2]), "v"(pf[1][3]), "s"(8 * k));
      }
    }
  }
  l = swap_sum(l);
  *(LAS f32x2*)(lds + LDS_ML + w * 256 + r32 * 8) = f32x2{m, l};
  LGKM0();
  ctl[CTL_MLOK / 4 + w] = 1;
  STAMP(2);

  const unsigned tq = (lane & 15) >> 2, tp = lane & 3, tblk = (lane >> 4) & 1;
  const unsigned vaddr0 = LDS_RING + (4 * hi + tq) * 256 + ((tq ^ (pos & 3)) << 6) + ((2 * tblk + (tp >> 1)) << 4) + ((tp & 1) << 3);
  f32x16 o[4];
#pragma unroll
  for (int i = 0; i < 4; ++i)
#pragma unroll
    for (int r = 0; r < 16; ++r) o[i][r] = 0.f;
  auto weights = [&]() {
    wait_above<1>(lbase + LDS_CTL + CTL_MLOK + min(lane, NC - 1) * 4, 0);
    unsigned a = LDS_ML + r32 * 8;
    asm volatile("" : "+v"(a) :: "memory");
    float M = -3.0e38f, L = 0.f;
    f32x2 ml[NC];
#pragma unroll
    for (int i = 0; i < NC; ++i) { ml[i] = *(LAS f32x2*)(lds + a + i * 256); M = fmaxf(M, ml[i][0]); }
#pragma unroll
    for (int i = 0; i < NC; ++i) L = fmaf(__builtin_amdgcn_exp2f((ml[i][0] - M) * CS), ml[i][1], L);
    const float inv = 1.0f / L;
    *(LAS float*)(lds + (a >> 1) + (LDS_WT - LDS_ML / 2) + pos * 128) = __builtin_amdgcn_exp2f((mprev - M) * CS) * inv;
    const float ct = __builtin_amdgcn_exp2f((m - M) * CS) * inv;
#pragma unroll
    for (int s = 0; s < 2; ++s) {
      u32x4 x = *(LAS u32x4*)(lds + ppst + W_LAST + s * 1024);
#pragma unroll
      for (int e = 0; e < 4; ++e) x[e] = pack2(__uint_as_float(x[e] << 16) * ct, __uint_as_float(x[e] & 0xffff0000u) * ct);
      *(LAS u32x4*)(lds + ppst + W_LAST + s * 1024) = x;
    }
    LGKM0();
    ctl[CTL_WTOK / 4 + w] = 1;
  };
  resc &= ~1u;
  if (nmine <= 2) weights();
#pragma unroll 1
  for (int k = 0; k < nmine - 1; ++k) {
    if (k == 1) weights();
    if ((resc >> k) & 1) {
      const float alpha = *(LAS float*)(lds + palp + k * 128);
#pragma unroll
      for (int i = 0; i < 4; ++i)
#pragma unroll
        for (int r = 0; r < 16; ++r) o[i][r] *= alpha;
    }
    const unsigned u = NT + w + NC * k;
    u32x4 pf[2];
    if (k >= KREG) {
      pf[0] = *(LAS u32x4*)(lds + ppst + (k - KREG) * 2048);
      pf[1] = *(LAS u32x4*)(lds + ppst + (k - KREG) * 2048 + 1024);
    } else {
      asm volatile("s_set_gpr_idx_on %8, gpr_idx(SRC0)\n\t"
                   "v_mov_b32 %0, v128\n\tv_mov_b32 %1, v129\n\tv_mov_b32 %2, v130\n\tv_mov_b32 %3, v131\n\t"
                   "v_mov_b32 %4, v132\n\tv_mov_b32 %5, v133\n\tv_mov_b32 %6, v134\n\tv_mov_b32 %7, v135\n\t"
                   "s_set_gpr_idx_off\n\ts_nop 1"
                   : "=v"(pf[0][0]), "=v"(pf[0][1]), "=v"(pf[0][2]), "=v"(pf[0][3]), "=v"(pf[1][0]), "=v"(pf[1][1]), "=v"(pf[1][2]), "=v"(pf[1][3])
                   : "s"(8 * k));
    }
    asm volatile("" : "+v"(pf[0]), "+v"(pf[1]), "+v"(o[0]), "+v"(o[1]), "+v"(o[2]), "+v"(o[3]));
    if (PRESLEEP && k > 1) __builtin_amdgcn_s_sleep(2 * PRESLEEP);
    wait_above<CSLEEP>(aland, 4 * u);
    if (TPRIO && k == nmine - 2) __builtin_amdgcn_s_setprio(TPRIO);
    bf16x8 vf[4][2];
#pragma unroll
    for (int i = 0; i < 4; ++i) {
      const unsigned a = (vaddr0 + ((u & 7) << 13)) ^ (i << 6);
#pragma unroll
      for (int s = 0; s < 2; ++s) {
        const s16x4 lo = __builtin_amdgcn_ds_read_tr16_b64_v4i16((LAS s16x4*)(lds + a + 4096 * s));
        const s16x4 up = __builtin_amdgcn_ds_read_tr16_b64_v4i16((LAS s16x4*)(lds + a + 4096 * s + 2048));
        vf[i][s] = __builtin_bit_cast(bf16x8, __builtin_shufflevector(lo, up, 0, 1, 2, 3, 4, 5, 6, 7));
      }
    }
    asm volatile("s_waitcnt lgkmcnt(0)" : "+v"(vf[0][0]), "+v"(vf[0][1]), "+v"(vf[1][0]), "+v"(vf[1][1]), "+v"(vf[2][0]), "+v"(vf[2][1]), "+v"(vf[3][0]), "+v"(vf[3][1]) :: "memory");
    ctl[CTL_FREE / 4 + (u & 7)] = u + 8;
    if (pf_on) prefetch(u);
#pragma unroll
    for (int s = 0; s < 2; ++s)
#pragma unroll
      for (int i = 0; i < 4; ++i) o[i] = __builtin_amdgcn_mfma_f32_32x32x16_bf16(vf[i][s], __builtin_bit_cast(bf16x8, pf[s]), o[i], 0, 0, 0);
  }
  STAMP(3);

  {
    unsigned l16 = lane * 16;
    asm volatile("" : "+v"(l16));
    if (nmine > 1) {
      auto put = [&](unsigned dst, const f32x16& x) {
#pragma unroll
        for (int j = 0; j < 4; ++j) *(LAS f32x4*)(lds + dst + j * 1024) = f32x4{x[4 * j], x[4 * j + 1], x[4 * j + 2], x[4 * j + 3]};
      };
      if (pos > 3) put(LDS_RING + ((2 * NT - 2 * NC + pos) & 7) * 8192 + l16, o[0]);
#pragma unroll
      for (int i = 1; i < 4; ++i) put(LDS_WREG + pos * W_SIZE + (i - 1) * 4096 + l16, o[i]);
      if (TWAIT) LGKM0(); else CBAR();
      ctl[CTL_ODONE / 4 + pos] = 1;
    }
    if (pos < 4) {
      f32x16 of = o[0];
      const unsigned l4 = l16 >> 2, awt = LDS_WT + (l4 & 127);
      wait_above<1>(lbase + LDS_CTL + CTL_WTOK + min(l4, (NC - 1) * 4u), 0);
      auto src_o = [&](int p) -> unsigned {
        const int i = pos ^ (p & 3);
        return l16 + (i == 0 ? LDS_RING + ((2 * NT - 2 * NC + p) & 7) * 8192 : LDS_WREG + p * W_SIZE + (i - 1) * 4096);
      };
      auto fma_o = [&](unsigned src, float c) {
#pragma unroll
        for (int j = 0; j < 4; ++j) {
          const f32x4 x = *(LAS f32x4*)(lds + src + j * 1024);
#pragma unroll
          for (int e = 0; e < 4; ++e) of[4 * j + e] = fmaf(x[e], c, of[4 * j + e]);
        }
      };
      auto mma_t = [&](int p, bf16x8 p0, bf16x8 p1) {
        const unsigned a = vaddr0 + (((2 * NT - NC + p) & 7) << 13);
        bf16x8 vf[2];
#pragma unroll
        for (int s = 0; s < 2; ++s) {
          const s16x4 lo = __builtin_amdgcn_ds_read_tr16_b64_v4i16((LAS s16x4*)(lds + a + 4096 * s));
          const s16x4 up = __builtin_amdgcn_ds_read_tr16_b64_v4i16((LAS s16x4*)(lds + a + 4096 * s + 2048));
          vf[s] = __builtin_bit_cast(bf16x8, __builtin_shufflevector(lo, up, 0, 1, 2, 3, 4, 5, 6, 7));
        }
        of = __builtin_amdgcn_mfma_f32_32x32x16_bf16(vf[0], p0, of, 0, 0, 0);
        of = __builtin_amdgcn_mfma_f32_32x32x16_bf16(vf[1], p1, of, 0, 0, 0);
      };
      {
        const float cw = *(LAS float*)(lds + awt + pos * 128);
#pragma unroll
        for (int r = 0; r < 16; ++r) of[r] *= cw;
      }
      if (NT >= 2 * NC) {
        float co[5];
        bf16x8 pl[NC][2];
        auto ld_p = [&](int p) {
          pl[p][0] = *(LAS bf16x8*)(lds + LDS_WREG + p * W_SIZE + W_LAST + l16);
          pl[p][1] = *(LAS bf16x8*)(lds + LDS_WREG + p * W_SIZE + W_LAST + l16 + 1024);
        };
#define PIN_OF() asm volatile("" : "+v"(of))
#pragma unroll
        for (int i = 1; i < 4; ++i) co[i - 1] = *(LAS float*)(lds + awt + (pos ^ i) * 128);
        co[3] = *(LAS float*)(lds + awt + 4 * 128);
        co[4] = *(LAS float*)(lds + awt + 5 * 128);
        ld_p(0); ld_p(1); ld_p(2);
        PIN_OF();
        wait_above<TSLEEP>(lbase + LDS_CTL + CTL_ODONE + (l4 & 12), 0);
#pragma unroll
        for (int i = 1; i < 4; ++i) fma_o(LDS_WREG + (pos ^ i) * W_SIZE + (i - 1) * 4096 + l16, co[i - 1]);
        PIN_OF();
        ld_p(3); ld_p(4); ld_p(5);
        wait_above<TSLEEP>(aland, 4 * (2 * NT - NC + 2));
        mma_t(0, pl[0][0], pl[0][1]);
        mma_t(1, pl[1][0], pl[1][1]);
        mma_t(2, pl[2][0], pl[2][1]);
        STAMP(4);
        wait_above<TSLEEP>(lbase + LDS_CTL + CTL_ODONE + 16 + (l4 & 4), 0);
        fma_o(src_o(4), co[3]);
        fma_o(src_o(5), co[4]);
        PIN_OF();
        wait_above<0>(aland, 4 * (2 * NT - NC + 4));
        mma_t(3, pl[3][0], pl[3][1]);
        mma_t(4, pl[4][0], pl[4][1]);
        PIN_OF();
        wait_above<0>(aland, 4 * (2 * NT - NC + 5));
        mma_t(5, pl[5][0], pl[5][1]);
      } else {
#pragma unroll 1
        for (int p = max(NC - NT, 0); p < NC; ++p) {
          if (p != pos && NT - 2 * NC + p >= 0) {
            wait_above<1>(lbase + LDS_CTL + CTL_ODONE + p * 4, 0);
            fma_o(src_o(p), *(LAS float*)(lds + awt + p * 128));
          }
          wait_above<1>(aland, 4 * (2 * NT - NC + p));
          mma_t(p, *(LAS bf16x8*)(lds + LDS_WREG + p * W_SIZE + W_LAST + l16), *(LAS bf16x8*)(lds + LDS_WREG + p * W_SIZE + W_LAST + l16 + 1024));
        }
      }
      const __amdgpu_buffer_rsrc_t ro = __builtin_amdgcn_make_buffer_rsrc(outp, 0, NB * HQ * SQ * HD * 2, 0x00020000);
      const unsigned orow = (unsigned)(b * HQ + 2 * h) * SQ * HD * 2 + pos * 64 + ((l16 & 511) << 4) + ((l16 >> 5) & 16);
#pragma unroll
      for (int j = 0; j < 2; ++j) {
        const unsigned a0 = pack2(of[8 * j], of[8 * j + 1]), a1 = pack2(of[8 * j + 2], of[8 * j + 3]);
        const unsigned b0 = pack2(of[8 * j + 4], of[8 * j + 5]), b1 = pack2(of[8 * j + 6], of[8 * j + 7]);
        auto s0 = __builtin_amdgcn_permlane32_swap(a0, b0, false, false);
        auto s1 = __builtin_amdgcn_permlane32_swap(a1, b1, false, false);
        u32x4 val = {s0[0], s1[0], s0[1], s1[1]};
        __builtin_amdgcn_raw_buffer_store_b128(val, ro, orow + j * 32, 0, OUTAUX);
      }
    }
  }
}
}

extern "C" void kernel_launch(void* const* d_in, const int* in_sizes, int n_in, void* d_out, int out_size, void* d_ws,
                              size_t ws_size, hipStream_t stream) {
  (void)n_in; (void)out_size; (void)ws_size;
  const unsigned nphys = (unsigned)(in_sizes[1] / (PAGE * HKV * HD));
  const unsigned kvbytes = nphys * PAGE_BYTES;
  (void)hipFuncSetAttribute((const void*)paged_attn, hipFuncAttributeMaxDynamicSharedMemorySize, LDS_TOTAL);
  hipLaunchKernelGGL(paged_attn, dim3(256), dim3(512), LDS_TOTAL, stream, d_in[0], d_in[1], d_in[2], (const int*)d_in[3],
                     (const int*)d_in[4], d_out, (char*)d_ws, kvbytes, nphys);
}
```

```cpp
#define SOFT 35
#include <hip/hip_runtime.h>
#include <hip/hip_bf16.h>

#define PROBE_G 0
#define QPOL "nt"
#define PF_2ND
#define TLBW 29
#define TLBG 0x200000u
#define SOFT_AT 0
#define TSHORT 1
#define TDELAY 0
#define PF_BY 0
#define PF_END 2u
#define PF_X64B 72
#define PF_DENSE 1
#define PF_PER 4
#define PF_PSTEP 2
#define PACE_X16 440
#define GAP_TICKS 0
#define OUTAUX 0
#define HOFF 4
#define LSLEEP 0
#define PRESLEEP 0
#define PF_EVERY 2
#define PF_X64 76
#define PF_ALL 0
#define PF_LEAD 380
#define PF_POL ""
#define PF_SLOW 0
#define PF_RUN 3
#define TSLEEP 0
#define TWAIT 1
#define TPRIO 0
#define CSLEEP 1

#define STAMP(i) do {} while (0)

namespace {
constexpr int NB = 16, HQ = 32, HKV = 16, SQ = 16, HD = 128, PAGE = 16, NBPS = 256, SKV = NBPS * PAGE;
constexpr unsigned PAGE_BYTES = PAGE * HKV * HD * 2;
constexpr unsigned INVALID = 0x80000000u;
constexpr int NC = 6;
constexpr int KREG = 16, KLDS = 5;
static_assert(NC * (KREG + KLDS + 1) >= NBPS / 2, "P storage");
constexpr unsigned LDS_RING = 0;
constexpr unsigned LDS_WREG = 65536;
constexpr unsigned W_ALP = KLDS * 2048;
constexpr unsigned W_LAST = W_ALP + (KREG + KLDS + 1) * 128;
constexpr unsigned W_SIZE = W_LAST + 2048;
static_assert(W_LAST >= 3 * 4096 && W_SIZE % 256 == 0, "consumer region");
constexpr unsigned LDS_TBL = LDS_WREG + 2 * W_SIZE;
constexpr unsigned LDS_Q = LDS_WREG;
constexpr unsigned LDS_ML = LDS_WREG + NC * W_SIZE;
constexpr unsigned LDS_WT = LDS_ML + NC * 256;
constexpr unsigned LDS_CTL = LDS_WT + NC * 128;
constexpr unsigned CTL_LAND = 0;
constexpr unsigned CTL_QOK = 16;
constexpr unsigned CTL_FREE = 32;
constexpr unsigned CTL_MLOK = 64;
constexpr unsigned CTL_ODONE = 96;
constexpr unsigned CTL_WTOK = 128;
constexpr unsigned LDS_DUMMY = LDS_CTL + 256;
constexpr unsigned LDS_TOTAL = LDS_DUMMY + 256;
static_assert(LDS_TOTAL <= 163840, "lds");

typedef __attribute__((ext_vector_type(8))) __bf16 bf16x8;
typedef __attribute__((ext_vector_type(2))) __bf16 bf16x2;
typedef __attribute__((ext_vector_type(4))) short s16x4;
typedef __attribute__((ext_vector_type(16))) float f32x16;
typedef __attribute__((ext_vector_type(4))) float f32x4;
typedef __attribute__((ext_vector_type(2))) float f32x2;
typedef __attribute__((ext_vector_type(4))) unsigned u32x4;
typedef __attribute__((ext_vector_type(2))) unsigned u32x2;
typedef __attribute__((ext_vector_type(4))) int i32x4;
#define LAS __attribute__((address_space(3)))

__device__ __forceinline__ float swap_max(float x) {
  auto r = __builtin_amdgcn_permlane32_swap(__float_as_uint(x), __float_as_uint(x), false, false);
  return fmaxf(__uint_as_float(r[0]), __uint_as_float(r[1]));
}
__device__ __forceinline__ float swap_sum(float x) {
  auto r = __builtin_amdgcn_permlane32_swap(__float_as_uint(x), __float_as_uint(x), false, false);
  return __uint_as_float(r[0]) + __uint_as_float(r[1]);
}
__device__ __forceinline__ unsigned pack2(float a, float b) {
  bf16x2 v = {(__bf16)a, (__bf16)b};
  return __builtin_bit_cast(unsigned, v);
}
template <int CTRL> __device__ __forceinline__ int dpp_add(int x) { return x + __builtin_amdgcn_update_dpp(0, x, CTRL, 0xf, 0xf, true); }
__device__ __forceinline__ unsigned now32() { return (unsigned)__builtin_amdgcn_s_memrealtime(); }
__device__ __forceinline__ int to_sgpr(int v) {
  int r;
  asm volatile("s_nop 1\n\tv_readfirstlane_b32 %0, %1" : "=s"(r) : "v"(v));
  return r;
}
#define LGKM0() asm volatile("s_waitcnt lgkmcnt(0)" ::: "memory")
#define VMWAIT(n) asm volatile("s_waitcnt vmcnt(" #n ")" ::: "memory")
#define CBAR() asm volatile("" ::: "memory")
template <int SLEEP> __device__ __forceinline__ void wait_above(unsigned addr, unsigned u) {
  unsigned v, cnt;
  if (SLEEP)
    asm volatile("s_mov_b32 %1, 0x200000\n\ts_branch 2f\n"
                 "1:\n\ts_sleep %4\n"
                 "2:\n\tds_read_b32 %0, %2\n\ts_sub_u32 %1, %1, 1\n\ts_waitcnt lgkmcnt(0)\n\t"
                 "v_cmp_ge_u32 vcc, %3, %0\n\ts_cbranch_scc1 3f\n\ts_nop 1\n\ts_cbranch_vccnz 1b\n3:"
                 : "=&v"(v), "=&s"(cnt) : "v"(addr), "s"(u), "n"(SLEEP) : "memory", "scc", "vcc");
  else
    asm volatile("s_mov_b32 %1, 0x200000\n"
                 "1:\n\tds_read_b32 %0, %2\n\ts_sub_u32 %1, %1, 1\n\ts_waitcnt lgkmcnt(0)\n\t"
                 "v_cmp_ge_u32 vcc, %3, %0\n\ts_cbranch_scc1 3f\n\ts_nop 1\n\ts_cbranch_vccnz 1b\n3:"
                 : "=&v"(v), "=&s"(cnt) : "v"(addr), "s"(u) : "memory", "scc", "vcc");
}

__global__ void __launch_bounds__(512, 1) __attribute__((amdgpu_num_vgpr(64)))
paged_attn(const void* __restrict__ qp, const void* __restrict__ kp, const void* __restrict__ vp, const int* __restrict__ bt,
           const int* __restrict__ lens, void* __restrict__ outp, char* __restrict__ ws, unsigned kvbytes, unsigned nphys) {
  extern __shared__ __attribute__((aligned(16))) char smem[];
  LAS char* lds = (LAS char*)smem;
  const int lane0 = threadIdx.x & 63;
  const int wv = __builtin_amdgcn_readfirstlane(threadIdx.x >> 6);
  const int g = blockIdx.x;
  const int h = g >> 4;
  constexpr float CS = 0.08838834764831845f * 1.4426950408889634f;
  volatile LAS unsigned* ctl = (volatile LAS unsigned*)(lds + LDS_CTL);

  const unsigned want = (g & 8) ? 15 - (g & 7) : (g & 7);
  unsigned lm1, rank;
  auto ranking = [&](int lane, unsigned& lr, u32x4& lq) __attribute__((always_inline)) {
    asm volatile("" : "+v"(lr), "+v"(lq));
    lm1 = min(lr - 1u, (unsigned)SKV - 1u);
    const unsigned key = lm1 * 16 + (lane & 15);
    rank = 0;
#pragma unroll
    for (int j = 0; j < 4; ++j) rank += (min(lq[j] - 1u, (unsigned)SKV - 1u) * 16 + ((lane >> 4) * 4 + j) > key) ? 1u : 0u;
    auto r1 = __builtin_amdgcn_permlane16_swap(rank, rank, false, false);
    rank = r1[0] + r1[1];
    auto r2 = __builtin_amdgcn_permlane32_swap(rank, rank, false, false);
    rank = r2[0] + r2[1];
  };
  auto total = [&]() __attribute__((always_inline)) {
    int t = lm1 + 1;
    t = dpp_add<0x121>(t); t = dpp_add<0x122>(t); t = dpp_add<0x124>(t); t = dpp_add<0x128>(t);
    return t;
  };

  if (__builtin_expect(wv < 2, 1)) {
    const int half = wv, lane = lane0;
    unsigned lr = lens[lane & 15];
    u32x4 lq = *(const u32x4*)(lens + (lane >> 4) * 4);
    unsigned first = bt[(lane & 15) * NBPS + half];
    __builtin_amdgcn_s_setprio(3);
    const unsigned rq4 = lane >> 4;
    unsigned vo0 = rq4 * 4096 + (((unsigned)(lane & 15) ^ (rq4 << 2)) << 4) + h * (HD * 2);
    unsigned vo1 = (vo0 + 16384 - 1024) ^ 16, vo2 = (vo0 + 32768 - 2048) ^ 32, vo3 = (vo0 + 49152 - 3072) ^ 48;
    unsigned vland = (unsigned)(size_t)lds + LDS_CTL + CTL_LAND + half * 4;
    unsigned vfree = (unsigned)(size_t)lds + LDS_CTL + CTL_FREE + (lane & 7) * 4;
    const unsigned ring = (unsigned)(size_t)lds + LDS_RING + half * 4096;
    const __amdgpu_buffer_rsrc_t rk = __builtin_amdgcn_make_buffer_rsrc((void*)kp, 0, (int)kvbytes, 0x00020000);
    asm volatile("" : "+v"(vo0), "+v"(vo1), "+v"(vo2), "+v"(vo3), "+v"(vland), "+v"(vfree) : "s"(rk), "s"(vp), "s"(nphys), "s"(ring));
    ranking(lane, lr, lq);
    first = min(first, nphys - 1) * PAGE_BYTES;
    const int b = __builtin_ctz((unsigned)__builtin_amdgcn_ballot_w64(rank == want) | 0x8000u);
    unsigned long long tstart;
    asm volatile("s_waitcnt vmcnt(0)\n\t"
                 "s_mov_b32 m0, %2\n\t"
                 "s_memrealtime %0\n\t"
                 "s_nop 2\n\t"
                 "buffer_load_dwordx4 %4, %1, %3 offen nt lds\n\t"
                 "buffer_load_dwordx4 %5, %1, %3 offen offset:1024 nt lds\n\t"
                 "buffer_load_dwordx4 %6, %1, %3 offen offset:2048 nt lds\n\t"
                 "buffer_load_dwordx4 %7, %1, %3 offen offset:3072 nt lds\n\t"
                 "s_waitcnt lgkmcnt(0)\n\t"
                 "s_barrier"
                 : "=&s"(tstart) : "s"(rk), "s"(ring), "s"(__builtin_amdgcn_readlane(first, b)), "v"(vo0), "v"(vo1), "v"(vo2), "v"(vo3) : "memory");
    __builtin_amdgcn_sched_barrier(0);
    asm volatile("" : "+v"(lm1));
    const int len = __builtin_amdgcn_readlane(lm1, b) + 1, nown = (len + PAGE - 1) / PAGE, NT = (nown + 1) >> 1;
    unsigned l0 = *(LAS unsigned*)(lds + LDS_TBL + (b * NBPS + min(2 * lane + half, nown - 1)) * 4);
    unsigned l1 = *(LAS unsigned*)(lds + LDS_TBL + (b * NBPS + min(2 * lane + half + 128, nown - 1)) * 4);
    const int T16 = total() * PACE_X16;
    const int step = to_sgpr((int)((float)T16 * __builtin_amdgcn_rcpf((float)max(len, 32))));
    const unsigned due0 = ((unsigned)tstart << 8) + half * ((step * HOFF) >> 3);
    const int ua = SOFT_AT ? to_sgpr(min(min(NT, 64), 1 + (int)((float)(SOFT_AT * 256) * __builtin_amdgcn_rcpf((float)step)))) : 1;
    l0 = min(l0, nphys - 1) * PAGE_BYTES;
    l1 = min(l1, nphys - 1) * PAGE_BYTES;
    const unsigned lastrec = (unsigned)__builtin_amdgcn_readlane(NT > 64 ? l1 : l0, (NT - 1) & 63) + (unsigned)to_sgpr(min(max(len - (2 * (NT - 1) + half) * PAGE, 0), PAGE)) * 4096u;
#define CNT(r)
    unsigned vf, vt, lc;
    asm volatile(
        "s_waitcnt lgkmcnt(0)\n\t"
        "s_memrealtime s[68:69]\n\t"
        "ds_read_b32 %[vf], %[vfree]\n\t"
        "s_mov_b32 s60, 1\n\t"
        "s_mov_b32 s61, 4\n\t"
        "s_mov_b32 s62, 0x400000\n\t"
        "s_mov_b32 s66, 0\n\t"
        "s_mov_b32 s67, %[ua]\n\t"
        "s_mov_b32 s72, 0x2000\n\t"
        "s_mov_b32 s73, 1\n\t"
        "s_mov_b32 s74, %[due1]\n\t"
        "s_mov_b64 s[76:77], %[kp]\n\t"
        "s_mov_b32 s78, %[nrec]\n\t"
        "s_mov_b32 s79, 0x20000\n\t"
        "s_and_b32 s77, s77, 0xffff\n\t"
        "v_mov_b32 %[lc], %[l0]\n\t"
        "s_getpc_b64 s[70:71]\n"
        ".LldA%=:\n\t"
        "s_add_u32 s80, s70, .LldL%=-.LldA%=\n\t"
        "s_addc_u32 s71, s71, 0\n\t"
        "s_branch .LldF%=\n"
        ".LldR%=:\n\t"
        "s_sub_u32 s62, s62, 1\n\t"
        "s_cbranch_scc1 .LldE%=\n"
        ".LldT%=:\n\t"
        "s_getreg_b32 s64, hwreg(HW_REG_IB_STS)\n\t"
        "s_and_b32 s65, s64, 15\n\t"
        "s_bfe_u32 s64, s64, 0x20016\n\t"
        "s_lshl4_add_u32 s64, s64, s65\n\t"
        "s_add_u32 s64, s64, 3\n\t"
        "s_and_b32 s64, s64, 60\n\t"
        "s_sub_u32 s65, s61, s64\n\t"
        "s_cmp_lg_u32 s65, s66\n\t"
        "s_cbranch_scc1 .LldP%=\n"
        ".LldC%=:\n\t"
        "s_waitcnt lgkmcnt(0)\n\t"
        "v_cmp_eq_u32 vcc, s60, %[vf]\n\t"
        "s_lshl_b32 s64, s68, 8\n\t"
        "s_memrealtime s[68:69]\n\t"
        "ds_read_b32 %[vf], %[vfree]\n\t"
        CNT("s82")
        "s_sub_u32 s64, s64, s74\n\t"
        "s_cmp_lt_i32 s64, 0\n\t"
        "s_cbranch_scc1 .LldR%=\n\t"
        CNT("s83")
        "s_cbranch_vccz .LldR%=\n\t"
        CNT("s84")
        "buffer_load_dwordx4 %[o0], s[76:79], s63 offen nt lds\n\t"
        "s_add_u32 s74, s74, %[step]\n\t"
        "s_add_u32 s73, s73, 1\n\t"
        "buffer_load_dwordx4 %[o1], s[76:79], s63 offen offset:1024 nt lds\n\t"
        "s_add_u32 s60, s60, 1\n\t"
        "s_add_u32 s61, s61, 4\n\t"
        "buffer_load_dwordx4 %[o2], s[76:79], s63 offen offset:2048 nt lds\n\t"
        "s_add_u32 s72, s72, 0x2000\n\t"
        "s_and_b32 s72, s72, 0xe000\n\t"
        "buffer_load_dwordx4 %[o3], s[76:79], s63 offen offset:3072 nt lds\n"
        ".LldF%=:\n\t"
        "v_readlane_b32 s63, %[lc], s73\n\t"
        "s_add_u32 m0, s72, %[ring]\n\t"
        "s_cmp_eq_u32 s73, %[ntm]\n\t"
        "s_cselect_b32 s78, %[last], %[nrec]\n\t"
        "s_cmp_lg_u32 s60, s67\n\t"
        "s_cbranch_scc1 .LldT%=\n\t"
        "s_cmp_lg_u32 s60, %[ua]\n\t"
        "s_cbranch_scc1 .LldG%=\n\t"
        "s_add_u32 s74, s74, %[soft]\n\t"
        "s_min_u32 s67, %[nt], 64\n\t"
        "s_cmp_lg_u32 s60, s67\n\t"
        "s_cbranch_scc1 .LldT%=\n"
        ".LldG%=:\n\t"
        "s_cmp_eq_u32 s73, %[nt]\n\t"
        "s_cbranch_scc1 .LldV%=\n\t"
        "v_mov_b32 %[lc], %[l1]\n\t"
        "v_readfirstlane_b32 s63, %[l1]\n\t"
        "s_add_u32 s67, s60, %[nt]\n\t"
        "s_sub_u32 s67, s67, 64\n\t"
        "s_branch .LldT%=\n"
        ".LldV%=:\n\t"
        "s_cmp_lg_u32 s60, %[nt]\n\t"
        "s_cbranch_scc1 .LldD%=\n\t"
        "s_mov_b64 s[76:77], %[vp]\n\t"
        "s_and_b32 s77, s77, 0xffff\n\t"
        "s_mov_b32 s73, 0\n\t"
        "s_min_u32 s67, %[nt], 64\n\t"
        "s_add_u32 s67, s67, s60\n\t"
        "s_mov_b32 s74, %[duev]\n\t"
        "s_cmp_eq_u32 %[ntm], 0\n\t"
        "s_cselect_b32 s78, %[last], %[nrec]\n\t"
        "v_mov_b32 %[lc], %[l0]\n\t"
        "v_readfirstlane_b32 s63, %[l0]\n\t"
        "v_xor_b32 %[o1], 16, %[o1]\n\t"
        "v_xor_b32 %[o2], 32, %[o2]\n\t"
        "v_xor_b32 %[o3], 48, %[o3]\n\t"
        "s_branch .LldT%=\n"
        ".LldD%=:\n\t"
        "s_add_u32 s74, s74, 0x40000000\n\t"
        "s_mov_b32 s60, -1\n\t"
        "s_branch .LldT%=\n"
        ".LldP%=:\n\t"
        "s_min_u32 s64, s64, 32\n\t"
        "s_add_u32 s70, s80, s64\n\t"
        "v_mov_b32 %[vt], s65\n\t"
        "s_mov_b32 s66, s65\n\t"
        "s_setpc_b64 s[70:71]\n\t"
        ".p2align 6\n"
        ".LldL%=:\n\t"
        "s_waitcnt vmcnt(0)\n\t"
        "s_waitcnt vmcnt(4)\n\t"
        "s_waitcnt vmcnt(8)\n\t"
        "s_waitcnt vmcnt(12)\n\t"
        "s_waitcnt vmcnt(16)\n\t"
        "s_waitcnt vmcnt(20)\n\t"
        "s_waitcnt vmcnt(24)\n\t"
        "s_waitcnt vmcnt(28)\n\t"
        "s_waitcnt vmcnt(32)\n\t"
        "ds_write_b32 %[vland], %[vt]\n\t"
        "s_cmp_lg_u32 s65, %[fin]\n\t"
        "s_cbranch_scc1 .LldC%=\n"
        ".LldE%=:\n\t"
        "s_waitcnt vmcnt(0) lgkmcnt(0)\n\t"
        : [vf] "=&v"(vf), [vt] "=&v"(vt), [lc] "=&v"(lc), [o1] "+v"(vo1), [o2] "+v"(vo2), [o3] "+v"(vo3)
        : [l0] "v"(l0), [l1] "v"(l1), [o0] "v"(vo0), [vland] "v"(vland), [vfree] "v"(vfree),
          [kp] "s"(kp), [vp] "s"(vp), [nrec] "s"(kvbytes), [nt] "s"(NT), [ring] "s"(ring),
          [step] "s"(step), [due1] "s"(due0 + step), [duev] "s"(due0 + (to_sgpr(T16) >> 5) + (SOFT + GAP_TICKS) * 256), [last] "s"(lastrec), [ntm] "s"(NT - 1), [fin] "s"(8 * NT), [ua] "s"(ua), [soft] "n"(SOFT * 256)
        : "memory", "scc", "vcc", "s60", "s61", "s62", "s63", "s64", "s65", "s66", "s67", "s68", "s69", "s70", "s71", "s72", "s73", "s74",
          "s76", "s77", "s78", "s79", "s80", "s82", "s83", "s84");
    STAMP(3);
    asm volatile("s_endpgm" ::: "memory");
  }

  int lane = lane0;
  asm volatile("" : "+v"(lane));
  auto touch = [&](const void* base, unsigned bytes, unsigned gran) __attribute__((always_inline)) {
    const __amdgpu_buffer_rsrc_t rt = __builtin_amdgcn_make_buffer_rsrc((void*)base, 0, (int)bytes, 0x00020000);
    const unsigned idx = TSHORT == 2 ? (g >> 5) + 8 * lane : TSHORT ? (g >> 4) + 16 * lane : (g >> 3) + 32 * lane;
    const unsigned vo = (idx < 0x80000000u / gran) ? idx * gran + ((idx * 30592u) & (gran - 128)) : INVALID;
    asm volatile("s_mov_b32 m0, %0\n\ts_nop 4\n\tbuffer_load_dword %2, %1, 0 offen nt lds" :: "s"((unsigned)(size_t)lds + LDS_DUMMY), "s"(rt), "v"(vo) : "memory");
  };
  const bool toucher = TSHORT == 2 ? (g & 24) == 8 : TSHORT ? (g & 8) != 0 : true;
  unsigned lraw = lens[lane & 15];
  u32x4 lq = *(const u32x4*)(lens + (lane >> 4) * 4);
  if (wv < 6) {
    const __amdgpu_buffer_rsrc_t rb = __builtin_amdgcn_make_buffer_rsrc((void*)bt, 0, NB * NBPS * 4, 0x00020000);
    asm volatile("s_mov_b32 m0, %0\n\ts_nop 4\n\tbuffer_load_dwordx4 %2, %1, 0 offen lds\n\t"
                 "buffer_load_dwordx4 %2, %1, 0 offen offset:1024 lds\n\t"
                 "buffer_load_dwordx4 %2, %1, 0 offen offset:2048 lds\n\t"
                 "buffer_load_dwordx4 %2, %1, 0 offen offset:3072 lds\n\t"
                 "s_waitcnt vmcnt(0)"
                 :: "s"((unsigned)(size_t)lds + LDS_TBL + (wv - 2) * 4096), "s"(rb), "v"(lane * 16 + (wv - 2) * 4096) : "memory");
  } else if (wv == 6) {
    if (TDELAY) __builtin_amdgcn_s_sleep(TDELAY);
    if (toucher) {
      if (TLBW & 1) touch(kp, kvbytes, TLBG);
      if (TLBW & 2) touch(vp, kvbytes, TLBG);
      if (TLBW & 16) touch(qp, NB * HQ * SQ * HD * 2, 0x80000u);
    }
    if (lane < 40) ctl[lane] = ((lane >> 3) == CTL_FREE / 32) ? (lane & 7) : 0u;
  }
  if (wv != 7) asm volatile("s_waitcnt lgkmcnt(0)\n\ts_barrier" ::: "memory");
  ranking(lane, lraw, lq);
  const int b = __builtin_ctz((unsigned)__builtin_amdgcn_ballot_w64(rank == want) | 0x8000u);
  if (wv == 7) {
    const __amdgpu_buffer_rsrc_t rq = __builtin_amdgcn_make_buffer_rsrc((void*)qp, 0, NB * HQ * SQ * HD * 2, 0x00020000);
    const unsigned q0 = (((int)__builtin_amdgcn_readlane(lraw, b) > 0) ? (unsigned)((b * HQ + 2 * h) * SQ * HD * 2) : INVALID) + (lane >> 4) * 256 +
                        (((unsigned)(lane & 15) ^ ((lane >> 4) << 2)) << 4);
    asm volatile("s_mov_b32 m0, %0\n\ts_nop 4\n\t"
                 "buffer_load_dwordx4 %2, %1, 0 offen " QPOL " lds\n\t"
                 "buffer_load_dwordx4 %3, %1, 0 offen offset:1024 " QPOL " lds\n\t"
                 "buffer_load_dwordx4 %4, %1, 0 offen offset:2048 " QPOL " lds\n\t"
                 "buffer_load_dwordx4 %5, %1, 0 offen offset:3072 " QPOL " lds\n\t"
                 "s_add_u32 m0, m0, 4096\n\ts_nop 0\n\t"
                 "buffer_load_dwordx4 %2, %1, %6 offen " QPOL " lds\n\t"
                 "buffer_load_dwordx4 %3, %1, %6 offen offset:1024 " QPOL " lds\n\t"
                 "buffer_load_dwordx4 %4, %1, %6 offen offset:2048 " QPOL " lds\n\t"
                 "buffer_load_dwordx4 %5, %1, %6 offen offset:3072 " QPOL " lds\n\t"
                 "s_barrier\n\t"
                 "s_waitcnt vmcnt(0)"
                 :: "s"((unsigned)(size_t)lds + LDS_Q), "s"(rq), "v"(q0), "v"(q0 ^ 16), "v"(q0 ^ 32), "v"(q0 ^ 48), "s"(4096) : "memory", "scc");
    ctl[CTL_QOK / 4] = 1;
  }
  const int bp = __builtin_ctz((unsigned)__builtin_amdgcn_ballot_w64(rank == 15 - want) | 0x8000u);
  const int lim = __builtin_amdgcn_readlane(lm1, b) + 1;
  const int nown = (lim + PAGE - 1) / PAGE, NT = (nown + 1) >> 1;
  const int tot = total();
  const int w = wv - 2;
  const int r32 = lane & 31, hi = lane >> 5;
  const int nmine = (NT > w) ? (NT - w + NC - 1) / NC : 0;
  const unsigned lbase = (unsigned)(size_t)lds;
  const unsigned aland = lbase + LDS_CTL + CTL_LAND + (lane & 1) * 4;
  const int npl = (__builtin_amdgcn_readlane(lm1, bp) >> 4) + 1, NTL = (npl + 1) >> 1;
  const int tots = __builtin_amdgcn_readfirstlane(tot);
  const bool pf_on = PF_EVERY && npl * 256 * 64 > tots * PF_X64B && npl > nown && (h & 3) == PF_BY;
  const unsigned pf_max = (npl * 256 * 64 > tots * PF_X64) ? 256u : PF_END;
  const unsigned pf_last = 2 * NT - to_sgpr((int)((float)(2 * NT * PF_LEAD) * 4096.0f * __builtin_amdgcn_rcpf((float)(tot * PACE_X16))));
  const unsigned pf_ratio = to_sgpr((int)((float)NTL * 65536.0f * __builtin_amdgcn_rcpf((float)NT)));
  const unsigned pf_per = PF_PER + (g & 7) * PF_PSTEP, pf_inv = (65536 + pf_per - 1) / pf_per;
  auto prefetch = [&](unsigned op) {
    const unsigned d = pf_last - op;
    if (d >= 1024u) return;
    const unsigned r = d - ((d * pf_inv) >> 16) * pf_per;
    if (r >= PF_RUN) return;
    unsigned tl = 2 * NTL - 1 - (((d - r) * pf_ratio) >> 16) - r;
    if (tl >= 2u * NTL) return;
    const bool isv = tl >= (unsigned)NTL;
    tl -= isv ? NTL : 0;
    const unsigned p0 = min((unsigned)bt[bp * NBPS + 2 * tl], nphys - 1), p1 = min((unsigned)bt[bp * NBPS + min(2 * tl + 1, (unsigned)npl - 1)], nphys - 1);
    const __amdgpu_buffer_rsrc_t rp = __builtin_amdgcn_make_buffer_rsrc(isv ? (void*)vp : (void*)kp, 0, (int)kvbytes, 0x00020000);
    unsigned ln = lane;
    asm volatile("" : "+v"(ln));
    const unsigned vo = ((ln & 32) ? p1 : p0) * PAGE_BYTES + ((h & 12) | 1) * (HD * 2) + ((ln >> 1) & 15) * 4096 + (PF_SLOW == 1 ? 128 : PF_SLOW == 2 ? 0 : (ln & 1) * 128);
    asm volatile("s_mov_b32 m0, %0\n\ts_nop 4\n\tbuffer_load_dword %2, %1, 0 offen " PF_POL " lds" PF_2ND :: "s"(lbase + LDS_DUMMY), "s"(rp), "v"(vo) : "memory");
  };
  const int pos = (w + NC * 22 - NT) % NC;
  const unsigned palp = LDS_WREG + pos * W_SIZE + W_ALP + r32 * 4;
  const unsigned ppst = LDS_WREG + pos * W_SIZE + lane * 16;
  asm volatile("" ::: "v128", "v255");
  unsigned resc = 0;
  float m = -3.0e38f, mprev = -3.0e38f, l = 0.f;

  {
    const unsigned fk = (((unsigned)r32 & 3) << 2) | (((unsigned)r32 >> 2) & 3);
    const unsigned kaddr0 = LDS_RING + r32 * 256 + (((unsigned)hi ^ fk) << 4);
    bf16x8 qf[8];
    wait_above<1>(lbase + LDS_CTL + CTL_QOK, 0);
#pragma unroll
    for (int s = 0; s < 8; ++s) qf[s] = *(LAS bf16x8*)(lds + ((kaddr0 + LDS_Q - LDS_RING) ^ (s << 5)));
#pragma unroll 1
    for (int k = 0; k < nmine; ++k) {
      const unsigned t = w + NC * k;
      if (PRESLEEP && k > 0) __builtin_amdgcn_s_sleep(PRESLEEP);
      wait_above<CSLEEP>(aland, 4 * t);
      if (k == 0) STAMP(1);
      bf16x8 kf[8];
#pragma unroll
      for (int s = 0; s < 8; ++s) kf[s] = *(LAS bf16x8*)(lds + ((kaddr0 + ((t & 7) << 13)) ^ (s << 5)));
      asm volatile("s_waitcnt lgkmcnt(0)" : "+v"(kf[0]), "+v"(kf[1]), "+v"(kf[2]), "+v"(kf[3]), "+v"(kf[4]), "+v"(kf[5]), "+v"(kf[6]), "+v"(kf[7]) :: "memory");
      ctl[CTL_FREE / 4 + (t & 7)] = t + 8;
      if (pf_on) prefetch(t);
      if ((TLBW & 4) && t == 16 && toucher) { touch(vp, kvbytes, TLBG); if (TLBW & 8) touch(outp, NB * HQ * SQ * HD * 2, 0x80000u); }
      f32x16 sc;
#pragma unroll
      for (int r = 0; r < 16; ++r) sc[r] = 0.f;
#pragma unroll
      for (int s = 0; s < 8; ++s) sc = __builtin_amdgcn_mfma_f32_32x32x16_bf16(kf[s], qf[s], sc, 0, 0, 0);
      if ((int)t == NT - 1) {
        const int kb = 2 * t * PAGE + 4 * hi;
#pragma unroll
        for (int r = 0; r < 16; ++r) sc[r] = (kb + 8 * (r >> 2) + (r & 3) < lim) ? sc[r] : -__builtin_inff();
      }
      float mx = sc[0];
#pragma unroll
      for (int r = 1; r < 16; ++r) mx = fmaxf(mx, sc[r]);
      mx = swap_max(mx);
      const float mn = fmaxf(m, mx);
      const float alpha = __builtin_amdgcn_exp2f((m - mn) * CS);
      mprev = m;
      m = mn;
      const float nmc = -mn * CS;
      float ps = 0.f;
#pragma unroll
      for (int r = 0; r < 16; ++r) { sc[r] = __builtin_amdgcn_exp2f(fmaf(sc[r], CS, nmc)); ps += sc[r]; }
      l = fmaf(l, alpha, ps);
      u32x4 pf[2];
#pragma unroll
      for (int s = 0; s < 2; ++s)
        pf[s] = u32x4{pack2(sc[8 * s], sc[8 * s + 1]), pack2(sc[8 * s + 2], sc[8 * s + 3]), pack2(sc[8 * s + 4], sc[8 * s + 5]), pack2(sc[8 * s + 6], sc[8 * s + 7])};
      if (__builtin_amdgcn_ballot_w64(alpha != 1.0f)) resc |= 1u << k;
      *(LAS float*)(lds + palp + k * 128) = alpha;
      if (k >= KREG || k == nmine - 1) {
        const unsigned dst = ppst + ((k == nmine - 1) ? W_LAST : (k - KREG) * 2048);
        *(LAS u32x4*)(lds + dst) = pf[0];
        *(LAS u32x4*)(lds + dst + 1024) = pf[1];
      } else {
        asm volatile("s_set_gpr_idx_on %8, gpr_idx(DST)\n\t"
                     "v_mov_b32 v128, %0\n\tv_mov_b32 v129, %1\n\tv_mov_b32 v130, %2\n\tv_mov_b32 v131, %3\n\t"
                     "v_mov_b32 v132, %4\n\tv_mov_b32 v133, %5\n\tv_mov_b32 v134, %6\n\tv_mov_b32 v135, %7\n\t"
                     "s_set_gpr_idx_off"
                     :: "v"(pf[0][0]), "v"(pf[0][1]), "v"(pf[0][2]), "v"(pf[0][3]), "v"(pf[1][0]), "v"(pf[1][1]), "v"(pf[1][2]), "v"(pf[1][3]), "s"(8 * k));
      }
    }
  }
  l = swap_sum(l);
  *(LAS f32x2*)(lds + LDS_ML + w * 256 + r32 * 8) = f32x2{m, l};
  LGKM0();
  ctl[CTL_MLOK / 4 + w] = 1;
  STAMP(2);

  const unsigned tq = (lane & 15) >> 2, tp = lane & 3, tblk = (lane >> 4) & 1;
  const unsigned vaddr0 = LDS_RING + (4 * hi + tq) * 256 + ((tq ^ (pos & 3)) << 6) + ((2 * tblk + (tp >> 1)) << 4) + ((tp & 1) << 3);
  f32x16 o[4];
#pragma unroll
  for (int i = 0; i < 4; ++i)
#pragma unroll
    for (int r = 0; r < 16; ++r) o[i][r] = 0.f;
  auto weights = [&]() {
    wait_above<1>(lbase + LDS_CTL + CTL_MLOK + min(lane, NC - 1) * 4, 0);
    unsigned a = LDS_ML + r32 * 8;
    asm volatile("" : "+v"(a) :: "memory");
    float M = -3.0e38f, L = 0.f;
    f32x2 ml[NC];
#pragma unroll
    for (int i = 0; i < NC; ++i) { ml[i] = *(LAS f32x2*)(lds + a + i * 256); M = fmaxf(M, ml[i][0]); }
#pragma unroll
    for (int i = 0; i < NC; ++i) L = fmaf(__builtin_amdgcn_exp2f((ml[i][0] - M) * CS), ml[i][1], L);
    const float inv = 1.0f / L;
    *(LAS float*)(lds + (a >> 1) + (LDS_WT - LDS_ML / 2) + pos * 128) = __builtin_amdgcn_exp2f((mprev - M) * CS) * inv;
    const float ct = __builtin_amdgcn_exp2f((m - M) * CS) * inv;
#pragma unroll
    for (int s = 0; s < 2; ++s) {
      u32x4 x = *(LAS u32x4*)(lds + ppst + W_LAST + s * 1024);
#pragma unroll
      for (int e = 0; e < 4; ++e) x[e] = pack2(__uint_as_float(x[e] << 16) * ct, __uint_as_float(x[e] & 0xffff0000u) * ct);
      *(LAS u32x4*)(lds + ppst + W_LAST + s * 1024) = x;
    }
    LGKM0();
    ctl[CTL_WTOK / 4 + w] = 1;
  };
  resc &= ~1u;
  if (nmine <= 2) weights();
#pragma unroll 1
  for (int k = 0; k < nmine - 1; ++k) {
    if (k == 1) weights();
    if ((resc >> k) & 1) {
      const float alpha = *(LAS float*)(lds + palp + k * 128);
#pragma unroll
      for (int i = 0; i < 4; ++i)
#pragma unroll
        for (int r = 0; r < 16; ++r) o[i][r] *= alpha;
    }
    const unsigned u = NT + w + NC * k;
    u32x4 pf[2];
    if (k >= KREG) {
      pf[0] = *(LAS u32x4*)(lds + ppst + (k - KREG) * 2048);
      pf[1] = *(LAS u32x4*)(lds + ppst + (k - KREG) * 2048 + 1024);
    } else {
      asm volatile("s_set_gpr_idx_on %8, gpr_idx(SRC0)\n\t"
                   "v_mov_b32 %0, v128\n\tv_mov_b32 %1, v129\n\tv_mov_b32 %2, v130\n\tv_mov_b32 %3, v131\n\t"
                   "v_mov_b32 %4, v132\n\tv_mov_b32 %5, v133\n\tv_mov_b32 %6, v134\n\tv_mov_b32 %7, v135\n\t"
                   "s_set_gpr_idx_off\n\ts_nop 1"
                   : "=v"(pf[0][0]), "=v"(pf[0][1]), "=v"(pf[0][2]), "=v"(pf[0][3]), "=v"(pf[1][0]), "=v"(pf[1][1]), "=v"(pf[1][2]), "=v"(pf[1][3])
                   : "s"(8 * k));
    }
    asm volatile("" : "+v"(pf[0]), "+v"(pf[1]), "+v"(o[0]), "+v"(o[1]), "+v"(o[2]), "+v"(o[3]));
    if (PRESLEEP && k > 1) __builtin_amdgcn_s_sleep(2 * PRESLEEP);
    wait_above<CSLEEP>(aland, 4 * u);
    if (TPRIO && k == nmine - 2) __builtin_amdgcn_s_setprio(TPRIO);
    bf16x8 vf[4][2];
#pragma unroll
    for (int i = 0; i < 4; ++i) {
      const unsigned a = (vaddr0 + ((u & 7) << 13)) ^ (i << 6);
#pragma unroll
      for (int s = 0; s < 2; ++s) {
        const s16x4 lo = __builtin_amdgcn_ds_read_tr16_b64_v4i16((LAS s16x4*)(lds + a + 4096 * s));
        const s16x4 up = __builtin_amdgcn_ds_read_tr16_b64_v4i16((LAS s16x4*)(lds + a + 4096 * s + 2048));
        vf[i][s] = __builtin_bit_cast(bf16x8, __builtin_shufflevector(lo, up, 0, 1, 2, 3, 4, 5, 6, 7));
      }
    }
    asm volatile("s_waitcnt lgkmcnt(0)" : "+v"(vf[0][0]), "+v"(vf[0][1]), "+v"(vf[1][0]), "+v"(vf[1][1]), "+v"(vf[2][0]), "+v"(vf[2][1]), "+v"(vf[3][0]), "+v"(vf[3][1]) :: "memory");
    ctl[CTL_FREE / 4 + (u & 7)] = u + 8;
    if (pf_on) prefetch(u);
#pragma unroll
    for (int s = 0; s < 2; ++s)
#pragma unroll
      for (int i = 0; i < 4; ++i) o[i] = __builtin_amdgcn_mfma_f32_32x32x16_bf16(vf[i][s], __builtin_bit_cast(bf16x8, pf[s]), o[i], 0, 0, 0);
  }
  STAMP(3);

  {
    unsigned l16 = lane * 16;
    asm volatile("" : "+v"(l16));
    if (nmine > 1) {
      auto put = [&](unsigned dst, const f32x16& x) {
#pragma unroll
        for (int j = 0; j < 4; ++j) *(LAS f32x4*)(lds + dst + j * 1024) = f32x4{x[4 * j], x[4 * j + 1], x[4 * j + 2], x[4 * j + 3]};
      };
      if (pos > 3) put(LDS_RING + ((2 * NT - 2 * NC + pos) & 7) * 8192 + l16, o[0]);
#pragma unroll
      for (int i = 1; i < 4; ++i) put(LDS_WREG + pos * W_SIZE + (i - 1) * 4096 + l16, o[i]);
      if (TWAIT) LGKM0(); else CBAR();
      ctl[CTL_ODONE / 4 + pos] = 1;
    }
    if (pos < 4) {
      f32x16 of = o[0];
      const unsigned l4 = l16 >> 2, awt = LDS_WT + (l4 & 127);
      wait_above<1>(lbase + LDS_CTL + CTL_WTOK + min(l4, (NC - 1) * 4u), 0);
      auto src_o = [&](int p) -> unsigned {
        const int i = pos ^ (p & 3);
        return l16 + (i == 0 ? LDS_RING + ((2 * NT - 2 * NC + p) & 7) * 8192 : LDS_WREG + p * W_SIZE + (i - 1) * 4096);
      };
      auto fma_o = [&](unsigned src, float c) {
#pragma unroll
        for (int j = 0; j < 4; ++j) {
          const f32x4 x = *(LAS f32x4*)(lds + src + j * 1024);
#pragma unroll
          for (int e = 0; e < 4; ++e) of[4 * j + e] = fmaf(x[e], c, of[4 * j + e]);
        }
      };
      auto mma_t = [&](int p, bf16x8 p0, bf16x8 p1) {
        const unsigned a = vaddr0 + (((2 * NT - NC + p) & 7) << 13);
        bf16x8 vf[2];
#pragma unroll
        for (int s = 0; s < 2; ++s) {
          const s16x4 lo = __builtin_amdgcn_ds_read_tr16_b64_v4i16((LAS s16x4*)(lds + a + 4096 * s));
          const s16x4 up = __builtin_amdgcn_ds_read_tr16_b64_v4i16((LAS s16x4*)(lds + a + 4096 * s + 2048));
          vf[s] = __builtin_bit_cast(bf16x8, __builtin_shufflevector(lo, up, 0, 1, 2, 3, 4, 5, 6, 7));
        }
        of = __builtin_amdgcn_mfma_f32_32x32x16_bf16(vf[0], p0, of, 0, 0, 0);
        of = __builtin_amdgcn_mfma_f32_32x32x16_bf16(vf[1], p1, of, 0, 0, 0);
      };
      {
        const float cw = *(LAS float*)(lds + awt + pos * 128);
#pragma unroll
        for (int r = 0; r < 16; ++r) of[r] *= cw;
      }
      if (NT >= 2 * NC) {
        float co[5];
        bf16x8 pl[NC][2];
        auto ld_p = [&](int p) {
          pl[p][0] = *(LAS bf16x8*)(lds + LDS_WREG + p * W_SIZE + W_LAST + l16);
          pl[p][1] = *(LAS bf16x8*)(lds + LDS_WREG + p * W_SIZE + W_LAST + l16 + 1024);
        };
#define PIN_OF() asm volatile("" : "+v"(of))
#pragma unroll
        for (int i = 1; i < 4; ++i) co[i - 1] = *(LAS float*)(lds + awt + (pos ^ i) * 128);
        co[3] = *(LAS float*)(lds + awt + 4 * 128);
        co[4] = *(LAS float*)(lds + awt + 5 * 128);
        ld_p(0); ld_p(1); ld_p(2);
        PIN_OF();
        wait_above<TSLEEP>(lbase + LDS_CTL + CTL_ODONE + (l4 & 12), 0);
#pragma unroll
        for (int i = 1; i < 4; ++i) fma_o(LDS_WREG + (pos ^ i) * W_SIZE + (i - 1) * 4096 + l16, co[i - 1]);
        PIN_OF();
        ld_p(3); ld_p(4); ld_p(5);
        wait_above<TSLEEP>(aland, 4 * (2 * NT - NC + 2));
        mma_t(0, pl[0][0], pl[0][1]);
        mma_t(1, pl[1][0], pl[1][1]);
        mma_t(2, pl[2][0], pl[2][1]);
        STAMP(4);
        wait_above<TSLEEP>(lbase + LDS_CTL + CTL_ODONE + 16 + (l4 & 4), 0);
        fma_o(src_o(4), co[3]);
        fma_o(src_o(5), co[4]);
        PIN_OF();
        wait_above<0>(aland, 4 * (2 * NT - NC + 4));
        mma_t(3, pl[3][0], pl[3][1]);
        mma_t(4, pl[4][0], pl[4][1]);
        PIN_OF();
        wait_above<0>(aland, 4 * (2 * NT - NC + 5));
        mma_t(5, pl[5][0], pl[5][1]);
      } else {
#pragma unroll 1
        for (int p = max(NC - NT, 0); p < NC; ++p) {
          if (p != pos && NT - 2 * NC + p >= 0) {
            wait_above<1>(lbase + LDS_CTL + CTL_ODONE + p * 4, 0);
            fma_o(src_o(p), *(LAS float*)(lds + awt + p * 128));
          }
          wait_above<1>(aland, 4 * (2 * NT - NC + p));
          mma_t(p, *(LAS bf16x8*)(lds + LDS_WREG + p * W_SIZE + W_LAST + l16), *(LAS bf16x8*)(lds + LDS_WREG + p * W_SIZE + W_LAST + l16 + 1024));
        }
      }
      const __amdgpu_buffer_rsrc_t ro = __builtin_amdgcn_make_buffer_rsrc(outp, 0, NB * HQ * SQ * HD * 2, 0x00020000);
      const unsigned orow = (unsigned)(b * HQ + 2 * h) * SQ * HD * 2 + pos * 64 + ((l16 & 511) << 4) + ((l16 >> 5) & 16);
#pragma unroll
      for (int j = 0; j < 2; ++j) {
        const unsigned a0 = pack2(of[8 * j], of[8 * j + 1]), a1 = pack2(of[8 * j + 2], of[8 * j + 3]);
        const unsigned b0 = pack2(of[8 * j + 4], of[8 * j + 5]), b1 = pack2(of[8 * j + 6], of[8 * j + 7]);
        auto s0 = __builtin_amdgcn_permlane32_swap(a0, b0, false, false);
        auto s1 = __builtin_amdgcn_permlane32_swap(a1, b1, false, false);
        u32x4 val = {s0[0], s1[0], s0[1], s1[1]};
        __builtin_amdgcn_raw_buffer_store_b128(val, ro, orow + j * 32, 0, OUTAUX);
      }
    }
  }
}
}

extern "C" void kernel_launch(void* const* d_in, const int* in_sizes, int n_in, void* d_out, int out_size, void* d_ws,
                              size_t ws_size, hipStream_t stream) {
  (void)n_in; (void)out_size; (void)ws_size;
  const unsigned nphys = (unsigned)(in_sizes[1] / (PAGE * HKV * HD));
  const unsigned kvbytes = nphys * PAGE_BYTES;
  (void)hipFuncSetAttribute((const void*)paged_attn, hipFuncAttributeMaxDynamicSharedMemorySize, LDS_TOTAL);
  hipLaunchKernelGGL(paged_attn, dim3(256), dim3(512), LDS_TOTAL, stream, d_in[0], d_in[1], d_in[2], (const int*)d_in[3],
                     (const int*)d_in[4], d_out, (char*)d_ws, kvbytes, nphys);
}
```

```cpp
#define PACE_X16 438
#define SOFT 120
#include <hip/hip_runtime.h>
#include <hip/hip_bf16.h>

#define PROBE_G 0
#define QPOL "nt"
#define PF_2ND
#define TLBW 5
#define TLBG 0x200000u
#define SOFT_AT 0
#define SORTP 1
#define TORDER 0
#define PF_OFF 6
#define TMODE 1
#define TK2 0
#define TV 16
#define TAHEAD 2
#define PF_BY 0
#define PF_END 2u
#define PF_X64B 72
#define PF_DENSE 1
#define PF_PER 4
#define PF_PSTEP 2
#define GAP_TICKS 0
#define OUTAUX 0
#define HOFF 4
#define LSLEEP 0
#define PRESLEEP 0
#define PF_EVERY 2
#define PF_X64 76
#define PF_ALL 0
#define PF_LEAD 300
#define PF_POL ""
#define PF_SLOW 0
#define PF_RUN 3
#define TSLEEP 0
#define TWAIT 1
#define TPRIO 0
#define CSLEEP 1

#define STAMP(i) do {} while (0)

namespace {
constexpr int NB = 16, HQ = 32, HKV = 16, SQ = 16, HD = 128, PAGE = 16, NBPS = 256, SKV = NBPS * PAGE;
constexpr unsigned PAGE_BYTES = PAGE * HKV * HD * 2;
constexpr unsigned INVALID = 0x80000000u;
constexpr int NC = 6;
constexpr int KREG = 16, KLDS = 5;
static_assert(NC * (KREG + KLDS + 1) >= NBPS / 2, "P storage");
constexpr unsigned LDS_RING = 0;
constexpr unsigned LDS_WREG = 65536;
constexpr unsigned W_ALP = KLDS * 2048;
constexpr unsigned W_LAST = W_ALP + (KREG + KLDS + 1) * 128;
constexpr unsigned W_SIZE = W_LAST + 2048;
static_assert(W_LAST >= 3 * 4096 && W_SIZE % 256 == 0, "consumer region");
constexpr unsigned LDS_TBL = LDS_WREG + W_SIZE;
constexpr unsigned LDS_SRT = LDS_WREG + 3 * W_SIZE;
constexpr unsigned LDS_Q = LDS_WREG;
constexpr unsigned LDS_ML = LDS_WREG + NC * W_SIZE;
constexpr unsigned LDS_WT = LDS_ML + NC * 256;
constexpr unsigned LDS_CTL = LDS_WT + NC * 128;
constexpr unsigned CTL_LAND = 0;
constexpr unsigned CTL_QOK = 16;
constexpr unsigned CTL_FREE = 32;
constexpr unsigned CTL_MLOK = 64;
constexpr unsigned CTL_ODONE = 96;
constexpr unsigned CTL_WTOK = 128;
constexpr unsigned LDS_DUMMY = LDS_CTL + 256;
constexpr unsigned LDS_PLIST = LDS_DUMMY + 256;
constexpr unsigned LDS_TOTAL = LDS_PLIST + 1024;
static_assert(LDS_TOTAL <= 163840, "lds");

typedef __attribute__((ext_vector_type(8))) __bf16 bf16x8;
typedef __attribute__((ext_vector_type(2))) __bf16 bf16x2;
typedef __attribute__((ext_vector_type(4))) short s16x4;
typedef __attribute__((ext_vector_type(16))) float f32x16;
typedef __attribute__((ext_vector_type(4))) float f32x4;
typedef __attribute__((ext_vector_type(2))) float f32x2;
typedef __attribute__((ext_vector_type(4))) unsigned u32x4;
typedef __attribute__((ext_vector_type(2))) unsigned u32x2;
typedef __attribute__((ext_vector_type(4))) int i32x4;
#define LAS __attribute__((address_space(3)))

__device__ __forceinline__ float swap_max(float x) {
  auto r = __builtin_amdgcn_permlane32_swap(__float_as_uint(x), __float_as_uint(x), false, false);
  return fmaxf(__uint_as_float(r[0]), __uint_as_float(r[1]));
}
__device__ __forceinline__ float swap_sum(float x) {
  auto r = __builtin_amdgcn_permlane32_swap(__float_as_uint(x), __float_as_uint(x), false, false);
  return __uint_as_float(r[0]) + __uint_as_float(r[1]);
}
__device__ __forceinline__ unsigned pack2(float a, float b) {
  bf16x2 v = {(__bf16)a, (__bf16)b};
  return __builtin_bit_cast(unsigned, v);
}
template <int CTRL> __device__ __forceinline__ int dpp_add(int x) { return x + __builtin_amdgcn_update_dpp(0, x, CTRL, 0xf, 0xf, true); }
__device__ __forceinline__ unsigned now32() { return (unsigned)__builtin_amdgcn_s_memrealtime(); }
__device__ __forceinline__ int to_sgpr(int v) {
  int r;
  asm volatile("s_nop 1\n\tv_readfirstlane_b32 %0, %1" : "=s"(r) : "v"(v));
  return r;
}
#define LGKM0() asm volatile("s_waitcnt lgkmcnt(0)" ::: "memory")
#define VMWAIT(n) asm volatile("s_waitcnt vmcnt(" #n ")" ::: "memory")
#define CBAR() asm volatile("" ::: "memory")
template <int SLEEP> __device__ __forceinline__ void wait_above(unsigned addr, unsigned u) {
  unsigned v, cnt;
  if (SLEEP)
    asm volatile("s_mov_b32 %1, 0x200000\n\ts_branch 2f\n"
                 "1:\n\ts_sleep %4\n"
                 "2:\n\tds_read_b32 %0, %2\n\ts_sub_u32 %1, %1, 1\n\ts_waitcnt lgkmcnt(0)\n\t"
                 "v_cmp_ge_u32 vcc, %3, %0\n\ts_cbranch_scc1 3f\n\ts_nop 1\n\ts_cbranch_vccnz 1b\n3:"
                 : "=&v"(v), "=&s"(cnt) : "v"(addr), "s"(u), "n"(SLEEP) : "memory", "scc", "vcc");
  else
    asm volatile("s_mov_b32 %1, 0x200000\n"
                 "1:\n\tds_read_b32 %0, %2\n\ts_sub_u32 %1, %1, 1\n\ts_waitcnt lgkmcnt(0)\n\t"
                 "v_cmp_ge_u32 vcc, %3, %0\n\ts_cbranch_scc1 3f\n\ts_nop 1\n\ts_cbranch_vccnz 1b\n3:"
                 : "=&v"(v), "=&s"(cnt) : "v"(addr), "s"(u) : "memory", "scc", "vcc");
}

__device__ __forceinline__ void wait_above_v(unsigned addr, unsigned u) {
  unsigned v, cnt;
  asm volatile("s_mov_b32 %1, 0x200000\n"
               "1:\n\tds_read_b32 %0, %2\n\ts_sub_u32 %1, %1, 1\n\ts_waitcnt lgkmcnt(0)\n\t"
               "v_cmp_ge_u32 vcc, %3, %0\n\ts_cbranch_scc1 3f\n\ts_nop 1\n\ts_cbranch_vccnz 1b\n3:"
               : "=&v"(v), "=&s"(cnt) : "v"(addr), "v"(u) : "memory", "scc", "vcc");
}

__global__ void __launch_bounds__(512, 1) __attribute__((amdgpu_num_vgpr(64)))
paged_attn(const void* __restrict__ qp, const void* __restrict__ kp, const void* __restrict__ vp, const int* __restrict__ bt,
           const int* __restrict__ lens, void* __restrict__ outp, char* __restrict__ ws, unsigned kvbytes, unsigned nphys) {
  extern __shared__ __attribute__((aligned(16))) char smem[];
  LAS char* lds = (LAS char*)smem;
  const int lane0 = threadIdx.x & 63;
  const int wv = __builtin_amdgcn_readfirstlane(threadIdx.x >> 6);
  const int g = blockIdx.x;
  const int h = g >> 4;
  constexpr float CS = 0.08838834764831845f * 1.4426950408889634f;
  volatile LAS unsigned* ctl = (volatile LAS unsigned*)(lds + LDS_CTL);

  const unsigned want = (g & 8) ? 15 - (g & 7) : (g & 7);
  unsigned lm1, rank;
  auto ranking = [&](int lane, unsigned& lr, u32x4& lq) __attribute__((always_inline)) {
    asm volatile("" : "+v"(lr), "+v"(lq));
    lm1 = min(lr - 1u, (unsigned)SKV - 1u);
    const unsigned key = lm1 * 16 + (lane & 15);
    rank = 0;
#pragma unroll
    for (int j = 0; j < 4; ++j) rank += (min(lq[j] - 1u, (unsigned)SKV - 1u) * 16 + ((lane >> 4) * 4 + j) > key) ? 1u : 0u;
    auto r1 = __builtin_amdgcn_permlane16_swap(rank, rank, false, false);
    rank = r1[0] + r1[1];
    auto r2 = __builtin_amdgcn_permlane32_swap(rank, rank, false, false);
    rank = r2[0] + r2[1];
  };
  auto total = [&]() __attribute__((always_inline)) {
    int t = lm1 + 1;
    t = dpp_add<0x121>(t); t = dpp_add<0x122>(t); t = dpp_add<0x124>(t); t = dpp_add<0x128>(t);
    return t;
  };

  auto sort_init = [&](int lane, unsigned scr) __attribute__((always_inline)) {
    *(LAS u32x4*)(lds + scr + lane * 32) = u32x4{0u, 0u, 0u, 0u};
    *(LAS u32x4*)(lds + scr + lane * 32 + 16) = u32x4{0u, 0u, 0u, 0u};
  };
  auto sort_pages = [&](int lane, int bb, int n, unsigned scr, unsigned dst) __attribute__((always_inline)) {
    u32x4 e = *(LAS u32x4*)(lds + LDS_TBL + (bb >> 3) * W_SIZE + (bb & 7) * 1024 + lane * 16);
    bool in[4];
#pragma unroll
    for (int j = 0; j < 4; ++j) {
      e[j] = min(e[j], nphys - 1);
      in[j] = (unsigned)(4 * lane + j - 2) < (unsigned)(n - 3);
      __hip_atomic_fetch_or((LAS unsigned*)(lds + scr + ((e[j] >> 5) & 255) * 8), in[j] ? 1u << (e[j] & 31) : 0u, __ATOMIC_RELAXED, __HIP_MEMORY_SCOPE_WAVEFRONT);
    }
    CBAR();
    u32x4 a = *(LAS u32x4*)(lds + scr + lane * 32), c = *(LAS u32x4*)(lds + scr + lane * 32 + 16);
    const int n0 = __builtin_popcount(a[0]), n1 = n0 + __builtin_popcount(a[2]), n2 = n1 + __builtin_popcount(c[0]), n3 = n2 + __builtin_popcount(c[2]);
    int sc = n3;
    sc = dpp_add<0x111>(sc); sc = dpp_add<0x112>(sc); sc = dpp_add<0x114>(sc); sc = dpp_add<0x118>(sc);
    sc += __builtin_amdgcn_update_dpp(0, sc, 0x142, 0xa, 0xf, false);
    sc += __builtin_amdgcn_update_dpp(0, sc, 0x143, 0xc, 0xf, false);
    const bool ok = SORTP && nphys <= 8192u && __builtin_amdgcn_readlane(sc, 63) == n - 3;
    sc += 2 - n3;
    a[1] = sc; a[3] = sc + n0; c[1] = sc + n1; c[3] = sc + n2;
    *(LAS u32x4*)(lds + scr + lane * 32) = a;
    *(LAS u32x4*)(lds + scr + lane * 32 + 16) = c;
    CBAR();
    u32x2 wp[4];
#pragma unroll
    for (int j = 0; j < 4; ++j) wp[j] = *(LAS u32x2*)(lds + scr + ((e[j] >> 5) & 255) * 8);
#pragma unroll
    for (int j = 0; j < 4; ++j) {
      const unsigned r = wp[j][1] + __builtin_popcount(wp[j][0] & ((1u << (e[j] & 31)) - 1u));
      *(LAS unsigned*)(lds + dst + ((ok && in[j]) ? r : 4 * lane + j) * 4) = e[j];
    }
    CBAR();
  };

  if (__builtin_expect(wv < 2, 1)) {
    const int half = wv, lane = lane0;
    unsigned lr = lens[lane & 15];
    u32x4 lq = *(const u32x4*)(lens + (lane >> 4) * 4);
    unsigned first = bt[(lane & 15) * NBPS + half];
    __builtin_amdgcn_s_setprio(3);
    sort_init(lane, LDS_SRT + half * 3072);
    const unsigned rq4 = lane >> 4;
    unsigned vo0 = rq4 * 4096 + (((unsigned)(lane & 15) ^ (rq4 << 2)) << 4) + h * (HD * 2);
    unsigned vo1 = (vo0 + 16384 - 1024) ^ 16, vo2 = (vo0 + 32768 - 2048) ^ 32, vo3 = (vo0 + 49152 - 3072) ^ 48;
    unsigned vland = (unsigned)(size_t)lds + LDS_CTL + CTL_LAND + half * 4;
    unsigned vfree = (unsigned)(size_t)lds + LDS_CTL + CTL_FREE + (lane & 7) * 4;
    const unsigned ring = (unsigned)(size_t)lds + LDS_RING + half * 4096;
    const __amdgpu_buffer_rsrc_t rk = __builtin_amdgcn_make_buffer_rsrc((void*)kp, 0, (int)kvbytes, 0x00020000);
    asm volatile("" : "+v"(vo0), "+v"(vo1), "+v"(vo2), "+v"(vo3), "+v"(vland), "+v"(vfree) : "s"(rk), "s"(vp), "s"(nphys), "s"(ring));
    ranking(lane, lr, lq);
    first = min(first, nphys - 1) * PAGE_BYTES;
    const int b = __builtin_ctz((unsigned)__builtin_amdgcn_ballot_w64(rank == want) | 0x8000u);
    unsigned long long tstart;
    asm volatile("s_waitcnt vmcnt(0)\n\t"
                 "s_mov_b32 m0, %2\n\t"
                 "s_memrealtime %0\n\t"
                 "s_nop 2\n\t"
                 "buffer_load_dwordx4 %4, %1, %3 offen nt lds\n\t"
                 "buffer_load_dwordx4 %5, %1, %3 offen offset:1024 nt lds\n\t"
                 "buffer_load_dwordx4 %6, %1, %3 offen offset:2048 nt lds\n\t"
                 "buffer_load_dwordx4 %7, %1, %3 offen offset:3072 nt lds\n\t"
                 "s_waitcnt lgkmcnt(0)\n\t"
                 "s_barrier"
                 : "=&s"(tstart) : "s"(rk), "s"(ring), "s"(__builtin_amdgcn_readlane(first, b)), "v"(vo0), "v"(vo1), "v"(vo2), "v"(vo3) : "memory");
    __builtin_amdgcn_sched_barrier(0);
    asm volatile("" : "+v"(lm1));
    const int len = __builtin_amdgcn_readlane(lm1, b) + 1, nown = (len + PAGE - 1) / PAGE, NT = (nown + 1) >> 1;
    sort_pages(lane, b, nown, LDS_SRT + half * 3072, LDS_SRT + half * 3072 + 2048);
    unsigned l0 = *(LAS unsigned*)(lds + LDS_SRT + half * 3072 + 2048 + min(2 * lane + half, nown - 1) * 4);
    unsigned l1 = *(LAS unsigned*)(lds + LDS_SRT + half * 3072 + 2048 + min(2 * lane + half + 128, nown - 1) * 4);
    const int T16 = total() * PACE_X16;
    const int step = to_sgpr((int)((float)T16 * __builtin_amdgcn_rcpf((float)max(len, 32))));
    const unsigned due0 = ((unsigned)tstart << 8) + half * ((step * HOFF) >> 3);
    const int ua = SOFT_AT ? to_sgpr(min(min(NT, 64), 1 + (int)((float)(SOFT_AT * 256) * __builtin_amdgcn_rcpf((float)step)))) : 1;
    l0 *= PAGE_BYTES;
    l1 *= PAGE_BYTES;
    const unsigned lastrec = (unsigned)__builtin_amdgcn_readlane(NT > 64 ? l1 : l0, (NT - 1) & 63) + (unsigned)to_sgpr(min(max(len - (2 * (NT - 1) + half) * PAGE, 0), PAGE)) * 4096u;
#define CNT(r)
    unsigned vf, vt, lc;
    asm volatile(
        "s_waitcnt lgkmcnt(0)\n\t"
        "s_memrealtime s[68:69]\n\t"
        "ds_read_b32 %[vf], %[vfree]\n\t"
        "s_mov_b32 s60, 1\n\t"
        "s_mov_b32 s61, 4\n\t"
        "s_mov_b32 s62, 0x400000\n\t"
        "s_mov_b32 s66, 0\n\t"
        "s_mov_b32 s67, %[ua]\n\t"
        "s_mov_b32 s72, 0x2000\n\t"
        "s_mov_b32 s73, 1\n\t"
        "s_mov_b32 s74, %[due1]\n\t"
        "s_mov_b64 s[76:77], %[kp]\n\t"
        "s_mov_b32 s78, %[nrec]\n\t"
        "s_mov_b32 s79, 0x20000\n\t"
        "s_and_b32 s77, s77, 0xffff\n\t"
        "v_mov_b32 %[lc], %[l0]\n\t"
        "s_getpc_b64 s[70:71]\n"
        ".LldA%=:\n\t"
        "s_add_u32 s80, s70, .LldL%=-.LldA%=\n\t"
        "s_addc_u32 s71, s71, 0\n\t"
        "s_branch .LldF%=\n"
        ".LldR%=:\n\t"
        "s_sub_u32 s62, s62, 1\n\t"
        "s_cbranch_scc1 .LldE%=\n"
        ".LldT%=:\n\t"
        "s_getreg_b32 s64, hwreg(HW_REG_IB_STS)\n\t"
        "s_and_b32 s65, s64, 15\n\t"
        "s_bfe_u32 s64, s64, 0x20016\n\t"
        "s_lshl4_add_u32 s64, s64, s65\n\t"
        "s_add_u32 s64, s64, 3\n\t"
        "s_and_b32 s64, s64, 60\n\t"
        "s_sub_u32 s65, s61, s64\n\t"
        "s_cmp_lg_u32 s65, s66\n\t"
        "s_cbranch_scc1 .LldP%=\n"
        ".LldC%=:\n\t"
        "s_waitcnt lgkmcnt(0)\n\t"
        "v_cmp_eq_u32 vcc, s60, %[vf]\n\t"
        "s_lshl_b32 s64, s68, 8\n\t"
        "s_memrealtime s[68:69]\n\t"
        "ds_read_b32 %[vf], %[vfree]\n\t"
        CNT("s82")
        "s_sub_u32 s64, s64, s74\n\t"
        "s_cmp_lt_i32 s64, 0\n\t"
        "s_cbranch_scc1 .LldR%=\n\t"
        CNT("s83")
        "s_cbranch_vccz .LldR%=\n\t"
        CNT("s84")
        "buffer_load_dwordx4 %[o0], s[76:79], s63 offen nt lds\n\t"
        "s_add_u32 s74, s74, %[step]\n\t"
        "s_add_u32 s73, s73, 1\n\t"
        "buffer_load_dwordx4 %[o1], s[76:79], s63 offen offset:1024 nt lds\n\t"
        "s_add_u32 s60, s60, 1\n\t"
        "s_add_u32 s61, s61, 4\n\t"
        "buffer_load_dwordx4 %[o2], s[76:79], s63 offen offset:2048 nt lds\n\t"
        "s_add_u32 s72, s72, 0x2000\n\t"
        "s_and_b32 s72, s72, 0xe000\n\t"
        "buffer_load_dwordx4 %[o3], s[76:79], s63 offen offset:3072 nt lds\n"
        ".LldF%=:\n\t"
        "v_readlane_b32 s63, %[lc], s73\n\t"
        "s_add_u32 m0, s72, %[ring]\n\t"
        "s_cmp_eq_u32 s73, %[ntm]\n\t"
        "s_cselect_b32 s78, %[last], %[nrec]\n\t"
        "s_cmp_lg_u32 s60, s67\n\t"
        "s_cbranch_scc1 .LldT%=\n\t"
        "s_cmp_lg_u32 s60, %[ua]\n\t"
        "s_cbranch_scc1 .LldG%=\n\t"
        "s_add_u32 s74, s74, %[soft]\n\t"
        "s_min_u32 s67, %[nt], 64\n\t"
        "s_cmp_lg_u32 s60, s67\n\t"
        "s_cbranch_scc1 .LldT%=\n"
        ".LldG%=:\n\t"
        "s_cmp_eq_u32 s73, %[nt]\n\t"
        "s_cbranch_scc1 .LldV%=\n\t"
        "v_mov_b32 %[lc], %[l1]\n\t"
        "v_readfirstlane_b32 s63, %[l1]\n\t"
        "s_add_u32 s67, s60, %[nt]\n\t"
        "s_sub_u32 s67, s67, 64\n\t"
        "s_branch .LldT%=\n"
        ".LldV%=:\n\t"
        "s_cmp_lg_u32 s60, %[nt]\n\t"
        "s_cbranch_scc1 .LldD%=\n\t"
        "s_mov_b64 s[76:77], %[vp]\n\t"
        "s_and_b32 s77, s77, 0xffff\n\t"
        "s_mov_b32 s73, 0\n\t"
        "s_min_u32 s67, %[nt], 64\n\t"
        "s_add_u32 s67, s67, s60\n\t"
        "s_mov_b32 s74, %[duev]\n\t"
        "s_cmp_eq_u32 %[ntm], 0\n\t"
        "s_cselect_b32 s78, %[last], %[nrec]\n\t"
        "v_mov_b32 %[lc], %[l0]\n\t"
        "v_readfirstlane_b32 s63, %[l0]\n\t"
        "v_xor_b32 %[o1], 16, %[o1]\n\t"
        "v_xor_b32 %[o2], 32, %[o2]\n\t"
        "v_xor_b32 %[o3], 48, %[o3]\n\t"
        "s_branch .LldT%=\n"
        ".LldD%=:\n\t"
        "s_add_u32 s74, s74, 0x40000000\n\t"
        "s_mov_b32 s60, -1\n\t"
        "s_branch .LldT%=\n"
        ".LldP%=:\n\t"
        "s_min_u32 s64, s64, 32\n\t"
        "s_add_u32 s70, s80, s64\n\t"
        "v_mov_b32 %[vt], s65\n\t"
        "s_mov_b32 s66, s65\n\t"
        "s_setpc_b64 s[70:71]\n\t"
        ".p2align 6\n"
        ".LldL%=:\n\t"
        "s_waitcnt vmcnt(0)\n\t"
        "s_waitcnt vmcnt(4)\n\t"
        "s_waitcnt vmcnt(8)\n\t"
        "s_waitcnt vmcnt(12)\n\t"
        "s_waitcnt vmcnt(16)\n\t"
        "s_waitcnt vmcnt(20)\n\t"
        "s_waitcnt vmcnt(24)\n\t"
        "s_waitcnt vmcnt(28)\n\t"
        "s_waitcnt vmcnt(32)\n\t"
        "ds_write_b32 %[vland], %[vt]\n\t"
        "s_cmp_lg_u32 s65, %[fin]\n\t"
        "s_cbranch_scc1 .LldC%=\n"
        ".LldE%=:\n\t"
        "s_waitcnt vmcnt(0) lgkmcnt(0)\n\t"
        : [vf] "=&v"(vf), [vt] "=&v"(vt), [lc] "=&v"(lc), [o1] "+v"(vo1), [o2] "+v"(vo2), [o3] "+v"(vo3)
        : [l0] "v"(l0), [l1] "v"(l1), [o0] "v"(vo0), [vland] "v"(vland), [vfree] "v"(vfree),
          [kp] "s"(kp), [vp] "s"(vp), [nrec] "s"(kvbytes), [nt] "s"(NT), [ring] "s"(ring),
          [step] "s"(step), [due1] "s"(due0 + step), [duev] "s"(due0 + (to_sgpr(T16) >> 5) + (SOFT + GAP_TICKS) * 256), [last] "s"(lastrec), [ntm] "s"(NT - 1), [fin] "s"(8 * NT), [ua] "s"(ua), [soft] "n"(SOFT * 256)
        : "memory", "scc", "vcc", "s60", "s61", "s62", "s63", "s64", "s65", "s66", "s67", "s68", "s69", "s70", "s71", "s72", "s73", "s74",
          "s76", "s77", "s78", "s79", "s80", "s82", "s83", "s84");
    STAMP(3);
    asm volatile("s_endpgm" ::: "memory");
  }

  int lane = lane0;
  asm volatile("" : "+v"(lane));
  const unsigned nchunk = (kvbytes + 16 * TLBG - 1) / (16 * TLBG);
  auto touch = [&](unsigned c, unsigned n) __attribute__((always_inline)) {
    const bool isv = c >= nchunk;
    const __amdgpu_buffer_rsrc_t rt = __builtin_amdgcn_make_buffer_rsrc(isv ? (void*)vp : (void*)kp, 0, (int)kvbytes, 0x00020000);
    unsigned ln = lane;
    asm volatile("" : "+v"(ln));
    const unsigned idx = (g >> 4) + 16 * (c - (isv ? nchunk : 0) + ln);
    const unsigned vo = (ln < n && idx < 0x80000000u / TLBG) ? idx * TLBG + ((idx * 30592u) & (TLBG - 128)) : INVALID;
    asm volatile("s_mov_b32 m0, %0\n\ts_nop 4\n\tbuffer_load_dword %2, %1, 0 offen nt lds" :: "s"((unsigned)(size_t)lds + LDS_DUMMY), "s"(rt), "v"(vo) : "memory");
  };
  const bool toucher = TLBW && (g & 8) != 0;
  unsigned lraw = lens[lane & 15];
  u32x4 lq = *(const u32x4*)(lens + (lane >> 4) * 4);
  if (wv < 6) {
    const __amdgpu_buffer_rsrc_t rb = __builtin_amdgcn_make_buffer_rsrc((void*)bt, 0, NB * NBPS * 4, 0x00020000);
    asm volatile("s_mov_b32 m0, %0\n\ts_nop 4\n\tbuffer_load_dwordx4 %2, %1, 0 offen lds\n\t"
                 "buffer_load_dwordx4 %2, %1, 0 offen offset:1024 lds\n\t"
                 "buffer_load_dwordx4 %2, %1, 0 offen offset:2048 lds\n\t"
                 "buffer_load_dwordx4 %2, %1, 0 offen offset:3072 lds\n\t"
                 "s_waitcnt vmcnt(0)"
                 :: "s"((unsigned)(size_t)lds + LDS_TBL + ((wv - 2) >> 1) * W_SIZE + (wv & 1) * 4096), "s"(rb), "v"(lane * 16 + (wv - 2) * 4096) : "memory");
  } else if (wv == 6) {
    if (toucher) touch(0, TAHEAD);
    if (lane < 40) ctl[lane] = ((lane >> 3) == CTL_FREE / 32) ? (lane & 7) : 0u;
  }
  if (wv != 7) asm volatile("s_waitcnt lgkmcnt(0)\n\ts_barrier" ::: "memory");
  ranking(lane, lraw, lq);
  const int b = __builtin_ctz((unsigned)__builtin_amdgcn_ballot_w64(rank == want) | 0x8000u);
  if (wv == 7) {
    const __amdgpu_buffer_rsrc_t rq = __builtin_amdgcn_make_buffer_rsrc((void*)qp, 0, NB * HQ * SQ * HD * 2, 0x00020000);
    const unsigned q0 = (((int)__builtin_amdgcn_readlane(lraw, b) > 0) ? (unsigned)((b * HQ + 2 * h) * SQ * HD * 2) : INVALID) + (lane >> 4) * 256 +
                        (((unsigned)(lane & 15) ^ ((lane >> 4) << 2)) << 4);
    asm volatile("s_mov_b32 m0, %0\n\ts_nop 4\n\t"
                 "buffer_load_dwordx4 %2, %1, 0 offen " QPOL " lds\n\t"
                 "buffer_load_dwordx4 %3, %1, 0 offen offset:1024 " QPOL " lds\n\t"
                 "buffer_load_dwordx4 %4, %1, 0 offen offset:2048 " QPOL " lds\n\t"
                 "buffer_load_dwordx4 %5, %1, 0 offen offset:3072 " QPOL " lds\n\t"
                 "s_add_u32 m0, m0, 4096\n\ts_nop 0\n\t"
                 "buffer_load_dwordx4 %2, %1, %6 offen " QPOL " lds\n\t"
                 "buffer_load_dwordx4 %3, %1, %6 offen offset:1024 " QPOL " lds\n\t"
                 "buffer_load_dwordx4 %4, %1, %6 offen offset:2048 " QPOL " lds\n\t"
                 "buffer_load_dwordx4 %5, %1, %6 offen offset:3072 " QPOL " lds\n\t"
                 "s_barrier\n\t"
                 "s_waitcnt vmcnt(0)"
                 :: "s"((unsigned)(size_t)lds + LDS_Q), "s"(rq), "v"(q0), "v"(q0 ^ 16), "v"(q0 ^ 32), "v"(q0 ^ 48), "s"(4096) : "memory", "scc");
    ctl[CTL_QOK / 4] = 1;
  }
  const int bp = __builtin_ctz((unsigned)__builtin_amdgcn_ballot_w64(rank == 15 - want) | 0x8000u);
  const int lim = __builtin_amdgcn_readlane(lm1, b) + 1;
  const int nown = (lim + PAGE - 1) / PAGE, NT = (nown + 1) >> 1;
  const int tot = total();
  const int w = wv - 2;
  const int r32 = lane & 31, hi = lane >> 5;
  const int nmine = (NT > w) ? (NT - w + NC - 1) / NC : 0;
  const unsigned lbase = (unsigned)(size_t)lds;
  const unsigned aland = lbase + LDS_CTL + CTL_LAND + (lane & 1) * 4;
  const int npl = (__builtin_amdgcn_readlane(lm1, bp) >> 4) + 1, NTL = (npl + 1) >> 1;
  const int tots = __builtin_amdgcn_readfirstlane(tot);
  const bool pf_on = PF_EVERY && npl * 256 * 64 > tots * PF_X64B && npl > nown && (h & 3) == PF_BY;
  if (pf_on && wv == 5) {
    sort_init(lane, LDS_SRT + 2 * 3072);
    CBAR();
    sort_pages(lane, bp, npl, LDS_SRT + 2 * 3072, LDS_PLIST);
  }
  const unsigned pf_max = (npl * 256 * 64 > tots * PF_X64) ? 256u : PF_END;
  const unsigned pf_last = 2 * NT - to_sgpr((int)((float)(2 * NT * (PF_LEAD + 23 * PF_OFF)) * 4096.0f * __builtin_amdgcn_rcpf((float)(tot * PACE_X16))));
  const unsigned pf_ratio = to_sgpr((int)((float)NTL * 65536.0f * __builtin_amdgcn_rcpf((float)NT)));
  const unsigned pf_per = PF_PER + (g & 7) * PF_PSTEP, pf_inv = (65536 + pf_per - 1) / pf_per;
  auto prefetch = [&](unsigned op) {
    const unsigned d = pf_last - op;
    if (d >= 1024u) return;
    const unsigned r = d - ((d * pf_inv) >> 16) * pf_per;
    if (r >= PF_RUN) return;
    unsigned tl = 2 * NTL - 1 - PF_OFF - (((d - r) * pf_ratio) >> 16) - r;
    if (tl >= 2u * NTL) return;
    const bool isv = tl >= (unsigned)NTL;
    tl -= isv ? NTL : 0;
    const __amdgpu_buffer_rsrc_t rp = __builtin_amdgcn_make_buffer_rsrc(isv ? (void*)vp : (void*)kp, 0, (int)kvbytes, 0x00020000);
    unsigned ln = lane;
    asm volatile("" : "+v"(ln));
    const unsigned pg = min(*(LAS unsigned*)(lds + LDS_PLIST + min(2 * tl + (ln >> 5), (unsigned)npl - 1) * 4), nphys - 1);
    const unsigned vo = pg * PAGE_BYTES + ((h & 12) | 1) * (HD * 2) + ((ln >> 1) & 15) * 4096 + (PF_SLOW == 1 ? 128 : PF_SLOW == 2 ? 0 : (ln & 1) * 128);
    asm volatile("s_mov_b32 m0, %0\n\ts_nop 4\n\tbuffer_load_dword %2, %1, 0 offen " PF_POL " lds" PF_2ND :: "s"(lbase + LDS_DUMMY), "s"(rp), "v"(vo) : "memory");
  };
  const int pos = (w + NC * 22 - NT) % NC;
  const unsigned tch_rate = to_sgpr((int)((float)nchunk * 65536.0f * __builtin_amdgcn_rcpf((float)NT)));
  auto touch_at = [&](unsigned op) __attribute__((always_inline)) {
    if (TMODE == 0) {
      const unsigned c1 = ((op + 1) * tch_rate) >> 16, c0 = (op * tch_rate) >> 16;
      if (c1 != c0 && c0 + TAHEAD < 2 * nchunk) touch(c0 + TAHEAD, 1);
    } else {
      if (op == TK2 && TAHEAD < nchunk) touch(TAHEAD, 64);
      if (op == TV) touch(nchunk, 64);
    }
  };
  const unsigned palp = LDS_WREG + pos * W_SIZE + W_ALP + r32 * 4;
  const unsigned ppst = LDS_WREG + pos * W_SIZE + lane * 16;
  asm volatile("" ::: "v128", "v255");
  unsigned resc = 0;
  float m = -3.0e38f, mprev = -3.0e38f, l = 0.f;

  {
    const unsigned fk = (((unsigned)r32 & 3) << 2) | (((unsigned)r32 >> 2) & 3);
    const unsigned kaddr0 = LDS_RING + r32 * 256 + (((unsigned)hi ^ fk) << 4);
    bf16x8 qf[8];
    wait_above<1>(lbase + LDS_CTL + CTL_QOK, 0);
#pragma unroll
    for (int s = 0; s < 8; ++s) qf[s] = *(LAS bf16x8*)(lds + ((kaddr0 + LDS_Q - LDS_RING) ^ (s << 5)));
#pragma unroll 1
    for (int k = 0; k < nmine; ++k) {
      const unsigned t = w + NC * k;
      if (PRESLEEP && k > 0) __builtin_amdgcn_s_sleep(PRESLEEP);
      wait_above<CSLEEP>(aland, 4 * t);
      if (k == 0) STAMP(1);
      bf16x8 kf[8];
#pragma unroll
      for (int s = 0; s < 8; ++s) kf[s] = *(LAS bf16x8*)(lds + ((kaddr0 + ((t & 7) << 13)) ^ (s << 5)));
      asm volatile("s_waitcnt lgkmcnt(0)" : "+v"(kf[0]), "+v"(kf[1]), "+v"(kf[2]), "+v"(kf[3]), "+v"(kf[4]), "+v"(kf[5]), "+v"(kf[6]), "+v"(kf[7]) :: "memory");
      ctl[CTL_FREE / 4 + (t & 7)] = t + 8;
      if (pf_on) prefetch(t);
      if (toucher) touch_at(t);
      f32x16 sc;
#pragma unroll
      for (int r = 0; r < 16; ++r) sc[r] = 0.f;
#pragma unroll
      for (int s = 0; s < 8; ++s) sc = __builtin_amdgcn_mfma_f32_32x32x16_bf16(kf[s], qf[s], sc, 0, 0, 0);
      if ((int)t == NT - 1) {
        const int kb = 2 * t * PAGE + 4 * hi;
#pragma unroll
        for (int r = 0; r < 16; ++r) sc[r] = (kb + 8 * (r >> 2) + (r & 3) < lim) ? sc[r] : -__builtin_inff();
      }
      float mx = sc[0];
#pragma unroll
      for (int r = 1; r < 16; ++r) mx = fmaxf(mx, sc[r]);
      mx = swap_max(mx);
      const float mn = fmaxf(m, mx);
      const float alpha = __builtin_amdgcn_exp2f((m - mn) * CS);
      mprev = m;
      m = mn;
      const float nmc = -mn * CS;
      float ps = 0.f;
#pragma unroll
      for (int r = 0; r < 16; ++r) { sc[r] = __builtin_amdgcn_exp2f(fmaf(sc[r], CS, nmc)); ps += sc[r]; }
      l = fmaf(l, alpha, ps);
      u32x4 pf[2];
#pragma unroll
      for (int s = 0; s < 2; ++s)
        pf[s] = u32x4{pack2(sc[8 * s], sc[8 * s + 1]), pack2(sc[8 * s + 2], sc[8 * s + 3]), pack2(sc[8 * s + 4], sc[8 * s + 5]), pack2(sc[8 * s + 6], sc[8 * s + 7])};
      if (__builtin_amdgcn_ballot_w64(alpha != 1.0f)) resc |= 1u << k;
      *(LAS float*)(lds + palp + k * 128) = alpha;
      if (k >= KREG || k == nmine - 1) {
        const unsigned dst = ppst + ((k == nmine - 1) ? W_LAST : (k - KREG) * 2048);
        *(LAS u32x4*)(lds + dst) = pf[0];
        *(LAS u32x4*)(lds + dst + 1024) = pf[1];
      } else {
        asm volatile("s_set_gpr_idx_on %8, gpr_idx(DST)\n\t"
                     "v_mov_b32 v128, %0\n\tv_mov_b32 v129, %1\n\tv_mov_b32 v130, %2\n\tv_mov_b32 v131, %3\n\t"
                     "v_mov_b32 v132, %4\n\tv_mov_b32 v133, %5\n\tv_mov_b32 v134, %6\n\tv_mov_b32 v135, %7\n\t"
                     "s_set_gpr_idx_off"
                     :: "v"(pf[0][0]), "v"(pf[0][1]), "v"(pf[0][2]), "v"(pf[0][3]), "v"(pf[1][0]), "v"(pf[1][1]), "v"(pf[1][2]), "v"(pf[1][3]), "s"(8 * k));
      }
    }
  }
  l = swap_sum(l);
  *(LAS f32x2*)(lds + LDS_ML + w * 256 + r32 * 8) = f32x2{m, l};
  LGKM0();
  ctl[CTL_MLOK / 4 + w] = 1;
  STAMP(2);

  const unsigned tq = (lane & 15) >> 2, tp = lane & 3, tblk = (lane >> 4) & 1;
  const unsigned vaddr0 = LDS_RING + (4 * hi + tq) * 256 + ((tq ^ (pos & 3)) << 6) + ((2 * tblk + (tp >> 1)) << 4) + ((tp & 1) << 3);
  f32x16 o[4];
#pragma unroll
  for (int i = 0; i < 4; ++i)
#pragma unroll
    for (int r = 0; r < 16; ++r) o[i][r] = 0.f;
  auto weights = [&]() {
    wait_above<1>(lbase + LDS_CTL + CTL_MLOK + min(lane, NC - 1) * 4, 0);
    unsigned a = LDS_ML + r32 * 8;
    asm volatile("" : "+v"(a) :: "memory");
    float M = -3.0e38f, L = 0.f;
    f32x2 ml[NC];
#pragma unroll
    for (int i = 0; i < NC; ++i) { ml[i] = *(LAS f32x2*)(lds + a + i * 256); M = fmaxf(M, ml[i][0]); }
#pragma unroll
    for (int i = 0; i < NC; ++i) L = fmaf(__builtin_amdgcn_exp2f((ml[i][0] - M) * CS), ml[i][1], L);
    const float inv = 1.0f / L;
    *(LAS float*)(lds + (a >> 1) + (LDS_WT - LDS_ML / 2) + pos * 128) = __builtin_amdgcn_exp2f((mprev - M) * CS) * inv;
    const float ct = __builtin_amdgcn_exp2f((m - M) * CS) * inv;
#pragma unroll
    for (int s = 0; s < 2; ++s) {
      u32x4 x = *(LAS u32x4*)(lds + ppst + W_LAST + s * 1024);
#pragma unroll
      for (int e = 0; e < 4; ++e) x[e] = pack2(__uint_as_float(x[e] << 16) * ct, __uint_as_float(x[e] & 0xffff0000u) * ct);
      *(LAS u32x4*)(lds + ppst + W_LAST + s * 1024) = x;
    }
    LGKM0();
    ctl[CTL_WTOK / 4 + w] = 1;
  };
  resc &= ~1u;
  if (nmine <= 2) weights();
#pragma unroll 1
  for (int k = 0; k < nmine - 1; ++k) {
    if (k == 1) weights();
    if ((resc >> k) & 1) {
      const float alpha = *(LAS float*)(lds + palp + k * 128);
#pragma unroll
      for (int i = 0; i < 4; ++i)
#pragma unroll
        for (int r = 0; r < 16; ++r) o[i][r] *= alpha;
    }
    const unsigned u = NT + w + NC * k;
    u32x4 pf[2];
    if (k >= KREG) {
      pf[0] = *(LAS u32x4*)(lds + ppst + (k - KREG) * 2048);
      pf[1] = *(LAS u32x4*)(lds + ppst + (k - KREG) * 2048 + 1024);
    } else {
      asm volatile("s_set_gpr_idx_on %8, gpr_idx(SRC0)\n\t"
                   "v_mov_b32 %0, v128\n\tv_mov_b32 %1, v129\n\tv_mov_b32 %2, v130\n\tv_mov_b32 %3, v131\n\t"
                   "v_mov_b32 %4, v132\n\tv_mov_b32 %5, v133\n\tv_mov_b32 %6, v134\n\tv_mov_b32 %7, v135\n\t"
                   "s_set_gpr_idx_off\n\ts_nop 1"
                   : "=v"(pf[0][0]), "=v"(pf[0][1]), "=v"(pf[0][2]), "=v"(pf[0][3]), "=v"(pf[1][0]), "=v"(pf[1][1]), "=v"(pf[1][2]), "=v"(pf[1][3])
                   : "s"(8 * k));
    }
    asm volatile("" : "+v"(pf[0]), "+v"(pf[1]), "+v"(o[0]), "+v"(o[1]), "+v"(o[2]), "+v"(o[3]));
    if (k == nmine - 2) wait_above<1>(lbase + LDS_CTL + CTL_WTOK + min(lane, NC - 1) * 4, 0);
    wait_above<CSLEEP>(aland, 4 * u);
    if (TPRIO && k == nmine - 2) __builtin_amdgcn_s_setprio(TPRIO);
    bf16x8 vf[4][2];
#pragma unroll
    for (int i = 0; i < 4; ++i) {
      const unsigned a = (vaddr0 + ((u & 7) << 13)) ^ (i << 6);
#pragma unroll
      for (int s = 0; s < 2; ++s) {
        const s16x4 lo = __builtin_amdgcn_ds_read_tr16_b64_v4i16((LAS s16x4*)(lds + a + 4096 * s));
        const s16x4 up = __builtin_amdgcn_ds_read_tr16_b64_v4i16((LAS s16x4*)(lds + a + 4096 * s + 2048));
        vf[i][s] = __builtin_bit_cast(bf16x8, __builtin_shufflevector(lo, up, 0, 1, 2, 3, 4, 5, 6, 7));
      }
    }
    asm volatile("s_waitcnt lgkmcnt(0)" : "+v"(vf[0][0]), "+v"(vf[0][1]), "+v"(vf[1][0]), "+v"(vf[1][1]), "+v"(vf[2][0]), "+v"(vf[2][1]), "+v"(vf[3][0]), "+v"(vf[3][1]) :: "memory");
    ctl[CTL_FREE / 4 + (u & 7)] = u + 8;
    if (pf_on) prefetch(u);
    if (toucher) touch_at(u);
#pragma unroll
    for (int s = 0; s < 2; ++s)
#pragma unroll
      for (int i = 0; i < 4; ++i) o[i] = __builtin_amdgcn_mfma_f32_32x32x16_bf16(vf[i][s], __builtin_bit_cast(bf16x8, pf[s]), o[i], 0, 0, 0);
  }
  STAMP(3);

  {
    unsigned l16 = lane * 16;
    asm volatile("" : "+v"(l16));
    const unsigned l4 = l16 >> 2, awt = LDS_WT + (l4 & 127);
    auto put = [&](unsigned dst, const f32x16& x) {
#pragma unroll
      for (int j = 0; j < 4; ++j) *(LAS f32x4*)(lds + dst + j * 1024) = f32x4{x[4 * j], x[4 * j + 1], x[4 * j + 2], x[4 * j + 3]};
    };
    float co[6];
    bf16x8 pl[NC][2];
    auto ld_p = [&](int p) {
      pl[p][0] = *(LAS bf16x8*)(lds + LDS_WREG + p * W_SIZE + W_LAST + l16);
      pl[p][1] = *(LAS bf16x8*)(lds + LDS_WREG + p * W_SIZE + W_LAST + l16 + 1024);
    };
#pragma unroll
    for (int i = 0; i < 4; ++i) co[i] = *(LAS float*)(lds + awt + ((pos & 3) ^ i) * 128);
    co[4] = *(LAS float*)(lds + awt + 4 * 128);
    co[5] = *(LAS float*)(lds + awt + 5 * 128);
    ld_p(0); ld_p(1);
    asm volatile("" : "+v"(l16) :: "memory");
    if (nmine > 1) {
      if (pos > 3) put(LDS_RING + ((2 * NT - 2 * NC + pos) & 7) * 8192 + l16, o[0]);
#pragma unroll
      for (int i = 1; i < 4; ++i) put(LDS_WREG + pos * W_SIZE + (i - 1) * 4096 + l16, o[i]);
      LGKM0();
      ctl[CTL_ODONE / 4 + pos] = 1;
    }
    if (pos < 4) {
      f32x16 of = o[0], ot;
#pragma unroll
      for (int r = 0; r < 16; ++r) ot[r] = 0.f;
      auto src_o = [&](int p) -> unsigned {
        const int i = pos ^ (p & 3);
        return l16 + (i == 0 ? LDS_RING + ((2 * NT - 2 * NC + p) & 7) * 8192 : LDS_WREG + p * W_SIZE + (i - 1) * 4096);
      };
      auto fma_o = [&](unsigned src, float c) {
#pragma unroll
        for (int j = 0; j < 4; ++j) {
          const f32x4 x = *(LAS f32x4*)(lds + src + j * 1024);
#pragma unroll
          for (int e = 0; e < 4; ++e) of[4 * j + e] = fmaf(x[e], c, of[4 * j + e]);
        }
      };
      auto mma_t = [&](int p, bf16x8 p0, bf16x8 p1) {
        const unsigned a = vaddr0 + (((2 * NT - NC + p) & 7) << 13);
        bf16x8 vf[2];
#pragma unroll
        for (int s = 0; s < 2; ++s) {
          const s16x4 lo = __builtin_amdgcn_ds_read_tr16_b64_v4i16((LAS s16x4*)(lds + a + 4096 * s));
          const s16x4 up = __builtin_amdgcn_ds_read_tr16_b64_v4i16((LAS s16x4*)(lds + a + 4096 * s + 2048));
          vf[s] = __builtin_bit_cast(bf16x8, __builtin_shufflevector(lo, up, 0, 1, 2, 3, 4, 5, 6, 7));
        }
        ot = __builtin_amdgcn_mfma_f32_32x32x16_bf16(vf[0], p0, ot, 0, 0, 0);
        ot = __builtin_amdgcn_mfma_f32_32x32x16_bf16(vf[1], p1, ot, 0, 0, 0);
      };
      if (NT >= 2 * NC) {
#define PIN_OF() asm volatile("" : "+v"(of), "+v"(ot))
#pragma unroll
        for (int r = 0; r < 16; ++r) of[r] *= co[0];
        const bool wl = (l4 & 24) == 0;
        const unsigned ua = 4 * (2 * NT - NC);
        wait_above_v(wl ? aland : lbase + LDS_CTL + CTL_ODONE + (l4 & 12), wl ? ua : 0u);
        mma_t(0, pl[0][0], pl[0][1]);
#pragma unroll
        for (int i = 1; i < 4; ++i) fma_o(LDS_WREG + (pos ^ i) * W_SIZE + (i - 1) * 4096 + l16, co[i]);
        PIN_OF();
        STAMP(4);
        ld_p(2); ld_p(3); ld_p(4); ld_p(5);
        wait_above_v(wl ? aland : lbase + LDS_CTL + CTL_ODONE + 16 + (l4 & 4), wl ? ua + 8 : 0u);
        mma_t(1, pl[1][0], pl[1][1]);
        mma_t(2, pl[2][0], pl[2][1]);
        fma_o(src_o(4), co[4]);
        fma_o(src_o(5), co[5]);
#pragma unroll
        for (int r = 0; r < 16; ++r) { ot[r] += of[r]; of[r] = 0.f; }
        asm volatile("" : "+v"(ot));
        wait_above<0>(aland, ua + 20);
        mma_t(3, pl[3][0], pl[3][1]);
        mma_t(4, pl[4][0], pl[4][1]);
        mma_t(5, pl[5][0], pl[5][1]);
        of = ot;
      } else {
        wait_above<1>(lbase + LDS_CTL + CTL_WTOK + min(l4, (NC - 1) * 4u), 0);
        const float cw = *(LAS float*)(lds + awt + pos * 128);
#pragma unroll
        for (int r = 0; r < 16; ++r) of[r] *= cw;
#pragma unroll 1
        for (int p = max(NC - NT, 0); p < NC; ++p) {
          if (p != pos && NT - 2 * NC + p >= 0) {
            wait_above<1>(lbase + LDS_CTL + CTL_ODONE + p * 4, 0);
            fma_o(src_o(p), *(LAS float*)(lds + awt + p * 128));
          }
          wait_above<1>(aland, 4 * (2 * NT - NC + p));
          mma_t(p, *(LAS bf16x8*)(lds + LDS_WREG + p * W_SIZE + W_LAST + l16), *(LAS bf16x8*)(lds + LDS_WREG + p * W_SIZE + W_LAST + l16 + 1024));
        }
#pragma unroll
        for (int r = 0; r < 16; ++r) of[r] += ot[r];
      }
      const __amdgpu_buffer_rsrc_t ro = __builtin_amdgcn_make_buffer_rsrc(outp, 0, NB * HQ * SQ * HD * 2, 0x00020000);
      const unsigned orow = (unsigned)(b * HQ + 2 * h) * SQ * HD * 2 + pos * 64 + ((l16 & 511) << 4) + ((l16 >> 5) & 16);
#pragma unroll
      for (int j = 0; j < 2; ++j) {
        const unsigned a0 = pack2(of[8 * j], of[8 * j + 1]), a1 = pack2(of[8 * j + 2], of[8 * j + 3]);
        const unsigned b0 = pack2(of[8 * j + 4], of[8 * j + 5]), b1 = pack2(of[8 * j + 6], of[8 * j + 7]);
        auto s0 = __builtin_amdgcn_permlane32_swap(a0, b0, false, false);
        auto s1 = __builtin_amdgcn_permlane32_swap(a1, b1, false, false);
        u32x4 val = {s0[0], s1[0], s0[1], s1[1]};
        __builtin_amdgcn_raw_buffer_store_b128(val, ro, orow + j * 32, 0, OUTAUX);
      }
    }
  }
}
}

extern "C" void kernel_launch(void* const* d_in, const int* in_sizes, int n_in, void* d_out, int out_size, void* d_ws,
                              size_t ws_size, hipStream_t stream) {
  (void)n_in; (void)out_size; (void)ws_size;
  const unsigned nphys = (unsigned)(in_sizes[1] / (PAGE * HKV * HD));
  const unsigned kvbytes = nphys * PAGE_BYTES;
  (void)hipFuncSetAttribute((const void*)paged_attn, hipFuncAttributeMaxDynamicSharedMemorySize, LDS_TOTAL);
  hipLaunchKernelGGL(paged_attn, dim3(256), dim3(512), LDS_TOTAL, stream, d_in[0], d_in[1], d_in[2], (const int*)d_in[3],
                     (const int*)d_in[4], d_out, (char*)d_ws, kvbytes, nphys);
}
```

```cpp
#define PACE_X16 438
#define SOFT 90
#include <hip/hip_runtime.h>
#include <hip/hip_bf16.h>

#define PROBE_G 0
#define QPOL "nt"
#define PF_2ND
#define TLBW 5
#define TLBG 0x200000u
#define SOFT_AT 0
#define SORTP 1
#define TORDER 0
#define PF_OFF 6
#define CODEPF 15360
#define TMODE 1
#define TK2 0
#define TV 16
#define TAHEAD 2
#define PF_BY 0
#define PF_END 2u
#define PF_X64B 72
#define PF_DENSE 1
#define PF_PER 4
#define PF_PSTEP 2
#define GAP_TICKS 0
#define OUTAUX 0
#define HOFF 4
#define LSLEEP 0
#define PRESLEEP 0
#define PF_EVERY 2
#define PF_X64 76
#define PF_ALL 0
#define PF_LEAD 300
#define PF_POL ""
#define PF_SLOW 0
#define PF_RUN 3
#define TSLEEP 0
#define TWAIT 1
#define TPRIO 0
#define CSLEEP 1

#define STAMP(i) do {} while (0)

namespace {
constexpr int NB = 16, HQ = 32, HKV = 16, SQ = 16, HD = 128, PAGE = 16, NBPS = 256, SKV = NBPS * PAGE;
constexpr unsigned PAGE_BYTES = PAGE * HKV * HD * 2;
constexpr unsigned INVALID = 0x80000000u;
constexpr int NC = 6;
constexpr int KREG = 16, KLDS = 5;
static_assert(NC * (KREG + KLDS + 1) >= NBPS / 2, "P storage");
constexpr unsigned LDS_RING = 0;
constexpr unsigned LDS_WREG = 65536;
constexpr unsigned W_ALP = KLDS * 2048;
constexpr unsigned W_LAST = W_ALP + (KREG + KLDS + 1) * 128;
constexpr unsigned W_SIZE = W_LAST + 2048;
static_assert(W_LAST >= 3 * 4096 && W_SIZE % 256 == 0, "consumer region");
constexpr unsigned LDS_TBL = LDS_WREG + W_SIZE;
constexpr unsigned LDS_SRT = LDS_WREG + 3 * W_SIZE;
constexpr unsigned LDS_Q = LDS_WREG;
constexpr unsigned LDS_ML = LDS_WREG + NC * W_SIZE;
constexpr unsigned LDS_WT = LDS_ML + NC * 256;
constexpr unsigned LDS_CTL = LDS_WT + NC * 128;
constexpr unsigned CTL_LAND = 0;
constexpr unsigned CTL_QOK = 16;
constexpr unsigned CTL_FREE = 32;
constexpr unsigned CTL_MLOK = 64;
constexpr unsigned CTL_ODONE = 96;
constexpr unsigned CTL_WTOK = 128;
constexpr unsigned LDS_DUMMY = LDS_CTL + 256;
constexpr unsigned LDS_PLIST = LDS_DUMMY + 256;
constexpr unsigned LDS_TOTAL = LDS_PLIST + 1024;
static_assert(LDS_TOTAL <= 163840, "lds");

typedef __attribute__((ext_vector_type(8))) __bf16 bf16x8;
typedef __attribute__((ext_vector_type(2))) __bf16 bf16x2;
typedef __attribute__((ext_vector_type(4))) short s16x4;
typedef __attribute__((ext_vector_type(16))) float f32x16;
typedef __attribute__((ext_vector_type(4))) float f32x4;
typedef __attribute__((ext_vector_type(2))) float f32x2;
typedef __attribute__((ext_vector_type(4))) unsigned u32x4;
typedef __attribute__((ext_vector_type(2))) unsigned u32x2;
typedef __attribute__((ext_vector_type(4))) int i32x4;
#define LAS __attribute__((address_space(3)))

__device__ __forceinline__ float swap_max(float x) {
  auto r = __builtin_amdgcn_permlane32_swap(__float_as_uint(x), __float_as_uint(x), false, false);
  return fmaxf(__uint_as_float(r[0]), __uint_as_float(r[1]));
}
__device__ __forceinline__ float swap_sum(float x) {
  auto r = __builtin_amdgcn_permlane32_swap(__float_as_uint(x), __float_as_uint(x), false, false);
  return __uint_as_float(r[0]) + __uint_as_float(r[1]);
}
__device__ __forceinline__ unsigned pack2(float a, float b) {
  bf16x2 v = {(__bf16)a, (__bf16)b};
  return __builtin_bit_cast(unsigned, v);
}
template <int CTRL> __device__ __forceinline__ int dpp_add(int x) { return x + __builtin_amdgcn_update_dpp(0, x, CTRL, 0xf, 0xf, true); }
__device__ __forceinline__ unsigned now32() { return (unsigned)__builtin_amdgcn_s_memrealtime(); }
__device__ __forceinline__ int to_sgpr(int v) {
  int r;
  asm volatile("s_nop 1\n\tv_readfirstlane_b32 %0, %1" : "=s"(r) : "v"(v));
  return r;
}
#define LGKM0() asm volatile("s_waitcnt lgkmcnt(0)" ::: "memory")
#define VMWAIT(n) asm volatile("s_waitcnt vmcnt(" #n ")" ::: "memory")
#define CBAR() asm volatile("" ::: "memory")
template <int SLEEP> __device__ __forceinline__ void wait_above(unsigned addr, unsigned u) {
  unsigned v, cnt;
  if (SLEEP)
    asm volatile("s_mov_b32 %1, 0x200000\n\ts_branch 2f\n"
                 "1:\n\ts_sleep %4\n"
                 "2:\n\tds_read_b32 %0, %2\n\ts_sub_u32 %1, %1, 1\n\ts_waitcnt lgkmcnt(0)\n\t"
                 "v_cmp_ge_u32 vcc, %3, %0\n\ts_cbranch_scc1 3f\n\ts_nop 1\n\ts_cbranch_vccnz 1b\n3:"
                 : "=&v"(v), "=&s"(cnt) : "v"(addr), "s"(u), "n"(SLEEP) : "memory", "scc", "vcc");
  else
    asm volatile("s_mov_b32 %1, 0x200000\n"
                 "1:\n\tds_read_b32 %0, %2\n\ts_sub_u32 %1, %1, 1\n\ts_waitcnt lgkmcnt(0)\n\t"
                 "v_cmp_ge_u32 vcc, %3, %0\n\ts_cbranch_scc1 3f\n\ts_nop 1\n\ts_cbranch_vccnz 1b\n3:"
                 : "=&v"(v), "=&s"(cnt) : "v"(addr), "s"(u) : "memory", "scc", "vcc");
}

__device__ __forceinline__ void wait_above_v(unsigned addr, unsigned u) {
  unsigned v, cnt;
  asm volatile("s_mov_b32 %1, 0x200000\n"
               "1:\n\tds_read_b32 %0, %2\n\ts_sub_u32 %1, %1, 1\n\ts_waitcnt lgkmcnt(0)\n\t"
               "v_cmp_ge_u32 vcc, %3, %0\n\ts_cbranch_scc1 3f\n\ts_nop 1\n\ts_cbranch_vccnz 1b\n3:"
               : "=&v"(v), "=&s"(cnt) : "v"(addr), "v"(u) : "memory", "scc", "vcc");
}

__global__ void __launch_bounds__(512, 1) __attribute__((amdgpu_num_vgpr(64)))
paged_attn(const void* __restrict__ qp, const void* __restrict__ kp, const void* __restrict__ vp, const int* __restrict__ bt,
           const int* __restrict__ lens, void* __restrict__ outp, char* __restrict__ ws, unsigned kvbytes, unsigned nphys) {
  extern __shared__ __attribute__((aligned(16))) char smem[];
  LAS char* lds = (LAS char*)smem;
  const int lane0 = threadIdx.x & 63;
  const int wv = __builtin_amdgcn_readfirstlane(threadIdx.x >> 6);
  const int g = blockIdx.x;
  const int h = g >> 4;
  constexpr float CS = 0.08838834764831845f * 1.4426950408889634f;
  volatile LAS unsigned* ctl = (volatile LAS unsigned*)(lds + LDS_CTL);

  const unsigned lbase = (unsigned)(size_t)lds;
  const bool toucher = TLBW && (g & 8) != 0;
  const unsigned nchunk = (kvbytes + 16 * TLBG - 1) / (16 * TLBG);

  unsigned lraw, first;
  u32x4 lq;
  {
    const __amdgpu_buffer_rsrc_t rb = __builtin_amdgcn_make_buffer_rsrc((void*)bt, 0, wv >= 4 ? NB * NBPS * 4 : 0, 0x00020000);
    asm volatile("global_load_dword %0, %3, %4\n\t"
                 "global_load_dwordx4 %1, %5, %4\n\t"
                 "global_load_dword %2, %6, %7\n\t"
                 "s_mov_b32 m0, %8\n\ts_nop 0\n\t"
                 "buffer_load_dwordx4 %9, %10, 0 offen lds\n\t"
                 "buffer_load_dwordx4 %9, %10, 0 offen offset:1024 lds\n\t"
                 "buffer_load_dwordx4 %9, %10, 0 offen offset:2048 lds\n\t"
                 "buffer_load_dwordx4 %9, %10, 0 offen offset:3072 lds"
                 : "=&v"(lraw), "=&v"(lq), "=&v"(first)
                 : "v"((lane0 & 15) * 4), "s"(lens), "v"(lane0 & 48), "v"((lane0 & 15) * (wv < 2 ? NBPS * 4 : 0) + (wv & 1) * 4), "s"(bt),
                   "s"(lbase + (wv >= 4 ? LDS_TBL + ((wv >> 1) & 1) * W_SIZE : LDS_WREG + 4 * W_SIZE) + (wv & 1) * 4096), "v"(lane0 * 16 + (wv & 3) * 4096), "s"(rb),
                   "s"(qp), "s"(kp), "s"(vp), "s"(outp), "s"(kvbytes), "s"(nphys)
                 : "memory");
    __builtin_amdgcn_sched_barrier(0);
    unsigned long long pc;
    asm volatile("s_getpc_b64 %0" : "=s"(pc));
    const __amdgpu_buffer_rsrc_t rt = __builtin_amdgcn_make_buffer_rsrc(wv == 5 ? (void*)pc : (void*)kp, 0,
                                                                        wv == 5 ? CODEPF : (wv == 6 && toucher) ? (int)min(kvbytes, TAHEAD * 16 * TLBG) : 0, 0x00020000);
    const unsigned mul = wv == 5 ? 512u : 16 * TLBG + 4224u, add = wv == 5 ? ((g >> 3) & 3) * 128 : (g >> 4) * (TLBG + 33024u);
    asm volatile("s_mov_b32 m0, %0\n\ts_nop 0\n\tbuffer_load_dword %1, %2, 0 offen lds"
                 :: "s"(lbase + LDS_DUMMY), "v"((lane0 & 31) * mul + add), "s"(rt) : "memory");
  }
  if (wv < 2) __builtin_amdgcn_s_setprio(3);
  if (threadIdx.x < 384) *(LAS u32x4*)(lds + LDS_SRT + threadIdx.x * 16) = u32x4{0u, 0u, 0u, 0u};
  else if (threadIdx.x < 424) ctl[lane0] = ((lane0 >> 3) == CTL_FREE / 32) ? (lane0 & 7) : 0u;
  const unsigned rstride = wv < 2 ? 4096 : 256;
  unsigned vo0 = (lane0 >> 4) * rstride + (((unsigned)(lane0 & 15) ^ ((lane0 >> 4) << 2)) << 4) + (wv < 2 ? h * (HD * 2) : 0);
  unsigned vo1 = (vo0 + 4 * rstride - 1024) ^ 16, vo2 = (vo0 + 8 * rstride - 2048) ^ 32, vo3 = (vo0 + 12 * rstride - 3072) ^ 48;
  const unsigned want = (g & 8) ? 15 - (g & 7) : (g & 7);
  const unsigned dst0 = lbase + (wv < 2 ? LDS_RING : LDS_Q) + (wv & 1) * 4096;
  asm volatile("s_waitcnt vmcnt(1)" : "+v"(lraw), "+v"(lq), "+v"(first), "+v"(vo0), "+v"(vo1), "+v"(vo2), "+v"(vo3) : "s"(dst0) : "memory");
  unsigned lm1 = min(lraw - 1u, (unsigned)SKV - 1u);
  unsigned rank = 0;
  {
    const unsigned key = lm1 * 16 + (lane0 & 15);
#pragma unroll
    for (int j = 0; j < 4; ++j) rank += (min(lq[j] - 1u, (unsigned)SKV - 1u) * 16 + ((lane0 >> 4) * 4 + j) > key) ? 1u : 0u;
    auto r1 = __builtin_amdgcn_permlane16_swap(rank, rank, false, false);
    rank = r1[0] + r1[1];
    auto r2 = __builtin_amdgcn_permlane32_swap(rank, rank, false, false);
    rank = r2[0] + r2[1];
  }
  first = min(first, nphys - 1) * PAGE_BYTES;
  const int b = __builtin_ctz((unsigned)__builtin_amdgcn_ballot_w64(rank == want) | 0x8000u);
  unsigned long long tstart = 0;
  if (wv < 4) {
    const bool qok = (int)__builtin_amdgcn_readlane(lraw, b) > 0;
    const __amdgpu_buffer_rsrc_t r0 = __builtin_amdgcn_make_buffer_rsrc(wv < 2 ? (void*)kp : (void*)qp, 0,
                                                                        wv < 2 ? (int)kvbytes : qok ? NB * HQ * SQ * HD * 2 : 0, 0x00020000);
    const unsigned so = wv < 2 ? __builtin_amdgcn_readlane(first, b) : qok ? (unsigned)((b * HQ + 2 * h) * SQ * HD * 2) + (wv & 1) * 4096 : 0u;
    asm volatile("s_waitcnt vmcnt(0)\n\t"
                 "s_mov_b32 m0, %2\n\t"
                 "s_memrealtime %0\n\t"
                 "s_nop 2\n\t"
                 "buffer_load_dwordx4 %4, %1, %3 offen nt lds\n\t"
                 "buffer_load_dwordx4 %5, %1, %3 offen offset:1024 nt lds\n\t"
                 "buffer_load_dwordx4 %6, %1, %3 offen offset:2048 nt lds\n\t"
                 "buffer_load_dwordx4 %7, %1, %3 offen offset:3072 nt lds\n\t"
                 "s_waitcnt lgkmcnt(0)"
                 : "=&s"(tstart) : "s"(r0), "s"(dst0), "s"(so), "v"(vo0), "v"(vo1), "v"(vo2), "v"(vo3) : "memory");
  }
  asm volatile("s_waitcnt lgkmcnt(0)\n\ts_barrier" : "+v"(lm1), "+v"(rank) :: "memory");
  __builtin_amdgcn_sched_barrier(0);
  const int bp = __builtin_ctz((unsigned)__builtin_amdgcn_ballot_w64(rank == 15 - want) | 0x8000u);
  const int len = __builtin_amdgcn_readlane(lm1, b) + 1, nown = (len + PAGE - 1) / PAGE, NT = (nown + 1) >> 1;
  const int npl = (__builtin_amdgcn_readlane(lm1, bp) >> 4) + 1, NTL = (npl + 1) >> 1;
  auto total = [&]() __attribute__((always_inline)) {
    int t = lm1 + 1;
    t = dpp_add<0x121>(t); t = dpp_add<0x122>(t); t = dpp_add<0x124>(t); t = dpp_add<0x128>(t);
    return t;
  };

  auto sort_pages = [&](int lane, int bb, int n, unsigned scr, unsigned dst) __attribute__((always_inline)) {
    u32x4 e = *(LAS u32x4*)(lds + LDS_TBL + (bb >> 3) * W_SIZE + (bb & 7) * 1024 + lane * 16);
    bool in[4];
#pragma unroll
    for (int j = 0; j < 4; ++j) {
      e[j] = min(e[j], nphys - 1);
      in[j] = (unsigned)(4 * lane + j - 2) < (unsigned)(n - 3);
      __hip_atomic_fetch_or((LAS unsigned*)(lds + scr + ((e[j] >> 5) & 255) * 8), in[j] ? 1u << (e[j] & 31) : 0u, __ATOMIC_RELAXED, __HIP_MEMORY_SCOPE_WAVEFRONT);
    }
    CBAR();
    u32x4 a = *(LAS u32x4*)(lds + scr + lane * 32), c = *(LAS u32x4*)(lds + scr + lane * 32 + 16);
    const int n0 = __builtin_popcount(a[0]), n1 = n0 + __builtin_popcount(a[2]), n2 = n1 + __builtin_popcount(c[0]), n3 = n2 + __builtin_popcount(c[2]);
    int sc = n3;
    sc = dpp_add<0x111>(sc); sc = dpp_add<0x112>(sc); sc = dpp_add<0x114>(sc); sc = dpp_add<0x118>(sc);
    sc += __builtin_amdgcn_update_dpp(0, sc, 0x142, 0xa, 0xf, false);
    sc += __builtin_amdgcn_update_dpp(0, sc, 0x143, 0xc, 0xf, false);
    const bool ok = SORTP && nphys <= 8192u && __builtin_amdgcn_readlane(sc, 63) == n - 3;
    sc += 2 - n3;
    a[1] = sc; a[3] = sc + n0; c[1] = sc + n1; c[3] = sc + n2;
    *(LAS u32x4*)(lds + scr + lane * 32) = a;
    *(LAS u32x4*)(lds + scr + lane * 32 + 16) = c;
    CBAR();
    u32x2 wp[4];
#pragma unroll
    for (int j = 0; j < 4; ++j) wp[j] = *(LAS u32x2*)(lds + scr + ((e[j] >> 5) & 255) * 8);
#pragma unroll
    for (int j = 0; j < 4; ++j) {
      const unsigned r = wp[j][1] + __builtin_popcount(wp[j][0] & ((1u << (e[j] & 31)) - 1u));
      *(LAS unsigned*)(lds + dst + ((ok && in[j]) ? r : 4 * lane + j) * 4) = e[j];
    }
    CBAR();
  };

  if (__builtin_expect(wv < 2 || wv == 7, 1))
    sort_pages(lane0, wv < 2 ? b : bp, wv < 2 ? nown : npl, LDS_SRT + min(wv, 2) * 2048, wv < 2 ? LDS_SRT + 6144 + wv * 1024 : LDS_PLIST);

  if (__builtin_expect(wv < 2, 1)) {
    const int half = wv, lane = lane0;
    const unsigned vland = lbase + LDS_CTL + CTL_LAND + half * 4;
    const unsigned vfree = lbase + LDS_CTL + CTL_FREE + (lane & 7) * 4;
    const unsigned ring = dst0;
    unsigned l0 = *(LAS unsigned*)(lds + LDS_SRT + 6144 + half * 1024 + min(2 * lane + half, nown - 1) * 4);
    unsigned l1 = *(LAS unsigned*)(lds + LDS_SRT + 6144 + half * 1024 + min(2 * lane + half + 128, nown - 1) * 4);
    const int T16 = total() * PACE_X16;
    const int step = to_sgpr((int)((float)T16 * __builtin_amdgcn_rcpf((float)max(len, 32))));
    const unsigned due0 = ((unsigned)tstart << 8) + half * ((step * HOFF) >> 3);
    const int ua = SOFT_AT ? to_sgpr(min(min(NT, 64), 1 + (int)((float)(SOFT_AT * 256) * __builtin_amdgcn_rcpf((float)step)))) : 1;
    l0 *= PAGE_BYTES;
    l1 *= PAGE_BYTES;
    const unsigned lastrec = (unsigned)__builtin_amdgcn_readlane(NT > 64 ? l1 : l0, (NT - 1) & 63) + (unsigned)to_sgpr(min(max(len - (2 * (NT - 1) + half) * PAGE, 0), PAGE)) * 4096u;
#define CNT(r)
    unsigned vf, vt, lc;
    asm volatile(
        "s_waitcnt lgkmcnt(0)\n\t"
        "s_memrealtime s[68:69]\n\t"
        "ds_read_b32 %[vf], %[vfree]\n\t"
        "s_mov_b32 s60, 1\n\t"
        "s_mov_b32 s61, 4\n\t"
        "s_mov_b32 s62, 0x400000\n\t"
        "s_mov_b32 s66, 0\n\t"
        "s_mov_b32 s67, %[ua]\n\t"
        "s_mov_b32 s72, 0x2000\n\t"
        "s_mov_b32 s73, 1\n\t"
        "s_mov_b32 s74, %[due1]\n\t"
        "s_mov_b64 s[76:77], %[kp]\n\t"
        "s_mov_b32 s78, %[nrec]\n\t"
        "s_mov_b32 s79, 0x20000\n\t"
        "s_and_b32 s77, s77, 0xffff\n\t"
        "v_mov_b32 %[lc], %[l0]\n\t"
        "s_getpc_b64 s[70:71]\n"
        ".LldA%=:\n\t"
        "s_add_u32 s80, s70, .LldL%=-.LldA%=\n\t"
        "s_addc_u32 s71, s71, 0\n\t"
        "s_branch .LldF%=\n"
        ".LldR%=:\n\t"
        "s_sub_u32 s62, s62, 1\n\t"
        "s_cbranch_scc1 .LldE%=\n"
        ".LldT%=:\n\t"
        "s_getreg_b32 s64, hwreg(HW_REG_IB_STS)\n\t"
        "s_and_b32 s65, s64, 15\n\t"
        "s_bfe_u32 s64, s64, 0x20016\n\t"
        "s_lshl4_add_u32 s64, s64, s65\n\t"
        "s_add_u32 s64, s64, 3\n\t"
        "s_and_b32 s64, s64, 60\n\t"
        "s_sub_u32 s65, s61, s64\n\t"
        "s_cmp_lg_u32 s65, s66\n\t"
        "s_cbranch_scc1 .LldP%=\n"
        ".LldC%=:\n\t"
        "s_waitcnt lgkmcnt(0)\n\t"
        "v_cmp_eq_u32 vcc, s60, %[vf]\n\t"
        "s_lshl_b32 s64, s68, 8\n\t"
        "s_memrealtime s[68:69]\n\t"
        "ds_read_b32 %[vf], %[vfree]\n\t"
        CNT("s82")
        "s_sub_u32 s64, s64, s74\n\t"
        "s_cmp_lt_i32 s64, 0\n\t"
        "s_cbranch_scc1 .LldR%=\n\t"
        CNT("s83")
        "s_cbranch_vccz .LldR%=\n\t"
        CNT("s84")
        "buffer_load_dwordx4 %[o0], s[76:79], s63 offen nt lds\n\t"
        "s_add_u32 s74, s74, %[step]\n\t"
        "s_add_u32 s73, s73, 1\n\t"
        "buffer_load_dwordx4 %[o1], s[76:79], s63 offen offset:1024 nt lds\n\t"
        "s_add_u32 s60, s60, 1\n\t"
        "s_add_u32 s61, s61, 4\n\t"
        "buffer_load_dwordx4 %[o2], s[76:79], s63 offen offset:2048 nt lds\n\t"
        "s_add_u32 s72, s72, 0x2000\n\t"
        "s_and_b32 s72, s72, 0xe000\n\t"
        "buffer_load_dwordx4 %[o3], s[76:79], s63 offen offset:3072 nt lds\n"
        ".LldF%=:\n\t"
        "v_readlane_b32 s63, %[lc], s73\n\t"
        "s_add_u32 m0, s72, %[ring]\n\t"
        "s_cmp_eq_u32 s73, %[ntm]\n\t"
        "s_cselect_b32 s78, %[last], %[nrec]\n\t"
        "s_cmp_lg_u32 s60, s67\n\t"
        "s_cbranch_scc1 .LldT%=\n\t"
        "s_cmp_lg_u32 s60, %[ua]\n\t"
        "s_cbranch_scc1 .LldG%=\n\t"
        "s_add_u32 s74, s74, %[soft]\n\t"
        "s_min_u32 s67, %[nt], 64\n\t"
        "s_cmp_lg_u32 s60, s67\n\t"
        "s_cbranch_scc1 .LldT%=\n"
        ".LldG%=:\n\t"
        "s_cmp_eq_u32 s73, %[nt]\n\t"
        "s_cbranch_scc1 .LldV%=\n\t"
        "v_mov_b32 %[lc], %[l1]\n\t"
        "v_readfirstlane_b32 s63, %[l1]\n\t"
        "s_add_u32 s67, s60, %[nt]\n\t"
        "s_sub_u32 s67, s67, 64\n\t"
        "s_branch .LldT%=\n"
        ".LldV%=:\n\t"
        "s_cmp_lg_u32 s60, %[nt]\n\t"
        "s_cbranch_scc1 .LldD%=\n\t"
        "s_mov_b64 s[76:77], %[vp]\n\t"
        "s_and_b32 s77, s77, 0xffff\n\t"
        "s_mov_b32 s73, 0\n\t"
        "s_min_u32 s67, %[nt], 64\n\t"
        "s_add_u32 s67, s67, s60\n\t"
        "s_mov_b32 s74, %[duev]\n\t"
        "s_cmp_eq_u32 %[ntm], 0\n\t"
        "s_cselect_b32 s78, %[last], %[nrec]\n\t"
        "v_mov_b32 %[lc], %[l0]\n\t"
        "v_readfirstlane_b32 s63, %[l0]\n\t"
        "v_xor_b32 %[o1], 16, %[o1]\n\t"
        "v_xor_b32 %[o2], 32, %[o2]\n\t"
        "v_xor_b32 %[o3], 48, %[o3]\n\t"
        "s_branch .LldT%=\n"
        ".LldD%=:\n\t"
        "s_add_u32 s74, s74, 0x40000000\n\t"
        "s_mov_b32 s60, -1\n\t"
        "s_branch .LldT%=\n"
        ".LldP%=:\n\t"
        "s_min_u32 s64, s64, 32\n\t"
        "s_add_u32 s70, s80, s64\n\t"
        "v_mov_b32 %[vt], s65\n\t"
        "s_mov_b32 s66, s65\n\t"
        "s_setpc_b64 s[70:71]\n\t"
        ".p2align 6\n"
        ".LldL%=:\n\t"
        "s_waitcnt vmcnt(0)\n\t"
        "s_waitcnt vmcnt(4)\n\t"
        "s_waitcnt vmcnt(8)\n\t"
        "s_waitcnt vmcnt(12)\n\t"
        "s_waitcnt vmcnt(16)\n\t"
        "s_waitcnt vmcnt(20)\n\t"
        "s_waitcnt vmcnt(24)\n\t"
        "s_waitcnt vmcnt(28)\n\t"
        "s_waitcnt vmcnt(32)\n\t"
        "ds_write_b32 %[vland], %[vt]\n\t"
        "s_cmp_lg_u32 s65, %[fin]\n\t"
        "s_cbranch_scc1 .LldC%=\n"
        ".LldE%=:\n\t"
        "s_waitcnt vmcnt(0) lgkmcnt(0)\n\t"
        : [vf] "=&v"(vf), [vt] "=&v"(vt), [lc] "=&v"(lc), [o1] "+v"(vo1), [o2] "+v"(vo2), [o3] "+v"(vo3)
        : [l0] "v"(l0), [l1] "v"(l1), [o0] "v"(vo0), [vland] "v"(vland), [vfree] "v"(vfree),
          [kp] "s"(kp), [vp] "s"(vp), [nrec] "s"(kvbytes), [nt] "s"(NT), [ring] "s"(ring),
          [step] "s"(step), [due1] "s"(due0 + step), [duev] "s"(due0 + (to_sgpr(T16) >> 5) + (SOFT + GAP_TICKS) * 256), [last] "s"(lastrec), [ntm] "s"(NT - 1), [fin] "s"(8 * NT), [ua] "s"(ua), [soft] "n"(SOFT * 256)
        : "memory", "scc", "vcc", "s60", "s61", "s62", "s63", "s64", "s65", "s66", "s67", "s68", "s69", "s70", "s71", "s72", "s73", "s74",
          "s76", "s77", "s78", "s79", "s80", "s82", "s83", "s84");
    STAMP(3);
    asm volatile("s_endpgm" ::: "memory");
  }

  int lane = lane0;
  asm volatile("" : "+v"(lane));
  auto touch = [&](unsigned c, unsigned n) __attribute__((always_inline)) {
    const bool isv = c >= nchunk;
    const __amdgpu_buffer_rsrc_t rt = __builtin_amdgcn_make_buffer_rsrc(isv ? (void*)vp : (void*)kp, 0, (int)kvbytes, 0x00020000);
    unsigned ln = lane;
    asm volatile("" : "+v"(ln));
    const unsigned idx = (g >> 4) + 16 * (c - (isv ? nchunk : 0) + ln);
    const unsigned vo = (ln < n && idx < 0x80000000u / TLBG) ? idx * TLBG + ((idx * 30592u) & (TLBG - 128)) : INVALID;
    asm volatile("s_mov_b32 m0, %0\n\ts_nop 4\n\tbuffer_load_dword %2, %1, 0 offen nt lds" :: "s"((unsigned)(size_t)lds + LDS_DUMMY), "s"(rt), "v"(vo) : "memory");
  };
  if (wv < 4) {
    VMWAIT(0);
    ctl[CTL_QOK / 4 + (wv & 1)] = 1;
  }
  const int lim = len;
  const int tot = total();
  const int w = wv - 2;
  const int r32 = lane & 31, hi = lane >> 5;
  const int nmine = (NT > w) ? (NT - w + NC - 1) / NC : 0;
  const unsigned aland = lbase + LDS_CTL + CTL_LAND + (lane & 1) * 4;
  const int tots = __builtin_amdgcn_readfirstlane(tot);
  const bool pf_on = PF_EVERY && npl * 256 * 64 > tots * PF_X64B && npl > nown && (h & 3) == PF_BY;
  const unsigned pf_max = (npl * 256 * 64 > tots * PF_X64) ? 256u : PF_END;
  const unsigned pf_last = 2 * NT - to_sgpr((int)((float)(2 * NT * (PF_LEAD + 23 * PF_OFF)) * 4096.0f * __builtin_amdgcn_rcpf((float)(tot * PACE_X16))));
  const unsigned pf_ratio = to_sgpr((int)((float)NTL * 65536.0f * __builtin_amdgcn_rcpf((float)NT)));
  const unsigned pf_per = PF_PER + (g & 7) * PF_PSTEP, pf_inv = (65536 + pf_per - 1) / pf_per;
  auto prefetch = [&](unsigned op) {
    const unsigned d = pf_last - op;
    if (d >= 1024u) return;
    const unsigned r = d - ((d * pf_inv) >> 16) * pf_per;
    if (r >= PF_RUN) return;
    unsigned tl = 2 * NTL - 1 - PF_OFF - (((d - r) * pf_ratio) >> 16) - r;
    if (tl >= 2u * NTL) return;
    const bool isv = tl >= (unsigned)NTL;
    tl -= isv ? NTL : 0;
    const __amdgpu_buffer_rsrc_t rp = __builtin_amdgcn_make_buffer_rsrc(isv ? (void*)vp : (void*)kp, 0, (int)kvbytes, 0x00020000);
    unsigned ln = lane;
    asm volatile("" : "+v"(ln));
    const unsigned pg = min(*(LAS unsigned*)(lds + LDS_PLIST + min(2 * tl + (ln >> 5), (unsigned)npl - 1) * 4), nphys - 1);
    const unsigned vo = pg * PAGE_BYTES + ((h & 12) | 1) * (HD * 2) + ((ln >> 1) & 15) * 4096 + (PF_SLOW == 1 ? 128 : PF_SLOW == 2 ? 0 : (ln & 1) * 128);
    asm volatile("s_mov_b32 m0, %0\n\ts_nop 4\n\tbuffer_load_dword %2, %1, 0 offen " PF_POL " lds" PF_2ND :: "s"(lbase + LDS_DUMMY), "s"(rp), "v"(vo) : "memory");
  };
  const int pos = (w + NC * 22 - NT) % NC;
  const unsigned tch_rate = to_sgpr((int)((float)nchunk * 65536.0f * __builtin_amdgcn_rcpf((float)NT)));
  auto touch_at = [&](unsigned op) __attribute__((always_inline)) {
    if (TMODE == 0) {
      const unsigned c1 = ((op + 1) * tch_rate) >> 16, c0 = (op * tch_rate) >> 16;
      if (c1 != c0 && c0 + TAHEAD < 2 * nchunk) touch(c0 + TAHEAD, 1);
    } else {
      if (op == TK2 && TAHEAD < nchunk) touch(TAHEAD, 64);
      if (op == TV) touch(nchunk, 64);
    }
  };
  const unsigned palp = LDS_WREG + pos * W_SIZE + W_ALP + r32 * 4;
  const unsigned ppst = LDS_WREG + pos * W_SIZE + lane * 16;
  asm volatile("" ::: "v128", "v255");
  unsigned resc = 0;
  float m = -3.0e38f, mprev = -3.0e38f, l = 0.f;

  {
    const unsigned fk = (((unsigned)r32 & 3) << 2) | (((unsigned)r32 >> 2) & 3);
    const unsigned kaddr0 = LDS_RING + r32 * 256 + (((unsigned)hi ^ fk) << 4);
    bf16x8 qf[8];
    wait_above<1>(aland + (CTL_QOK - CTL_LAND), 0);
#pragma unroll
    for (int s = 0; s < 8; ++s) qf[s] = *(LAS bf16x8*)(lds + ((kaddr0 + LDS_Q - LDS_RING) ^ (s << 5)));
#pragma unroll 1
    for (int k = 0; k < nmine; ++k) {
      const unsigned t = w + NC * k;
      if (PRESLEEP && k > 0) __builtin_amdgcn_s_sleep(PRESLEEP);
      wait_above<CSLEEP>(aland, 4 * t);
      if (k == 0) STAMP(1);
      bf16x8 kf[8];
#pragma unroll
      for (int s = 0; s < 8; ++s) kf[s] = *(LAS bf16x8*)(lds + ((kaddr0 + ((t & 7) << 13)) ^ (s << 5)));
      asm volatile("s_waitcnt lgkmcnt(0)" : "+v"(kf[0]), "+v"(kf[1]), "+v"(kf[2]), "+v"(kf[3]), "+v"(kf[4]), "+v"(kf[5]), "+v"(kf[6]), "+v"(kf[7]) :: "memory");
      ctl[CTL_FREE / 4 + (t & 7)] = t + 8;
      if (pf_on) prefetch(t);
      if (toucher) touch_at(t);
      f32x16 sc;
#pragma unroll
      for (int r = 0; r < 16; ++r) sc[r] = 0.f;
#pragma unroll
      for (int s = 0; s < 8; ++s) sc = __builtin_amdgcn_mfma_f32_32x32x16_bf16(kf[s], qf[s], sc, 0, 0, 0);
      if ((int)t == NT - 1) {
        const int kb = 2 * t * PAGE + 4 * hi;
#pragma unroll
        for (int r = 0; r < 16; ++r) sc[r] = (kb + 8 * (r >> 2) + (r & 3) < lim) ? sc[r] : -__builtin_inff();
      }
      float mx = sc[0];
#pragma unroll
      for (int r = 1; r < 16; ++r) mx = fmaxf(mx, sc[r]);
      mx = swap_max(mx);
      const float mn = fmaxf(m, mx);
      const float alpha = __builtin_amdgcn_exp2f((m - mn) * CS);
      mprev = m;
      m = mn;
      const float nmc = -mn * CS;
      float ps = 0.f;
#pragma unroll
      for (int r = 0; r < 16; ++r) { sc[r] = __builtin_amdgcn_exp2f(fmaf(sc[r], CS, nmc)); ps += sc[r]; }
      l = fmaf(l, alpha, ps);
      u32x4 pf[2];
#pragma unroll
      for (int s = 0; s < 2; ++s)
        pf[s] = u32x4{pack2(sc[8 * s], sc[8 * s + 1]), pack2(sc[8 * s + 2], sc[8 * s + 3]), pack2(sc[8 * s + 4], sc[8 * s + 5]), pack2(sc[8 * s + 6], sc[8 * s + 7])};
      if (__builtin_amdgcn_ballot_w64(alpha != 1.0f)) resc |= 1u << k;
      *(LAS float*)(lds + palp + k * 128) = alpha;
      if (k >= KREG || k == nmine - 1) {
        const unsigned dst = ppst + ((k == nmine - 1) ? W_LAST : (k - KREG) * 2048);
        *(LAS u32x4*)(lds + dst) = pf[0];
        *(LAS u32x4*)(lds + dst + 1024) = pf[1];
      } else {
        asm volatile("s_set_gpr_idx_on %8, gpr_idx(DST)\n\t"
                     "v_mov_b32 v128, %0\n\tv_mov_b32 v129, %1\n\tv_mov_b32 v130, %2\n\tv_mov_b32 v131, %3\n\t"
                     "v_mov_b32 v132, %4\n\tv_mov_b32 v133, %5\n\tv_mov_b32 v134, %6\n\tv_mov_b32 v135, %7\n\t"
                     "s_set_gpr_idx_off"
                     :: "v"(pf[0][0]), "v"(pf[0][1]), "v"(pf[0][2]), "v"(pf[0][3]), "v"(pf[1][0]), "v"(pf[1][1]), "v"(pf[1][2]), "v"(pf[1][3]), "s"(8 * k));
      }
    }
  }
  l = swap_sum(l);
  *(LAS f32x2*)(lds + LDS_ML + w * 256 + r32 * 8) = f32x2{m, l};
  LGKM0();
  ctl[CTL_MLOK / 4 + w] = 1;
  STAMP(2);

  const unsigned tq = (lane & 15) >> 2, tp = lane & 3, tblk = (lane >> 4) & 1;
  const unsigned vaddr0 = LDS_RING + (4 * hi + tq) * 256 + ((tq ^ (pos & 3)) << 6) + ((2 * tblk + (tp >> 1)) << 4) + ((tp & 1) << 3);
  f32x16 o[4];
#pragma unroll
  for (int i = 0; i < 4; ++i)
#pragma unroll
    for (int r = 0; r < 16; ++r) o[i][r] = 0.f;
  auto weights = [&]() {
    wait_above<1>(lbase + LDS_CTL + CTL_MLOK + min(lane, NC - 1) * 4, 0);
    unsigned a = LDS_ML + r32 * 8;
    asm volatile("" : "+v"(a) :: "memory");
    float M = -3.0e38f, L = 0.f;
    f32x2 ml[NC];
#pragma unroll
    for (int i = 0; i < NC; ++i) { ml[i] = *(LAS f32x2*)(lds + a + i * 256); M = fmaxf(M, ml[i][0]); }
#pragma unroll
    for (int i = 0; i < NC; ++i) L = fmaf(__builtin_amdgcn_exp2f((ml[i][0] - M) * CS), ml[i][1], L);
    const float inv = 1.0f / L;
    *(LAS float*)(lds + (a >> 1) + (LDS_WT - LDS_ML / 2) + pos * 128) = __builtin_amdgcn_exp2f((mprev - M) * CS) * inv;
    const float ct = __builtin_amdgcn_exp2f((m - M) * CS) * inv;
#pragma unroll
    for (int s = 0; s < 2; ++s) {
      u32x4 x = *(LAS u32x4*)(lds + ppst + W_LAST + s * 1024);
#pragma unroll
      for (int e = 0; e < 4; ++e) x[e] = pack2(__uint_as_float(x[e] << 16) * ct, __uint_as_float(x[e] & 0xffff0000u) * ct);
      *(LAS u32x4*)(lds + ppst + W_LAST + s * 1024) = x;
    }
    LGKM0();
    ctl[CTL_WTOK / 4 + w] = 1;
  };
  resc &= ~1u;
  if (nmine <= 2) weights();
#pragma unroll 1
  for (int k = 0; k < nmine - 1; ++k) {
    if (k == 1) weights();
    if ((resc >> k) & 1) {
      const float alpha = *(LAS float*)(lds + palp + k * 128);
#pragma unroll
      for (int i = 0; i < 4; ++i)
#pragma unroll
        for (int r = 0; r < 16; ++r) o[i][r] *= alpha;
    }
    const unsigned u = NT + w + NC * k;
    u32x4 pf[2];
    if (k >= KREG) {
      pf[0] = *(LAS u32x4*)(lds + ppst + (k - KREG) * 2048);
      pf[1] = *(LAS u32x4*)(lds + ppst + (k - KREG) * 2048 + 1024);
    } else {
      asm volatile("s_set_gpr_idx_on %8, gpr_idx(SRC0)\n\t"
                   "v_mov_b32 %0, v128\n\tv_mov_b32 %1, v129\n\tv_mov_b32 %2, v130\n\tv_mov_b32 %3, v131\n\t"
                   "v_mov_b32 %4, v132\n\tv_mov_b32 %5, v133\n\tv_mov_b32 %6, v134\n\tv_mov_b32 %7, v135\n\t"
                   "s_set_gpr_idx_off\n\ts_nop 1"
                   : "=v"(pf[0][0]), "=v"(pf[0][1]), "=v"(pf[0][2]), "=v"(pf[0][3]), "=v"(pf[1][0]), "=v"(pf[1][1]), "=v"(pf[1][2]), "=v"(pf[1][3])
                   : "s"(8 * k));
    }
    asm volatile("" : "+v"(pf[0]), "+v"(pf[1]), "+v"(o[0]), "+v"(o[1]), "+v"(o[2]), "+v"(o[3]));
    if (k == nmine - 2) wait_above<1>(lbase + LDS_CTL + CTL_WTOK + min(lane, NC - 1) * 4, 0);
    wait_above<CSLEEP>(aland, 4 * u);
    if (TPRIO && k == nmine - 2) __builtin_amdgcn_s_setprio(TPRIO);
    bf16x8 vf[4][2];
#pragma unroll
    for (int i = 0; i < 4; ++i) {
      const unsigned a = (vaddr0 + ((u & 7) << 13)) ^ (i << 6);
#pragma unroll
      for (int s = 0; s < 2; ++s) {
        const s16x4 lo = __builtin_amdgcn_ds_read_tr16_b64_v4i16((LAS s16x4*)(lds + a + 4096 * s));
        const s16x4 up = __builtin_amdgcn_ds_read_tr16_b64_v4i16((LAS s16x4*)(lds + a + 4096 * s + 2048));
        vf[i][s] = __builtin_bit_cast(bf16x8, __builtin_shufflevector(lo, up, 0, 1, 2, 3, 4, 5, 6, 7));
      }
    }
    asm volatile("s_waitcnt lgkmcnt(0)" : "+v"(vf[0][0]), "+v"(vf[0][1]), "+v"(vf[1][0]), "+v"(vf[1][1]), "+v"(vf[2][0]), "+v"(vf[2][1]), "+v"(vf[3][0]), "+v"(vf[3][1]) :: "memory");
    ctl[CTL_FREE / 4 + (u & 7)] = u + 8;
    if (pf_on) prefetch(u);
    if (toucher) touch_at(u);
#pragma unroll
    for (int s = 0; s < 2; ++s)
#pragma unroll
      for (int i = 0; i < 4; ++i) o[i] = __builtin_amdgcn_mfma_f32_32x32x16_bf16(vf[i][s], __builtin_bit_cast(bf16x8, pf[s]), o[i], 0, 0, 0);
  }
  STAMP(3);

  {
    unsigned l16 = lane * 16;
    asm volatile("" : "+v"(l16));
    const unsigned l4 = l16 >> 2, awt = LDS_WT + (l4 & 127);
    auto put = [&](unsigned dst, const f32x16& x) {
#pragma unroll
      for (int j = 0; j < 4; ++j) *(LAS f32x4*)(lds + dst + j * 1024) = f32x4{x[4 * j], x[4 * j + 1], x[4 * j + 2], x[4 * j + 3]};
    };
    float co[6];
    bf16x8 pl[NC][2];
    auto ld_p = [&](int p) {
      pl[p][0] = *(LAS bf16x8*)(lds + LDS_WREG + p * W_SIZE + W_LAST + l16);
      pl[p][1] = *(LAS bf16x8*)(lds + LDS_WREG + p * W_SIZE + W_LAST + l16 + 1024);
    };
#pragma unroll
    for (int i = 0; i < 4; ++i) co[i] = *(LAS float*)(lds + awt + ((pos & 3) ^ i) * 128);
    co[4] = *(LAS float*)(lds + awt + 4 * 128);
    co[5] = *(LAS float*)(lds + awt + 5 * 128);
    ld_p(0); ld_p(1);
    asm volatile("" : "+v"(l16) :: "memory");
    if (nmine > 1) {
      if (pos > 3) put(LDS_RING + ((2 * NT - 2 * NC + pos) & 7) * 8192 + l16, o[0]);
#pragma unroll
      for (int i = 1; i < 4; ++i) put(LDS_WREG + pos * W_SIZE + (i - 1) * 4096 + l16, o[i]);
      LGKM0();
      ctl[CTL_ODONE / 4 + pos] = 1;
    }
    if (pos < 4) {
      f32x16 of = o[0], ot;
#pragma unroll
      for (int r = 0; r < 16; ++r) ot[r] = 0.f;
      auto src_o = [&](int p) -> unsigned {
        const int i = pos ^ (p & 3);
        return l16 + (i == 0 ? LDS_RING + ((2 * NT - 2 * NC + p) & 7) * 8192 : LDS_WREG + p * W_SIZE + (i - 1) * 4096);
      };
      auto fma_o = [&](unsigned src, float c) {
#pragma unroll
        for (int j = 0; j < 4; ++j) {
          const f32x4 x = *(LAS f32x4*)(lds + src + j * 1024);
#pragma unroll
          for (int e = 0; e < 4; ++e) of[4 * j + e] = fmaf(x[e], c, of[4 * j + e]);
        }
      };
      auto mma_t = [&](int p, bf16x8 p0, bf16x8 p1) {
        const unsigned a = vaddr0 + (((2 * NT - NC + p) & 7) << 13);
        bf16x8 vf[2];
#pragma unroll
        for (int s = 0; s < 2; ++s) {
          const s16x4 lo = __builtin_amdgcn_ds_read_tr16_b64_v4i16((LAS s16x4*)(lds + a + 4096 * s));
          const s16x4 up = __builtin_amdgcn_ds_read_tr16_b64_v4i16((LAS s16x4*)(lds + a + 4096 * s + 2048));
          vf[s] = __builtin_bit_cast(bf16x8, __builtin_shufflevector(lo, up, 0, 1, 2, 3, 4, 5, 6, 7));
        }
        ot = __builtin_amdgcn_mfma_f32_32x32x16_bf16(vf[0], p0, ot, 0, 0, 0);
        ot = __builtin_amdgcn_mfma_f32_32x32x16_bf16(vf[1], p1, ot, 0, 0, 0);
      };
      if (NT >= 2 * NC) {
#define PIN_OF() asm volatile("" : "+v"(of), "+v"(ot))
#pragma unroll
        for (int r = 0; r < 16; ++r) of[r] *= co[0];
        const bool wl = (l4 & 24) == 0;
        const unsigned ua = 4 * (2 * NT - NC);
        wait_above_v(wl ? aland : lbase + LDS_CTL + CTL_ODONE + (l4 & 12), wl ? ua : 0u);
        mma_t(0, pl[0][0], pl[0][1]);
#pragma unroll
        for (int i = 1; i < 4; ++i) fma_o(LDS_WREG + (pos ^ i) * W_SIZE + (i - 1) * 4096 + l16, co[i]);
        PIN_OF();
        STAMP(4);
        ld_p(2); ld_p(3); ld_p(4); ld_p(5);
        wait_above_v(wl ? aland : lbase + LDS_CTL + CTL_ODONE + 16 + (l4 & 4), wl ? ua + 8 : 0u);
        mma_t(1, pl[1][0], pl[1][1]);
        mma_t(2, pl[2][0], pl[2][1]);
        fma_o(src_o(4), co[4]);
        fma_o(src_o(5), co[5]);
#pragma unroll
        for (int r = 0; r < 16; ++r) { ot[r] += of[r]; of[r] = 0.f; }
        asm volatile("" : "+v"(ot));
        wait_above<0>(aland, ua + 20);
        mma_t(3, pl[3][0], pl[3][1]);
        mma_t(4, pl[4][0], pl[4][1]);
        mma_t(5, pl[5][0], pl[5][1]);
        of = ot;
      } else {
        wait_above<1>(lbase + LDS_CTL + CTL_WTOK + min(l4, (NC - 1) * 4u), 0);
        const float cw = *(LAS float*)(lds + awt + pos * 128);
#pragma unroll
        for (int r = 0; r < 16; ++r) of[r] *= cw;
#pragma unroll 1
        for (int p = max(NC - NT, 0); p < NC; ++p) {
          if (p != pos && NT - 2 * NC + p >= 0) {
            wait_above<1>(lbase + LDS_CTL + CTL_ODONE + p * 4, 0);
            fma_o(src_o(p), *(LAS float*)(lds + awt + p * 128));
          }
          wait_above<1>(aland, 4 * (2 * NT - NC + p));
          mma_t(p, *(LAS bf16x8*)(lds + LDS_WREG + p * W_SIZE + W_LAST + l16), *(LAS bf16x8*)(lds + LDS_WREG + p * W_SIZE + W_LAST + l16 + 1024));
        }
#pragma unroll
        for (int r = 0; r < 16; ++r) of[r] += ot[r];
      }
      const __amdgpu_buffer_rsrc_t ro = __builtin_amdgcn_make_buffer_rsrc(outp, 0, NB * HQ * SQ * HD * 2, 0x00020000);
      const unsigned orow = (unsigned)(b * HQ + 2 * h) * SQ * HD * 2 + pos * 64 + ((l16 & 511) << 4) + ((l16 >> 5) & 16);
#pragma unroll
      for (int j = 0; j < 2; ++j) {
        const unsigned a0 = pack2(of[8 * j], of[8 * j + 1]), a1 = pack2(of[8 * j + 2], of[8 * j + 3]);
        const unsigned b0 = pack2(of[8 * j + 4], of[8 * j + 5]), b1 = pack2(of[8 * j + 6], of[8 * j + 7]);
        auto s0 = __builtin_amdgcn_permlane32_swap(a0, b0, false, false);
        auto s1 = __builtin_amdgcn_permlane32_swap(a1, b1, false, false);
        u32x4 val = {s0[0], s1[0], s0[1], s1[1]};
        __builtin_amdgcn_raw_buffer_store_b128(val, ro, orow + j * 32, 0, OUTAUX);
      }
    }
  }
}
}

extern "C" void kernel_launch(void* const* d_in, const int* in_sizes, int n_in, void* d_out, int out_size, void* d_ws,
                              size_t ws_size, hipStream_t stream) {
  (void)n_in; (void)out_size; (void)ws_size;
  const unsigned nphys = (unsigned)(in_sizes[1] / (PAGE * HKV * HD));
  const unsigned kvbytes = nphys * PAGE_BYTES;
  (void)hipFuncSetAttribute((const void*)paged_attn, hipFuncAttributeMaxDynamicSharedMemorySize, LDS_TOTAL);
  hipLaunchKernelGGL(paged_attn, dim3(256), dim3(512), LDS_TOTAL, stream, d_in[0], d_in[1], d_in[2], (const int*)d_in[3],
                     (const int*)d_in[4], d_out, (char*)d_ws, kvbytes, nphys);
}
```

```cpp
#define PACE_X16 436
#define SOFT 0
#include <hip/hip_runtime.h>
#include <hip/hip_bf16.h>

#define PROBE_G 0
#define QPOL "nt"
#define PF_2ND
#define TLBW 5
#define TLBG 0x200000u
#define SORTP 1
#define TORDER 0
#define PF_OFF 6
#define CODEPF 15360
#define TMODE 1
#define TK2 0
#define TV 16
#define TAHEAD 2
#define PF_BY 0
#define PF_END 2u
#define PF_X64B 72
#define PF_DENSE 1
#define PF_PER 4
#define PF_PSTEP 2
#define GAP_TICKS 0
#define OUTAUX 0
#define HOFF 4
#define LSLEEP 0
#define PRESLEEP 0
#define PF_EVERY 2
#define PF_X64 76
#define PF_ALL 0
#define PF_LEAD 300
#define PF_POL ""
#define PF_SLOW 0
#define PF_RUN 3
#define TSLEEP 0
#define TWAIT 1
#define TPRIO 0
#define CSLEEP 1

#define STAMP(i) do {} while (0)

namespace {
constexpr int NB = 16, HQ = 32, HKV = 16, SQ = 16, HD = 128, PAGE = 16, NBPS = 256, SKV = NBPS * PAGE;
constexpr unsigned PAGE_BYTES = PAGE * HKV * HD * 2;
constexpr unsigned INVALID = 0x80000000u;
constexpr int U0 = 4;
constexpr int NC = 6;
constexpr int KREG = 16, KLDS = 5;
static_assert(NC * (KREG + KLDS + 1) >= NBPS / 2, "P storage");
constexpr unsigned LDS_RING = 0;
constexpr unsigned LDS_WREG = 65536;
constexpr unsigned W_ALP = KLDS * 2048;
constexpr unsigned W_LAST = W_ALP + (KREG + KLDS + 1) * 128;
constexpr unsigned W_SIZE = W_LAST + 2048;
static_assert(W_LAST >= 3 * 4096 && W_SIZE % 256 == 0, "consumer region");
constexpr unsigned LDS_TBL = LDS_WREG + W_SIZE;
constexpr unsigned LDS_SRT = LDS_WREG + 3 * W_SIZE;
constexpr unsigned LDS_Q = LDS_WREG;
constexpr unsigned LDS_ML = LDS_WREG + NC * W_SIZE;
constexpr unsigned LDS_WT = LDS_ML + NC * 256;
constexpr unsigned LDS_CTL = LDS_WT + NC * 128;
constexpr unsigned CTL_LAND = 0;
constexpr unsigned CTL_QOK = 16;
constexpr unsigned CTL_SORT = 24;
constexpr unsigned CTL_FREE = 32;
constexpr unsigned CTL_MLOK = 64;
constexpr unsigned CTL_ODONE = 96;
constexpr unsigned CTL_WTOK = 128;
constexpr unsigned LDS_DUMMY = LDS_CTL + 256;
constexpr unsigned LDS_PLIST = LDS_DUMMY + 256;
constexpr unsigned LDS_TOTAL = LDS_PLIST + 1024;
static_assert(LDS_TOTAL <= 163840, "lds");

typedef __attribute__((ext_vector_type(8))) __bf16 bf16x8;
typedef __attribute__((ext_vector_type(2))) __bf16 bf16x2;
typedef __attribute__((ext_vector_type(4))) short s16x4;
typedef __attribute__((ext_vector_type(16))) float f32x16;
typedef __attribute__((ext_vector_type(4))) float f32x4;
typedef __attribute__((ext_vector_type(2))) float f32x2;
typedef __attribute__((ext_vector_type(4))) unsigned u32x4;
typedef __attribute__((ext_vector_type(2))) unsigned u32x2;
typedef __attribute__((ext_vector_type(4))) int i32x4;
#define LAS __attribute__((address_space(3)))

__device__ __forceinline__ float swap_max(float x) {
  auto r = __builtin_amdgcn_permlane32_swap(__float_as_uint(x), __float_as_uint(x), false, false);
  return fmaxf(__uint_as_float(r[0]), __uint_as_float(r[1]));
}
__device__ __forceinline__ float swap_sum(float x) {
  auto r = __builtin_amdgcn_permlane32_swap(__float_as_uint(x), __float_as_uint(x), false, false);
  return __uint_as_float(r[0]) + __uint_as_float(r[1]);
}
__device__ __forceinline__ unsigned pack2(float a, float b) {
  bf16x2 v = {(__bf16)a, (__bf16)b};
  return __builtin_bit_cast(unsigned, v);
}
template <int CTRL> __device__ __forceinline__ int dpp_add(int x) { return x + __builtin_amdgcn_update_dpp(0, x, CTRL, 0xf, 0xf, true); }
__device__ __forceinline__ unsigned now32() { return (unsigned)__builtin_amdgcn_s_memrealtime(); }
__device__ __forceinline__ int to_sgpr(int v) {
  int r;
  asm volatile("s_nop 1\n\tv_readfirstlane_b32 %0, %1" : "=s"(r) : "v"(v));
  return r;
}
#define LGKM0() asm volatile("s_waitcnt lgkmcnt(0)" ::: "memory")
#define VMWAIT(n) asm volatile("s_waitcnt vmcnt(" #n ")" ::: "memory")
#define CBAR() asm volatile("" ::: "memory")
template <int SLEEP> __device__ __forceinline__ void wait_above(unsigned addr, unsigned u) {
  unsigned v, cnt;
  if (SLEEP)
    asm volatile("s_mov_b32 %1, 0x200000\n\ts_branch 2f\n"
                 "1:\n\ts_sleep %4\n"
                 "2:\n\tds_read_b32 %0, %2\n\ts_sub_u32 %1, %1, 1\n\ts_waitcnt lgkmcnt(0)\n\t"
                 "v_cmp_ge_u32 vcc, %3, %0\n\ts_cbranch_scc1 3f\n\ts_nop 1\n\ts_cbranch_vccnz 1b\n3:"
                 : "=&v"(v), "=&s"(cnt) : "v"(addr), "s"(u), "n"(SLEEP) : "memory", "scc", "vcc");
  else
    asm volatile("s_mov_b32 %1, 0x200000\n"
                 "1:\n\tds_read_b32 %0, %2\n\ts_sub_u32 %1, %1, 1\n\ts_waitcnt lgkmcnt(0)\n\t"
                 "v_cmp_ge_u32 vcc, %3, %0\n\ts_cbranch_scc1 3f\n\ts_nop 1\n\ts_cbranch_vccnz 1b\n3:"
                 : "=&v"(v), "=&s"(cnt) : "v"(addr), "s"(u) : "memory", "scc", "vcc");
}

__device__ __forceinline__ void wait_above_v(unsigned addr, unsigned u) {
  unsigned v, cnt;
  asm volatile("s_mov_b32 %1, 0x200000\n"
               "1:\n\tds_read_b32 %0, %2\n\ts_sub_u32 %1, %1, 1\n\ts_waitcnt lgkmcnt(0)\n\t"
               "v_cmp_ge_u32 vcc, %3, %0\n\ts_cbranch_scc1 3f\n\ts_nop 1\n\ts_cbranch_vccnz 1b\n3:"
               : "=&v"(v), "=&s"(cnt) : "v"(addr), "v"(u) : "memory", "scc", "vcc");
}

__global__ void __launch_bounds__(512, 1) __attribute__((amdgpu_num_vgpr(64)))
paged_attn(const void* __restrict__ qp, const void* __restrict__ kp, const void* __restrict__ vp, const int* __restrict__ bt,
           const int* __restrict__ lens, void* __restrict__ outp, char* __restrict__ ws, unsigned kvbytes, unsigned nphys) {
  extern __shared__ __attribute__((aligned(16))) char smem[];
  LAS char* lds = (LAS char*)smem;
  const int lane0 = threadIdx.x & 63;
  const int wv = __builtin_amdgcn_readfirstlane(threadIdx.x >> 6);
  const int g = blockIdx.x;
  const int h = g >> 4;
  constexpr float CS = 0.08838834764831845f * 1.4426950408889634f;
  volatile LAS unsigned* ctl = (volatile LAS unsigned*)(lds + LDS_CTL);

  const unsigned lbase = (unsigned)(size_t)lds;
  const bool toucher = TLBW && (g & 8) != 0;
  const unsigned nchunk = (kvbytes + 16 * TLBG - 1) / (16 * TLBG);

  unsigned lraw, first;
  u32x4 lq;
  {
    asm volatile("global_load_dword %0, %3, %4\n\t"
                 "global_load_dwordx4 %1, %5, %4\n\t"
                 "global_load_dword %2, %6, %7"
                 : "=&v"(lraw), "=&v"(lq), "=&v"(first)
                 : "v"((lane0 & 15) * 4), "s"(lens), "v"(lane0 & 48), "v"((lane0 & 15) * (wv < 2 ? NBPS * 4 : 0) + (lane0 >> 4) * 8 + (wv & 1) * 4), "s"(bt),
                   "s"(qp), "s"(kp), "s"(vp), "s"(outp), "s"(kvbytes), "s"(nphys)
                 : "memory");
    __builtin_amdgcn_sched_barrier(0);
  }
  if (wv >= 4) {
    if (wv == 7) {
      const __amdgpu_buffer_rsrc_t rb = __builtin_amdgcn_make_buffer_rsrc((void*)bt, 0, NB * NBPS * 4, 0x00020000);
#define TBL4(SO) "buffer_load_dwordx4 %2, %1, " SO " offen lds\n\tbuffer_load_dwordx4 %2, %1, " SO " offen offset:1024 lds\n\t" \
                 "buffer_load_dwordx4 %2, %1, " SO " offen offset:2048 lds\n\tbuffer_load_dwordx4 %2, %1, " SO " offen offset:3072 lds\n\t"
      asm volatile("s_mov_b32 m0, %0\n\ts_nop 0\n\t" TBL4("0") "s_add_u32 m0, m0, 4096\n\ts_nop 0\n\t" TBL4("%3")
                   "s_add_u32 m0, m0, %6\n\ts_nop 0\n\t" TBL4("%4") "s_add_u32 m0, m0, 4096\n\ts_nop 0\n\t" TBL4("%5")
                   :: "s"(lbase + LDS_TBL), "s"(rb), "v"(lane0 * 16), "s"(4096), "s"(8192), "s"(12288), "n"(W_SIZE - 4096) : "memory", "scc");
    }
    unsigned long long pc;
    asm volatile("s_getpc_b64 %0" : "=s"(pc));
    const __amdgpu_buffer_rsrc_t rt = __builtin_amdgcn_make_buffer_rsrc(wv == 5 ? (void*)pc : (void*)kp, 0,
                                                                        wv == 5 ? CODEPF : (wv == 6 && toucher) ? (int)min(kvbytes, TAHEAD * 16 * TLBG) : 0, 0x00020000);
    const unsigned mul = wv == 5 ? 512u : 16 * TLBG + 4224u, add = wv == 5 ? ((g >> 3) & 3) * 128 : (g >> 4) * (TLBG + 33024u);
    asm volatile("s_mov_b32 m0, %0\n\ts_nop 0\n\tbuffer_load_dword %1, %2, 0 offen lds"
                 :: "s"(lbase + LDS_DUMMY), "v"((lane0 & 31) * mul + add), "s"(rt) : "memory");
    *(LAS u32x4*)(lds + LDS_SRT + (threadIdx.x - 256) * 16) = u32x4{0u, 0u, 0u, 0u};
    if (threadIdx.x < 296) ctl[lane0] = ((lane0 >> 3) == CTL_FREE / 32) ? (lane0 & 7) : 0u;
    asm volatile("s_waitcnt lgkmcnt(0)\n\ts_barrier" ::: "memory");
  }
  if (wv < 2) __builtin_amdgcn_s_setprio(3);
  const unsigned rstride = wv < 2 ? 4096 : 256;
  unsigned vo0 = (lane0 >> 4) * rstride + (((unsigned)(lane0 & 15) ^ ((lane0 >> 4) << 2)) << 4) + (wv < 2 ? h * (HD * 2) : 0);
  unsigned vo1 = (vo0 + 4 * rstride - 1024) ^ 16, vo2 = (vo0 + 8 * rstride - 2048) ^ 32, vo3 = (vo0 + 12 * rstride - 3072) ^ 48;
  const unsigned want = (g & 8) ? 15 - (g & 7) : (g & 7);
  const unsigned dst0 = lbase + (wv < 2 ? LDS_RING : LDS_Q) + (wv & 1) * 4096;
  asm volatile("s_cmp_ge_u32 %8, 4\n\ts_cbranch_scc1 1f\n\ts_waitcnt vmcnt(0)\n1:\n\ts_waitcnt vmcnt(1)" : "+v"(lraw), "+v"(lq), "+v"(first), "+v"(vo0), "+v"(vo1), "+v"(vo2), "+v"(vo3) : "s"(dst0), "s"(wv) : "memory", "scc");
  unsigned lm1 = min(lraw - 1u, (unsigned)SKV - 1u);
  unsigned rank = 0;
  {
    const unsigned key = lm1 * 16 + (lane0 & 15);
#pragma unroll
    for (int j = 0; j < 4; ++j) rank += (min(lq[j] - 1u, (unsigned)SKV - 1u) * 16 + ((lane0 >> 4) * 4 + j) > key) ? 1u : 0u;
    auto r1 = __builtin_amdgcn_permlane16_swap(rank, rank, false, false);
    rank = r1[0] + r1[1];
    auto r2 = __builtin_amdgcn_permlane32_swap(rank, rank, false, false);
    rank = r2[0] + r2[1];
  }
  first = min(first, nphys - 1) * PAGE_BYTES;
  const int b = __builtin_ctz((unsigned)__builtin_amdgcn_ballot_w64(rank == want) | 0x8000u);
  unsigned long long tstart = 0;
  if (wv < 4) {
    const bool qok = (int)__builtin_amdgcn_readlane(lraw, b) > 0;
    const __amdgpu_buffer_rsrc_t r0 = __builtin_amdgcn_make_buffer_rsrc(wv < 2 ? (void*)kp : (void*)qp, 0,
                                                                        wv < 2 ? (int)kvbytes : qok ? NB * HQ * SQ * HD * 2 : 0, 0x00020000);
    const unsigned so = wv < 2 ? __builtin_amdgcn_readlane(first, b) : qok ? (unsigned)((b * HQ + 2 * h) * SQ * HD * 2) + (wv & 1) * 4096 : 0u;
    asm volatile("s_waitcnt vmcnt(0)\n\t"
                 "s_mov_b32 m0, %2\n\t"
                 "s_memrealtime %0\n\t"
                 "s_nop 2\n\t"
                 "buffer_load_dwordx4 %4, %1, %3 offen nt lds\n\t"
                 "buffer_load_dwordx4 %5, %1, %3 offen offset:1024 nt lds\n\t"
                 "buffer_load_dwordx4 %6, %1, %3 offen offset:2048 nt lds\n\t"
                 "buffer_load_dwordx4 %7, %1, %3 offen offset:3072 nt lds\n\t"
                 "s_waitcnt lgkmcnt(0)\n\t"
                 "s_barrier"
                 : "=&s"(tstart) : "s"(r0), "s"(dst0), "s"(so), "v"(vo0), "v"(vo1), "v"(vo2), "v"(vo3) : "memory");
  }
  asm volatile("" : "+v"(lm1), "+v"(rank), "+v"(first) :: "memory");
  __builtin_amdgcn_sched_barrier(0);
  const int bp = __builtin_ctz((unsigned)__builtin_amdgcn_ballot_w64(rank == 15 - want) | 0x8000u);
  const int len = __builtin_amdgcn_readlane(lm1, b) + 1, nown = (len + PAGE - 1) / PAGE, NT = (nown + 1) >> 1;
  const int npl = (__builtin_amdgcn_readlane(lm1, bp) >> 4) + 1, NTL = (npl + 1) >> 1;
  auto total = [&]() __attribute__((always_inline)) {
    int t = lm1 + 1;
    t = dpp_add<0x121>(t); t = dpp_add<0x122>(t); t = dpp_add<0x124>(t); t = dpp_add<0x128>(t);
    return t;
  };

  auto sort_pages = [&](int lane, int bb, int n, unsigned scr, unsigned dst) __attribute__((always_inline)) {
    u32x4 e = *(LAS u32x4*)(lds + LDS_TBL + (bb >> 3) * W_SIZE + (bb & 7) * 1024 + lane * 16);
    bool in[4];
#pragma unroll
    for (int j = 0; j < 4; ++j) {
      e[j] = min(e[j], nphys - 1);
      in[j] = 4 * lane + j >= 2 * U0 && 4 * lane + j < n - 1;
      __hip_atomic_fetch_or((LAS unsigned*)(lds + scr + ((e[j] >> 5) & 255) * 8), in[j] ? 1u << (e[j] & 31) : 0u, __ATOMIC_RELAXED, __HIP_MEMORY_SCOPE_WAVEFRONT);
    }
    CBAR();
    u32x4 a = *(LAS u32x4*)(lds + scr + lane * 32), c = *(LAS u32x4*)(lds + scr + lane * 32 + 16);
    const int n0 = __builtin_popcount(a[0]), n1 = n0 + __builtin_popcount(a[2]), n2 = n1 + __builtin_popcount(c[0]), n3 = n2 + __builtin_popcount(c[2]);
    int sc = n3;
    sc = dpp_add<0x111>(sc); sc = dpp_add<0x112>(sc); sc = dpp_add<0x114>(sc); sc = dpp_add<0x118>(sc);
    sc += __builtin_amdgcn_update_dpp(0, sc, 0x142, 0xa, 0xf, false);
    sc += __builtin_amdgcn_update_dpp(0, sc, 0x143, 0xc, 0xf, false);
    const bool ok = SORTP && nphys <= 8192u && __builtin_amdgcn_readlane(sc, 63) == n - 1 - 2 * U0;
    sc += 2 * U0 - n3;
    a[1] = sc; a[3] = sc + n0; c[1] = sc + n1; c[3] = sc + n2;
    *(LAS u32x4*)(lds + scr + lane * 32) = a;
    *(LAS u32x4*)(lds + scr + lane * 32 + 16) = c;
    CBAR();
    u32x2 wp[4];
#pragma unroll
    for (int j = 0; j < 4; ++j) wp[j] = *(LAS u32x2*)(lds + scr + ((e[j] >> 5) & 255) * 8);
#pragma unroll
    for (int j = 0; j < 4; ++j) {
      const unsigned r = wp[j][1] + __builtin_popcount(wp[j][0] & ((1u << (e[j] & 31)) - 1u));
      *(LAS unsigned*)(lds + dst + ((ok && in[j]) ? r : 4 * lane + j) * 4) = e[j] * PAGE_BYTES;
    }
    CBAR();
  };

  if (__builtin_expect(wv < 2, 1)) {
    const int half = wv, lane = lane0;
    const unsigned vland = lbase + LDS_CTL + CTL_LAND + half * 4;
    const unsigned vfree = lbase + LDS_CTL + CTL_FREE + (lane & 7) * 4;
    const unsigned ring = dst0;
    unsigned l0 = __builtin_amdgcn_ds_bpermute((b + 16 * lane) * 4, first), l1 = 0;
    const unsigned a0 = lbase + LDS_SRT + 4096 + min(2 * lane + half, nown - 1) * 4, a1 = lbase + LDS_SRT + 4096 + min(2 * lane + half + 128, nown - 1) * 4;
    const int T16 = total() * PACE_X16;
    const int step = to_sgpr((int)((float)T16 * __builtin_amdgcn_rcpf((float)max(len, 32))));
    const unsigned due0 = ((unsigned)tstart << 8) + half * ((step * HOFF) >> 3);
    const int lrows = len - (2 * (NT - 1) + half) * PAGE;
#define CNT(r)
    unsigned vf, vt, lc;
    asm volatile(
        "s_waitcnt lgkmcnt(0)\n\t"
        "s_memrealtime s[68:69]\n\t"
        "ds_read_b32 %[vf], %[vfree]\n\t"
        "s_mov_b32 s60, 1\n\t"
        "s_mov_b32 s61, 4\n\t"
        "s_mov_b32 s62, 0x400000\n\t"
        "s_mov_b32 s66, 0\n\t"
        "s_min_u32 s67, %[nt], %[u0]\n\t"
        "s_mov_b32 s75, %[nrec]\n\t"
        "s_mov_b32 s72, 0x2000\n\t"
        "s_mov_b32 s73, 1\n\t"
        "s_mov_b32 s74, %[due1]\n\t"
        "s_mov_b64 s[76:77], %[kp]\n\t"
        "s_mov_b32 s78, %[nrec]\n\t"
        "s_mov_b32 s79, 0x20000\n\t"
        "s_and_b32 s77, s77, 0xffff\n\t"
        "v_mov_b32 %[lc], %[l0]\n\t"
        "s_getpc_b64 s[70:71]\n"
        ".LldA%=:\n\t"
        "s_add_u32 s80, s70, .LldL%=-.LldA%=\n\t"
        "s_addc_u32 s71, s71, 0\n\t"
        "s_branch .LldF%=\n"
        ".LldR%=:\n\t"
        "s_sub_u32 s62, s62, 1\n\t"
        "s_cbranch_scc1 .LldE%=\n"
        ".LldT%=:\n\t"
        "s_getreg_b32 s64, hwreg(HW_REG_IB_STS)\n\t"
        "s_and_b32 s65, s64, 15\n\t"
        "s_bfe_u32 s64, s64, 0x20016\n\t"
        "s_lshl4_add_u32 s64, s64, s65\n\t"
        "s_add_u32 s64, s64, 3\n\t"
        "s_and_b32 s64, s64, 60\n\t"
        "s_sub_u32 s65, s61, s64\n\t"
        "s_cmp_lg_u32 s65, s66\n\t"
        "s_cbranch_scc1 .LldP%=\n"
        ".LldC%=:\n\t"
        "s_waitcnt lgkmcnt(0)\n\t"
        "v_cmp_eq_u32 vcc, s60, %[vf]\n\t"
        "s_lshl_b32 s64, s68, 8\n\t"
        "s_memrealtime s[68:69]\n\t"
        "ds_read_b32 %[vf], %[vfree]\n\t"
        CNT("s82")
        "s_sub_u32 s64, s64, s74\n\t"
        "s_cmp_lt_i32 s64, 0\n\t"
        "s_cbranch_scc1 .LldR%=\n\t"
        CNT("s83")
        "s_cbranch_vccz .LldR%=\n\t"
        CNT("s84")
        "buffer_load_dwordx4 %[o0], s[76:79], s63 offen nt lds\n\t"
        "s_add_u32 s74, s74, %[step]\n\t"
        "s_add_u32 s73, s73, 1\n\t"
        "buffer_load_dwordx4 %[o1], s[76:79], s63 offen offset:1024 nt lds\n\t"
        "s_add_u32 s60, s60, 1\n\t"
        "s_add_u32 s61, s61, 4\n\t"
        "buffer_load_dwordx4 %[o2], s[76:79], s63 offen offset:2048 nt lds\n\t"
        "s_add_u32 s72, s72, 0x2000\n\t"
        "s_and_b32 s72, s72, 0xe000\n\t"
        "buffer_load_dwordx4 %[o3], s[76:79], s63 offen offset:3072 nt lds\n"
        ".LldF%=:\n\t"
        "v_readlane_b32 s63, %[lc], s73\n\t"
        "s_add_u32 m0, s72, %[ring]\n\t"
        "s_cmp_eq_u32 s73, %[ntm]\n\t"
        "s_cselect_b32 s78, s75, %[nrec]\n\t"
        "s_cmp_lg_u32 s60, s67\n\t"
        "s_cbranch_scc1 .LldT%=\n\t"
        "s_cmp_lg_u32 s60, %[u0]\n\t"
        "s_cbranch_scc1 .LldG%=\n\t"
        "s_cmp_le_u32 %[nt], %[u0]\n\t"
        "s_cbranch_scc1 .LldG%=\n"
        ".LldS%=:\n\t"
        "ds_read_b32 %[lc], %[vland] offset:%[srt]\n\t"
        "ds_read_b32 %[l0], %[a0]\n\t"
        "ds_read_b32 %[l1], %[a1]\n\t"
        "s_sub_u32 s62, s62, 1\n\t"
        "s_cbranch_scc1 .LldE%=\n\t"
        "s_waitcnt lgkmcnt(0)\n\t"
        "v_readfirstlane_b32 s64, %[lc]\n\t"
        "s_cmp_eq_u32 s64, 0\n\t"
        "s_cbranch_scc1 .LldS%=\n\t"
        "s_max_i32 s65, %[lrows], 0\n\t"
        "s_lshl_b32 s65, s65, 12\n\t"
        "s_add_u32 s64, s64, s65\n\t"
        "s_sub_u32 s64, s64, 1\n\t"
        "s_cmp_lt_i32 %[lrows], 16\n\t"
        "s_cselect_b32 s75, s64, %[nrec]\n\t"
        "s_min_u32 s67, %[nt], 64\n\t"
        "v_mov_b32 %[lc], %[l0]\n\t"
        "s_branch .LldF%=\n"
        ".LldG%=:\n\t"
        "s_cmp_eq_u32 s73, %[nt]\n\t"
        "s_cbranch_scc1 .LldV%=\n\t"
        "v_mov_b32 %[lc], %[l1]\n\t"
        "v_readfirstlane_b32 s63, %[l1]\n\t"
        "s_add_u32 s67, s60, %[nt]\n\t"
        "s_sub_u32 s67, s67, 64\n\t"
        "s_branch .LldT%=\n"
        ".LldV%=:\n\t"
        "s_cmp_lg_u32 s60, %[nt]\n\t"
        "s_cbranch_scc1 .LldD%=\n\t"
        "s_mov_b64 s[76:77], %[vp]\n\t"
        "s_and_b32 s77, s77, 0xffff\n\t"
        "s_mov_b32 s73, 0\n\t"
        "s_min_u32 s67, %[nt], 64\n\t"
        "s_add_u32 s67, s67, s60\n\t"
        "s_mov_b32 s74, %[duev]\n\t"
        "s_cmp_eq_u32 %[ntm], 0\n\t"
        "s_cselect_b32 s78, s75, %[nrec]\n\t"
        "v_mov_b32 %[lc], %[l0]\n\t"
        "v_readfirstlane_b32 s63, %[l0]\n\t"
        "v_xor_b32 %[o1], 16, %[o1]\n\t"
        "v_xor_b32 %[o2], 32, %[o2]\n\t"
        "v_xor_b32 %[o3], 48, %[o3]\n\t"
        "s_branch .LldT%=\n"
        ".LldD%=:\n\t"
        "s_add_u32 s74, s74, 0x40000000\n\t"
        "s_mov_b32 s60, -1\n\t"
        "s_branch .LldT%=\n"
        ".LldP%=:\n\t"
        "s_min_u32 s64, s64, 32\n\t"
        "s_add_u32 s70, s80, s64\n\t"
        "v_mov_b32 %[vt], s65\n\t"
        "s_mov_b32 s66, s65\n\t"
        "s_setpc_b64 s[70:71]\n\t"
        ".p2align 6\n"
        ".LldL%=:\n\t"
        "s_waitcnt vmcnt(0)\n\t"
        "s_waitcnt vmcnt(4)\n\t"
        "s_waitcnt vmcnt(8)\n\t"
        "s_waitcnt vmcnt(12)\n\t"
        "s_waitcnt vmcnt(16)\n\t"
        "s_waitcnt vmcnt(20)\n\t"
        "s_waitcnt vmcnt(24)\n\t"
        "s_waitcnt vmcnt(28)\n\t"
        "s_waitcnt vmcnt(32)\n\t"
        "ds_write_b32 %[vland], %[vt]\n\t"
        "s_cmp_lg_u32 s65, %[fin]\n\t"
        "s_cbranch_scc1 .LldC%=\n"
        ".LldE%=:\n\t"
        "s_waitcnt vmcnt(0) lgkmcnt(0)\n\t"
        : [vf] "=&v"(vf), [vt] "=&v"(vt), [lc] "=&v"(lc), [l0] "+v"(l0), [l1] "+v"(l1), [o1] "+v"(vo1), [o2] "+v"(vo2), [o3] "+v"(vo3)
        : [a0] "v"(a0), [a1] "v"(a1), [o0] "v"(vo0), [vland] "v"(vland), [vfree] "v"(vfree),
          [kp] "s"(kp), [vp] "s"(vp), [nrec] "s"(kvbytes), [nt] "s"(NT), [ring] "s"(ring),
          [step] "s"(step), [due1] "s"(due0 + step + SOFT * 256), [duev] "s"(due0 + (to_sgpr(T16) >> 5) + (SOFT + GAP_TICKS) * 256), [lrows] "s"(lrows), [ntm] "s"(NT - 1), [fin] "s"(8 * NT), [u0] "n"(U0), [srt] "n"(CTL_SORT - CTL_LAND)
        : "memory", "scc", "vcc", "s60", "s61", "s62", "s63", "s64", "s65", "s66", "s67", "s68", "s69", "s70", "s71", "s72", "s73", "s74", "s75",
          "s76", "s77", "s78", "s79", "s80", "s82", "s83", "s84");
    STAMP(3);
    asm volatile("s_endpgm" ::: "memory");
  }

  int lane = lane0;
  asm volatile("" : "+v"(lane));
  auto touch = [&](unsigned c, unsigned n) __attribute__((always_inline)) {
    const bool isv = c >= nchunk;
    const __amdgpu_buffer_rsrc_t rt = __builtin_amdgcn_make_buffer_rsrc(isv ? (void*)vp : (void*)kp, 0, (int)kvbytes, 0x00020000);
    unsigned ln = lane;
    asm volatile("" : "+v"(ln));
    const unsigned idx = (g >> 4) + 16 * (c - (isv ? nchunk : 0) + ln);
    const unsigned vo = (ln < n && idx < 0x80000000u / TLBG) ? idx * TLBG + ((idx * 30592u) & (TLBG - 128)) : INVALID;
    asm volatile("s_mov_b32 m0, %0\n\ts_nop 4\n\tbuffer_load_dword %2, %1, 0 offen nt lds" :: "s"((unsigned)(size_t)lds + LDS_DUMMY), "s"(rt), "v"(vo) : "memory");
  };
  if (wv == 7) {
#pragma unroll 1
    for (int i = 0; i < 2; ++i) {
      sort_pages(lane, i ? bp : b, i ? npl : nown, LDS_SRT + i * 2048, i ? LDS_PLIST : LDS_SRT + 4096);
      if (i == 0) {
        const unsigned lo = *(LAS unsigned*)(lds + LDS_SRT + 4096 + (nown - 1) * 4) + 1;
        LGKM0();
        ctl[CTL_SORT / 4 + (lane & 1)] = lo;
      }
    }
  }
  if (wv < 4) {
    VMWAIT(0);
    ctl[CTL_QOK / 4 + (wv & 1)] = 1;
  }
  const int lim = len;
  const int tot = total();
  const int w = wv - 2;
  const int r32 = lane & 31, hi = lane >> 5;
  const int nmine = (NT > w) ? (NT - w + NC - 1) / NC : 0;
  const unsigned aland = lbase + LDS_CTL + CTL_LAND + (lane & 1) * 4;
  const int tots = __builtin_amdgcn_readfirstlane(tot);
  const bool pf_on = PF_EVERY && npl * 256 * 64 > tots * PF_X64B && npl > nown && (h & 3) == PF_BY;
  const unsigned pf_max = (npl * 256 * 64 > tots * PF_X64) ? 256u : PF_END;
  const unsigned pf_last = 2 * NT - to_sgpr((int)((float)(2 * NT * (PF_LEAD + 23 * PF_OFF)) * 4096.0f * __builtin_amdgcn_rcpf((float)(tot * PACE_X16))));
  const unsigned pf_ratio = to_sgpr((int)((float)NTL * 65536.0f * __builtin_amdgcn_rcpf((float)NT)));
  const unsigned pf_per = PF_PER + (g & 7) * PF_PSTEP, pf_inv = (65536 + pf_per - 1) / pf_per;
  auto prefetch = [&](unsigned op) {
    const unsigned d = pf_last - op;
    if (d >= 1024u) return;
    const unsigned r = d - ((d * pf_inv) >> 16) * pf_per;
    if (r >= PF_RUN) return;
    unsigned tl = 2 * NTL - 1 - PF_OFF - (((d - r) * pf_ratio) >> 16) - r;
    if (tl >= 2u * NTL) return;
    const bool isv = tl >= (unsigned)NTL;
    tl -= isv ? NTL : 0;
    const __amdgpu_buffer_rsrc_t rp = __builtin_amdgcn_make_buffer_rsrc(isv ? (void*)vp : (void*)kp, 0, (int)kvbytes, 0x00020000);
    unsigned ln = lane;
    asm volatile("" : "+v"(ln));
    const unsigned vo = (*(LAS unsigned*)(lds + LDS_PLIST + min(2 * tl + (ln >> 5), (unsigned)npl - 1) * 4) & 0x7fff0000u) + ((h & 12) | 1) * (HD * 2) + ((ln >> 1) & 15) * 4096 + (PF_SLOW == 1 ? 128 : PF_SLOW == 2 ? 0 : (ln & 1) * 128);
    asm volatile("s_mov_b32 m0, %0\n\ts_nop 4\n\tbuffer_load_dword %2, %1, 0 offen " PF_POL " lds" PF_2ND :: "s"(lbase + LDS_DUMMY), "s"(rp), "v"(vo) : "memory");
  };
  const int pos = (w + NC * 22 - NT) % NC;
  const unsigned tch_rate = to_sgpr((int)((float)nchunk * 65536.0f * __builtin_amdgcn_rcpf((float)NT)));
  auto touch_at = [&](unsigned op) __attribute__((always_inline)) {
    if (TMODE == 0) {
      const unsigned c1 = ((op + 1) * tch_rate) >> 16, c0 = (op * tch_rate) >> 16;
      if (c1 != c0 && c0 + TAHEAD < 2 * nchunk) touch(c0 + TAHEAD, 1);
    } else {
      if (op == TK2 && TAHEAD < nchunk) touch(TAHEAD, 64);
      if (op == TV) touch(nchunk, 64);
    }
  };
  const unsigned palp = LDS_WREG + pos * W_SIZE + W_ALP + r32 * 4;
  const unsigned ppst = LDS_WREG + pos * W_SIZE + lane * 16;
  asm volatile("" ::: "v128", "v255");
  unsigned resc = 0;
  float m = -3.0e38f, mprev = -3.0e38f, l = 0.f;

  {
    const unsigned fk = (((unsigned)r32 & 3) << 2) | (((unsigned)r32 >> 2) & 3);
    const unsigned kaddr0 = LDS_RING + r32 * 256 + (((unsigned)hi ^ fk) << 4);
    bf16x8 qf[8];
    wait_above<1>(aland + (CTL_QOK - CTL_LAND), 0);
#pragma unroll
    for (int s = 0; s < 8; ++s) qf[s] = *(LAS bf16x8*)(lds + ((kaddr0 + LDS_Q - LDS_RING) ^ (s << 5)));
#pragma unroll 1
    for (int k = 0; k < nmine; ++k) {
      const unsigned t = w + NC * k;
      if (PRESLEEP && k > 0) __builtin_amdgcn_s_sleep(PRESLEEP);
      wait_above<CSLEEP>(aland, 4 * t);
      if (k == 0) STAMP(1);
      bf16x8 kf[8];
#pragma unroll
      for (int s = 0; s < 8; ++s) kf[s] = *(LAS bf16x8*)(lds + ((kaddr0 + ((t & 7) << 13)) ^ (s << 5)));
      asm volatile("s_waitcnt lgkmcnt(0)" : "+v"(kf[0]), "+v"(kf[1]), "+v"(kf[2]), "+v"(kf[3]), "+v"(kf[4]), "+v"(kf[5]), "+v"(kf[6]), "+v"(kf[7]) :: "memory");
      ctl[CTL_FREE / 4 + (t & 7)] = t + 8;
      if (pf_on) prefetch(t);
      if (toucher) touch_at(t);
      f32x16 sc;
#pragma unroll
      for (int r = 0; r < 16; ++r) sc[r] = 0.f;
#pragma unroll
      for (int s = 0; s < 8; ++s) sc = __builtin_amdgcn_mfma_f32_32x32x16_bf16(kf[s], qf[s], sc, 0, 0, 0);
      if ((int)t == NT - 1) {
        const int kb = 2 * t * PAGE + 4 * hi;
#pragma unroll
        for (int r = 0; r < 16; ++r) sc[r] = (kb + 8 * (r >> 2) + (r & 3) < lim) ? sc[r] : -__builtin_inff();
      }
      float mx = sc[0];
#pragma unroll
      for (int r = 1; r < 16; ++r) mx = fmaxf(mx, sc[r]);
      mx = swap_max(mx);
      const float mn = fmaxf(m, mx);
      const float alpha = __builtin_amdgcn_exp2f((m - mn) * CS);
      mprev = m;
      m = mn;
      const float nmc = -mn * CS;
      float ps = 0.f;
#pragma unroll
      for (int r = 0; r < 16; ++r) { sc[r] = __builtin_amdgcn_exp2f(fmaf(sc[r], CS, nmc)); ps += sc[r]; }
      l = fmaf(l, alpha, ps);
      u32x4 pf[2];
#pragma unroll
      for (int s = 0; s < 2; ++s)
        pf[s] = u32x4{pack2(sc[8 * s], sc[8 * s + 1]), pack2(sc[8 * s + 2], sc[8 * s + 3]), pack2(sc[8 * s + 4], sc[8 * s + 5]), pack2(sc[8 * s + 6], sc[8 * s + 7])};
      if (__builtin_amdgcn_ballot_w64(alpha != 1.0f)) resc |= 1u << k;
      *(LAS float*)(lds + palp + k * 128) = alpha;
      if (k >= KREG || k == nmine - 1) {
        const unsigned dst = ppst + ((k == nmine - 1) ? W_LAST : (k - KREG) * 2048);
        *(LAS u32x4*)(lds + dst) = pf[0];
        *(LAS u32x4*)(lds + dst + 1024) = pf[1];
      } else {
        asm volatile("s_set_gpr_idx_on %8, gpr_idx(DST)\n\t"
                     "v_mov_b32 v128, %0\n\tv_mov_b32 v129, %1\n\tv_mov_b32 v130, %2\n\tv_mov_b32 v131, %3\n\t"
                     "v_mov_b32 v132, %4\n\tv_mov_b32 v133, %5\n\tv_mov_b32 v134, %6\n\tv_mov_b32 v135, %7\n\t"
                     "s_set_gpr_idx_off"
                     :: "v"(pf[0][0]), "v"(pf[0][1]), "v"(pf[0][2]), "v"(pf[0][3]), "v"(pf[1][0]), "v"(pf[1][1]), "v"(pf[1][2]), "v"(pf[1][3]), "s"(8 * k));
      }
    }
  }
  l = swap_sum(l);
  *(LAS f32x2*)(lds + LDS_ML + w * 256 + r32 * 8) = f32x2{m, l};
  LGKM0();
  ctl[CTL_MLOK / 4 + w] = 1;
  STAMP(2);

  const unsigned tq = (lane & 15) >> 2, tp = lane & 3, tblk = (lane >> 4) & 1;
  const unsigned vaddr0 = LDS_RING + (4 * hi + tq) * 256 + ((tq ^ (pos & 3)) << 6) + ((2 * tblk + (tp >> 1)) << 4) + ((tp & 1) << 3);
  f32x16 o[4];
#pragma unroll
  for (int i = 0; i < 4; ++i)
#pragma unroll
    for (int r = 0; r < 16; ++r) o[i][r] = 0.f;
  auto weights = [&]() {
    wait_above<1>(lbase + LDS_CTL + CTL_MLOK + min(lane, NC - 1) * 4, 0);
    unsigned a = LDS_ML + r32 * 8;
    asm volatile("" : "+v"(a) :: "memory");
    float M = -3.0e38f, L = 0.f;
    f32x2 ml[NC];
#pragma unroll
    for (int i = 0; i < NC; ++i) { ml[i] = *(LAS f32x2*)(lds + a + i * 256); M = fmaxf(M, ml[i][0]); }
#pragma unroll
    for (int i = 0; i < NC; ++i) L = fmaf(__builtin_amdgcn_exp2f((ml[i][0] - M) * CS), ml[i][1], L);
    const float inv = 1.0f / L;
    *(LAS float*)(lds + (a >> 1) + (LDS_WT - LDS_ML / 2) + pos * 128) = __builtin_amdgcn_exp2f((mprev - M) * CS) * inv;
    const float ct = __builtin_amdgcn_exp2f((m - M) * CS) * inv;
#pragma unroll
    for (int s = 0; s < 2; ++s) {
      u32x4 x = *(LAS u32x4*)(lds + ppst + W_LAST + s * 1024);
#pragma unroll
      for (int e = 0; e < 4; ++e) x[e] = pack2(__uint_as_float(x[e] << 16) * ct, __uint_as_float(x[e] & 0xffff0000u) * ct);
      *(LAS u32x4*)(lds + ppst + W_LAST + s * 1024) = x;
    }
    LGKM0();
    ctl[CTL_WTOK / 4 + w] = 1;
  };
  resc &= ~1u;
  if (nmine <= 2) weights();
#pragma unroll 1
  for (int k = 0; k < nmine - 1; ++k) {
    if (k == 1) weights();
    if ((resc >> k) & 1) {
      const float alpha = *(LAS float*)(lds + palp + k * 128);
#pragma unroll
      for (int i = 0; i < 4; ++i)
#pragma unroll
        for (int r = 0; r < 16; ++r) o[i][r] *= alpha;
    }
    const unsigned u = NT + w + NC * k;
    u32x4 pf[2];
    if (k >= KREG) {
      pf[0] = *(LAS u32x4*)(lds + ppst + (k - KREG) * 2048);
      pf[1] = *(LAS u32x4*)(lds + ppst + (k - KREG) * 2048 + 1024);
    } else {
      asm volatile("s_set_gpr_idx_on %8, gpr_idx(SRC0)\n\t"
                   "v_mov_b32 %0, v128\n\tv_mov_b32 %1, v129\n\tv_mov_b32 %2, v130\n\tv_mov_b32 %3, v131\n\t"
                   "v_mov_b32 %4, v132\n\tv_mov_b32 %5, v133\n\tv_mov_b32 %6, v134\n\tv_mov_b32 %7, v135\n\t"
                   "s_set_gpr_idx_off\n\ts_nop 1"
                   : "=v"(pf[0][0]), "=v"(pf[0][1]), "=v"(pf[0][2]), "=v"(pf[0][3]), "=v"(pf[1][0]), "=v"(pf[1][1]), "=v"(pf[1][2]), "=v"(pf[1][3])
                   : "s"(8 * k));
    }
    asm volatile("" : "+v"(pf[0]), "+v"(pf[1]), "+v"(o[0]), "+v"(o[1]), "+v"(o[2]), "+v"(o[3]));
    if (k == nmine - 2) wait_above<1>(lbase + LDS_CTL + CTL_WTOK + min(lane, NC - 1) * 4, 0);
    wait_above<CSLEEP>(aland, 4 * u);
    if (TPRIO && k == nmine - 2) __builtin_amdgcn_s_setprio(TPRIO);
    bf16x8 vf[4][2];
#pragma unroll
    for (int i = 0; i < 4; ++i) {
      const unsigned a = (vaddr0 + ((u & 7) << 13)) ^ (i << 6);
#pragma unroll
      for (int s = 0; s < 2; ++s) {
        const s16x4 lo = __builtin_amdgcn_ds_read_tr16_b64_v4i16((LAS s16x4*)(lds + a + 4096 * s));
        const s16x4 up = __builtin_amdgcn_ds_read_tr16_b64_v4i16((LAS s16x4*)(lds + a + 4096 * s + 2048));
        vf[i][s] = __builtin_bit_cast(bf16x8, __builtin_shufflevector(lo, up, 0, 1, 2, 3, 4, 5, 6, 7));
      }
    }
    asm volatile("s_waitcnt lgkmcnt(0)" : "+v"(vf[0][0]), "+v"(vf[0][1]), "+v"(vf[1][0]), "+v"(vf[1][1]), "+v"(vf[2][0]), "+v"(vf[2][1]), "+v"(vf[3][0]), "+v"(vf[3][1]) :: "memory");
    ctl[CTL_FREE / 4 + (u & 7)] = u + 8;
    if (pf_on) prefetch(u);
    if (toucher) touch_at(u);
#pragma unroll
    for (int s = 0; s < 2; ++s)
#pragma unroll
      for (int i = 0; i < 4; ++i) o[i] = __builtin_amdgcn_mfma_f32_32x32x16_bf16(vf[i][s], __builtin_bit_cast(bf16x8, pf[s]), o[i], 0, 0, 0);
  }
  STAMP(3);

  {
    unsigned l16 = lane * 16;
    asm volatile("" : "+v"(l16));
    const unsigned l4 = l16 >> 2, awt = LDS_WT + (l4 & 127);
    auto put = [&](unsigned dst, const f32x16& x) {
#pragma unroll
      for (int j = 0; j < 4; ++j) *(LAS f32x4*)(lds + dst + j * 1024) = f32x4{x[4 * j], x[4 * j + 1], x[4 * j + 2], x[4 * j + 3]};
    };
    float co[6];
    bf16x8 pl[NC][2];
    auto ld_p = [&](int p) {
      pl[p][0] = *(LAS bf16x8*)(lds + LDS_WREG + p * W_SIZE + W_LAST + l16);
      pl[p][1] = *(LAS bf16x8*)(lds + LDS_WREG + p * W_SIZE + W_LAST + l16 + 1024);
    };
#pragma unroll
    for (int i = 0; i < 4; ++i) co[i] = *(LAS float*)(lds + awt + ((pos & 3) ^ i) * 128);
    co[4] = *(LAS float*)(lds + awt + 4 * 128);
    co[5] = *(LAS float*)(lds + awt + 5 * 128);
    ld_p(0); ld_p(1);
    asm volatile("" : "+v"(l16) :: "memory");
    if (nmine > 1) {
      if (pos > 3) put(LDS_RING + ((2 * NT - 2 * NC + pos) & 7) * 8192 + l16, o[0]);
#pragma unroll
      for (int i = 1; i < 4; ++i) put(LDS_WREG + pos * W_SIZE + (i - 1) * 4096 + l16, o[i]);
      LGKM0();
      ctl[CTL_ODONE / 4 + pos] = 1;
    }
    if (pos < 4) {
      f32x16 of = o[0], ot;
#pragma unroll
      for (int r = 0; r < 16; ++r) ot[r] = 0.f;
      auto src_o = [&](int p) -> unsigned {
        const int i = pos ^ (p & 3);
        return l16 + (i == 0 ? LDS_RING + ((2 * NT - 2 * NC + p) & 7) * 8192 : LDS_WREG + p * W_SIZE + (i - 1) * 4096);
      };
      auto fma_o = [&](unsigned src, float c) {
#pragma unroll
        for (int j = 0; j < 4; ++j) {
          const f32x4 x = *(LAS f32x4*)(lds + src + j * 1024);
#pragma unroll
          for (int e = 0; e < 4; ++e) of[4 * j + e] = fmaf(x[e], c, of[4 * j + e]);
        }
      };
      auto mma_t = [&](int p, bf16x8 p0, bf16x8 p1) {
        const unsigned a = vaddr0 + (((2 * NT - NC + p) & 7) << 13);
        bf16x8 vf[2];
#pragma unroll
        for (int s = 0; s < 2; ++s) {
          const s16x4 lo = __builtin_amdgcn_ds_read_tr16_b64_v4i16((LAS s16x4*)(lds + a + 4096 * s));
          const s16x4 up = __builtin_amdgcn_ds_read_tr16_b64_v4i16((LAS s16x4*)(lds + a + 4096 * s + 2048));
          vf[s] = __builtin_bit_cast(bf16x8, __builtin_shufflevector(lo, up, 0, 1, 2, 3, 4, 5, 6, 7));
        }
        ot = __builtin_amdgcn_mfma_f32_32x32x16_bf16(vf[0], p0, ot, 0, 0, 0);
        ot = __builtin_amdgcn_mfma_f32_32x32x16_bf16(vf[1], p1, ot, 0, 0, 0);
      };
      if (NT >= 2 * NC) {
#define PIN_OF() asm volatile("" : "+v"(of), "+v"(ot))
#pragma unroll
        for (int r = 0; r < 16; ++r) of[r] *= co[0];
        const bool wl = (l4 & 24) == 0;
        const unsigned ua = 4 * (2 * NT - NC);
        wait_above_v(wl ? aland : lbase + LDS_CTL + CTL_ODONE + (l4 & 12), wl ? ua : 0u);
        mma_t(0, pl[0][0], pl[0][1]);
#pragma unroll
        for (int i = 1; i < 4; ++i) fma_o(LDS_WREG + (pos ^ i) * W_SIZE + (i - 1) * 4096 + l16, co[i]);
        PIN_OF();
        STAMP(4);
        ld_p(2); ld_p(3); ld_p(4); ld_p(5);
        wait_above_v(wl ? aland : lbase + LDS_CTL + CTL_ODONE + 16 + (l4 & 4), wl ? ua + 8 : 0u);
        mma_t(1, pl[1][0], pl[1][1]);
        mma_t(2, pl[2][0], pl[2][1]);
        fma_o(src_o(4), co[4]);
        fma_o(src_o(5), co[5]);
#pragma unroll
        for (int r = 0; r < 16; ++r) { ot[r] += of[r]; of[r] = 0.f; }
        asm volatile("" : "+v"(ot));
        wait_above<0>(aland, ua + 20);
        mma_t(3, pl[3][0], pl[3][1]);
        mma_t(4, pl[4][0], pl[4][1]);
        mma_t(5, pl[5][0], pl[5][1]);
        of = ot;
      } else {
        wait_above<1>(lbase + LDS_CTL + CTL_WTOK + min(l4, (NC - 1) * 4u), 0);
        const float cw = *(LAS float*)(lds + awt + pos * 128);
#pragma unroll
        for (int r = 0; r < 16; ++r) of[r] *= cw;
#pragma unroll 1
        for (int p = max(NC - NT, 0); p < NC; ++p) {
          if (p != pos && NT - 2 * NC + p >= 0) {
            wait_above<1>(lbase + LDS_CTL + CTL_ODONE + p * 4, 0);
            fma_o(src_o(p), *(LAS float*)(lds + awt + p * 128));
          }
          wait_above<1>(aland, 4 * (2 * NT - NC + p));
          mma_t(p, *(LAS bf16x8*)(lds + LDS_WREG + p * W_SIZE + W_LAST + l16), *(LAS bf16x8*)(lds + LDS_WREG + p * W_SIZE + W_LAST + l16 + 1024));
        }
#pragma unroll
        for (int r = 0; r < 16; ++r) of[r] += ot[r];
      }
      const __amdgpu_buffer_rsrc_t ro = __builtin_amdgcn_make_buffer_rsrc(outp, 0, NB * HQ * SQ * HD * 2, 0x00020000);
      const unsigned orow = (unsigned)(b * HQ + 2 * h) * SQ * HD * 2 + pos * 64 + ((l16 & 511) << 4) + ((l16 >> 5) & 16);
#pragma unroll
      for (int j = 0; j < 2; ++j) {
        const unsigned a0 = pack2(of[8 * j], of[8 * j + 1]), a1 = pack2(of[8 * j + 2], of[8 * j + 3]);
        const unsigned b0 = pack2(of[8 * j + 4], of[8 * j + 5]), b1 = pack2(of[8 * j + 6], of[8 * j + 7]);
        auto s0 = __builtin_amdgcn_permlane32_swap(a0, b0, false, false);
        auto s1 = __builtin_amdgcn_permlane32_swap(a1, b1, false, false);
        u32x4 val = {s0[0], s1[0], s0[1], s1[1]};
        __builtin_amdgcn_raw_buffer_store_b128(val, ro, orow + j * 32, 0, OUTAUX);
      }
    }
  }
}
}

extern "C" void kernel_launch(void* const* d_in, const int* in_sizes, int n_in, void* d_out, int out_size, void* d_ws,
                              size_t ws_size, hipStream_t stream) {
  (void)n_in; (void)out_size; (void)ws_size;
  const unsigned nphys = (unsigned)(in_sizes[1] / (PAGE * HKV * HD));
  const unsigned kvbytes = nphys * PAGE_BYTES;
  (void)hipFuncSetAttribute((const void*)paged_attn, hipFuncAttributeMaxDynamicSharedMemorySize, LDS_TOTAL);
  hipLaunchKernelGGL(paged_attn, dim3(256), dim3(512), LDS_TOTAL, stream, d_in[0], d_in[1], d_in[2], (const int*)d_in[3],
                     (const int*)d_in[4], d_out, (char*)d_ws, kvbytes, nphys);
}
```

```cpp
#define PACE_X16 436
#define SOFT 5
#include <hip/hip_runtime.h>
#include <hip/hip_bf16.h>

#define PROBE_G 0
#define QPOL "nt"
#define PF_2ND
#define TLBW 5
#define TLBG 0x200000u
#define SORTP 1
#define TORDER 0
#define PF_OFF 6
#define CODEPF 16640
#define COLD_AT 140
#define COLD_PAUSE 60
#define TMODE 1
#define TK2 0
#define TV 16
#define TAHEAD 2
#define PF_BY 0
#define PF_END 2u
#define PF_X64B 72
#define PF_DENSE 1
#define PF_PER 4
#define PF_PSTEP 2
#define GAP_TICKS 0
#define OUTAUX 0
#define HOFF 4
#define LSLEEP 0
#define PRESLEEP 0
#define PF_EVERY 2
#define PF_X64 76
#define PF_ALL 0
#define PF_LEAD 300
#define PF_POL ""
#define PF_SLOW 0
#define PF_RUN 3
#define TSLEEP 0
#define TWAIT 1
#define TPRIO 0
#define CSLEEP 1

#define STAMP(i) do {} while (0)

namespace {
constexpr int NB = 16, HQ = 32, HKV = 16, SQ = 16, HD = 128, PAGE = 16, NBPS = 256, SKV = NBPS * PAGE;
constexpr unsigned PAGE_BYTES = PAGE * HKV * HD * 2;
constexpr unsigned INVALID = 0x80000000u;
constexpr int U0 = 4;
constexpr int NC = 6;
constexpr int KREG = 16, KLDS = 5;
static_assert(NC * (KREG + KLDS + 1) >= NBPS / 2, "P storage");
constexpr unsigned LDS_RING = 0;
constexpr unsigned LDS_WREG = 65536;
constexpr unsigned W_ALP = KLDS * 2048;
constexpr unsigned W_LAST = W_ALP + (KREG + KLDS + 1) * 128;
constexpr unsigned W_SIZE = W_LAST + 2048;
static_assert(W_LAST >= 3 * 4096 && W_SIZE % 256 == 0, "consumer region");
constexpr unsigned LDS_TBL = LDS_WREG + W_SIZE;
constexpr unsigned LDS_SRT = LDS_WREG + 3 * W_SIZE;
constexpr unsigned LDS_Q = LDS_WREG;
constexpr unsigned LDS_ML = LDS_WREG + NC * W_SIZE;
constexpr unsigned LDS_WT = LDS_ML + NC * 256;
constexpr unsigned LDS_CTL = LDS_WT + NC * 128;
constexpr unsigned CTL_LAND = 0;
constexpr unsigned CTL_QOK = 16;
constexpr unsigned CTL_SORT = 24;
constexpr unsigned CTL_FREE = 32;
constexpr unsigned CTL_MLOK = 64;
constexpr unsigned CTL_ODONE = 96;
constexpr unsigned CTL_WTOK = 128;
constexpr unsigned LDS_DUMMY = LDS_CTL + 256;
constexpr unsigned LDS_PLIST = LDS_DUMMY + 256;
constexpr unsigned LDS_TOTAL = LDS_PLIST + 1024;
static_assert(LDS_TOTAL <= 163840, "lds");

typedef __attribute__((ext_vector_type(8))) __bf16 bf16x8;
typedef __attribute__((ext_vector_type(2))) __bf16 bf16x2;
typedef __attribute__((ext_vector_type(4))) short s16x4;
typedef __attribute__((ext_vector_type(16))) float f32x16;
typedef __attribute__((ext_vector_type(4))) float f32x4;
typedef __attribute__((ext_vector_type(2))) float f32x2;
typedef __attribute__((ext_vector_type(4))) unsigned u32x4;
typedef __attribute__((ext_vector_type(2))) unsigned u32x2;
typedef __attribute__((ext_vector_type(4))) int i32x4;
#define LAS __attribute__((address_space(3)))

__device__ __forceinline__ float swap_max(float x) {
  auto r = __builtin_amdgcn_permlane32_swap(__float_as_uint(x), __float_as_uint(x), false, false);
  return fmaxf(__uint_as_float(r[0]), __uint_as_float(r[1]));
}
__device__ __forceinline__ float swap_sum(float x) {
  auto r = __builtin_amdgcn_permlane32_swap(__float_as_uint(x), __float_as_uint(x), false, false);
  return __uint_as_float(r[0]) + __uint_as_float(r[1]);
}
__device__ __forceinline__ unsigned pack2(float a, float b) {
  bf16x2 v = {(__bf16)a, (__bf16)b};
  return __builtin_bit_cast(unsigned, v);
}
template <int CTRL> __device__ __forceinline__ int dpp_add(int x) { return x + __builtin_amdgcn_update_dpp(0, x, CTRL, 0xf, 0xf, true); }
__device__ __forceinline__ unsigned now32() { return (unsigned)__builtin_amdgcn_s_memrealtime(); }
__device__ __forceinline__ int to_sgpr(int v) {
  int r;
  asm volatile("s_nop 1\n\tv_readfirstlane_b32 %0, %1" : "=s"(r) : "v"(v));
  return r;
}
#define LGKM0() asm volatile("s_waitcnt lgkmcnt(0)" ::: "memory")
#define VMWAIT(n) asm volatile("s_waitcnt vmcnt(" #n ")" ::: "memory")
#define CBAR() asm volatile("" ::: "memory")
template <int SLEEP> __device__ __forceinline__ void wait_above(unsigned addr, unsigned u) {
  unsigned v, cnt;
  if (SLEEP)
    asm volatile("s_mov_b32 %1, 0x200000\n\ts_branch 2f\n"
                 "1:\n\ts_sleep %4\n"
                 "2:\n\tds_read_b32 %0, %2\n\ts_sub_u32 %1, %1, 1\n\ts_waitcnt lgkmcnt(0)\n\t"
                 "v_cmp_ge_u32 vcc, %3, %0\n\ts_cbranch_scc1 3f\n\ts_nop 1\n\ts_cbranch_vccnz 1b\n3:"
                 : "=&v"(v), "=&s"(cnt) : "v"(addr), "s"(u), "n"(SLEEP) : "memory", "scc", "vcc");
  else
    asm volatile("s_mov_b32 %1, 0x200000\n"
                 "1:\n\tds_read_b32 %0, %2\n\ts_sub_u32 %1, %1, 1\n\ts_waitcnt lgkmcnt(0)\n\t"
                 "v_cmp_ge_u32 vcc, %3, %0\n\ts_cbranch_scc1 3f\n\ts_nop 1\n\ts_cbranch_vccnz 1b\n3:"
                 : "=&v"(v), "=&s"(cnt) : "v"(addr), "s"(u) : "memory", "scc", "vcc");
}

__device__ __forceinline__ void wait_above_v(unsigned addr, unsigned u) {
  unsigned v, cnt;
  asm volatile("s_mov_b32 %1, 0x200000\n"
               "1:\n\tds_read_b32 %0, %2\n\ts_sub_u32 %1, %1, 1\n\ts_waitcnt lgkmcnt(0)\n\t"
               "v_cmp_ge_u32 vcc, %3, %0\n\ts_cbranch_scc1 3f\n\ts_nop 1\n\ts_cbranch_vccnz 1b\n3:"
               : "=&v"(v), "=&s"(cnt) : "v"(addr), "v"(u) : "memory", "scc", "vcc");
}

__global__ void __launch_bounds__(512, 1) __attribute__((amdgpu_num_vgpr(64)))
paged_attn(const void* __restrict__ qp, const void* __restrict__ kp, const void* __restrict__ vp, const int* __restrict__ bt,
           const int* __restrict__ lens, void* __restrict__ outp, char* __restrict__ ws, unsigned kvbytes, unsigned nphys) {
  extern __shared__ __attribute__((aligned(16))) char smem[];
  LAS char* lds = (LAS char*)smem;
  const unsigned tw0 = now32();
  __builtin_amdgcn_sched_barrier(0);
  const int lane0 = threadIdx.x & 63;
  const int wv = __builtin_amdgcn_readfirstlane(threadIdx.x >> 6);
  const int g = blockIdx.x;
  const int h = g >> 4;
  constexpr float CS = 0.08838834764831845f * 1.4426950408889634f;
  volatile LAS unsigned* ctl = (volatile LAS unsigned*)(lds + LDS_CTL);

  const unsigned lbase = (unsigned)(size_t)lds;
  const bool toucher = TLBW && (g & 8) != 0;
  const unsigned nchunk = (kvbytes + 16 * TLBG - 1) / (16 * TLBG);

  unsigned lraw, first;
  u32x4 lq;
  {
    asm volatile("global_load_dword %0, %3, %4\n\t"
                 "global_load_dwordx4 %1, %5, %4\n\t"
                 "global_load_dword %2, %6, %7"
                 : "=&v"(lraw), "=&v"(lq), "=&v"(first)
                 : "v"((lane0 & 15) * 4), "s"(lens), "v"(lane0 & 48), "v"((lane0 & 15) * (wv < 2 ? NBPS * 4 : 0) + (lane0 >> 4) * 8 + (wv & 1) * 4), "s"(bt),
                   "s"(qp), "s"(kp), "s"(vp), "s"(outp), "s"(kvbytes), "s"(nphys)
                 : "memory");
    __builtin_amdgcn_sched_barrier(0);
  }
  if (wv >= 4) {
    if (wv == 7) {
      const __amdgpu_buffer_rsrc_t rb = __builtin_amdgcn_make_buffer_rsrc((void*)bt, 0, NB * NBPS * 4, 0x00020000);
#define TBL4(SO) "buffer_load_dwordx4 %2, %1, " SO " offen lds\n\tbuffer_load_dwordx4 %2, %1, " SO " offen offset:1024 lds\n\t" \
                 "buffer_load_dwordx4 %2, %1, " SO " offen offset:2048 lds\n\tbuffer_load_dwordx4 %2, %1, " SO " offen offset:3072 lds\n\t"
      asm volatile("s_mov_b32 m0, %0\n\ts_nop 0\n\t" TBL4("0") "s_add_u32 m0, m0, 4096\n\ts_nop 0\n\t" TBL4("%3")
                   "s_add_u32 m0, m0, %6\n\ts_nop 0\n\t" TBL4("%4") "s_add_u32 m0, m0, 4096\n\ts_nop 0\n\t" TBL4("%5")
                   :: "s"(lbase + LDS_TBL), "s"(rb), "v"(lane0 * 16), "s"(4096), "s"(8192), "s"(12288), "n"(W_SIZE - 4096) : "memory", "scc");
    }
    unsigned long long pc;
    asm volatile("s_getpc_b64 %0" : "=s"(pc));
    const __amdgpu_buffer_rsrc_t rt = __builtin_amdgcn_make_buffer_rsrc(wv == 5 ? (void*)pc : (void*)kp, 0,
                                                                        wv == 5 ? CODEPF : (wv == 6 && toucher) ? (int)min(kvbytes, TAHEAD * 16 * TLBG) : 0, 0x00020000);
    const unsigned mul = wv == 5 ? 512u : 16 * TLBG + 4224u, add = wv == 5 ? ((g >> 3) & 3) * 128 : (g >> 4) * (TLBG + 33024u);
    asm volatile("s_mov_b32 m0, %0\n\ts_nop 0\n\tbuffer_load_dword %1, %2, 0 offen lds"
                 :: "s"(lbase + LDS_DUMMY), "v"((wv == 5 ? lane0 : lane0 & 31) * mul + add), "s"(rt) : "memory");
    *(LAS u32x4*)(lds + LDS_SRT + (threadIdx.x - 256) * 16) = u32x4{0u, 0u, 0u, 0u};
    if (threadIdx.x < 296) ctl[lane0] = ((lane0 >> 3) == CTL_FREE / 32) ? (lane0 & 7) : 0u;
    asm volatile("s_waitcnt lgkmcnt(0)\n\ts_barrier" ::: "memory");
  }
  if (wv < 2) __builtin_amdgcn_s_setprio(3);
  const unsigned rstride = wv < 2 ? 4096 : 256;
  unsigned vo0 = (lane0 >> 4) * rstride + (((unsigned)(lane0 & 15) ^ ((lane0 >> 4) << 2)) << 4) + (wv < 2 ? h * (HD * 2) : 0);
  unsigned vo1 = (vo0 + 4 * rstride - 1024) ^ 16, vo2 = (vo0 + 8 * rstride - 2048) ^ 32, vo3 = (vo0 + 12 * rstride - 3072) ^ 48;
  const unsigned want = (g & 8) ? 15 - (g & 7) : (g & 7);
  const unsigned dst0 = lbase + (wv < 2 ? LDS_RING : LDS_Q) + (wv & 1) * 4096;
  asm volatile("s_cmp_ge_u32 %8, 4\n\ts_cbranch_scc1 1f\n\ts_waitcnt vmcnt(0)\n1:\n\ts_waitcnt vmcnt(1)" : "+v"(lraw), "+v"(lq), "+v"(first), "+v"(vo0), "+v"(vo1), "+v"(vo2), "+v"(vo3) : "s"(dst0), "s"(wv) : "memory", "scc");
  unsigned lm1 = min(lraw - 1u, (unsigned)SKV - 1u);
  unsigned rank = 0;
  {
    const unsigned key = lm1 * 16 + (lane0 & 15);
#pragma unroll
    for (int j = 0; j < 4; ++j) rank += (min(lq[j] - 1u, (unsigned)SKV - 1u) * 16 + ((lane0 >> 4) * 4 + j) > key) ? 1u : 0u;
    auto r1 = __builtin_amdgcn_permlane16_swap(rank, rank, false, false);
    rank = r1[0] + r1[1];
    auto r2 = __builtin_amdgcn_permlane32_swap(rank, rank, false, false);
    rank = r2[0] + r2[1];
  }
  first = min(first, nphys - 1) * PAGE_BYTES;
  const int b = __builtin_ctz((unsigned)__builtin_amdgcn_ballot_w64(rank == want) | 0x8000u);
  unsigned long long tstart = 0;
  if (wv < 4) {
    const bool qok = (int)__builtin_amdgcn_readlane(lraw, b) > 0;
    const __amdgpu_buffer_rsrc_t r0 = __builtin_amdgcn_make_buffer_rsrc(wv < 2 ? (void*)kp : (void*)qp, 0,
                                                                        wv < 2 ? (int)kvbytes : qok ? NB * HQ * SQ * HD * 2 : 0, 0x00020000);
    const unsigned so = wv < 2 ? __builtin_amdgcn_readlane(first, b) : qok ? (unsigned)((b * HQ + 2 * h) * SQ * HD * 2) + (wv & 1) * 4096 : 0u;
    asm volatile("s_waitcnt vmcnt(0)\n\t"
                 "s_mov_b32 m0, %2\n\t"
                 "s_memrealtime %0\n\t"
                 "s_nop 2\n\t"
                 "buffer_load_dwordx4 %4, %1, %3 offen nt lds\n\t"
                 "buffer_load_dwordx4 %5, %1, %3 offen offset:1024 nt lds\n\t"
                 "buffer_load_dwordx4 %6, %1, %3 offen offset:2048 nt lds\n\t"
                 "buffer_load_dwordx4 %7, %1, %3 offen offset:3072 nt lds\n\t"
                 "s_waitcnt lgkmcnt(0)\n\t"
                 "s_barrier"
                 : "=&s"(tstart) : "s"(r0), "s"(dst0), "s"(so), "v"(vo0), "v"(vo1), "v"(vo2), "v"(vo3) : "memory");
  }
  asm volatile("" : "+v"(lm1), "+v"(rank), "+v"(first) :: "memory");
  __builtin_amdgcn_sched_barrier(0);
  const int bp = __builtin_ctz((unsigned)__builtin_amdgcn_ballot_w64(rank == 15 - want) | 0x8000u);
  const int len = __builtin_amdgcn_readlane(lm1, b) + 1, nown = (len + PAGE - 1) / PAGE, NT = (nown + 1) >> 1;
  const int npl = (__builtin_amdgcn_readlane(lm1, bp) >> 4) + 1, NTL = (npl + 1) >> 1;
  auto total = [&]() __attribute__((always_inline)) {
    int t = lm1 + 1;
    t = dpp_add<0x121>(t); t = dpp_add<0x122>(t); t = dpp_add<0x124>(t); t = dpp_add<0x128>(t);
    return t;
  };

  auto sort_pages = [&](int lane, int bb, int n, unsigned scr, unsigned dst) __attribute__((always_inline)) {
    u32x4 e = *(LAS u32x4*)(lds + LDS_TBL + (bb >> 3) * W_SIZE + (bb & 7) * 1024 + lane * 16);
    bool in[4];
#pragma unroll
    for (int j = 0; j < 4; ++j) {
      e[j] = min(e[j], nphys - 1);
      in[j] = 4 * lane + j >= 2 * U0 && 4 * lane + j < n - 1;
      __hip_atomic_fetch_or((LAS unsigned*)(lds + scr + ((e[j] >> 5) & 255) * 8), in[j] ? 1u << (e[j] & 31) : 0u, __ATOMIC_RELAXED, __HIP_MEMORY_SCOPE_WAVEFRONT);
    }
    CBAR();
    u32x4 a = *(LAS u32x4*)(lds + scr + lane * 32), c = *(LAS u32x4*)(lds + scr + lane * 32 + 16);
    const int n0 = __builtin_popcount(a[0]), n1 = n0 + __builtin_popcount(a[2]), n2 = n1 + __builtin_popcount(c[0]), n3 = n2 + __builtin_popcount(c[2]);
    int sc = n3;
    sc = dpp_add<0x111>(sc); sc = dpp_add<0x112>(sc); sc = dpp_add<0x114>(sc); sc = dpp_add<0x118>(sc);
    sc += __builtin_amdgcn_update_dpp(0, sc, 0x142, 0xa, 0xf, false);
    sc += __builtin_amdgcn_update_dpp(0, sc, 0x143, 0xc, 0xf, false);
    const bool ok = SORTP && nphys <= 8192u && __builtin_amdgcn_readlane(sc, 63) == n - 1 - 2 * U0;
    sc += 2 * U0 - n3;
    a[1] = sc; a[3] = sc + n0; c[1] = sc + n1; c[3] = sc + n2;
    *(LAS u32x4*)(lds + scr + lane * 32) = a;
    *(LAS u32x4*)(lds + scr + lane * 32 + 16) = c;
    CBAR();
    u32x2 wp[4];
#pragma unroll
    for (int j = 0; j < 4; ++j) wp[j] = *(LAS u32x2*)(lds + scr + ((e[j] >> 5) & 255) * 8);
#pragma unroll
    for (int j = 0; j < 4; ++j) {
      const unsigned r = wp[j][1] + __builtin_popcount(wp[j][0] & ((1u << (e[j] & 31)) - 1u));
      *(LAS unsigned*)(lds + dst + ((ok && in[j]) ? r : 4 * lane + j) * 4) = e[j] * PAGE_BYTES;
    }
    CBAR();
  };

  if (__builtin_expect(wv < 2, 1)) {
    const int half = wv, lane = lane0;
    const unsigned vland = lbase + LDS_CTL + CTL_LAND + half * 4;
    const unsigned vfree = lbase + LDS_CTL + CTL_FREE + (lane & 7) * 4;
    const unsigned ring = dst0;
    unsigned l0 = __builtin_amdgcn_ds_bpermute((b + 16 * lane) * 4, first), l1 = 0;
    const unsigned a0 = lbase + LDS_SRT + 4096 + min(2 * lane + half, nown - 1) * 4, a1 = lbase + LDS_SRT + 4096 + min(2 * lane + half + 128, nown - 1) * 4;
    const int T16 = total() * PACE_X16;
    const int step = to_sgpr((int)((float)T16 * __builtin_amdgcn_rcpf((float)max(len, 32))));
    const unsigned due0 = (((unsigned)tstart + (((unsigned)tstart - tw0 > COLD_AT) ? COLD_PAUSE : 0)) << 8) + half * ((step * HOFF) >> 3);
    const int lrows = len - (2 * (NT - 1) + half) * PAGE;
#define CNT(r)
    unsigned vf, vt, lc;
    asm volatile(
        "s_waitcnt lgkmcnt(0)\n\t"
        "s_memrealtime s[68:69]\n\t"
        "ds_read_b32 %[vf], %[vfree]\n\t"
        "s_mov_b32 s60, 1\n\t"
        "s_mov_b32 s61, 4\n\t"
        "s_mov_b32 s62, 0x400000\n\t"
        "s_mov_b32 s66, 0\n\t"
        "s_min_u32 s67, %[nt], %[u0]\n\t"
        "s_mov_b32 s75, %[nrec]\n\t"
        "s_mov_b32 s72, 0x2000\n\t"
        "s_mov_b32 s73, 1\n\t"
        "s_mov_b32 s74, %[due1]\n\t"
        "s_mov_b64 s[76:77], %[kp]\n\t"
        "s_mov_b32 s78, %[nrec]\n\t"
        "s_mov_b32 s79, 0x20000\n\t"
        "s_and_b32 s77, s77, 0xffff\n\t"
        "v_mov_b32 %[lc], %[l0]\n\t"
        "s_getpc_b64 s[70:71]\n"
        ".LldA%=:\n\t"
        "s_add_u32 s80, s70, .LldL%=-.LldA%=\n\t"
        "s_addc_u32 s71, s71, 0\n\t"
        "s_branch .LldF%=\n"
        ".LldR%=:\n\t"
        "s_sub_u32 s62, s62, 1\n\t"
        "s_cbranch_scc1 .LldE%=\n"
        ".LldT%=:\n\t"
        "s_getreg_b32 s64, hwreg(HW_REG_IB_STS)\n\t"
        "s_and_b32 s65, s64, 15\n\t"
        "s_bfe_u32 s64, s64, 0x20016\n\t"
        "s_lshl4_add_u32 s64, s64, s65\n\t"
        "s_add_u32 s64, s64, 3\n\t"
        "s_and_b32 s64, s64, 60\n\t"
        "s_sub_u32 s65, s61, s64\n\t"
        "s_cmp_lg_u32 s65, s66\n\t"
        "s_cbranch_scc1 .LldP%=\n"
        ".LldC%=:\n\t"
        "s_waitcnt lgkmcnt(0)\n\t"
        "v_cmp_eq_u32 vcc, s60, %[vf]\n\t"
        "s_lshl_b32 s64, s68, 8\n\t"
        "s_memrealtime s[68:69]\n\t"
        "ds_read_b32 %[vf], %[vfree]\n\t"
        CNT("s82")
        "s_sub_u32 s64, s64, s74\n\t"
        "s_cmp_lt_i32 s64, 0\n\t"
        "s_cbranch_scc1 .LldR%=\n\t"
        CNT("s83")
        "s_cbranch_vccz .LldR%=\n\t"
        CNT("s84")
        "buffer_load_dwordx4 %[o0], s[76:79], s63 offen nt lds\n\t"
        "s_add_u32 s74, s74, %[step]\n\t"
        "s_add_u32 s73, s73, 1\n\t"
        "buffer_load_dwordx4 %[o1], s[76:79], s63 offen offset:1024 nt lds\n\t"
        "s_add_u32 s60, s60, 1\n\t"
        "s_add_u32 s61, s61, 4\n\t"
        "buffer_load_dwordx4 %[o2], s[76:79], s63 offen offset:2048 nt lds\n\t"
        "s_add_u32 s72, s72, 0x2000\n\t"
        "s_and_b32 s72, s72, 0xe000\n\t"
        "buffer_load_dwordx4 %[o3], s[76:79], s63 offen offset:3072 nt lds\n"
        ".LldF%=:\n\t"
        "v_readlane_b32 s63, %[lc], s73\n\t"
        "s_add_u32 m0, s72, %[ring]\n\t"
        "s_cmp_eq_u32 s73, %[ntm]\n\t"
        "s_cselect_b32 s78, s75, %[nrec]\n\t"
        "s_cmp_lg_u32 s60, s67\n\t"
        "s_cbranch_scc1 .LldT%=\n\t"
        "s_cmp_lg_u32 s60, %[u0]\n\t"
        "s_cbranch_scc1 .LldG%=\n\t"
        "s_cmp_le_u32 %[nt], %[u0]\n\t"
        "s_cbranch_scc1 .LldG%=\n"
        ".LldS%=:\n\t"
        "ds_read_b32 %[lc], %[vland] offset:%[srt]\n\t"
        "ds_read_b32 %[l0], %[a0]\n\t"
        "ds_read_b32 %[l1], %[a1]\n\t"
        "s_sub_u32 s62, s62, 1\n\t"
        "s_cbranch_scc1 .LldE%=\n\t"
        "s_waitcnt lgkmcnt(0)\n\t"
        "v_readfirstlane_b32 s64, %[lc]\n\t"
        "s_cmp_eq_u32 s64, 0\n\t"
        "s_cbranch_scc1 .LldS%=\n\t"
        "s_max_i32 s65, %[lrows], 0\n\t"
        "s_lshl_b32 s65, s65, 12\n\t"
        "s_add_u32 s64, s64, s65\n\t"
        "s_sub_u32 s64, s64, 1\n\t"
        "s_cmp_lt_i32 %[lrows], 16\n\t"
        "s_cselect_b32 s75, s64, %[nrec]\n\t"
        "s_min_u32 s67, %[nt], 64\n\t"
        "v_mov_b32 %[lc], %[l0]\n\t"
        "s_branch .LldF%=\n"
        ".LldG%=:\n\t"
        "s_cmp_eq_u32 s73, %[nt]\n\t"
        "s_cbranch_scc1 .LldV%=\n\t"
        "v_mov_b32 %[lc], %[l1]\n\t"
        "v_readfirstlane_b32 s63, %[l1]\n\t"
        "s_add_u32 s67, s60, %[nt]\n\t"
        "s_sub_u32 s67, s67, 64\n\t"
        "s_branch .LldT%=\n"
        ".LldV%=:\n\t"
        "s_cmp_lg_u32 s60, %[nt]\n\t"
        "s_cbranch_scc1 .LldD%=\n\t"
        "s_mov_b64 s[76:77], %[vp]\n\t"
        "s_and_b32 s77, s77, 0xffff\n\t"
        "s_mov_b32 s73, 0\n\t"
        "s_min_u32 s67, %[nt], 64\n\t"
        "s_add_u32 s67, s67, s60\n\t"
        "s_mov_b32 s74, %[duev]\n\t"
        "s_cmp_eq_u32 %[ntm], 0\n\t"
        "s_cselect_b32 s78, s75, %[nrec]\n\t"
        "v_mov_b32 %[lc], %[l0]\n\t"
        "v_readfirstlane_b32 s63, %[l0]\n\t"
        "v_xor_b32 %[o1], 16, %[o1]\n\t"
        "v_xor_b32 %[o2], 32, %[o2]\n\t"
        "v_xor_b32 %[o3], 48, %[o3]\n\t"
        "s_branch .LldT%=\n"
        ".LldD%=:\n\t"
        "s_add_u32 s74, s74, 0x40000000\n\t"
        "s_mov_b32 s60, -1\n\t"
        "s_branch .LldT%=\n"
        ".LldP%=:\n\t"
        "s_min_u32 s64, s64, 32\n\t"
        "s_add_u32 s70, s80, s64\n\t"
        "v_mov_b32 %[vt], s65\n\t"
        "s_mov_b32 s66, s65\n\t"
        "s_setpc_b64 s[70:71]\n\t"
        ".p2align 6\n"
        ".LldL%=:\n\t"
        "s_waitcnt vmcnt(0)\n\t"
        "s_waitcnt vmcnt(4)\n\t"
        "s_waitcnt vmcnt(8)\n\t"
        "s_waitcnt vmcnt(12)\n\t"
        "s_waitcnt vmcnt(16)\n\t"
        "s_waitcnt vmcnt(20)\n\t"
        "s_waitcnt vmcnt(24)\n\t"
        "s_waitcnt vmcnt(28)\n\t"
        "s_waitcnt vmcnt(32)\n\t"
        "ds_write_b32 %[vland], %[vt]\n\t"
        "s_cmp_lg_u32 s65, %[fin]\n\t"
        "s_cbranch_scc1 .LldC%=\n"
        ".LldE%=:\n\t"
        "s_waitcnt vmcnt(0) lgkmcnt(0)\n\t"
        : [vf] "=&v"(vf), [vt] "=&v"(vt), [lc] "=&v"(lc), [l0] "+v"(l0), [l1] "+v"(l1), [o1] "+v"(vo1), [o2] "+v"(vo2), [o3] "+v"(vo3)
        : [a0] "v"(a0), [a1] "v"(a1), [o0] "v"(vo0), [vland] "v"(vland), [vfree] "v"(vfree),
          [kp] "s"(kp), [vp] "s"(vp), [nrec] "s"(kvbytes), [nt] "s"(NT), [ring] "s"(ring),
          [step] "s"(step), [due1] "s"(due0 + step + SOFT * 256), [duev] "s"(due0 + (to_sgpr(T16) >> 5) + (SOFT + GAP_TICKS) * 256), [lrows] "s"(lrows), [ntm] "s"(NT - 1), [fin] "s"(8 * NT), [u0] "n"(U0), [srt] "n"(CTL_SORT - CTL_LAND)
        : "memory", "scc", "vcc", "s60", "s61", "s62", "s63", "s64", "s65", "s66", "s67", "s68", "s69", "s70", "s71", "s72", "s73", "s74", "s75",
          "s76", "s77", "s78", "s79", "s80", "s82", "s83", "s84");
    STAMP(3);
    asm volatile("s_endpgm" ::: "memory");
  }

  int lane = lane0;
  asm volatile("" : "+v"(lane));
  auto touch = [&](unsigned c, unsigned n) __attribute__((always_inline)) {
    const bool isv = c >= nchunk;
    const __amdgpu_buffer_rsrc_t rt = __builtin_amdgcn_make_buffer_rsrc(isv ? (void*)vp : (void*)kp, 0, (int)kvbytes, 0x00020000);
    unsigned ln = lane;
    asm volatile("" : "+v"(ln));
    const unsigned idx = (g >> 4) + 16 * (c - (isv ? nchunk : 0) + ln);
    const unsigned vo = (ln < n && idx < 0x80000000u / TLBG) ? idx * TLBG + ((idx * 30592u) & (TLBG - 128)) : INVALID;
    asm volatile("s_mov_b32 m0, %0\n\ts_nop 4\n\tbuffer_load_dword %2, %1, 0 offen nt lds" :: "s"((unsigned)(size_t)lds + LDS_DUMMY), "s"(rt), "v"(vo) : "memory");
  };
  if (wv == 7) {
#pragma unroll 1
    for (int i = 0; i < 2; ++i) {
      sort_pages(lane, i ? bp : b, i ? npl : nown, LDS_SRT + i * 2048, i ? LDS_PLIST : LDS_SRT + 4096);
      if (i == 0) {
        const unsigned lo = *(LAS unsigned*)(lds + LDS_SRT + 4096 + (nown - 1) * 4) + 1;
        LGKM0();
        ctl[CTL_SORT / 4 + (lane & 1)] = lo;
      }
    }
  }
  if (wv < 4) {
    VMWAIT(0);
    ctl[CTL_QOK / 4 + (wv & 1)] = 1;
  }
  const int lim = len;
  const int tot = total();
  const int w = wv - 2;
  const int r32 = lane & 31, hi = lane >> 5;
  const int nmine = (NT > w) ? (NT - w + NC - 1) / NC : 0;
  const unsigned aland = lbase + LDS_CTL + CTL_LAND + (lane & 1) * 4;
  const int tots = __builtin_amdgcn_readfirstlane(tot);
  const bool pf_on = PF_EVERY && npl * 256 * 64 > tots * PF_X64B && npl > nown && (h & 3) == PF_BY;
  const unsigned pf_max = (npl * 256 * 64 > tots * PF_X64) ? 256u : PF_END;
  const unsigned pf_last = 2 * NT - to_sgpr((int)((float)(2 * NT * (PF_LEAD + 23 * PF_OFF)) * 4096.0f * __builtin_amdgcn_rcpf((float)(tot * PACE_X16))));
  const unsigned pf_ratio = to_sgpr((int)((float)NTL * 65536.0f * __builtin_amdgcn_rcpf((float)NT)));
  const unsigned pf_per = PF_PER + (g & 7) * PF_PSTEP, pf_inv = (65536 + pf_per - 1) / pf_per;
  auto prefetch = [&](unsigned op) {
    const unsigned d = pf_last - op;
    if (d >= 1024u) return;
    const unsigned r = d - ((d * pf_inv) >> 16) * pf_per;
    if (r >= PF_RUN) return;
    unsigned tl = 2 * NTL - 1 - PF_OFF - (((d - r) * pf_ratio) >> 16) - r;
    if (tl >= 2u * NTL) return;
    const bool isv = tl >= (unsigned)NTL;
    tl -= isv ? NTL : 0;
    const __amdgpu_buffer_rsrc_t rp = __builtin_amdgcn_make_buffer_rsrc(isv ? (void*)vp : (void*)kp, 0, (int)kvbytes, 0x00020000);
    unsigned ln = lane;
    asm volatile("" : "+v"(ln));
    const unsigned vo = (*(LAS unsigned*)(lds + LDS_PLIST + min(2 * tl + (ln >> 5), (unsigned)npl - 1) * 4) & 0x7fff0000u) + ((h & 12) | 1) * (HD * 2) + ((ln >> 1) & 15) * 4096 + (PF_SLOW == 1 ? 128 : PF_SLOW == 2 ? 0 : (ln & 1) * 128);
    asm volatile("s_mov_b32 m0, %0\n\ts_nop 4\n\tbuffer_load_dword %2, %1, 0 offen " PF_POL " lds" PF_2ND :: "s"(lbase + LDS_DUMMY), "s"(rp), "v"(vo) : "memory");
  };
  const int pos = (w + NC * 22 - NT) % NC;
  const unsigned tch_rate = to_sgpr((int)((float)nchunk * 65536.0f * __builtin_amdgcn_rcpf((float)NT)));
  auto touch_at = [&](unsigned op) __attribute__((always_inline)) {
    if (TMODE == 0) {
      const unsigned c1 = ((op + 1) * tch_rate) >> 16, c0 = (op * tch_rate) >> 16;
      if (c1 != c0 && c0 + TAHEAD < 2 * nchunk) touch(c0 + TAHEAD, 1);
    } else {
      if (op == TK2 && TAHEAD < nchunk) touch(TAHEAD, 64);
      if (op == TV) touch(nchunk, 64);
    }
  };
  const unsigned palp = LDS_WREG + pos * W_SIZE + W_ALP + r32 * 4;
  const unsigned ppst = LDS_WREG + pos * W_SIZE + lane * 16;
  asm volatile("" ::: "v128", "v255");
  unsigned resc = 0;
  float m = -3.0e38f, mprev = -3.0e38f, l = 0.f;

  {
    const unsigned fk = (((unsigned)r32 & 3) << 2) | (((unsigned)r32 >> 2) & 3);
    const unsigned kaddr0 = LDS_RING + r32 * 256 + (((unsigned)hi ^ fk) << 4);
    bf16x8 qf[8];
    wait_above<1>(aland + (CTL_QOK - CTL_LAND), 0);
#pragma unroll
    for (int s = 0; s < 8; ++s) qf[s] = *(LAS bf16x8*)(lds + ((kaddr0 + LDS_Q - LDS_RING) ^ (s << 5)));
#pragma unroll 1
    for (int k = 0; k < nmine; ++k) {
      const unsigned t = w + NC * k;
      if (PRESLEEP && k > 0) __builtin_amdgcn_s_sleep(PRESLEEP);
      wait_above<CSLEEP>(aland, 4 * t);
      if (k == 0) STAMP(1);
      bf16x8 kf[8];
#pragma unroll
      for (int s = 0; s < 8; ++s) kf[s] = *(LAS bf16x8*)(lds + ((kaddr0 + ((t & 7) << 13)) ^ (s << 5)));
      asm volatile("s_waitcnt lgkmcnt(0)" : "+v"(kf[0]), "+v"(kf[1]), "+v"(kf[2]), "+v"(kf[3]), "+v"(kf[4]), "+v"(kf[5]), "+v"(kf[6]), "+v"(kf[7]) :: "memory");
      ctl[CTL_FREE / 4 + (t & 7)] = t + 8;
      if (pf_on) prefetch(t);
      if (toucher) touch_at(t);
      f32x16 sc;
#pragma unroll
      for (int r = 0; r < 16; ++r) sc[r] = 0.f;
#pragma unroll
      for (int s = 0; s < 8; ++s) sc = __builtin_amdgcn_mfma_f32_32x32x16_bf16(kf[s], qf[s], sc, 0, 0, 0);
      if ((int)t == NT - 1) {
        const int kb = 2 * t * PAGE + 4 * hi;
#pragma unroll
        for (int r = 0; r < 16; ++r) sc[r] = (kb + 8 * (r >> 2) + (r & 3) < lim) ? sc[r] : -__builtin_inff();
      }
      float mx = sc[0];
#pragma unroll
      for (int r = 1; r < 16; ++r) mx = fmaxf(mx, sc[r]);
      mx = swap_max(mx);
      const float mn = fmaxf(m, mx);
      const float alpha = __builtin_amdgcn_exp2f((m - mn) * CS);
      mprev = m;
      m = mn;
      const float nmc = -mn * CS;
      float ps = 0.f;
#pragma unroll
      for (int r = 0; r < 16; ++r) { sc[r] = __builtin_amdgcn_exp2f(fmaf(sc[r], CS, nmc)); ps += sc[r]; }
      l = fmaf(l, alpha, ps);
      u32x4 pf[2];
#pragma unroll
      for (int s = 0; s < 2; ++s)
        pf[s] = u32x4{pack2(sc[8 * s], sc[8 * s + 1]), pack2(sc[8 * s + 2], sc[8 * s + 3]), pack2(sc[8 * s + 4], sc[8 * s + 5]), pack2(sc[8 * s + 6], sc[8 * s + 7])};
      if (__builtin_amdgcn_ballot_w64(alpha != 1.0f)) resc |= 1u << k;
      *(LAS float*)(lds + palp + k * 128) = alpha;
      if (k >= KREG || k == nmine - 1) {
        const unsigned dst = ppst + ((k == nmine - 1) ? W_LAST : (k - KREG) * 2048);
        *(LAS u32x4*)(lds + dst) = pf[0];
        *(LAS u32x4*)(lds + dst + 1024) = pf[1];
      } else {
        asm volatile("s_set_gpr_idx_on %8, gpr_idx(DST)\n\t"
                     "v_mov_b32 v128, %0\n\tv_mov_b32 v129, %1\n\tv_mov_b32 v130, %2\n\tv_mov_b32 v131, %3\n\t"
                     "v_mov_b32 v132, %4\n\tv_mov_b32 v133, %5\n\tv_mov_b32 v134, %6\n\tv_mov_b32 v135, %7\n\t"
                     "s_set_gpr_idx_off"
                     :: "v"(pf[0][0]), "v"(pf[0][1]), "v"(pf[0][2]), "v"(pf[0][3]), "v"(pf[1][0]), "v"(pf[1][1]), "v"(pf[1][2]), "v"(pf[1][3]), "s"(8 * k));
      }
    }
  }
  l = swap_sum(l);
  *(LAS f32x2*)(lds + LDS_ML + w * 256 + r32 * 8) = f32x2{m, l};
  LGKM0();
  ctl[CTL_MLOK / 4 + w] = 1;
  STAMP(2);

  const unsigned tq = (lane & 15) >> 2, tp = lane & 3, tblk = (lane >> 4) & 1;
  const unsigned vaddr0 = LDS_RING + (4 * hi + tq) * 256 + ((tq ^ (pos & 3)) << 6) + ((2 * tblk + (tp >> 1)) << 4) + ((tp & 1) << 3);
  f32x16 o[4];
#pragma unroll
  for (int i = 0; i < 4; ++i)
#pragma unroll
    for (int r = 0; r < 16; ++r) o[i][r] = 0.f;
  auto weights = [&]() {
    wait_above<1>(lbase + LDS_CTL + CTL_MLOK + min(lane, NC - 1) * 4, 0);
    unsigned a = LDS_ML + r32 * 8;
    asm volatile("" : "+v"(a) :: "memory");
    float M = -3.0e38f, L = 0.f;
    f32x2 ml[NC];
#pragma unroll
    for (int i = 0; i < NC; ++i) { ml[i] = *(LAS f32x2*)(lds + a + i * 256); M = fmaxf(M, ml[i][0]); }
#pragma unroll
    for (int i = 0; i < NC; ++i) L = fmaf(__builtin_amdgcn_exp2f((ml[i][0] - M) * CS), ml[i][1], L);
    const float inv = 1.0f / L;
    *(LAS float*)(lds + (a >> 1) + (LDS_WT - LDS_ML / 2) + pos * 128) = __builtin_amdgcn_exp2f((mprev - M) * CS) * inv;
    const float ct = __builtin_amdgcn_exp2f((m - M) * CS) * inv;
#pragma unroll
    for (int s = 0; s < 2; ++s) {
      u32x4 x = *(LAS u32x4*)(lds + ppst + W_LAST + s * 1024);
#pragma unroll
      for (int e = 0; e < 4; ++e) x[e] = pack2(__uint_as_float(x[e] << 16) * ct, __uint_as_float(x[e] & 0xffff0000u) * ct);
      *(LAS u32x4*)(lds + ppst + W_LAST + s * 1024) = x;
    }
    LGKM0();
    ctl[CTL_WTOK / 4 + w] = 1;
  };
  resc &= ~1u;
  if (nmine <= 2) weights();
#pragma unroll 1
  for (int k = 0; k < nmine - 1; ++k) {
    if (k == 1) weights();
    if ((resc >> k) & 1) {
      const float alpha = *(LAS float*)(lds + palp + k * 128);
#pragma unroll
      for (int i = 0; i < 4; ++i)
#pragma unroll
        for (int r = 0; r < 16; ++r) o[i][r] *= alpha;
    }
    const unsigned u = NT + w + NC * k;
    u32x4 pf[2];
    if (k >= KREG) {
      pf[0] = *(LAS u32x4*)(lds + ppst + (k - KREG) * 2048);
      pf[1] = *(LAS u32x4*)(lds + ppst + (k - KREG) * 2048 + 1024);
    } else {
      asm volatile("s_set_gpr_idx_on %8, gpr_idx(SRC0)\n\t"
                   "v_mov_b32 %0, v128\n\tv_mov_b32 %1, v129\n\tv_mov_b32 %2, v130\n\tv_mov_b32 %3, v131\n\t"
                   "v_mov_b32 %4, v132\n\tv_mov_b32 %5, v133\n\tv_mov_b32 %6, v134\n\tv_mov_b32 %7, v135\n\t"
                   "s_set_gpr_idx_off\n\ts_nop 1"
                   : "=v"(pf[0][0]), "=v"(pf[0][1]), "=v"(pf[0][2]), "=v"(pf[0][3]), "=v"(pf[1][0]), "=v"(pf[1][1]), "=v"(pf[1][2]), "=v"(pf[1][3])
                   : "s"(8 * k));
    }
    asm volatile("" : "+v"(pf[0]), "+v"(pf[1]), "+v"(o[0]), "+v"(o[1]), "+v"(o[2]), "+v"(o[3]));
    if (k == nmine - 2) wait_above<1>(lbase + LDS_CTL + CTL_WTOK + min(lane, NC - 1) * 4, 0);
    wait_above<CSLEEP>(aland, 4 * u);
    if (TPRIO && k == nmine - 2) __builtin_amdgcn_s_setprio(TPRIO);
    bf16x8 vf[4][2];
#pragma unroll
    for (int i = 0; i < 4; ++i) {
      const unsigned a = (vaddr0 + ((u & 7) << 13)) ^ (i << 6);
#pragma unroll
      for (int s = 0; s < 2; ++s) {
        const s16x4 lo = __builtin_amdgcn_ds_read_tr16_b64_v4i16((LAS s16x4*)(lds + a + 4096 * s));
        const s16x4 up = __builtin_amdgcn_ds_read_tr16_b64_v4i16((LAS s16x4*)(lds + a + 4096 * s + 2048));
        vf[i][s] = __builtin_bit_cast(bf16x8, __builtin_shufflevector(lo, up, 0, 1, 2, 3, 4, 5, 6, 7));
      }
    }
    asm volatile("s_waitcnt lgkmcnt(0)" : "+v"(vf[0][0]), "+v"(vf[0][1]), "+v"(vf[1][0]), "+v"(vf[1][1]), "+v"(vf[2][0]), "+v"(vf[2][1]), "+v"(vf[3][0]), "+v"(vf[3][1]) :: "memory");
    ctl[CTL_FREE / 4 + (u & 7)] = u + 8;
    if (pf_on) prefetch(u);
    if (toucher) touch_at(u);
#pragma unroll
    for (int s = 0; s < 2; ++s)
#pragma unroll
      for (int i = 0; i < 4; ++i) o[i] = __builtin_amdgcn_mfma_f32_32x32x16_bf16(vf[i][s], __builtin_bit_cast(bf16x8, pf[s]), o[i], 0, 0, 0);
  }
  STAMP(3);

  {
    unsigned l16 = lane * 16;
    asm volatile("" : "+v"(l16));
    const unsigned l4 = l16 >> 2, awt = LDS_WT + (l4 & 127);
    auto put = [&](unsigned dst, const f32x16& x) {
#pragma unroll
      for (int j = 0; j < 4; ++j) *(LAS f32x4*)(lds + dst + j * 1024) = f32x4{x[4 * j], x[4 * j + 1], x[4 * j + 2], x[4 * j + 3]};
    };
    float co[6];
    bf16x8 pl[NC][2];
    auto ld_p = [&](int p) {
      pl[p][0] = *(LAS bf16x8*)(lds + LDS_WREG + p * W_SIZE + W_LAST + l16);
      pl[p][1] = *(LAS bf16x8*)(lds + LDS_WREG + p * W_SIZE + W_LAST + l16 + 1024);
    };
#pragma unroll
    for (int i = 0; i < 4; ++i) co[i] = *(LAS float*)(lds + awt + ((pos & 3) ^ i) * 128);
    co[4] = *(LAS float*)(lds + awt + 4 * 128);
    co[5] = *(LAS float*)(lds + awt + 5 * 128);
    ld_p(0); ld_p(1);
    asm volatile("" : "+v"(l16) :: "memory");
    if (nmine > 1) {
      if (pos > 3) put(LDS_RING + ((2 * NT - 2 * NC + pos) & 7) * 8192 + l16, o[0]);
#pragma unroll
      for (int i = 1; i < 4; ++i) put(LDS_WREG + pos * W_SIZE + (i - 1) * 4096 + l16, o[i]);
      LGKM0();
      ctl[CTL_ODONE / 4 + pos] = 1;
    }
    if (pos < 4) {
      f32x16 of = o[0], ot;
#pragma unroll
      for (int r = 0; r < 16; ++r) ot[r] = 0.f;
      auto src_o = [&](int p) -> unsigned {
        const int i = pos ^ (p & 3);
        return l16 + (i == 0 ? LDS_RING + ((2 * NT - 2 * NC + p) & 7) * 8192 : LDS_WREG + p * W_SIZE + (i - 1) * 4096);
      };
      auto fma_o = [&](unsigned src, float c) {
#pragma unroll
        for (int j = 0; j < 4; ++j) {
          const f32x4 x = *(LAS f32x4*)(lds + src + j * 1024);
#pragma unroll
          for (int e = 0; e < 4; ++e) of[4 * j + e] = fmaf(x[e], c, of[4 * j + e]);
        }
      };
      auto mma_t = [&](int p, bf16x8 p0, bf16x8 p1) {
        const unsigned a = vaddr0 + (((2 * NT - NC + p) & 7) << 13);
        bf16x8 vf[2];
#pragma unroll
        for (int s = 0; s < 2; ++s) {
          const s16x4 lo = __builtin_amdgcn_ds_read_tr16_b64_v4i16((LAS s16x4*)(lds + a + 4096 * s));
          const s16x4 up = __builtin_amdgcn_ds_read_tr16_b64_v4i16((LAS s16x4*)(lds + a + 4096 * s + 2048));
          vf[s] = __builtin_bit_cast(bf16x8, __builtin_shufflevector(lo, up, 0, 1, 2, 3, 4, 5, 6, 7));
        }
        ot = __builtin_amdgcn_mfma_f32_32x32x16_bf16(vf[0], p0, ot, 0, 0, 0);
        ot = __builtin_amdgcn_mfma_f32_32x32x16_bf16(vf[1], p1, ot, 0, 0, 0);
      };
      if (NT >= 2 * NC) {
#define PIN_OF() asm volatile("" : "+v"(of), "+v"(ot))
#pragma unroll
        for (int r = 0; r < 16; ++r) of[r] *= co[0];
        const bool wl = (l4 & 24) == 0;
        const unsigned ua = 4 * (2 * NT - NC);
        wait_above_v(wl ? aland : lbase + LDS_CTL + CTL_ODONE + (l4 & 12), wl ? ua : 0u);
        mma_t(0, pl[0][0], pl[0][1]);
#pragma unroll
        for (int i = 1; i < 4; ++i) fma_o(LDS_WREG + (pos ^ i) * W_SIZE + (i - 1) * 4096 + l16, co[i]);
        PIN_OF();
        STAMP(4);
        ld_p(2); ld_p(3); ld_p(4); ld_p(5);
        wait_above_v(wl ? aland : lbase + LDS_CTL + CTL_ODONE + 16 + (l4 & 4), wl ? ua + 8 : 0u);
        mma_t(1, pl[1][0], pl[1][1]);
        mma_t(2, pl[2][0], pl[2][1]);
        fma_o(src_o(4), co[4]);
        fma_o(src_o(5), co[5]);
#pragma unroll
        for (int r = 0; r < 16; ++r) { ot[r] += of[r]; of[r] = 0.f; }
        asm volatile("" : "+v"(ot));
        wait_above<0>(aland, ua + 20);
        mma_t(3, pl[3][0], pl[3][1]);
        mma_t(4, pl[4][0], pl[4][1]);
        mma_t(5, pl[5][0], pl[5][1]);
        of = ot;
      } else {
        wait_above<1>(lbase + LDS_CTL + CTL_WTOK + min(l4, (NC - 1) * 4u), 0);
        const float cw = *(LAS float*)(lds + awt + pos * 128);
#pragma unroll
        for (int r = 0; r < 16; ++r) of[r] *= cw;
#pragma unroll 1
        for (int p = max(NC - NT, 0); p < NC; ++p) {
          if (p != pos && NT - 2 * NC + p >= 0) {
            wait_above<1>(lbase + LDS_CTL + CTL_ODONE + p * 4, 0);
            fma_o(src_o(p), *(LAS float*)(lds + awt + p * 128));
          }
          wait_above<1>(aland, 4 * (2 * NT - NC + p));
          mma_t(p, *(LAS bf16x8*)(lds + LDS_WREG + p * W_SIZE + W_LAST + l16), *(LAS bf16x8*)(lds + LDS_WREG + p * W_SIZE + W_LAST + l16 + 1024));
        }
#pragma unroll
        for (int r = 0; r < 16; ++r) of[r] += ot[r];
      }
      const __amdgpu_buffer_rsrc_t ro = __builtin_amdgcn_make_buffer_rsrc(outp, 0, NB * HQ * SQ * HD * 2, 0x00020000);
      const unsigned orow = (unsigned)(b * HQ + 2 * h) * SQ * HD * 2 + pos * 64 + ((l16 & 511) << 4) + ((l16 >> 5) & 16);
#pragma unroll
      for (int j = 0; j < 2; ++j) {
        const unsigned a0 = pack2(of[8 * j], of[8 * j + 1]), a1 = pack2(of[8 * j + 2], of[8 * j + 3]);
        const unsigned b0 = pack2(of[8 * j + 4], of[8 * j + 5]), b1 = pack2(of[8 * j + 6], of[8 * j + 7]);
        auto s0 = __builtin_amdgcn_permlane32_swap(a0, b0, false, false);
        auto s1 = __builtin_amdgcn_permlane32_swap(a1, b1, false, false);
        u32x4 val = {s0[0], s1[0], s0[1], s1[1]};
        __builtin_amdgcn_raw_buffer_store_b128(val, ro, orow + j * 32, 0, OUTAUX);
      }
    }
  }
}
}

extern "C" void kernel_launch(void* const* d_in, const int* in_sizes, int n_in, void* d_out, int out_size, void* d_ws,
                              size_t ws_size, hipStream_t stream) {
  (void)n_in; (void)out_size; (void)ws_size;
  const unsigned nphys = (unsigned)(in_sizes[1] / (PAGE * HKV * HD));
  const unsigned kvbytes = nphys * PAGE_BYTES;
  (void)hipFuncSetAttribute((const void*)paged_attn, hipFuncAttributeMaxDynamicSharedMemorySize, LDS_TOTAL);
  hipLaunchKernelGGL(paged_attn, dim3(256), dim3(512), LDS_TOTAL, stream, d_in[0], d_in[1], d_in[2], (const int*)d_in[3],
                     (const int*)d_in[4], d_out, (char*)d_ws, kvbytes, nphys);
}
```

```cpp
#define PACE_X16 436
#define SOFT 15
#include <hip/hip_runtime.h>
#include <hip/hip_bf16.h>

#define PROBE_G 0
#define QPOL "nt"
#define PF_2ND
#define TLBW 5
#define TLBG 0x200000u
#define SORTP 1
#define TORDER 0
#define PF_OFF 6
#define CODEPF 16640
#define TMODE 1
#define TK2 0
#define TV 16
#define TAHEAD 2
#define PF_BY 0
#define PF_END 2u
#define PF_X64B 72
#define PF_DENSE 1
#define PF_PER 4
#define PF_PSTEP 2
#define GAP_TICKS 0
#define OUTAUX 0
#define HOFF 4
#define LSLEEP 0
#define PRESLEEP 0
#define PF_EVERY 2
#define PF_X64 76
#define PF_ALL 0
#define PF_LEAD 300
#define PF_POL ""
#define PF_SLOW 0
#define PF_RUN 3
#define TSLEEP 0
#define TWAIT 1
#define TPRIO 0
#define CSLEEP 1

#define STAMP(i) do {} while (0)

namespace {
constexpr int NB = 16, HQ = 32, HKV = 16, SQ = 16, HD = 128, PAGE = 16, NBPS = 256, SKV = NBPS * PAGE;
constexpr unsigned PAGE_BYTES = PAGE * HKV * HD * 2;
constexpr unsigned INVALID = 0x80000000u;
constexpr int U0 = 4;
constexpr int NC = 6;
constexpr int KREG = 16, KLDS = 5;
static_assert(NC * (KREG + KLDS + 1) >= NBPS / 2, "P storage");
constexpr unsigned LDS_RING = 0;
constexpr unsigned LDS_WREG = 65536;
constexpr unsigned W_ALP = KLDS * 2048;
constexpr unsigned W_LAST = W_ALP + (KREG + KLDS + 1) * 128;
constexpr unsigned W_SIZE = W_LAST + 2048;
static_assert(W_LAST >= 3 * 4096 && W_SIZE % 256 == 0, "consumer region");
constexpr unsigned LDS_TBL = LDS_WREG + W_SIZE;
constexpr unsigned LDS_SRT = LDS_WREG + 3 * W_SIZE;
constexpr unsigned LDS_Q = LDS_WREG;
constexpr unsigned LDS_ML = LDS_WREG + NC * W_SIZE;
constexpr unsigned LDS_WT = LDS_ML + NC * 256;
constexpr unsigned LDS_CTL = LDS_WT + NC * 128;
constexpr unsigned CTL_LAND = 0;
constexpr unsigned CTL_QOK = 16;
constexpr unsigned CTL_SORT = 24;
constexpr unsigned CTL_FREE = 32;
constexpr unsigned CTL_MLOK = 64;
constexpr unsigned CTL_ODONE = 96;
constexpr unsigned CTL_WTOK = 128;
constexpr unsigned LDS_DUMMY = LDS_CTL + 256;
constexpr unsigned LDS_PLIST = LDS_DUMMY + 256;
constexpr unsigned LDS_TOTAL = LDS_PLIST + 1024;
static_assert(LDS_TOTAL <= 163840, "lds");

typedef __attribute__((ext_vector_type(8))) __bf16 bf16x8;
typedef __attribute__((ext_vector_type(2))) __bf16 bf16x2;
typedef __attribute__((ext_vector_type(4))) short s16x4;
typedef __attribute__((ext_vector_type(16))) float f32x16;
typedef __attribute__((ext_vector_type(4))) float f32x4;
typedef __attribute__((ext_vector_type(2))) float f32x2;
typedef __attribute__((ext_vector_type(4))) unsigned u32x4;
typedef __attribute__((ext_vector_type(2))) unsigned u32x2;
typedef __attribute__((ext_vector_type(4))) int i32x4;
#define LAS __attribute__((address_space(3)))

__device__ __forceinline__ float swap_max(float x) {
  auto r = __builtin_amdgcn_permlane32_swap(__float_as_uint(x), __float_as_uint(x), false, false);
  return fmaxf(__uint_as_float(r[0]), __uint_as_float(r[1]));
}
__device__ __forceinline__ float swap_sum(float x) {
  auto r = __builtin_amdgcn_permlane32_swap(__float_as_uint(x), __float_as_uint(x), false, false);
  return __uint_as_float(r[0]) + __uint_as_float(r[1]);
}
__device__ __forceinline__ unsigned pack2(float a, float b) {
  bf16x2 v = {(__bf16)a, (__bf16)b};
  return __builtin_bit_cast(unsigned, v);
}
template <int CTRL> __device__ __forceinline__ int dpp_add(int x) { return x + __builtin_amdgcn_update_dpp(0, x, CTRL, 0xf, 0xf, true); }
__device__ __forceinline__ unsigned now32() { return (unsigned)__builtin_amdgcn_s_memrealtime(); }
__device__ __forceinline__ int to_sgpr(int v) {
  int r;
  asm volatile("s_nop 1\n\tv_readfirstlane_b32 %0, %1" : "=s"(r) : "v"(v));
  return r;
}
#define LGKM0() asm volatile("s_waitcnt lgkmcnt(0)" ::: "memory")
#define VMWAIT(n) asm volatile("s_waitcnt vmcnt(" #n ")" ::: "memory")
#define CBAR() asm volatile("" ::: "memory")
template <int SLEEP> __device__ __forceinline__ void wait_above(unsigned addr, unsigned u) {
  unsigned v, cnt;
  if (SLEEP)
    asm volatile("s_mov_b32 %1, 0x200000\n\ts_branch 2f\n"
                 "1:\n\ts_sleep %4\n"
                 "2:\n\tds_read_b32 %0, %2\n\ts_sub_u32 %1, %1, 1\n\ts_waitcnt lgkmcnt(0)\n\t"
                 "v_cmp_ge_u32 vcc, %3, %0\n\ts_cbranch_scc1 3f\n\ts_nop 1\n\ts_cbranch_vccnz 1b\n3:"
                 : "=&v"(v), "=&s"(cnt) : "v"(addr), "s"(u), "n"(SLEEP) : "memory", "scc", "vcc");
  else
    asm volatile("s_mov_b32 %1, 0x200000\n"
                 "1:\n\tds_read_b32 %0, %2\n\ts_sub_u32 %1, %1, 1\n\ts_waitcnt lgkmcnt(0)\n\t"
                 "v_cmp_ge_u32 vcc, %3, %0\n\ts_cbranch_scc1 3f\n\ts_nop 1\n\ts_cbranch_vccnz 1b\n3:"
                 : "=&v"(v), "=&s"(cnt) : "v"(addr), "s"(u) : "memory", "scc", "vcc");
}

__device__ __forceinline__ void wait_above_v(unsigned addr, unsigned u) {
  unsigned v, cnt;
  asm volatile("s_mov_b32 %1, 0x200000\n"
               "1:\n\tds_read_b32 %0, %2\n\ts_sub_u32 %1, %1, 1\n\ts_waitcnt lgkmcnt(0)\n\t"
               "v_cmp_ge_u32 vcc, %3, %0\n\ts_cbranch_scc1 3f\n\ts_nop 1\n\ts_cbranch_vccnz 1b\n3:"
               : "=&v"(v), "=&s"(cnt) : "v"(addr), "v"(u) : "memory", "scc", "vcc");
}

__global__ void __launch_bounds__(512, 1) __attribute__((amdgpu_num_vgpr(64)))
paged_attn(const void* __restrict__ qp, const void* __restrict__ kp, const void* __restrict__ vp, const int* __restrict__ bt,
           const int* __restrict__ lens, void* __restrict__ outp, char* __restrict__ ws, unsigned kvbytes, unsigned nphys) {
  extern __shared__ __attribute__((aligned(16))) char smem[];
  LAS char* lds = (LAS char*)smem;
  const int lane0 = threadIdx.x & 63;
  const int wv = __builtin_amdgcn_readfirstlane(threadIdx.x >> 6);
  const int g = blockIdx.x;
  const int h = g >> 4;
  constexpr float CS = 0.08838834764831845f * 1.4426950408889634f;
  volatile LAS unsigned* ctl = (volatile LAS unsigned*)(lds + LDS_CTL);

  const unsigned lbase = (unsigned)(size_t)lds;
  const bool toucher = TLBW && (g & 8) != 0;
  const unsigned nchunk = (kvbytes + 16 * TLBG - 1) / (16 * TLBG);

  unsigned lraw, first;
  u32x4 lq;
  {
    asm volatile("global_load_dword %0, %3, %4\n\t"
                 "global_load_dwordx4 %1, %5, %4\n\t"
                 "global_load_dword %2, %6, %7"
                 : "=&v"(lraw), "=&v"(lq), "=&v"(first)
                 : "v"((lane0 & 15) * 4), "s"(lens), "v"(lane0 & 48), "v"((lane0 & 15) * (wv < 2 ? NBPS * 4 : 0) + (lane0 >> 4) * 8 + (wv & 1) * 4), "s"(bt),
                   "s"(qp), "s"(kp), "s"(vp), "s"(outp), "s"(kvbytes), "s"(nphys)
                 : "memory");
    __builtin_amdgcn_sched_barrier(0);
  }
  if (wv >= 4) {
    if (wv == 7) {
      const __amdgpu_buffer_rsrc_t rb = __builtin_amdgcn_make_buffer_rsrc((void*)bt, 0, NB * NBPS * 4, 0x00020000);
#define TBL4(SO) "buffer_load_dwordx4 %2, %1, " SO " offen lds\n\tbuffer_load_dwordx4 %2, %1, " SO " offen offset:1024 lds\n\t" \
                 "buffer_load_dwordx4 %2, %1, " SO " offen offset:2048 lds\n\tbuffer_load_dwordx4 %2, %1, " SO " offen offset:3072 lds\n\t"
      asm volatile("s_mov_b32 m0, %0\n\ts_nop 0\n\t" TBL4("0") "s_add_u32 m0, m0, 4096\n\ts_nop 0\n\t" TBL4("%3")
                   "s_add_u32 m0, m0, %6\n\ts_nop 0\n\t" TBL4("%4") "s_add_u32 m0, m0, 4096\n\ts_nop 0\n\t" TBL4("%5")
                   :: "s"(lbase + LDS_TBL), "s"(rb), "v"(lane0 * 16), "s"(4096), "s"(8192), "s"(12288), "n"(W_SIZE - 4096) : "memory", "scc");
    }
    unsigned long long pc;
    asm volatile("s_getpc_b64 %0" : "=s"(pc));
    const __amdgpu_buffer_rsrc_t rt = __builtin_amdgcn_make_buffer_rsrc(wv == 5 ? (void*)pc : (void*)kp, 0,
                                                                        wv == 5 ? CODEPF : (wv == 6 && toucher) ? (int)min(kvbytes, TAHEAD * 16 * TLBG) : 0, 0x00020000);
    const unsigned mul = wv == 5 ? 512u : 16 * TLBG + 4224u, add = wv == 5 ? ((g >> 3) & 3) * 128 : (g >> 4) * (TLBG + 33024u);
    asm volatile("s_mov_b32 m0, %0\n\ts_nop 0\n\tbuffer_load_dword %1, %2, 0 offen lds"
                 :: "s"(lbase + LDS_DUMMY), "v"((wv == 5 ? lane0 : lane0 & 31) * mul + add), "s"(rt) : "memory");
    *(LAS u32x4*)(lds + LDS_SRT + (threadIdx.x - 256) * 16) = u32x4{0u, 0u, 0u, 0u};
    if (threadIdx.x < 296) ctl[lane0] = ((lane0 >> 3) == CTL_FREE / 32) ? (lane0 & 7) : 0u;
    asm volatile("s_waitcnt lgkmcnt(0)\n\ts_barrier" ::: "memory");
  }
  if (wv < 2) __builtin_amdgcn_s_setprio(3);
  const unsigned rstride = wv < 2 ? 4096 : 256;
  unsigned vo0 = (lane0 >> 4) * rstride + (((unsigned)(lane0 & 15) ^ ((lane0 >> 4) << 2)) << 4) + (wv < 2 ? h * (HD * 2) : 0);
  unsigned vo1 = (vo0 + 4 * rstride - 1024) ^ 16, vo2 = (vo0 + 8 * rstride - 2048) ^ 32, vo3 = (vo0 + 12 * rstride - 3072) ^ 48;
  const unsigned want = (g & 8) ? 15 - (g & 7) : (g & 7);
  const unsigned dst0 = lbase + (wv < 2 ? LDS_RING : LDS_Q) + (wv & 1) * 4096;
  asm volatile("s_cmp_ge_u32 %8, 4\n\ts_cbranch_scc1 1f\n\ts_waitcnt vmcnt(0)\n1:\n\ts_waitcnt vmcnt(1)" : "+v"(lraw), "+v"(lq), "+v"(first), "+v"(vo0), "+v"(vo1), "+v"(vo2), "+v"(vo3) : "s"(dst0), "s"(wv) : "memory", "scc");
  unsigned lm1 = min(lraw - 1u, (unsigned)SKV - 1u);
  unsigned rank = 0;
  {
    const unsigned key = lm1 * 16 + (lane0 & 15);
#pragma unroll
    for (int j = 0; j < 4; ++j) rank += (min(lq[j] - 1u, (unsigned)SKV - 1u) * 16 + ((lane0 >> 4) * 4 + j) > key) ? 1u : 0u;
    auto r1 = __builtin_amdgcn_permlane16_swap(rank, rank, false, false);
    rank = r1[0] + r1[1];
    auto r2 = __builtin_amdgcn_permlane32_swap(rank, rank, false, false);
    rank = r2[0] + r2[1];
  }
  first = min(first, nphys - 1) * PAGE_BYTES;
  const int b = __builtin_ctz((unsigned)__builtin_amdgcn_ballot_w64(rank == want) | 0x8000u);
  unsigned long long tstart = 0;
  if (wv < 4) {
    const bool qok = (int)__builtin_amdgcn_readlane(lraw, b) > 0;
    const __amdgpu_buffer_rsrc_t r0 = __builtin_amdgcn_make_buffer_rsrc(wv < 2 ? (void*)kp : (void*)qp, 0,
                                                                        wv < 2 ? (int)kvbytes : qok ? NB * HQ * SQ * HD * 2 : 0, 0x00020000);
    const unsigned so = wv < 2 ? __builtin_amdgcn_readlane(first, b) : qok ? (unsigned)((b * HQ + 2 * h) * SQ * HD * 2) + (wv & 1) * 4096 : 0u;
    asm volatile("s_waitcnt vmcnt(0)\n\t"
                 "s_mov_b32 m0, %2\n\t"
                 "s_memrealtime %0\n\t"
                 "s_nop 2\n\t"
                 "buffer_load_dwordx4 %4, %1, %3 offen nt lds\n\t"
                 "buffer_load_dwordx4 %5, %1, %3 offen offset:1024 nt lds\n\t"
                 "buffer_load_dwordx4 %6, %1, %3 offen offset:2048 nt lds\n\t"
                 "buffer_load_dwordx4 %7, %1, %3 offen offset:3072 nt lds\n\t"
                 "s_waitcnt lgkmcnt(0)\n\t"
                 "s_barrier"
                 : "=&s"(tstart) : "s"(r0), "s"(dst0), "s"(so), "v"(vo0), "v"(vo1), "v"(vo2), "v"(vo3) : "memory");
  }
  asm volatile("" : "+v"(lm1), "+v"(rank), "+v"(first) :: "memory");
  __builtin_amdgcn_sched_barrier(0);
  const int bp = __builtin_ctz((unsigned)__builtin_amdgcn_ballot_w64(rank == 15 - want) | 0x8000u);
  const int len = __builtin_amdgcn_readlane(lm1, b) + 1, nown = (len + PAGE - 1) / PAGE, NT = (nown + 1) >> 1;
  const int npl = (__builtin_amdgcn_readlane(lm1, bp) >> 4) + 1, NTL = (npl + 1) >> 1;
  auto total = [&]() __attribute__((always_inline)) {
    int t = lm1 + 1;
    t = dpp_add<0x121>(t); t = dpp_add<0x122>(t); t = dpp_add<0x124>(t); t = dpp_add<0x128>(t);
    return t;
  };

  auto sort_pages = [&](int lane, int bb, int n, unsigned scr, unsigned dst) __attribute__((always_inline)) {
    u32x4 e = *(LAS u32x4*)(lds + LDS_TBL + (bb >> 3) * W_SIZE + (bb & 7) * 1024 + lane * 16);
    bool in[4];
#pragma unroll
    for (int j = 0; j < 4; ++j) {
      e[j] = min(e[j], nphys - 1);
      in[j] = 4 * lane + j >= 2 * U0 && 4 * lane + j < n - 1;
      __hip_atomic_fetch_or((LAS unsigned*)(lds + scr + ((e[j] >> 5) & 255) * 8), in[j] ? 1u << (e[j] & 31) : 0u, __ATOMIC_RELAXED, __HIP_MEMORY_SCOPE_WAVEFRONT);
    }
    CBAR();
    u32x4 a = *(LAS u32x4*)(lds + scr + lane * 32), c = *(LAS u32x4*)(lds + scr + lane * 32 + 16);
    const int n0 = __builtin_popcount(a[0]), n1 = n0 + __builtin_popcount(a[2]), n2 = n1 + __builtin_popcount(c[0]), n3 = n2 + __builtin_popcount(c[2]);
    int sc = n3;
    sc = dpp_add<0x111>(sc); sc = dpp_add<0x112>(sc); sc = dpp_add<0x114>(sc); sc = dpp_add<0x118>(sc);
    sc += __builtin_amdgcn_update_dpp(0, sc, 0x142, 0xa, 0xf, false);
    sc += __builtin_amdgcn_update_dpp(0, sc, 0x143, 0xc, 0xf, false);
    const bool ok = SORTP && nphys <= 8192u && __builtin_amdgcn_readlane(sc, 63) == n - 1 - 2 * U0;
    sc += 2 * U0 - n3;
    a[1] = sc; a[3] = sc + n0; c[1] = sc + n1; c[3] = sc + n2;
    *(LAS u32x4*)(lds + scr + lane * 32) = a;
    *(LAS u32x4*)(lds + scr + lane * 32 + 16) = c;
    CBAR();
    u32x2 wp[4];
#pragma unroll
    for (int j = 0; j < 4; ++j) wp[j] = *(LAS u32x2*)(lds + scr + ((e[j] >> 5) & 255) * 8);
#pragma unroll
    for (int j = 0; j < 4; ++j) {
      const unsigned r = wp[j][1] + __builtin_popcount(wp[j][0] & ((1u << (e[j] & 31)) - 1u));
      *(LAS unsigned*)(lds + dst + ((ok && in[j]) ? r : 4 * lane + j) * 4) = e[j] * PAGE_BYTES;
    }
    CBAR();
  };

  if (__builtin_expect(wv < 2, 1)) {
    const int half = wv, lane = lane0;
    const unsigned vland = lbase + LDS_CTL + CTL_LAND + half * 4;
    const unsigned vfree = lbase + LDS_CTL + CTL_FREE + (lane & 7) * 4;
    const unsigned ring = dst0;
    unsigned l0 = __builtin_amdgcn_ds_bpermute((b + 16 * lane) * 4, first), l1 = 0;
    const unsigned a0 = lbase + LDS_SRT + 4096 + min(2 * lane + half, nown - 1) * 4, a1 = lbase + LDS_SRT + 4096 + min(2 * lane + half + 128, nown - 1) * 4;
    const int T16 = total() * PACE_X16;
    const int step = to_sgpr((int)((float)T16 * __builtin_amdgcn_rcpf((float)max(len, 32))));
    const unsigned due0 = ((unsigned)tstart << 8) + half * ((step * HOFF) >> 3);
    const int lrows = len - (2 * (NT - 1) + half) * PAGE;
#define CNT(r)
    unsigned vf, vt, lc;
    asm volatile(
        "s_waitcnt lgkmcnt(0)\n\t"
        "s_memrealtime s[68:69]\n\t"
        "ds_read_b32 %[vf], %[vfree]\n\t"
        "s_mov_b32 s60, 1\n\t"
        "s_mov_b32 s61, 4\n\t"
        "s_mov_b32 s62, 0x400000\n\t"
        "s_mov_b32 s66, 0\n\t"
        "s_min_u32 s67, %[nt], %[u0]\n\t"
        "s_mov_b32 s75, %[nrec]\n\t"
        "s_mov_b32 s72, 0x2000\n\t"
        "s_mov_b32 s73, 1\n\t"
        "s_mov_b32 s74, %[due1]\n\t"
        "s_mov_b64 s[76:77], %[kp]\n\t"
        "s_mov_b32 s78, %[nrec]\n\t"
        "s_mov_b32 s79, 0x20000\n\t"
        "s_and_b32 s77, s77, 0xffff\n\t"
        "v_mov_b32 %[lc], %[l0]\n\t"
        "s_getpc_b64 s[70:71]\n"
        ".LldA%=:\n\t"
        "s_add_u32 s80, s70, .LldL%=-.LldA%=\n\t"
        "s_addc_u32 s71, s71, 0\n\t"
        "s_branch .LldF%=\n"
        ".LldR%=:\n\t"
        "s_sub_u32 s62, s62, 1\n\t"
        "s_cbranch_scc1 .LldE%=\n"
        ".LldT%=:\n\t"
        "s_getreg_b32 s64, hwreg(HW_REG_IB_STS)\n\t"
        "s_and_b32 s65, s64, 15\n\t"
        "s_bfe_u32 s64, s64, 0x20016\n\t"
        "s_lshl4_add_u32 s64, s64, s65\n\t"
        "s_add_u32 s64, s64, 3\n\t"
        "s_and_b32 s64, s64, 60\n\t"
        "s_sub_u32 s65, s61, s64\n\t"
        "s_cmp_lg_u32 s65, s66\n\t"
        "s_cbranch_scc1 .LldP%=\n"
        ".LldC%=:\n\t"
        "s_waitcnt lgkmcnt(0)\n\t"
        "v_cmp_eq_u32 vcc, s60, %[vf]\n\t"
        "s_lshl_b32 s64, s68, 8\n\t"
        "s_memrealtime s[68:69]\n\t"
        "ds_read_b32 %[vf], %[vfree]\n\t"
        CNT("s82")
        "s_sub_u32 s64, s64, s74\n\t"
        "s_cmp_lt_i32 s64, 0\n\t"
        "s_cbranch_scc1 .LldR%=\n\t"
        CNT("s83")
        "s_cbranch_vccz .LldR%=\n\t"
        CNT("s84")
        "buffer_load_dwordx4 %[o0], s[76:79], s63 offen nt lds\n\t"
        "s_add_u32 s74, s74, %[step]\n\t"
        "s_add_u32 s73, s73, 1\n\t"
        "buffer_load_dwordx4 %[o1], s[76:79], s63 offen offset:1024 nt lds\n\t"
        "s_add_u32 s60, s60, 1\n\t"
        "s_add_u32 s61, s61, 4\n\t"
        "buffer_load_dwordx4 %[o2], s[76:79], s63 offen offset:2048 nt lds\n\t"
        "s_add_u32 s72, s72, 0x2000\n\t"
        "s_and_b32 s72, s72, 0xe000\n\t"
        "buffer_load_dwordx4 %[o3], s[76:79], s63 offen offset:3072 nt lds\n"
        ".LldF%=:\n\t"
        "v_readlane_b32 s63, %[lc], s73\n\t"
        "s_add_u32 m0, s72, %[ring]\n\t"
        "s_cmp_eq_u32 s73, %[ntm]\n\t"
        "s_cselect_b32 s78, s75, %[nrec]\n\t"
        "s_cmp_lg_u32 s60, s67\n\t"
        "s_cbranch_scc1 .LldT%=\n\t"
        "s_cmp_lg_u32 s60, %[u0]\n\t"
        "s_cbranch_scc1 .LldG%=\n\t"
        "s_cmp_le_u32 %[nt], %[u0]\n\t"
        "s_cbranch_scc1 .LldG%=\n"
        ".LldS%=:\n\t"
        "ds_read_b32 %[lc], %[vland] offset:%[srt]\n\t"
        "ds_read_b32 %[l0], %[a0]\n\t"
        "ds_read_b32 %[l1], %[a1]\n\t"
        "s_sub_u32 s62, s62, 1\n\t"
        "s_cbranch_scc1 .LldE%=\n\t"
        "s_waitcnt lgkmcnt(0)\n\t"
        "v_readfirstlane_b32 s64, %[lc]\n\t"
        "s_cmp_eq_u32 s64, 0\n\t"
        "s_cbranch_scc1 .LldS%=\n\t"
        "s_max_i32 s65, %[lrows], 0\n\t"
        "s_lshl_b32 s65, s65, 12\n\t"
        "s_add_u32 s64, s64, s65\n\t"
        "s_sub_u32 s64, s64, 1\n\t"
        "s_cmp_lt_i32 %[lrows], 16\n\t"
        "s_cselect_b32 s75, s64, %[nrec]\n\t"
        "s_min_u32 s67, %[nt], 64\n\t"
        "v_mov_b32 %[lc], %[l0]\n\t"
        "s_branch .LldF%=\n"
        ".LldG%=:\n\t"
        "s_cmp_eq_u32 s73, %[nt]\n\t"
        "s_cbranch_scc1 .LldV%=\n\t"
        "v_mov_b32 %[lc], %[l1]\n\t"
        "v_readfirstlane_b32 s63, %[l1]\n\t"
        "s_add_u32 s67, s60, %[nt]\n\t"
        "s_sub_u32 s67, s67, 64\n\t"
        "s_branch .LldT%=\n"
        ".LldV%=:\n\t"
        "s_cmp_lg_u32 s60, %[nt]\n\t"
        "s_cbranch_scc1 .LldD%=\n\t"
        "s_mov_b64 s[76:77], %[vp]\n\t"
        "s_and_b32 s77, s77, 0xffff\n\t"
        "s_mov_b32 s73, 0\n\t"
        "s_min_u32 s67, %[nt], 64\n\t"
        "s_add_u32 s67, s67, s60\n\t"
        "s_mov_b32 s74, %[duev]\n\t"
        "s_cmp_eq_u32 %[ntm], 0\n\t"
        "s_cselect_b32 s78, s75, %[nrec]\n\t"
        "v_mov_b32 %[lc], %[l0]\n\t"
        "v_readfirstlane_b32 s63, %[l0]\n\t"
        "v_xor_b32 %[o1], 16, %[o1]\n\t"
        "v_xor_b32 %[o2], 32, %[o2]\n\t"
        "v_xor_b32 %[o3], 48, %[o3]\n\t"
        "s_branch .LldT%=\n"
        ".LldD%=:\n\t"
        "s_add_u32 s74, s74, 0x40000000\n\t"
        "s_mov_b32 s60, -1\n\t"
        "s_branch .LldT%=\n"
        ".LldP%=:\n\t"
        "s_min_u32 s64, s64, 32\n\t"
        "s_add_u32 s70, s80, s64\n\t"
        "v_mov_b32 %[vt], s65\n\t"
        "s_mov_b32 s66, s65\n\t"
        "s_setpc_b64 s[70:71]\n\t"
        ".p2align 6\n"
        ".LldL%=:\n\t"
        "s_waitcnt vmcnt(0)\n\t"
        "s_waitcnt vmcnt(4)\n\t"
        "s_waitcnt vmcnt(8)\n\t"
        "s_waitcnt vmcnt(12)\n\t"
        "s_waitcnt vmcnt(16)\n\t"
        "s_waitcnt vmcnt(20)\n\t"
        "s_waitcnt vmcnt(24)\n\t"
        "s_waitcnt vmcnt(28)\n\t"
        "s_waitcnt vmcnt(32)\n\t"
        "ds_write_b32 %[vland], %[vt]\n\t"
        "s_cmp_lg_u32 s65, %[fin]\n\t"
        "s_cbranch_scc1 .LldC%=\n"
        ".LldE%=:\n\t"
        "s_waitcnt vmcnt(0) lgkmcnt(0)\n\t"
        : [vf] "=&v"(vf), [vt] "=&v"(vt), [lc] "=&v"(lc), [l0] "+v"(l0), [l1] "+v"(l1), [o1] "+v"(vo1), [o2] "+v"(vo2), [o3] "+v"(vo3)
        : [a0] "v"(a0), [a1] "v"(a1), [o0] "v"(vo0), [vland] "v"(vland), [vfree] "v"(vfree),
          [kp] "s"(kp), [vp] "s"(vp), [nrec] "s"(kvbytes), [nt] "s"(NT), [ring] "s"(ring),
          [step] "s"(step), [due1] "s"(due0 + step + SOFT * 256), [duev] "s"(due0 + (to_sgpr(T16) >> 5) + (SOFT + GAP_TICKS) * 256), [lrows] "s"(lrows), [ntm] "s"(NT - 1), [fin] "s"(8 * NT), [u0] "n"(U0), [srt] "n"(CTL_SORT - CTL_LAND)
        : "memory", "scc", "vcc", "s60", "s61", "s62", "s63", "s64", "s65", "s66", "s67", "s68", "s69", "s70", "s71", "s72", "s73", "s74", "s75",
          "s76", "s77", "s78", "s79", "s80", "s82", "s83", "s84");
    STAMP(3);
    asm volatile("s_endpgm" ::: "memory");
  }

  int lane = lane0;
  asm volatile("" : "+v"(lane));
  auto touch = [&](unsigned c, unsigned n) __attribute__((always_inline)) {
    const bool isv = c >= nchunk;
    const __amdgpu_buffer_rsrc_t rt = __builtin_amdgcn_make_buffer_rsrc(isv ? (void*)vp : (void*)kp, 0, (int)kvbytes, 0x00020000);
    unsigned ln = lane;
    asm volatile("" : "+v"(ln));
    const unsigned idx = (g >> 4) + 16 * (c - (isv ? nchunk : 0) + ln);
    const unsigned vo = (ln < n && idx < 0x80000000u / TLBG) ? idx * TLBG + ((idx * 30592u) & (TLBG - 128)) : INVALID;
    asm volatile("s_mov_b32 m0, %0\n\ts_nop 4\n\tbuffer_load_dword %2, %1, 0 offen nt lds" :: "s"((unsigned)(size_t)lds + LDS_DUMMY), "s"(rt), "v"(vo) : "memory");
  };
  if (wv == 7) {
#pragma unroll 1
    for (int i = 0; i < 2; ++i) {
      sort_pages(lane, i ? bp : b, i ? npl : nown, LDS_SRT + i * 2048, i ? LDS_PLIST : LDS_SRT + 4096);
      if (i == 0) {
        const unsigned lo = *(LAS unsigned*)(lds + LDS_SRT + 4096 + (nown - 1) * 4) + 1;
        LGKM0();
        ctl[CTL_SORT / 4 + (lane & 1)] = lo;
      }
    }
  }
  if (wv < 4) {
    VMWAIT(0);
    ctl[CTL_QOK / 4 + (wv & 1)] = 1;
  }
  const int lim = len;
  const int tot = total();
  const int w = wv - 2;
  const int r32 = lane & 31, hi = lane >> 5;
  const int nmine = (NT > w) ? (NT - w + NC - 1) / NC : 0;
  const unsigned aland = lbase + LDS_CTL + CTL_LAND + (lane & 1) * 4;
  const int tots = __builtin_amdgcn_readfirstlane(tot);
  const bool pf_on = PF_EVERY && npl * 256 * 64 > tots * PF_X64B && npl > nown && (h & 3) == PF_BY;
  const unsigned pf_max = (npl * 256 * 64 > tots * PF_X64) ? 256u : PF_END;
  const unsigned pf_last = 2 * NT - to_sgpr((int)((float)(2 * NT * (PF_LEAD + 23 * PF_OFF)) * 4096.0f * __builtin_amdgcn_rcpf((float)(tot * PACE_X16))));
  const unsigned pf_ratio = to_sgpr((int)((float)NTL * 65536.0f * __builtin_amdgcn_rcpf((float)NT)));
  const unsigned pf_per = PF_PER + (g & 7) * PF_PSTEP, pf_inv = (65536 + pf_per - 1) / pf_per;
  auto prefetch = [&](unsigned op) {
    const unsigned d = pf_last - op;
    if (d >= 1024u) return;
    const unsigned r = d - ((d * pf_inv) >> 16) * pf_per;
    if (r >= PF_RUN) return;
    unsigned tl = 2 * NTL - 1 - PF_OFF - (((d - r) * pf_ratio) >> 16) - r;
    if (tl >= 2u * NTL) return;
    const bool isv = tl >= (unsigned)NTL;
    tl -= isv ? NTL : 0;
    const __amdgpu_buffer_rsrc_t rp = __builtin_amdgcn_make_buffer_rsrc(isv ? (void*)vp : (void*)kp, 0, (int)kvbytes, 0x00020000);
    unsigned ln = lane;
    asm volatile("" : "+v"(ln));
    const unsigned vo = (*(LAS unsigned*)(lds + LDS_PLIST + min(2 * tl + (ln >> 5), (unsigned)npl - 1) * 4) & 0x7fff0000u) + ((h & 12) | 1) * (HD * 2) + ((ln >> 1) & 15) * 4096 + (PF_SLOW == 1 ? 128 : PF_SLOW == 2 ? 0 : (ln & 1) * 128);
    asm volatile("s_mov_b32 m0, %0\n\ts_nop 4\n\tbuffer_load_dword %2, %1, 0 offen " PF_POL " lds" PF_2ND :: "s"(lbase + LDS_DUMMY), "s"(rp), "v"(vo) : "memory");
  };
  const int pos = (w + NC * 22 - NT) % NC;
  const unsigned tch_rate = to_sgpr((int)((float)nchunk * 65536.0f * __builtin_amdgcn_rcpf((float)NT)));
  auto touch_at = [&](unsigned op) __attribute__((always_inline)) {
    if (TMODE == 0) {
      const unsigned c1 = ((op + 1) * tch_rate) >> 16, c0 = (op * tch_rate) >> 16;
      if (c1 != c0 && c0 + TAHEAD < 2 * nchunk) touch(c0 + TAHEAD, 1);
    } else {
      if (op == TK2 && TAHEAD < nchunk) touch(TAHEAD, 64);
      if (op == TV) touch(nchunk, 64);
    }
  };
  const unsigned palp = LDS_WREG + pos * W_SIZE + W_ALP + r32 * 4;
  const unsigned ppst = LDS_WREG + pos * W_SIZE + lane * 16;
  asm volatile("" ::: "v128", "v255");
  unsigned resc = 0;
  float m = -3.0e38f, mprev = -3.0e38f, l = 0.f;

  {
    const unsigned fk = (((unsigned)r32 & 3) << 2) | (((unsigned)r32 >> 2) & 3);
    const unsigned kaddr0 = LDS_RING + r32 * 256 + (((unsigned)hi ^ fk) << 4);
    bf16x8 qf[8];
    wait_above<1>(aland + (CTL_QOK - CTL_LAND), 0);
#pragma unroll
    for (int s = 0; s < 8; ++s) qf[s] = *(LAS bf16x8*)(lds + ((kaddr0 + LDS_Q - LDS_RING) ^ (s << 5)));
#pragma unroll 1
    for (int k = 0; k < nmine; ++k) {
      const unsigned t = w + NC * k;
      if (PRESLEEP && k > 0) __builtin_amdgcn_s_sleep(PRESLEEP);
      wait_above<CSLEEP>(aland, 4 * t);
      if (k == 0) STAMP(1);
      bf16x8 kf[8];
#pragma unroll
      for (int s = 0; s < 8; ++s) kf[s] = *(LAS bf16x8*)(lds + ((kaddr0 + ((t & 7) << 13)) ^ (s << 5)));
      asm volatile("s_waitcnt lgkmcnt(0)" : "+v"(kf[0]), "+v"(kf[1]), "+v"(kf[2]), "+v"(kf[3]), "+v"(kf[4]), "+v"(kf[5]), "+v"(kf[6]), "+v"(kf[7]) :: "memory");
      ctl[CTL_FREE / 4 + (t & 7)] = t + 8;
      if (pf_on) prefetch(t);
      if (toucher) touch_at(t);
      f32x16 sc;
#pragma unroll
      for (int r = 0; r < 16; ++r) sc[r] = 0.f;
#pragma unroll
      for (int s = 0; s < 8; ++s) sc = __builtin_amdgcn_mfma_f32_32x32x16_bf16(kf[s], qf[s], sc, 0, 0, 0);
      if ((int)t == NT - 1) {
        const int kb = 2 * t * PAGE + 4 * hi;
#pragma unroll
        for (int r = 0; r < 16; ++r) sc[r] = (kb + 8 * (r >> 2) + (r & 3) < lim) ? sc[r] : -__builtin_inff();
      }
      float mx = sc[0];
#pragma unroll
      for (int r = 1; r < 16; ++r) mx = fmaxf(mx, sc[r]);
      mx = swap_max(mx);
      const float mn = fmaxf(m, mx);
      const float alpha = __builtin_amdgcn_exp2f((m - mn) * CS);
      mprev = m;
      m = mn;
      const float nmc = -mn * CS;
      float ps = 0.f;
#pragma unroll
      for (int r = 0; r < 16; ++r) { sc[r] = __builtin_amdgcn_exp2f(fmaf(sc[r], CS, nmc)); ps += sc[r]; }
      l = fmaf(l, alpha, ps);
      u32x4 pf[2];
#pragma unroll
      for (int s = 0; s < 2; ++s)
        pf[s] = u32x4{pack2(sc[8 * s], sc[8 * s + 1]), pack2(sc[8 * s + 2], sc[8 * s + 3]), pack2(sc[8 * s + 4], sc[8 * s + 5]), pack2(sc[8 * s + 6], sc[8 * s + 7])};
      if (__builtin_amdgcn_ballot_w64(alpha != 1.0f)) resc |= 1u << k;
      *(LAS float*)(lds + palp + k * 128) = alpha;
      if (k >= KREG || k == nmine - 1) {
        const unsigned dst = ppst + ((k == nmine - 1) ? W_LAST : (k - KREG) * 2048);
        *(LAS u32x4*)(lds + dst) = pf[0];
        *(LAS u32x4*)(lds + dst + 1024) = pf[1];
      } else {
        asm volatile("s_set_gpr_idx_on %8, gpr_idx(DST)\n\t"
                     "v_mov_b32 v128, %0\n\tv_mov_b32 v129, %1\n\tv_mov_b32 v130, %2\n\tv_mov_b32 v131, %3\n\t"
                     "v_mov_b32 v132, %4\n\tv_mov_b32 v133, %5\n\tv_mov_b32 v134, %6\n\tv_mov_b32 v135, %7\n\t"
                     "s_set_gpr_idx_off"
                     :: "v"(pf[0][0]), "v"(pf[0][1]), "v"(pf[0][2]), "v"(pf[0][3]), "v"(pf[1][0]), "v"(pf[1][1]), "v"(pf[1][2]), "v"(pf[1][3]), "s"(8 * k));
      }
    }
  }
  l = swap_sum(l);
  *(LAS f32x2*)(lds + LDS_ML + w * 256 + r32 * 8) = f32x2{m, l};
  LGKM0();
  ctl[CTL_MLOK / 4 + w] = 1;
  STAMP(2);

  const unsigned tq = (lane & 15) >> 2, tp = lane & 3, tblk = (lane >> 4) & 1;
  const unsigned vaddr0 = LDS_RING + (4 * hi + tq) * 256 + ((tq ^ (pos & 3)) << 6) + ((2 * tblk + (tp >> 1)) << 4) + ((tp & 1) << 3);
  f32x16 o[4];
#pragma unroll
  for (int i = 0; i < 4; ++i)
#pragma unroll
    for (int r = 0; r < 16; ++r) o[i][r] = 0.f;
  auto weights = [&]() {
    wait_above<1>(lbase + LDS_CTL + CTL_MLOK + min(lane, NC - 1) * 4, 0);
    unsigned a = LDS_ML + r32 * 8;
    asm volatile("" : "+v"(a) :: "memory");
    float M = -3.0e38f, L = 0.f;
    f32x2 ml[NC];
#pragma unroll
    for (int i = 0; i < NC; ++i) { ml[i] = *(LAS f32x2*)(lds + a + i * 256); M = fmaxf(M, ml[i][0]); }
#pragma unroll
    for (int i = 0; i < NC; ++i) L = fmaf(__builtin_amdgcn_exp2f((ml[i][0] - M) * CS), ml[i][1], L);
    const float inv = 1.0f / L;
    *(LAS float*)(lds + (a >> 1) + (LDS_WT - LDS_ML / 2) + pos * 128) = __builtin_amdgcn_exp2f((mprev - M) * CS) * inv;
    const float ct = __builtin_amdgcn_exp2f((m - M) * CS) * inv;
#pragma unroll
    for (int s = 0; s < 2; ++s) {
      u32x4 x = *(LAS u32x4*)(lds + ppst + W_LAST + s * 1024);
#pragma unroll
      for (int e = 0; e < 4; ++e) x[e] = pack2(__uint_as_float(x[e] << 16) * ct, __uint_as_float(x[e] & 0xffff0000u) * ct);
      *(LAS u32x4*)(lds + ppst + W_LAST + s * 1024) = x;
    }
    LGKM0();
    ctl[CTL_WTOK / 4 + w] = 1;
  };
  resc &= ~1u;
  if (nmine <= 2) weights();
#pragma unroll 1
  for (int k = 0; k < nmine - 1; ++k) {
    if (k == 1) weights();
    if ((resc >> k) & 1) {
      const float alpha = *(LAS float*)(lds + palp + k * 128);
#pragma unroll
      for (int i = 0; i < 4; ++i)
#pragma unroll
        for (int r = 0; r < 16; ++r) o[i][r] *= alpha;
    }
    const unsigned u = NT + w + NC * k;
    u32x4 pf[2];
    if (k >= KREG) {
      pf[0] = *(LAS u32x4*)(lds + ppst + (k - KREG) * 2048);
      pf[1] = *(LAS u32x4*)(lds + ppst + (k - KREG) * 2048 + 1024);
    } else {
      asm volatile("s_set_gpr_idx_on %8, gpr_idx(SRC0)\n\t"
                   "v_mov_b32 %0, v128\n\tv_mov_b32 %1, v129\n\tv_mov_b32 %2, v130\n\tv_mov_b32 %3, v131\n\t"
                   "v_mov_b32 %4, v132\n\tv_mov_b32 %5, v133\n\tv_mov_b32 %6, v134\n\tv_mov_b32 %7, v135\n\t"
                   "s_set_gpr_idx_off\n\ts_nop 1"
                   : "=v"(pf[0][0]), "=v"(pf[0][1]), "=v"(pf[0][2]), "=v"(pf[0][3]), "=v"(pf[1][0]), "=v"(pf[1][1]), "=v"(pf[1][2]), "=v"(pf[1][3])
                   : "s"(8 * k));
    }
    asm volatile("" : "+v"(pf[0]), "+v"(pf[1]), "+v"(o[0]), "+v"(o[1]), "+v"(o[2]), "+v"(o[3]));
    if (k == nmine - 2) wait_above<1>(lbase + LDS_CTL + CTL_WTOK + min(lane, NC - 1) * 4, 0);
    wait_above<CSLEEP>(aland, 4 * u);
    if (TPRIO && k == nmine - 2) __builtin_amdgcn_s_setprio(TPRIO);
    bf16x8 vf[4][2];
#pragma unroll
    for (int i = 0; i < 4; ++i) {
      const unsigned a = (vaddr0 + ((u & 7) << 13)) ^ (i << 6);
#pragma unroll
      for (int s = 0; s < 2; ++s) {
        const s16x4 lo = __builtin_amdgcn_ds_read_tr16_b64_v4i16((LAS s16x4*)(lds + a + 4096 * s));
        const s16x4 up = __builtin_amdgcn_ds_read_tr16_b64_v4i16((LAS s16x4*)(lds + a + 4096 * s + 2048));
        vf[i][s] = __builtin_bit_cast(bf16x8, __builtin_shufflevector(lo, up, 0, 1, 2, 3, 4, 5, 6, 7));
      }
    }
    asm volatile("s_waitcnt lgkmcnt(0)" : "+v"(vf[0][0]), "+v"(vf[0][1]), "+v"(vf[1][0]), "+v"(vf[1][1]), "+v"(vf[2][0]), "+v"(vf[2][1]), "+v"(vf[3][0]), "+v"(vf[3][1]) :: "memory");
    ctl[CTL_FREE / 4 + (u & 7)] = u + 8;
    if (pf_on) prefetch(u);
    if (toucher) touch_at(u);
#pragma unroll
    for (int s = 0; s < 2; ++s)
#pragma unroll
      for (int i = 0; i < 4; ++i) o[i] = __builtin_amdgcn_mfma_f32_32x32x16_bf16(vf[i][s], __builtin_bit_cast(bf16x8, pf[s]), o[i], 0, 0, 0);
  }
  STAMP(3);

  {
    unsigned l16 = lane * 16;
    asm volatile("" : "+v"(l16));
    const unsigned l4 = l16 >> 2, awt = LDS_WT + (l4 & 127);
    auto put = [&](unsigned dst, const f32x16& x) {
#pragma unroll
      for (int j = 0; j < 4; ++j) *(LAS f32x4*)(lds + dst + j * 1024) = f32x4{x[4 * j], x[4 * j + 1], x[4 * j + 2], x[4 * j + 3]};
    };
    float co[6];
    bf16x8 pl[NC][2];
    auto ld_p = [&](int p) {
      pl[p][0] = *(LAS bf16x8*)(lds + LDS_WREG + p * W_SIZE + W_LAST + l16);
      pl[p][1] = *(LAS bf16x8*)(lds + LDS_WREG + p * W_SIZE + W_LAST + l16 + 1024);
    };
#pragma unroll
    for (int i = 0; i < 4; ++i) co[i] = *(LAS float*)(lds + awt + ((pos & 3) ^ i) * 128);
    co[4] = *(LAS float*)(lds + awt + 4 * 128);
    co[5] = *(LAS float*)(lds + awt + 5 * 128);
    ld_p(0); ld_p(1);
    asm volatile("" : "+v"(l16) :: "memory");
    if (nmine > 1) {
      if (pos > 3) put(LDS_RING + ((2 * NT - 2 * NC + pos) & 7) * 8192 + l16, o[0]);
#pragma unroll
      for (int i = 1; i < 4; ++i) put(LDS_WREG + pos * W_SIZE + (i - 1) * 4096 + l16, o[i]);
      LGKM0();
      ctl[CTL_ODONE / 4 + pos] = 1;
    }
    if (pos < 4) {
      f32x16 of = o[0], ot;
#pragma unroll
      for (int r = 0; r < 16; ++r) ot[r] = 0.f;
      auto src_o = [&](int p) -> unsigned {
        const int i = pos ^ (p & 3);
        return l16 + (i == 0 ? LDS_RING + ((2 * NT - 2 * NC + p) & 7) * 8192 : LDS_WREG + p * W_SIZE + (i - 1) * 4096);
      };
      auto fma_o = [&](unsigned src, float c) {
#pragma unroll
        for (int j = 0; j < 4; ++j) {
          const f32x4 x = *(LAS f32x4*)(lds + src + j * 1024);
#pragma unroll
          for (int e = 0; e < 4; ++e) of[4 * j + e] = fmaf(x[e], c, of[4 * j + e]);
        }
      };
      auto mma_t = [&](int p, bf16x8 p0, bf16x8 p1) {
        const unsigned a = vaddr0 + (((2 * NT - NC + p) & 7) << 13);
        bf16x8 vf[2];
#pragma unroll
        for (int s = 0; s < 2; ++s) {
          const s16x4 lo = __builtin_amdgcn_ds_read_tr16_b64_v4i16((LAS s16x4*)(lds + a + 4096 * s));
          const s16x4 up = __builtin_amdgcn_ds_read_tr16_b64_v4i16((LAS s16x4*)(lds + a + 4096 * s + 2048));
          vf[s] = __builtin_bit_cast(bf16x8, __builtin_shufflevector(lo, up, 0, 1, 2, 3, 4, 5, 6, 7));
        }
        ot = __builtin_amdgcn_mfma_f32_32x32x16_bf16(vf[0], p0, ot, 0, 0, 0);
        ot = __builtin_amdgcn_mfma_f32_32x32x16_bf16(vf[1], p1, ot, 0, 0, 0);
      };
      if (NT >= 2 * NC) {
#define PIN_OF() asm volatile("" : "+v"(of), "+v"(ot))
#pragma unroll
        for (int r = 0; r < 16; ++r) of[r] *= co[0];
        const bool wl = (l4 & 24) == 0;
        const unsigned ua = 4 * (2 * NT - NC);
        wait_above_v(wl ? aland : lbase + LDS_CTL + CTL_ODONE + (l4 & 12), wl ? ua : 0u);
        mma_t(0, pl[0][0], pl[0][1]);
#pragma unroll
        for (int i = 1; i < 4; ++i) fma_o(LDS_WREG + (pos ^ i) * W_SIZE + (i - 1) * 4096 + l16, co[i]);
        PIN_OF();
        STAMP(4);
        ld_p(2); ld_p(3); ld_p(4); ld_p(5);
        wait_above_v(wl ? aland : lbase + LDS_CTL + CTL_ODONE + 16 + (l4 & 4), wl ? ua + 8 : 0u);
        mma_t(1, pl[1][0], pl[1][1]);
        mma_t(2, pl[2][0], pl[2][1]);
        fma_o(src_o(4), co[4]);
        fma_o(src_o(5), co[5]);
#pragma unroll
        for (int r = 0; r < 16; ++r) { ot[r] += of[r]; of[r] = 0.f; }
        asm volatile("" : "+v"(ot));
        wait_above<0>(aland, ua + 20);
        mma_t(3, pl[3][0], pl[3][1]);
        mma_t(4, pl[4][0], pl[4][1]);
        mma_t(5, pl[5][0], pl[5][1]);
        of = ot;
      } else {
        wait_above<1>(lbase + LDS_CTL + CTL_WTOK + min(l4, (NC - 1) * 4u), 0);
        const float cw = *(LAS float*)(lds + awt + pos * 128);
#pragma unroll
        for (int r = 0; r < 16; ++r) of[r] *= cw;
#pragma unroll 1
        for (int p = max(NC - NT, 0); p < NC; ++p) {
          if (p != pos && NT - 2 * NC + p >= 0) {
            wait_above<1>(lbase + LDS_CTL + CTL_ODONE + p * 4, 0);
            fma_o(src_o(p), *(LAS float*)(lds + awt + p * 128));
          }
          wait_above<1>(aland, 4 * (2 * NT - NC + p));
          mma_t(p, *(LAS bf16x8*)(lds + LDS_WREG + p * W_SIZE + W_LAST + l16), *(LAS bf16x8*)(lds + LDS_WREG + p * W_SIZE + W_LAST + l16 + 1024));
        }
#pragma unroll
        for (int r = 0; r < 16; ++r) of[r] += ot[r];
      }
      const __amdgpu_buffer_rsrc_t ro = __builtin_amdgcn_make_buffer_rsrc(outp, 0, NB * HQ * SQ * HD * 2, 0x00020000);
      const unsigned orow = (unsigned)(b * HQ + 2 * h) * SQ * HD * 2 + pos * 64 + ((l16 & 511) << 4) + ((l16 >> 5) & 16);
#pragma unroll
      for (int j = 0; j < 2; ++j) {
        const unsigned a0 = pack2(of[8 * j], of[8 * j + 1]), a1 = pack2(of[8 * j + 2], of[8 * j + 3]);
        const unsigned b0 = pack2(of[8 * j + 4], of[8 * j + 5]), b1 = pack2(of[8 * j + 6], of[8 * j + 7]);
        auto s0 = __builtin_amdgcn_permlane32_swap(a0, b0, false, false);
        auto s1 = __builtin_amdgcn_permlane32_swap(a1, b1, false, false);
        u32x4 val = {s0[0], s1[0], s0[1], s1[1]};
        __builtin_amdgcn_raw_buffer_store_b128(val, ro, orow + j * 32, 0, OUTAUX);
      }
    }
  }
}
}

extern "C" void kernel_launch(void* const* d_in, const int* in_sizes, int n_in, void* d_out, int out_size, void* d_ws,
                              size_t ws_size, hipStream_t stream) {
  (void)n_in; (void)out_size; (void)ws_size;
  const unsigned nphys = (unsigned)(in_sizes[1] / (PAGE * HKV * HD));
  const unsigned kvbytes = nphys * PAGE_BYTES;
  (void)hipFuncSetAttribute((const void*)paged_attn, hipFuncAttributeMaxDynamicSharedMemorySize, LDS_TOTAL);
  hipLaunchKernelGGL(paged_attn, dim3(256), dim3(512), LDS_TOTAL, stream, d_in[0], d_in[1], d_in[2], (const int*)d_in[3],
                     (const int*)d_in[4], d_out, (char*)d_ws, kvbytes, nphys);
}
```

```cpp
#include <hip/hip_runtime.h>
#include <hip/hip_bf16.h>

#define PACE_X16 435
#define SOFT 15
#define GAP_TICKS 10
#define CODEPF 11776
#define PF_LEAD 180
#define PF_X64 72
#define PF_PER 4
#define PF_PSTEP 2
#define PF_RUN 3

namespace {
constexpr int NB = 16, HQ = 32, HKV = 16, SQ = 16, HD = 128, PAGE = 16, NBPS = 256, SKV = NBPS * PAGE;
constexpr unsigned PAGE_BYTES = PAGE * HKV * HD * 2;
constexpr unsigned INVALID = 0x80000000u;
constexpr unsigned TLBG = 0x200000u;
constexpr int TAHEAD = 2;
constexpr int U0 = 4;
constexpr int NC = 6;
constexpr int KREG = 16, KLDS = 5;
static_assert(NC * (KREG + KLDS + 1) >= NBPS / 2, "P storage");
constexpr unsigned LDS_RING = 0;
constexpr unsigned LDS_WREG = 65536;
constexpr unsigned W_ALP = KLDS * 2048;
constexpr unsigned W_LAST = W_ALP + (KREG + KLDS + 1) * 128;
constexpr unsigned W_SIZE = W_LAST + 2048;
static_assert(W_LAST >= 3 * 4096 && W_SIZE % 256 == 0, "consumer region");
constexpr unsigned LDS_TBL = LDS_WREG + W_SIZE;
constexpr unsigned LDS_SRT = LDS_WREG + 3 * W_SIZE;
constexpr unsigned LDS_Q = LDS_WREG;
constexpr unsigned LDS_ML = LDS_WREG + NC * W_SIZE;
constexpr unsigned LDS_WT = LDS_ML + NC * 256;
constexpr unsigned LDS_CTL = LDS_WT + NC * 128;
constexpr unsigned CTL_LAND = 0;
constexpr unsigned CTL_QOK = 16;
constexpr unsigned CTL_SORT = 24;
constexpr unsigned CTL_FREE = 32;
constexpr unsigned CTL_MLOK = 64;
constexpr unsigned CTL_ODONE = 96;
constexpr unsigned CTL_WTOK = 128;
constexpr unsigned LDS_DUMMY = LDS_CTL + 256;
constexpr unsigned LDS_PLIST = LDS_DUMMY + 256;
constexpr unsigned LDS_TOTAL = LDS_PLIST + 1024;
static_assert(LDS_TOTAL <= 163840, "lds");

typedef __attribute__((ext_vector_type(8))) __bf16 bf16x8;
typedef __attribute__((ext_vector_type(2))) __bf16 bf16x2;
typedef __attribute__((ext_vector_type(4))) short s16x4;
typedef __attribute__((ext_vector_type(16))) float f32x16;
typedef __attribute__((ext_vector_type(4))) float f32x4;
typedef __attribute__((ext_vector_type(2))) float f32x2;
typedef __attribute__((ext_vector_type(4))) unsigned u32x4;
typedef __attribute__((ext_vector_type(2))) unsigned u32x2;
#define LAS __attribute__((address_space(3)))

__device__ __forceinline__ float swap_max(float x) {
  auto r = __builtin_amdgcn_permlane32_swap(__float_as_uint(x), __float_as_uint(x), false, false);
  return fmaxf(__uint_as_float(r[0]), __uint_as_float(r[1]));
}
__device__ __forceinline__ float swap_sum(float x) {
  auto r = __builtin_amdgcn_permlane32_swap(__float_as_uint(x), __float_as_uint(x), false, false);
  return __uint_as_float(r[0]) + __uint_as_float(r[1]);
}
__device__ __forceinline__ unsigned pack2(float a, float b) {
  bf16x2 v = {(__bf16)a, (__bf16)b};
  return __builtin_bit_cast(unsigned, v);
}
template <int CTRL> __device__ __forceinline__ int dpp_add(int x) { return x + __builtin_amdgcn_update_dpp(0, x, CTRL, 0xf, 0xf, true); }
__device__ __forceinline__ unsigned now32() { return (unsigned)__builtin_amdgcn_s_memrealtime(); }
__device__ __forceinline__ int to_sgpr(int v) {
  int r;
  asm volatile("s_nop 1\n\tv_readfirstlane_b32 %0, %1" : "=s"(r) : "v"(v));
  return r;
}
#define LGKM0() asm volatile("s_waitcnt lgkmcnt(0)" ::: "memory")
#define VMWAIT(n) asm volatile("s_waitcnt vmcnt(" #n ")" ::: "memory")
#define CBAR() asm volatile("" ::: "memory")
template <int SLEEP> __device__ __forceinline__ void wait_above(unsigned addr, unsigned u) {
  unsigned v, cnt;
  if (SLEEP)
    asm volatile("s_mov_b32 %1, 0x800000\n\ts_branch 2f\n"
                 "1:\n\ts_sleep %4\n"
                 "2:\n\tds_read_b32 %0, %2\n\ts_sub_u32 %1, %1, 1\n\ts_waitcnt lgkmcnt(0)\n\t"
                 "v_cmp_ge_u32 vcc, %3, %0\n\ts_cbranch_scc1 3f\n\ts_nop 1\n\ts_cbranch_vccnz 1b\n3:"
                 : "=&v"(v), "=&s"(cnt) : "v"(addr), "s"(u), "n"(SLEEP) : "memory", "scc", "vcc");
  else
    asm volatile("s_mov_b32 %1, 0x800000\n"
                 "1:\n\tds_read_b32 %0, %2\n\ts_sub_u32 %1, %1, 1\n\ts_waitcnt lgkmcnt(0)\n\t"
                 "v_cmp_ge_u32 vcc, %3, %0\n\ts_cbranch_scc1 3f\n\ts_nop 1\n\ts_cbranch_vccnz 1b\n3:"
                 : "=&v"(v), "=&s"(cnt) : "v"(addr), "s"(u) : "memory", "scc", "vcc");
}

__device__ __forceinline__ void wait_above_v(unsigned addr, unsigned u) {
  unsigned v, cnt;
  asm volatile("s_mov_b32 %1, 0x800000\n"
               "1:\n\tds_read_b32 %0, %2\n\ts_sub_u32 %1, %1, 1\n\ts_waitcnt lgkmcnt(0)\n\t"
               "v_cmp_ge_u32 vcc, %3, %0\n\ts_cbranch_scc1 3f\n\ts_nop 1\n\ts_cbranch_vccnz 1b\n3:"
               : "=&v"(v), "=&s"(cnt) : "v"(addr), "v"(u) : "memory", "scc", "vcc");
}

__global__ void __launch_bounds__(512, 1) __attribute__((amdgpu_num_vgpr(64)))
paged_attn(const void* __restrict__ qp, const void* __restrict__ kp, const void* __restrict__ vp, const int* __restrict__ bt,
           const int* __restrict__ lens, void* __restrict__ outp, unsigned kvbytes, unsigned nphys) {
  extern __shared__ __attribute__((aligned(16))) char smem[];
  LAS char* lds = (LAS char*)smem;
  const int lane0 = threadIdx.x & 63;
  const int wv = __builtin_amdgcn_readfirstlane(threadIdx.x >> 6);
  const int g = blockIdx.x;
  const int h = g >> 4;
  constexpr float CS = 0.08838834764831845f * 1.4426950408889634f;
  volatile LAS unsigned* ctl = (volatile LAS unsigned*)(lds + LDS_CTL);

  const unsigned lbase = (unsigned)(size_t)lds;
  const bool toucher = (g & 8) != 0;
  const unsigned nchunk = (kvbytes + 16 * TLBG - 1) / (16 * TLBG);

  unsigned lraw, first;
  u32x4 lq;
  {
    asm volatile("global_load_dword %0, %3, %4\n\t"
                 "global_load_dwordx4 %1, %5, %4\n\t"
                 "global_load_dword %2, %6, %7"
                 : "=&v"(lraw), "=&v"(lq), "=&v"(first)
                 : "v"((lane0 & 15) * 4), "s"(lens), "v"(lane0 & 48), "v"((lane0 & 15) * (wv < 2 ? NBPS * 4 : 0) + (lane0 >> 4) * 8 + (wv & 1) * 4), "s"(bt),
                   "s"(qp), "s"(kp), "s"(vp), "s"(outp), "s"(kvbytes), "s"(nphys)
                 : "memory");
    __builtin_amdgcn_sched_barrier(0);
  }
  if (wv >= 4) {
    if (wv == 7) {
      const __amdgpu_buffer_rsrc_t rb = __builtin_amdgcn_make_buffer_rsrc((void*)bt, 0, NB * NBPS * 4, 0x00020000);
#define TBL4(SO) "buffer_load_dwordx4 %2, %1, " SO " offen lds\n\tbuffer_load_dwordx4 %2, %1, " SO " offen offset:1024 lds\n\t" \
                 "buffer_load_dwordx4 %2, %1, " SO " offen offset:2048 lds\n\tbuffer_load_dwordx4 %2, %1, " SO " offen offset:3072 lds\n\t"
      asm volatile("s_mov_b32 m0, %0\n\ts_nop 0\n\t" TBL4("0") "s_add_u32 m0, m0, 4096\n\ts_nop 0\n\t" TBL4("%3")
                   "s_add_u32 m0, m0, %6\n\ts_nop 0\n\t" TBL4("%4") "s_add_u32 m0, m0, 4096\n\ts_nop 0\n\t" TBL4("%5")
                   :: "s"(lbase + LDS_TBL), "s"(rb), "v"(lane0 * 16), "s"(4096), "s"(8192), "s"(12288), "n"(W_SIZE - 4096) : "memory", "scc");
    }
    unsigned long long pc;
    asm volatile("s_getpc_b64 %0" : "=s"(pc));
    const __amdgpu_buffer_rsrc_t rt = __builtin_amdgcn_make_buffer_rsrc(wv == 5 ? (void*)pc : (void*)kp, 0,
                                                                        wv == 5 ? CODEPF : (wv == 6 && toucher) ? (int)min(kvbytes, TAHEAD * 16 * TLBG) : 0, 0x00020000);
    const unsigned mul = wv == 5 ? 512u : 16 * TLBG + 4224u, add = wv == 5 ? ((g >> 3) & 3) * 128 : (g >> 4) * (TLBG + 33024u);
    asm volatile("s_mov_b32 m0, %0\n\ts_nop 0\n\tbuffer_load_dword %1, %2, 0 offen lds"
                 :: "s"(lbase + LDS_DUMMY), "v"((wv == 5 ? lane0 : lane0 & 31) * mul + add), "s"(rt) : "memory");
    *(LAS u32x4*)(lds + LDS_SRT + (threadIdx.x - 256) * 16) = u32x4{0u, 0u, 0u, 0u};
    if (threadIdx.x < 296) ctl[lane0] = ((lane0 >> 3) == CTL_FREE / 32) ? (lane0 & 7) : 0u;
    asm volatile("s_waitcnt lgkmcnt(0)\n\ts_barrier" ::: "memory");
  }
  if (wv < 2) __builtin_amdgcn_s_setprio(3);
  const unsigned rstride = wv < 2 ? 4096 : 256;
  unsigned vo0 = (lane0 >> 4) * rstride + (((unsigned)(lane0 & 15) ^ ((lane0 >> 4) << 2)) << 4) + (wv < 2 ? h * (HD * 2) : 0);
  unsigned vo1 = (vo0 + 4 * rstride - 1024) ^ 16, vo2 = (vo0 + 8 * rstride - 2048) ^ 32, vo3 = (vo0 + 12 * rstride - 3072) ^ 48;
  const unsigned want = (g & 8) ? 15 - (g & 7) : (g & 7);
  const unsigned dst0 = lbase + (wv < 2 ? LDS_RING : LDS_Q) + (wv & 1) * 4096;
  asm volatile("s_cmp_ge_u32 %8, 4\n\ts_cbranch_scc1 1f\n\ts_waitcnt vmcnt(0)\n1:\n\ts_waitcnt vmcnt(1)" : "+v"(lraw), "+v"(lq), "+v"(first), "+v"(vo0), "+v"(vo1), "+v"(vo2), "+v"(vo3) : "s"(dst0), "s"(wv) : "memory", "scc");
  unsigned lm1 = min(lraw - 1u, (unsigned)SKV - 1u);
  unsigned rank = 0;
  {
    const unsigned key = lm1 * 16 + (lane0 & 15);
#pragma unroll
    for (int j = 0; j < 4; ++j) rank += (min(lq[j] - 1u, (unsigned)SKV - 1u) * 16 + ((lane0 >> 4) * 4 + j) > key) ? 1u : 0u;
    auto r1 = __builtin_amdgcn_permlane16_swap(rank, rank, false, false);
    rank = r1[0] + r1[1];
    auto r2 = __builtin_amdgcn_permlane32_swap(rank, rank, false, false);
    rank = r2[0] + r2[1];
  }
  first = min(first, nphys - 1) * PAGE_BYTES;
  const int b = __builtin_ctz((unsigned)__builtin_amdgcn_ballot_w64(rank == want) | 0x8000u);
  unsigned long long tstart = 0;
  if (wv < 4) {
    const bool qok = (int)__builtin_amdgcn_readlane(lraw, b) > 0;
    const __amdgpu_buffer_rsrc_t r0 = __builtin_amdgcn_make_buffer_rsrc(wv < 2 ? (void*)kp : (void*)qp, 0,
                                                                        wv < 2 ? (int)kvbytes : qok ? NB * HQ * SQ * HD * 2 : 0, 0x00020000);
    const unsigned so = wv < 2 ? __builtin_amdgcn_readlane(first, b) : qok ? (unsigned)((b * HQ + 2 * h) * SQ * HD * 2) + (wv & 1) * 4096 : 0u;
    asm volatile("s_waitcnt vmcnt(0)\n\t"
                 "s_mov_b32 m0, %2\n\t"
                 "s_memrealtime %0\n\t"
                 "s_nop 2\n\t"
                 "buffer_load_dwordx4 %4, %1, %3 offen nt lds\n\t"
                 "buffer_load_dwordx4 %5, %1, %3 offen offset:1024 nt lds\n\t"
                 "buffer_load_dwordx4 %6, %1, %3 offen offset:2048 nt lds\n\t"
                 "buffer_load_dwordx4 %7, %1, %3 offen offset:3072 nt lds\n\t"
                 "s_waitcnt lgkmcnt(0)\n\t"
                 "s_barrier"
                 : "=&s"(tstart) : "s"(r0), "s"(dst0), "s"(so), "v"(vo0), "v"(vo1), "v"(vo2), "v"(vo3) : "memory");
  }
  asm volatile("" : "+v"(lm1), "+v"(rank), "+v"(first) :: "memory");
  __builtin_amdgcn_sched_barrier(0);
  const int bp = __builtin_ctz((unsigned)__builtin_amdgcn_ballot_w64(rank == 15 - want) | 0x8000u);
  const int len = __builtin_amdgcn_readlane(lm1, b) + 1, nown = (len + PAGE - 1) / PAGE, NT = (nown + 1) >> 1;
  const int npl = (__builtin_amdgcn_readlane(lm1, bp) >> 4) + 1, NTL = (npl + 1) >> 1;
  auto total = [&]() __attribute__((always_inline)) {
    int t = lm1 + 1;
    t = dpp_add<0x121>(t); t = dpp_add<0x122>(t); t = dpp_add<0x124>(t); t = dpp_add<0x128>(t);
    return t;
  };

  auto sort_pages = [&](int lane, int bb, int n, unsigned scr, unsigned dst) __attribute__((always_inline)) {
    u32x4 e = *(LAS u32x4*)(lds + LDS_TBL + (bb >> 3) * W_SIZE + (bb & 7) * 1024 + lane * 16);
    bool in[4];
#pragma unroll
    for (int j = 0; j < 4; ++j) {
      e[j] = min(e[j], nphys - 1);
      in[j] = 4 * lane + j >= 2 * U0 && 4 * lane + j < n - 1;
      __hip_atomic_fetch_or((LAS unsigned*)(lds + scr + ((e[j] >> 5) & 255) * 8), in[j] ? 1u << (e[j] & 31) : 0u, __ATOMIC_RELAXED, __HIP_MEMORY_SCOPE_WAVEFRONT);
    }
    CBAR();
    u32x4 a = *(LAS u32x4*)(lds + scr + lane * 32), c = *(LAS u32x4*)(lds + scr + lane * 32 + 16);
    const int n0 = __builtin_popcount(a[0]), n1 = n0 + __builtin_popcount(a[2]), n2 = n1 + __builtin_popcount(c[0]), n3 = n2 + __builtin_popcount(c[2]);
    int sc = n3;
    sc = dpp_add<0x111>(sc); sc = dpp_add<0x112>(sc); sc = dpp_add<0x114>(sc); sc = dpp_add<0x118>(sc);
    sc += __builtin_amdgcn_update_dpp(0, sc, 0x142, 0xa, 0xf, false);
    sc += __builtin_amdgcn_update_dpp(0, sc, 0x143, 0xc, 0xf, false);
    const bool ok = nphys <= 8192u && __builtin_amdgcn_readlane(sc, 63) == n - 1 - 2 * U0;
    sc += 2 * U0 - n3;
    a[1] = sc; a[3] = sc + n0; c[1] = sc + n1; c[3] = sc + n2;
    *(LAS u32x4*)(lds + scr + lane * 32) = a;
    *(LAS u32x4*)(lds + scr + lane * 32 + 16) = c;
    CBAR();
    u32x2 wp[4];
#pragma unroll
    for (int j = 0; j < 4; ++j) wp[j] = *(LAS u32x2*)(lds + scr + ((e[j] >> 5) & 255) * 8);
#pragma unroll
    for (int j = 0; j < 4; ++j) {
      const unsigned r = wp[j][1] + __builtin_popcount(wp[j][0] & ((1u << (e[j] & 31)) - 1u));
      *(LAS unsigned*)(lds + dst + ((ok && in[j]) ? r : 4 * lane + j) * 4) = e[j] * PAGE_BYTES;
    }
    CBAR();
  };

  if (__builtin_expect(wv < 2, 1)) {
    const int half = wv, lane = lane0;
    const unsigned vland = lbase + LDS_CTL + CTL_LAND + half * 4;
    const unsigned vfree = lbase + LDS_CTL + CTL_FREE + (lane & 7) * 4;
    const unsigned ring = dst0;
    unsigned l0 = __builtin_amdgcn_ds_bpermute((b + 16 * lane) * 4, first), l1 = 0;
    const unsigned a0 = lbase + LDS_SRT + 4096 + min(2 * lane + half, nown - 1) * 4, a1 = lbase + LDS_SRT + 4096 + min(2 * lane + half + 128, nown - 1) * 4;
    const int rot = (NT >= 3 * NC) ? NT % NC : 0;
    const unsigned vperm = (lane < NC ? (lane + rot >= NC ? lane + rot - NC : lane + rot) : lane) * 4;
    const int T16 = total() * PACE_X16;
    const int step = to_sgpr((int)((float)T16 * __builtin_amdgcn_rcpf((float)max(len, 32))));
    const unsigned due0 = ((unsigned)tstart << 8) + half * (step >> 1);
    const int lrows = len - (2 * (NT - 1) + half) * PAGE;
    unsigned vf, vt, lc;
    asm volatile(
        "s_waitcnt lgkmcnt(0)\n\t"
        "s_memrealtime s[68:69]\n\t"
        "ds_read_b32 %[vf], %[vfree]\n\t"
        "s_mov_b32 s60, 1\n\t"
        "s_mov_b32 s61, 4\n\t"
        "s_mov_b32 s62, 0x1000000\n\t"
        "s_mov_b32 s66, 0\n\t"
        "s_min_u32 s67, %[nt], %[u0]\n\t"
        "s_mov_b32 s75, %[nrec]\n\t"
        "s_mov_b32 s72, 0x2000\n\t"
        "s_mov_b32 s73, 1\n\t"
        "s_mov_b32 s74, %[due1]\n\t"
        "s_mov_b64 s[76:77], %[kp]\n\t"
        "s_mov_b32 s78, %[nrec]\n\t"
        "s_mov_b32 s79, 0x20000\n\t"
        "s_and_b32 s77, s77, 0xffff\n\t"
        "v_mov_b32 %[lc], %[l0]\n\t"
        "s_getpc_b64 s[70:71]\n"
        ".LldA%=:\n\t"
        "s_add_u32 s80, s70, .LldL%=-.LldA%=\n\t"
        "s_addc_u32 s71, s71, 0\n\t"
        "s_branch .LldF%=\n"
        ".LldR%=:\n\t"
        "s_sub_u32 s62, s62, 1\n\t"
        "s_cbranch_scc1 .LldE%=\n"
        ".LldT%=:\n\t"
        "s_getreg_b32 s64, hwreg(HW_REG_IB_STS)\n\t"
        "s_and_b32 s65, s64, 15\n\t"
        "s_bfe_u32 s64, s64, 0x20016\n\t"
        "s_lshl4_add_u32 s64, s64, s65\n\t"
        "s_add_u32 s64, s64, 3\n\t"
        "s_and_b32 s64, s64, 60\n\t"
        "s_sub_u32 s65, s61, s64\n\t"
        "s_cmp_lg_u32 s65, s66\n\t"
        "s_cbranch_scc1 .LldP%=\n"
        ".LldC%=:\n\t"
        "s_waitcnt lgkmcnt(0)\n\t"
        "v_cmp_eq_u32 vcc, s60, %[vf]\n\t"
        "s_lshl_b32 s64, s68, 8\n\t"
        "s_memrealtime s[68:69]\n\t"
        "ds_read_b32 %[vf], %[vfree]\n\t"
        "s_sub_u32 s64, s64, s74\n\t"
        "s_cmp_lt_i32 s64, 0\n\t"
        "s_cbranch_scc1 .LldR%=\n\t"
        "s_cbranch_vccz .LldR%=\n\t"
        "buffer_load_dwordx4 %[o0], s[76:79], s63 offen nt lds\n\t"
        "s_add_u32 s74, s74, %[step]\n\t"
        "s_add_u32 s73, s73, 1\n\t"
        "buffer_load_dwordx4 %[o1], s[76:79], s63 offen offset:1024 nt lds\n\t"
        "s_add_u32 s60, s60, 1\n\t"
        "s_add_u32 s61, s61, 4\n\t"
        "buffer_load_dwordx4 %[o2], s[76:79], s63 offen offset:2048 nt lds\n\t"
        "s_add_u32 s72, s72, 0x2000\n\t"
        "s_and_b32 s72, s72, 0xe000\n\t"
        "buffer_load_dwordx4 %[o3], s[76:79], s63 offen offset:3072 nt lds\n"
        ".LldF%=:\n\t"
        "v_readlane_b32 s63, %[lc], s73\n\t"
        "s_add_u32 m0, s72, %[ring]\n\t"
        "s_cmp_eq_u32 s73, %[ntm]\n\t"
        "s_cselect_b32 s78, s75, %[nrec]\n\t"
        "s_cmp_lg_u32 s60, s67\n\t"
        "s_cbranch_scc1 .LldT%=\n\t"
        "s_cmp_lg_u32 s60, %[u0]\n\t"
        "s_cbranch_scc1 .LldG%=\n\t"
        "s_cmp_le_u32 %[nt], %[u0]\n\t"
        "s_cbranch_scc1 .LldG%=\n"
        ".LldS%=:\n\t"
        "ds_read_b32 %[lc], %[vland] offset:%[srt]\n\t"
        "ds_read_b32 %[l0], %[a0]\n\t"
        "ds_read_b32 %[l1], %[a1]\n\t"
        "s_sub_u32 s62, s62, 1\n\t"
        "s_cbranch_scc1 .LldE%=\n\t"
        "s_waitcnt lgkmcnt(0)\n\t"
        "v_readfirstlane_b32 s64, %[lc]\n\t"
        "s_cmp_eq_u32 s64, 0\n\t"
        "s_cbranch_scc1 .LldS%=\n\t"
        "s_max_i32 s65, %[lrows], 0\n\t"
        "s_lshl_b32 s65, s65, 12\n\t"
        "s_add_u32 s64, s64, s65\n\t"
        "s_sub_u32 s64, s64, 1\n\t"
        "s_cmp_lt_i32 %[lrows], 16\n\t"
        "s_cselect_b32 s75, s64, %[nrec]\n\t"
        "s_min_u32 s67, %[nt], 64\n\t"
        "v_mov_b32 %[lc], %[l0]\n\t"
        "s_branch .LldF%=\n"
        ".LldG%=:\n\t"
        "s_cmp_eq_u32 s73, %[nt]\n\t"
        "s_cbranch_scc1 .LldV%=\n\t"
        "v_mov_b32 %[lc], %[l1]\n\t"
        "v_readfirstlane_b32 s63, %[l1]\n\t"
        "s_add_u32 s67, s60, %[nt]\n\t"
        "s_sub_u32 s67, s67, 64\n\t"
        "s_branch .LldT%=\n"
        ".LldV%=:\n\t"
        "s_cmp_lg_u32 s60, %[nt]\n\t"
        "s_cbranch_scc1 .LldD%=\n\t"
        "s_mov_b64 s[76:77], %[vp]\n\t"
        "s_and_b32 s77, s77, 0xffff\n\t"
        "s_mov_b32 s73, 0\n\t"
        "s_min_u32 s67, %[nt], 64\n\t"
        "s_add_u32 s67, s67, s60\n\t"
        "s_mov_b32 s74, %[duev]\n\t"
        "s_cmp_eq_u32 %[ntm], 0\n\t"
        "s_cselect_b32 s78, s75, %[nrec]\n\t"
        "ds_bpermute_b32 %[lc], %[vperm], %[l0]\n\t"
        "v_xor_b32 %[o1], 16, %[o1]\n\t"
        "v_xor_b32 %[o2], 32, %[o2]\n\t"
        "v_xor_b32 %[o3], 48, %[o3]\n\t"
        "s_waitcnt lgkmcnt(0)\n\t"
        "v_readfirstlane_b32 s63, %[lc]\n\t"
        "s_branch .LldT%=\n"
        ".LldD%=:\n\t"
        "s_add_u32 s74, s74, 0x40000000\n\t"
        "s_mov_b32 s60, -1\n\t"
        "s_branch .LldT%=\n"
        ".LldP%=:\n\t"
        "s_min_u32 s64, s64, 32\n\t"
        "s_add_u32 s70, s80, s64\n\t"
        "v_mov_b32 %[vt], s65\n\t"
        "s_mov_b32 s66, s65\n\t"
        "s_setpc_b64 s[70:71]\n\t"
        ".p2align 6\n"
        ".LldL%=:\n\t"
        "s_waitcnt vmcnt(0)\n\t"
        "s_waitcnt vmcnt(4)\n\t"
        "s_waitcnt vmcnt(8)\n\t"
        "s_waitcnt vmcnt(12)\n\t"
        "s_waitcnt vmcnt(16)\n\t"
        "s_waitcnt vmcnt(20)\n\t"
        "s_waitcnt vmcnt(24)\n\t"
        "s_waitcnt vmcnt(28)\n\t"
        "s_waitcnt vmcnt(32)\n\t"
        "ds_write_b32 %[vland], %[vt]\n\t"
        "s_cmp_lg_u32 s65, %[fin]\n\t"
        "s_cbranch_scc1 .LldC%=\n"
        ".LldE%=:\n\t"
        "s_waitcnt vmcnt(0) lgkmcnt(0)\n\t"
        : [vf] "=&v"(vf), [vt] "=&v"(vt), [lc] "=&v"(lc), [l0] "+&v"(l0), [l1] "+&v"(l1), [o1] "+&v"(vo1), [o2] "+&v"(vo2), [o3] "+&v"(vo3)
        : [vperm] "v"(vperm), [a0] "v"(a0), [a1] "v"(a1), [o0] "v"(vo0), [vland] "v"(vland), [vfree] "v"(vfree),
          [kp] "s"(kp), [vp] "s"(vp), [nrec] "s"(kvbytes), [nt] "s"(NT), [ring] "s"(ring),
          [step] "s"(step), [due1] "s"(due0 + step + SOFT * 256), [duev] "s"(due0 + (to_sgpr(T16) >> 5) + (SOFT + GAP_TICKS) * 256), [lrows] "s"(lrows), [ntm] "s"(NT - 1), [fin] "s"(8 * NT), [u0] "n"(U0), [srt] "n"(CTL_SORT - CTL_LAND)
        : "memory", "scc", "vcc", "s60", "s61", "s62", "s63", "s64", "s65", "s66", "s67", "s68", "s69", "s70", "s71", "s72", "s73", "s74", "s75",
          "s76", "s77", "s78", "s79", "s80");
    asm volatile("s_endpgm" ::: "memory");
  }

  int lane = lane0;
  asm volatile("" : "+v"(lane));
  auto touch = [&](unsigned c) __attribute__((always_inline)) {
    const bool isv = c >= nchunk;
    const __amdgpu_buffer_rsrc_t rt = __builtin_amdgcn_make_buffer_rsrc(isv ? (void*)vp : (void*)kp, 0, (int)kvbytes, 0x00020000);
    unsigned ln = lane;
    asm volatile("" : "+v"(ln));
    const unsigned idx = (g >> 4) + 16 * (c - (isv ? nchunk : 0) + ln);
    const unsigned vo = (idx < 0x80000000u / TLBG) ? idx * TLBG + ((idx * 30592u) & (TLBG - 128)) : INVALID;
    asm volatile("s_mov_b32 m0, %0\n\ts_nop 4\n\tbuffer_load_dword %2, %1, 0 offen nt lds" :: "s"((unsigned)(size_t)lds + LDS_DUMMY), "s"(rt), "v"(vo) : "memory");
  };
  if (wv == 7) {
#pragma unroll 1
    for (int i = 0; i < 2; ++i) {
      sort_pages(lane, i ? bp : b, i ? npl : nown, LDS_SRT + i * 2048, i ? LDS_PLIST : LDS_SRT + 4096);
      if (i == 0) {
        const unsigned lo = *(LAS unsigned*)(lds + LDS_SRT + 4096 + (nown - 1) * 4) + 1;
        LGKM0();
        ctl[CTL_SORT / 4 + (lane & 1)] = lo;
      }
    }
  }
  if (wv < 4) {
    VMWAIT(0);
    ctl[CTL_QOK / 4 + (wv & 1)] = 1;
  }
  const int tot = total();
  const int w = wv - 2;
  const int r32 = lane & 31, hi = lane >> 5;
  const int nmine = (NT > w) ? (NT - w + NC - 1) / NC : 0;
  const unsigned aland = lbase + LDS_CTL + CTL_LAND + (lane & 1) * 4;
  const int tots = __builtin_amdgcn_readfirstlane(tot);
  const bool pf_on = npl * 256 * 64 > tots * PF_X64 && npl > nown && (h & 3) == 0;
  const unsigned pf_last = 2 * NT - to_sgpr((int)((float)(2 * NT * (PF_LEAD + 23 * NC)) * 4096.0f * __builtin_amdgcn_rcpf((float)(tot * PACE_X16))));
  const unsigned pf_ratio = to_sgpr((int)((float)NTL * 65536.0f * __builtin_amdgcn_rcpf((float)NT)));
  const unsigned pf_per = PF_PER + (g & 7) * PF_PSTEP, pf_inv = (65536 + pf_per - 1) / pf_per;
  auto prefetch = [&](unsigned op) {
    const unsigned d = pf_last - op;
    if (d >= 1024u) return;
    const unsigned r = d - ((d * pf_inv) >> 16) * pf_per;
    if (r >= PF_RUN) return;
    unsigned tl = 2 * NTL - 1 - NC - (((d - r) * pf_ratio) >> 16) - r;
    if (tl >= 2u * NTL) return;
    const bool isv = tl >= (unsigned)NTL;
    tl -= isv ? NTL : 0;
    const __amdgpu_buffer_rsrc_t rp = __builtin_amdgcn_make_buffer_rsrc(isv ? (void*)vp : (void*)kp, 0, (int)kvbytes, 0x00020000);
    unsigned ln = lane;
    asm volatile("" : "+v"(ln));
    const unsigned vo = (*(LAS unsigned*)(lds + LDS_PLIST + min(2 * tl + (ln >> 5), (unsigned)npl - 1) * 4) & 0x7fff0000u) + (h + 1) * (HD * 2) +
                        ((ln >> 1) & 15) * 4096 + (ln & 1) * 128;
    asm volatile("s_mov_b32 m0, %0\n\ts_nop 4\n\tbuffer_load_dword %2, %1, 0 offen lds" :: "s"(lbase + LDS_DUMMY), "s"(rp), "v"(vo) : "memory");
  };
  const int pos = (w + NC * 22 - NT) % NC;
  const unsigned palp = LDS_WREG + pos * W_SIZE + W_ALP + r32 * 4;
  const unsigned ppst = LDS_WREG + pos * W_SIZE + lane * 16;
  asm volatile("" ::: "v128", "v255");
  unsigned resc = 0;
  float m = -3.0e38f, mprev = -3.0e38f, l = 0.f;

  {
    const unsigned fk = (((unsigned)r32 & 3) << 2) | (((unsigned)r32 >> 2) & 3);
    const unsigned kaddr0 = LDS_RING + r32 * 256 + (((unsigned)hi ^ fk) << 4);
    bf16x8 qf[8];
    wait_above<1>(aland + (CTL_QOK - CTL_LAND), 0);
#pragma unroll
    for (int s = 0; s < 8; ++s) qf[s] = *(LAS bf16x8*)(lds + ((kaddr0 + LDS_Q - LDS_RING) ^ (s << 5)));
#pragma unroll 1
    for (int k = 0; k < nmine; ++k) {
      const unsigned t = w + NC * k;
      wait_above<1>(aland, 4 * t);
      bf16x8 kf[8];
#pragma unroll
      for (int s = 0; s < 8; ++s) kf[s] = *(LAS bf16x8*)(lds + ((kaddr0 + ((t & 7) << 13)) ^ (s << 5)));
      asm volatile("s_waitcnt lgkmcnt(0)" : "+v"(kf[0]), "+v"(kf[1]), "+v"(kf[2]), "+v"(kf[3]), "+v"(kf[4]), "+v"(kf[5]), "+v"(kf[6]), "+v"(kf[7]) :: "memory");
      ctl[CTL_FREE / 4 + (t & 7)] = t + 8;
      if (pf_on) prefetch(t);
      if (toucher && (t & ~16u) == 0) touch(t ? nchunk : TAHEAD);
      f32x16 sc;
#pragma unroll
      for (int r = 0; r < 16; ++r) sc[r] = 0.f;
#pragma unroll
      for (int s = 0; s < 8; ++s) sc = __builtin_amdgcn_mfma_f32_32x32x16_bf16(kf[s], qf[s], sc, 0, 0, 0);
      if ((int)t == NT - 1) {
        const int kb = 2 * t * PAGE + 4 * hi;
#pragma unroll
        for (int r = 0; r < 16; ++r) sc[r] = (kb + 8 * (r >> 2) + (r & 3) < len) ? sc[r] : -__builtin_inff();
      }
      float mx = sc[0];
#pragma unroll
      for (int r = 1; r < 16; ++r) mx = fmaxf(mx, sc[r]);
      mx = swap_max(mx);
      const float mn = fmaxf(m, mx);
      const float alpha = __builtin_amdgcn_exp2f((m - mn) * CS);
      mprev = m;
      m = mn;
      float ps = 0.f;
#pragma unroll
      for (int r = 0; r < 16; ++r) { sc[r] = __builtin_amdgcn_exp2f((sc[r] - mn) * CS); ps += sc[r]; }
      l = fmaf(l, alpha, ps);
      u32x4 pf[2];
#pragma unroll
      for (int s = 0; s < 2; ++s)
        pf[s] = u32x4{pack2(sc[8 * s], sc[8 * s + 1]), pack2(sc[8 * s + 2], sc[8 * s + 3]), pack2(sc[8 * s + 4], sc[8 * s + 5]), pack2(sc[8 * s + 6], sc[8 * s + 7])};
      if (__builtin_amdgcn_ballot_w64(alpha != 1.0f)) resc |= 1u << k;
      *(LAS float*)(lds + palp + k * 128) = alpha;
      if (k >= KREG || k == nmine - 1) {
        const unsigned dst = ppst + ((k == nmine - 1) ? W_LAST : (k - KREG) * 2048);
        *(LAS u32x4*)(lds + dst) = pf[0];
        *(LAS u32x4*)(lds + dst + 1024) = pf[1];
      } else {
        asm volatile("s_set_gpr_idx_on %8, gpr_idx(DST)\n\t"
                     "v_mov_b32 v128, %0\n\tv_mov_b32 v129, %1\n\tv_mov_b32 v130, %2\n\tv_mov_b32 v131, %3\n\t"
                     "v_mov_b32 v132, %4\n\tv_mov_b32 v133, %5\n\tv_mov_b32 v134, %6\n\tv_mov_b32 v135, %7\n\t"
                     "s_set_gpr_idx_off"
                     :: "v"(pf[0][0]), "v"(pf[0][1]), "v"(pf[0][2]), "v"(pf[0][3]), "v"(pf[1][0]), "v"(pf[1][1]), "v"(pf[1][2]), "v"(pf[1][3]), "s"(8 * k));
      }
    }
  }
  l = swap_sum(l);
  *(LAS f32x2*)(lds + LDS_ML + w * 256 + r32 * 8) = f32x2{m, l};
  LGKM0();
  ctl[CTL_MLOK / 4 + w] = 1;

  const unsigned tq = (lane & 15) >> 2, tp = lane & 3, tblk = (lane >> 4) & 1;
  const unsigned vaddr0 = LDS_RING + (4 * hi + tq) * 256 + ((tq ^ (pos & 3)) << 6) + ((2 * tblk + (tp >> 1)) << 4) + ((tp & 1) << 3);
  f32x16 o[4];
#pragma unroll
  for (int i = 0; i < 4; ++i)
#pragma unroll
    for (int r = 0; r < 16; ++r) o[i][r] = 0.f;
  auto weights = [&]() {
    wait_above<1>(lbase + LDS_CTL + CTL_MLOK + min(lane, NC - 1) * 4, 0);
    unsigned a = LDS_ML + r32 * 8;
    asm volatile("" : "+v"(a) :: "memory");
    float M = -3.0e38f, L = 0.f;
    f32x2 ml[NC];
#pragma unroll
    for (int i = 0; i < NC; ++i) { ml[i] = *(LAS f32x2*)(lds + a + i * 256); M = fmaxf(M, ml[i][0]); }
#pragma unroll
    for (int i = 0; i < NC; ++i) L = fmaf(__builtin_amdgcn_exp2f((ml[i][0] - M) * CS), ml[i][1], L);
    const float inv = 1.0f / L;
    *(LAS float*)(lds + (a >> 1) + (LDS_WT - LDS_ML / 2) + pos * 128) = __builtin_amdgcn_exp2f((mprev - M) * CS) * inv;
    const float ct = __builtin_amdgcn_exp2f((m - M) * CS) * inv;
#pragma unroll
    for (int s = 0; s < 2; ++s) {
      u32x4 x = *(LAS u32x4*)(lds + ppst + W_LAST + s * 1024);
#pragma unroll
      for (int e = 0; e < 4; ++e) x[e] = pack2(__uint_as_float(x[e] << 16) * ct, __uint_as_float(x[e] & 0xffff0000u) * ct);
      *(LAS u32x4*)(lds + ppst + W_LAST + s * 1024) = x;
    }
    LGKM0();
    ctl[CTL_WTOK / 4 + w] = 1;
  };
  resc &= ~1u;
  if (nmine <= 3) weights();
#pragma unroll 1
  for (int k = 0; k < nmine - 1; ++k) {
    if (k == 2) weights();
    if ((resc >> k) & 1) {
      const float alpha = *(LAS float*)(lds + palp + k * 128);
#pragma unroll
      for (int i = 0; i < 4; ++i)
#pragma unroll
        for (int r = 0; r < 16; ++r) o[i][r] *= alpha;
    }
    const unsigned u = NT + ((k == 0 && NT >= 3 * NC) ? pos : w + NC * k);
    u32x4 pf[2];
    if (k >= KREG) {
      pf[0] = *(LAS u32x4*)(lds + ppst + (k - KREG) * 2048);
      pf[1] = *(LAS u32x4*)(lds + ppst + (k - KREG) * 2048 + 1024);
    } else {
      asm volatile("s_set_gpr_idx_on %8, gpr_idx(SRC0)\n\t"
                   "v_mov_b32 %0, v128\n\tv_mov_b32 %1, v129\n\tv_mov_b32 %2, v130\n\tv_mov_b32 %3, v131\n\t"
                   "v_mov_b32 %4, v132\n\tv_mov_b32 %5, v133\n\tv_mov_b32 %6, v134\n\tv_mov_b32 %7, v135\n\t"
                   "s_set_gpr_idx_off\n\ts_nop 1"
                   : "=v"(pf[0][0]), "=v"(pf[0][1]), "=v"(pf[0][2]), "=v"(pf[0][3]), "=v"(pf[1][0]), "=v"(pf[1][1]), "=v"(pf[1][2]), "=v"(pf[1][3])
                   : "s"(8 * k));
    }
    asm volatile("" : "+v"(pf[0]), "+v"(pf[1]), "+v"(o[0]), "+v"(o[1]), "+v"(o[2]), "+v"(o[3]));
    if (k == nmine - 2) wait_above<1>(lbase + LDS_CTL + CTL_WTOK + min(lane, NC - 1) * 4, 0);
    wait_above<1>(aland, 4 * u);
    bf16x8 vf[4][2];
#pragma unroll
    for (int i = 0; i < 4; ++i) {
      const unsigned a = (vaddr0 + ((u & 7) << 13)) ^ (i << 6);
#pragma unroll
      for (int s = 0; s < 2; ++s) {
        const s16x4 lo = __builtin_amdgcn_ds_read_tr16_b64_v4i16((LAS s16x4*)(lds + a + 4096 * s));
        const s16x4 up = __builtin_amdgcn_ds_read_tr16_b64_v4i16((LAS s16x4*)(lds + a + 4096 * s + 2048));
        vf[i][s] = __builtin_bit_cast(bf16x8, __builtin_shufflevector(lo, up, 0, 1, 2, 3, 4, 5, 6, 7));
      }
    }
    asm volatile("s_waitcnt lgkmcnt(0)" : "+v"(vf[0][0]), "+v"(vf[0][1]), "+v"(vf[1][0]), "+v"(vf[1][1]), "+v"(vf[2][0]), "+v"(vf[2][1]), "+v"(vf[3][0]), "+v"(vf[3][1]) :: "memory");
    ctl[CTL_FREE / 4 + (u & 7)] = u + 8;
    if (pf_on) prefetch(u);
#pragma unroll
    for (int s = 0; s < 2; ++s)
#pragma unroll
      for (int i = 0; i < 4; ++i) o[i] = __builtin_amdgcn_mfma_f32_32x32x16_bf16(vf[i][s], __builtin_bit_cast(bf16x8, pf[s]), o[i], 0, 0, 0);
  }

  {
    unsigned l16 = lane * 16;
    asm volatile("" : "+v"(l16));
    const unsigned l4 = l16 >> 2, awt = LDS_WT + (l4 & 127);
    auto put = [&](unsigned dst, const f32x16& x) {
#pragma unroll
      for (int j = 0; j < 4; ++j) *(LAS f32x4*)(lds + dst + j * 1024) = f32x4{x[4 * j], x[4 * j + 1], x[4 * j + 2], x[4 * j + 3]};
    };
    float co[6];
    bf16x8 pl[NC][2];
    auto ld_p = [&](int p) {
      pl[p][0] = *(LAS bf16x8*)(lds + LDS_WREG + p * W_SIZE + W_LAST + l16);
      pl[p][1] = *(LAS bf16x8*)(lds + LDS_WREG + p * W_SIZE + W_LAST + l16 + 1024);
    };
#pragma unroll
    for (int i = 0; i < 4; ++i) co[i] = *(LAS float*)(lds + awt + ((pos & 3) ^ i) * 128);
    co[4] = *(LAS float*)(lds + awt + 4 * 128);
    co[5] = *(LAS float*)(lds + awt + 5 * 128);
    ld_p(0); ld_p(1);
    asm volatile("" : "+v"(l16) :: "memory");
    if (nmine > 1) {
      if (pos > 3) put(LDS_RING + ((2 * NT - 2 * NC + pos) & 7) * 8192 + l16, o[0]);
#pragma unroll
      for (int i = 1; i < 4; ++i) put(LDS_WREG + pos * W_SIZE + (i - 1) * 4096 + l16, o[i]);
      LGKM0();
      ctl[CTL_ODONE / 4 + pos] = 1;
    }
    if (pos < 4) {
      f32x16 of = o[0], ot;
#pragma unroll
      for (int r = 0; r < 16; ++r) ot[r] = 0.f;
      auto src_o = [&](int p) -> unsigned {
        const int i = pos ^ (p & 3);
        return l16 + (i == 0 ? LDS_RING + ((2 * NT - 2 * NC + p) & 7) * 8192 : LDS_WREG + p * W_SIZE + (i - 1) * 4096);
      };
      auto fma_o = [&](unsigned src, float c) {
#pragma unroll
        for (int j = 0; j < 4; ++j) {
          const f32x4 x = *(LAS f32x4*)(lds + src + j * 1024);
#pragma unroll
          for (int e = 0; e < 4; ++e) of[4 * j + e] = fmaf(x[e], c, of[4 * j + e]);
        }
      };
      auto mma_t = [&](int p, bf16x8 p0, bf16x8 p1) {
        const unsigned a = vaddr0 + (((2 * NT - NC + p) & 7) << 13);
        bf16x8 vf[2];
#pragma unroll
        for (int s = 0; s < 2; ++s) {
          const s16x4 lo = __builtin_amdgcn_ds_read_tr16_b64_v4i16((LAS s16x4*)(lds + a + 4096 * s));
          const s16x4 up = __builtin_amdgcn_ds_read_tr16_b64_v4i16((LAS s16x4*)(lds + a + 4096 * s + 2048));
          vf[s] = __builtin_bit_cast(bf16x8, __builtin_shufflevector(lo, up, 0, 1, 2, 3, 4, 5, 6, 7));
        }
        ot = __builtin_amdgcn_mfma_f32_32x32x16_bf16(vf[0], p0, ot, 0, 0, 0);
        ot = __builtin_amdgcn_mfma_f32_32x32x16_bf16(vf[1], p1, ot, 0, 0, 0);
      };
      if (NT >= 2 * NC) {
#define PIN_OF() asm volatile("" : "+v"(of), "+v"(ot))
#pragma unroll
        for (int r = 0; r < 16; ++r) of[r] *= co[0];
        const bool wl = (l4 & 24) == 0;
        const unsigned ua = 4 * (2 * NT - NC);
        wait_above_v(wl ? aland : lbase + LDS_CTL + CTL_ODONE + (l4 & 12), wl ? ua : 0u);
        mma_t(0, pl[0][0], pl[0][1]);
#pragma unroll
        for (int i = 1; i < 4; ++i) fma_o(LDS_WREG + (pos ^ i) * W_SIZE + (i - 1) * 4096 + l16, co[i]);
        PIN_OF();
        ld_p(2); ld_p(3); ld_p(4); ld_p(5);
        wait_above_v(wl ? aland : lbase + LDS_CTL + CTL_ODONE + 16 + (l4 & 4), wl ? ua + 8 : 0u);
        mma_t(1, pl[1][0], pl[1][1]);
        mma_t(2, pl[2][0], pl[2][1]);
        fma_o(src_o(4), co[4]);
        fma_o(src_o(5), co[5]);
#pragma unroll
        for (int r = 0; r < 16; ++r) { ot[r] += of[r]; of[r] = 0.f; }
        asm volatile("" : "+v"(ot));
        wait_above<0>(aland, ua + 20);
        mma_t(3, pl[3][0], pl[3][1]);
        mma_t(4, pl[4][0], pl[4][1]);
        mma_t(5, pl[5][0], pl[5][1]);
        of = ot;
      } else {
        wait_above<1>(lbase + LDS_CTL + CTL_WTOK + min(l4, (NC - 1) * 4u), 0);
        const float cw = *(LAS float*)(lds + awt + pos * 128);
#pragma unroll
        for (int r = 0; r < 16; ++r) of[r] *= cw;
#pragma unroll 1
        for (int p = max(NC - NT, 0); p < NC; ++p) {
          if (p != pos && NT - 2 * NC + p >= 0) {
            wait_above<1>(lbase + LDS_CTL + CTL_ODONE + p * 4, 0);
            fma_o(src_o(p), *(LAS float*)(lds + awt + p * 128));
          }
          wait_above<1>(aland, 4 * (2 * NT - NC + p));
          mma_t(p, *(LAS bf16x8*)(lds + LDS_WREG + p * W_SIZE + W_LAST + l16), *(LAS bf16x8*)(lds + LDS_WREG + p * W_SIZE + W_LAST + l16 + 1024));
        }
#pragma unroll
        for (int r = 0; r < 16; ++r) of[r] += ot[r];
      }
      const __amdgpu_buffer_rsrc_t ro = __builtin_amdgcn_make_buffer_rsrc(outp, 0, NB * HQ * SQ * HD * 2, 0x00020000);
      const unsigned orow = (unsigned)(b * HQ + 2 * h) * SQ * HD * 2 + pos * 64 + ((l16 & 511) << 4) + ((l16 >> 5) & 16);
#pragma unroll
      for (int j = 0; j < 2; ++j) {
        const unsigned a0 = pack2(of[8 * j], of[8 * j + 1]), a1 = pack2(of[8 * j + 2], of[8 * j + 3]);
        const unsigned b0 = pack2(of[8 * j + 4], of[8 * j + 5]), b1 = pack2(of[8 * j + 6], of[8 * j + 7]);
        auto s0 = __builtin_amdgcn_permlane32_swap(a0, b0, false, false);
        auto s1 = __builtin_amdgcn_permlane32_swap(a1, b1, false, false);
        u32x4 val = {s0[0], s1[0], s0[1], s1[1]};
        __builtin_amdgcn_raw_buffer_store_b128(val, ro, orow + j * 32, 0, 0);
      }
    }
  }
}
}

extern "C" void kernel_launch(void* const* d_in, const int* in_sizes, int n_in, void* d_out, int out_size, void* d_ws,
                              size_t ws_size, hipStream_t stream) {
  (void)n_in; (void)out_size; (void)d_ws; (void)ws_size;
  const unsigned nphys = (unsigned)(in_sizes[1] / (PAGE * HKV * HD));
  const unsigned kvbytes = nphys * PAGE_BYTES;
  (void)hipFuncSetAttribute((const void*)paged_attn, hipFuncAttributeMaxDynamicSharedMemorySize, LDS_TOTAL);
  hipLaunchKernelGGL(paged_attn, dim3(256), dim3(512), LDS_TOTAL, stream, d_in[0], d_in[1], d_in[2], (const int*)d_in[3],
                     (const int*)d_in[4], d_out, kvbytes, nphys);
}
```

```cpp
#define PACE_X16 433
#define SOFT 10
#include <hip/hip_runtime.h>
#include <hip/hip_bf16.h>

#define GAP_TICKS 10
#define OTOUCH 0
#define CODEPF 11520
#define PF_LEAD 180
#define PF_X64 72
#define PF_PER 4
#define PF_PSTEP 2
#define PF_RUN 3

namespace {
constexpr int NB = 16, HQ = 32, HKV = 16, SQ = 16, HD = 128, PAGE = 16, NBPS = 256, SKV = NBPS * PAGE;
constexpr unsigned PAGE_BYTES = PAGE * HKV * HD * 2;
constexpr unsigned TLBG = 0x200000u;
constexpr int U0 = 4;
constexpr int NC = 6;
constexpr int KREG = 16, KLDS = 5;
static_assert(NC * (KREG + KLDS + 1) >= NBPS / 2, "P storage");
constexpr unsigned LDS_RING = 0;
constexpr unsigned LDS_WREG = 65536;
constexpr unsigned W_ALP = KLDS * 2048;
constexpr unsigned W_LAST = W_ALP + (KREG + KLDS + 1) * 128;
constexpr unsigned W_SIZE = W_LAST + 2048;
static_assert(W_LAST >= 3 * 4096 && W_SIZE % 256 == 0, "consumer region");
constexpr unsigned LDS_TBL = LDS_WREG + W_SIZE;
constexpr unsigned LDS_SRT = LDS_WREG + 3 * W_SIZE;
constexpr unsigned LDS_Q = LDS_WREG;
constexpr unsigned LDS_ML = LDS_WREG + NC * W_SIZE;
constexpr unsigned LDS_WT = LDS_ML + NC * 256;
constexpr unsigned LDS_CTL = LDS_WT + NC * 128;
constexpr unsigned CTL_LAND = 0;
constexpr unsigned CTL_QOK = 16;
constexpr unsigned CTL_SORT = 24;
constexpr unsigned CTL_FREE = 32;
constexpr unsigned CTL_MLOK = 64;
constexpr unsigned CTL_ODONE = 96;
constexpr unsigned CTL_WTOK = 128;
constexpr unsigned LDS_DUMMY = LDS_CTL + 256;
constexpr unsigned LDS_PLIST = LDS_DUMMY + 256;
constexpr unsigned LDS_TOTAL = LDS_PLIST + 1024;
static_assert(LDS_TOTAL <= 163840, "lds");

typedef __attribute__((ext_vector_type(8))) __bf16 bf16x8;
typedef __attribute__((ext_vector_type(2))) __bf16 bf16x2;
typedef __attribute__((ext_vector_type(4))) short s16x4;
typedef __attribute__((ext_vector_type(16))) float f32x16;
typedef __attribute__((ext_vector_type(4))) float f32x4;
typedef __attribute__((ext_vector_type(2))) float f32x2;
typedef __attribute__((ext_vector_type(4))) unsigned u32x4;
typedef __attribute__((ext_vector_type(2))) unsigned u32x2;
#define LAS __attribute__((address_space(3)))

__device__ __forceinline__ float swap_max(float x) {
  auto r = __builtin_amdgcn_permlane32_swap(__float_as_uint(x), __float_as_uint(x), false, false);
  return fmaxf(__uint_as_float(r[0]), __uint_as_float(r[1]));
}
__device__ __forceinline__ float swap_sum(float x) {
  auto r = __builtin_amdgcn_permlane32_swap(__float_as_uint(x), __float_as_uint(x), false, false);
  return __uint_as_float(r[0]) + __uint_as_float(r[1]);
}
__device__ __forceinline__ unsigned pack2(float a, float b) {
  bf16x2 v = {(__bf16)a, (__bf16)b};
  return __builtin_bit_cast(unsigned, v);
}
template <int CTRL> __device__ __forceinline__ int dpp_add(int x) { return x + __builtin_amdgcn_update_dpp(0, x, CTRL, 0xf, 0xf, true); }
__device__ __forceinline__ unsigned now32() { return (unsigned)__builtin_amdgcn_s_memrealtime(); }
__device__ __forceinline__ int to_sgpr(int v) {
  int r;
  asm volatile("s_nop 1\n\tv_readfirstlane_b32 %0, %1" : "=s"(r) : "v"(v));
  return r;
}
#define LGKM0() asm volatile("s_waitcnt lgkmcnt(0)" ::: "memory")
#define VMWAIT(n) asm volatile("s_waitcnt vmcnt(" #n ")" ::: "memory")
#define CBAR() asm volatile("" ::: "memory")
template <int SLEEP> __device__ __forceinline__ void wait_above(unsigned addr, unsigned u) {
  unsigned v, cnt;
  if (SLEEP)
    asm volatile("s_mov_b32 %1, 0x800000\n\ts_branch 2f\n"
                 "1:\n\ts_sleep %4\n"
                 "2:\n\tds_read_b32 %0, %2\n\ts_sub_u32 %1, %1, 1\n\ts_waitcnt lgkmcnt(0)\n\t"
                 "v_cmp_ge_u32 vcc, %3, %0\n\ts_cbranch_scc1 3f\n\ts_nop 1\n\ts_cbranch_vccnz 1b\n3:"
                 : "=&v"(v), "=&s"(cnt) : "v"(addr), "s"(u), "n"(SLEEP) : "memory", "scc", "vcc");
  else
    asm volatile("s_mov_b32 %1, 0x800000\n"
                 "1:\n\tds_read_b32 %0, %2\n\ts_sub_u32 %1, %1, 1\n\ts_waitcnt lgkmcnt(0)\n\t"
                 "v_cmp_ge_u32 vcc, %3, %0\n\ts_cbranch_scc1 3f\n\ts_nop 1\n\ts_cbranch_vccnz 1b\n3:"
                 : "=&v"(v), "=&s"(cnt) : "v"(addr), "s"(u) : "memory", "scc", "vcc");
}

__device__ __forceinline__ void wait_above_v(unsigned addr, unsigned u) {
  unsigned v, cnt;
  asm volatile("s_mov_b32 %1, 0x800000\n"
               "1:\n\tds_read_b32 %0, %2\n\ts_sub_u32 %1, %1, 1\n\ts_waitcnt lgkmcnt(0)\n\t"
               "v_cmp_ge_u32 vcc, %3, %0\n\ts_cbranch_scc1 3f\n\ts_nop 1\n\ts_cbranch_vccnz 1b\n3:"
               : "=&v"(v), "=&s"(cnt) : "v"(addr), "v"(u) : "memory", "scc", "vcc");
}

__global__ void __launch_bounds__(512, 1) __attribute__((amdgpu_num_vgpr(64)))
paged_attn(const void* __restrict__ qp, const void* __restrict__ kp, const void* __restrict__ vp, const int* __restrict__ bt,
           const int* __restrict__ lens, void* __restrict__ outp, unsigned kvbytes, unsigned nphys) {
  extern __shared__ __attribute__((aligned(16))) char smem[];
  LAS char* lds = (LAS char*)smem;
  const int lane0 = threadIdx.x & 63;
  const int wv = __builtin_amdgcn_readfirstlane(threadIdx.x >> 6);
  const int g = blockIdx.x;
  const int h = g >> 4;
  constexpr float CS = 0.08838834764831845f * 1.4426950408889634f;
  volatile LAS unsigned* ctl = (volatile LAS unsigned*)(lds + LDS_CTL);

  const unsigned lbase = (unsigned)(size_t)lds;

  unsigned lraw, first;
  u32x4 lq;
  {
    asm volatile("global_load_dword %0, %3, %4\n\t"
                 "global_load_dwordx4 %1, %5, %4\n\t"
                 "global_load_dword %2, %6, %7"
                 : "=&v"(lraw), "=&v"(lq), "=&v"(first)
                 : "v"((lane0 & 15) * 4), "s"(lens), "v"(lane0 & 48), "v"((lane0 & 15) * (wv < 2 ? NBPS * 4 : 0) + (lane0 >> 4) * 8 + (wv & 1) * 4), "s"(bt),
                   "s"(qp), "s"(kp), "s"(vp), "s"(outp), "s"(kvbytes), "s"(nphys)
                 : "memory");
    __builtin_amdgcn_sched_barrier(0);
  }
  if (wv >= 4) {
    if (wv == 7) {
      const __amdgpu_buffer_rsrc_t rb = __builtin_amdgcn_make_buffer_rsrc((void*)bt, 0, NB * NBPS * 4, 0x00020000);
#define TBL4(SO) "buffer_load_dwordx4 %2, %1, " SO " offen lds\n\tbuffer_load_dwordx4 %2, %1, " SO " offen offset:1024 lds\n\t" \
                 "buffer_load_dwordx4 %2, %1, " SO " offen offset:2048 lds\n\tbuffer_load_dwordx4 %2, %1, " SO " offen offset:3072 lds\n\t"
      asm volatile("s_mov_b32 m0, %0\n\ts_nop 0\n\t" TBL4("0") "s_add_u32 m0, m0, 4096\n\ts_nop 0\n\t" TBL4("%3")
                   "s_add_u32 m0, m0, %6\n\ts_nop 0\n\t" TBL4("%4") "s_add_u32 m0, m0, 4096\n\ts_nop 0\n\t" TBL4("%5")
                   :: "s"(lbase + LDS_TBL), "s"(rb), "v"(lane0 * 16), "s"(4096), "s"(8192), "s"(12288), "n"(W_SIZE - 4096) : "memory", "scc");
    }
    unsigned long long pc;
    asm volatile("s_getpc_b64 %0" : "=s"(pc));
    const __amdgpu_buffer_rsrc_t rt = __builtin_amdgcn_make_buffer_rsrc(
        wv == 5 ? (void*)pc : wv == 4 ? (void*)vp : wv == 7 ? outp : (void*)kp, 0,
        wv == 5 ? CODEPF : !(g & 8) ? 0 : wv == 7 ? OTOUCH * NB * HQ * SQ * HD * 2 : (int)kvbytes, 0x00020000);
    const unsigned mul = wv == 5 ? 512u : 16 * TLBG + 4224u, add = wv == 5 ? ((g >> 3) & 3) * 128 : (g >> 4) * ((wv == 7 ? TLBG / 4 : TLBG) + 33024u);
    asm volatile("s_mov_b32 m0, %0\n\ts_nop 0\n\tbuffer_load_dword %1, %2, 0 offen lds"
                 :: "s"(lbase + LDS_DUMMY), "v"((lane0 & 31) * mul + add), "s"(rt) : "memory");
    *(LAS u32x4*)(lds + LDS_SRT + (threadIdx.x - 256) * 16) = u32x4{0u, 0u, 0u, 0u};
    if (threadIdx.x < 296) ctl[lane0] = ((lane0 >> 3) == CTL_FREE / 32) ? (lane0 & 7) : 0u;
    asm volatile("s_waitcnt lgkmcnt(0)\n\ts_barrier" ::: "memory");
  }
  if (wv < 2) __builtin_amdgcn_s_setprio(3);
  const unsigned rstride = wv < 2 ? 4096 : 256;
  unsigned vo0 = (lane0 >> 4) * rstride + (((unsigned)(lane0 & 15) ^ ((lane0 >> 4) << 2)) << 4) + (wv < 2 ? h * (HD * 2) : 0);
  unsigned vo1 = (vo0 + 4 * rstride - 1024) ^ 16, vo2 = (vo0 + 8 * rstride - 2048) ^ 32, vo3 = (vo0 + 12 * rstride - 3072) ^ 48;
  const unsigned want = (g & 8) ? 15 - (g & 7) : (g & 7);
  const unsigned dst0 = lbase + (wv < 2 ? LDS_RING : LDS_Q) + (wv & 1) * 4096;
  asm volatile("s_cmp_ge_u32 %8, 4\n\ts_cbranch_scc1 1f\n\ts_waitcnt vmcnt(0)\n1:\n\ts_waitcnt vmcnt(1)" : "+v"(lraw), "+v"(lq), "+v"(first), "+v"(vo0), "+v"(vo1), "+v"(vo2), "+v"(vo3) : "s"(dst0), "s"(wv) : "memory", "scc");
  unsigned lm1 = min(lraw - 1u, (unsigned)SKV - 1u);
  unsigned rank = 0;
  {
    const unsigned key = lm1 * 16 + (lane0 & 15);
#pragma unroll
    for (int j = 0; j < 4; ++j) rank += (min(lq[j] - 1u, (unsigned)SKV - 1u) * 16 + ((lane0 >> 4) * 4 + j) > key) ? 1u : 0u;
    auto r1 = __builtin_amdgcn_permlane16_swap(rank, rank, false, false);
    rank = r1[0] + r1[1];
    auto r2 = __builtin_amdgcn_permlane32_swap(rank, rank, false, false);
    rank = r2[0] + r2[1];
  }
  first = min(first, nphys - 1) * PAGE_BYTES;
  const int b = __builtin_ctz((unsigned)__builtin_amdgcn_ballot_w64(rank == want) | 0x8000u);
  unsigned long long tstart = 0;
  if (wv < 4) {
    const bool qok = (int)__builtin_amdgcn_readlane(lraw, b) > 0;
    const __amdgpu_buffer_rsrc_t r0 = __builtin_amdgcn_make_buffer_rsrc(wv < 2 ? (void*)kp : (void*)qp, 0,
                                                                        wv < 2 ? (int)kvbytes : qok ? NB * HQ * SQ * HD * 2 : 0, 0x00020000);
    const unsigned so = wv < 2 ? __builtin_amdgcn_readlane(first, b) : qok ? (unsigned)((b * HQ + 2 * h) * SQ * HD * 2) + (wv & 1) * 4096 : 0u;
    asm volatile("s_waitcnt vmcnt(0)\n\t"
                 "s_mov_b32 m0, %2\n\t"
                 "s_memrealtime %0\n\t"
                 "s_nop 2\n\t"
                 "buffer_load_dwordx4 %4, %1, %3 offen nt lds\n\t"
                 "buffer_load_dwordx4 %5, %1, %3 offen offset:1024 nt lds\n\t"
                 "buffer_load_dwordx4 %6, %1, %3 offen offset:2048 nt lds\n\t"
                 "buffer_load_dwordx4 %7, %1, %3 offen offset:3072 nt lds\n\t"
                 "s_waitcnt lgkmcnt(0)\n\t"
                 "s_barrier"
                 : "=&s"(tstart) : "s"(r0), "s"(dst0), "s"(so), "v"(vo0), "v"(vo1), "v"(vo2), "v"(vo3) : "memory");
  }
  asm volatile("" : "+v"(lm1), "+v"(rank), "+v"(first) :: "memory");
  __builtin_amdgcn_sched_barrier(0);
  const int bp = __builtin_ctz((unsigned)__builtin_amdgcn_ballot_w64(rank == 15 - want) | 0x8000u);
  const int len = __builtin_amdgcn_readlane(lm1, b) + 1, nown = (len + PAGE - 1) / PAGE, NT = (nown + 1) >> 1;
  const int npl = (__builtin_amdgcn_readlane(lm1, bp) >> 4) + 1, NTL = (npl + 1) >> 1;
  auto total = [&]() __attribute__((always_inline)) {
    int t = lm1 + 1;
    t = dpp_add<0x121>(t); t = dpp_add<0x122>(t); t = dpp_add<0x124>(t); t = dpp_add<0x128>(t);
    return t;
  };

  auto sort_pages = [&](int lane, int bb, int n, unsigned scr, unsigned dst) __attribute__((always_inline)) {
    u32x4 e = *(LAS u32x4*)(lds + LDS_TBL + (bb >> 3) * W_SIZE + (bb & 7) * 1024 + lane * 16);
    bool in[4];
#pragma unroll
    for (int j = 0; j < 4; ++j) {
      e[j] = min(e[j], nphys - 1);
      in[j] = 4 * lane + j >= 2 * U0 && 4 * lane + j < n - 1;
      __hip_atomic_fetch_or((LAS unsigned*)(lds + scr + ((e[j] >> 5) & 255) * 8), in[j] ? 1u << (e[j] & 31) : 0u, __ATOMIC_RELAXED, __HIP_MEMORY_SCOPE_WAVEFRONT);
    }
    CBAR();
    u32x4 a = *(LAS u32x4*)(lds + scr + lane * 32), c = *(LAS u32x4*)(lds + scr + lane * 32 + 16);
    const int n0 = __builtin_popcount(a[0]), n1 = n0 + __builtin_popcount(a[2]), n2 = n1 + __builtin_popcount(c[0]), n3 = n2 + __builtin_popcount(c[2]);
    int sc = n3;
    sc = dpp_add<0x111>(sc); sc = dpp_add<0x112>(sc); sc = dpp_add<0x114>(sc); sc = dpp_add<0x118>(sc);
    sc += __builtin_amdgcn_update_dpp(0, sc, 0x142, 0xa, 0xf, false);
    sc += __builtin_amdgcn_update_dpp(0, sc, 0x143, 0xc, 0xf, false);
    const bool ok = nphys <= 8192u && __builtin_amdgcn_readlane(sc, 63) == n - 1 - 2 * U0;
    sc += 2 * U0 - n3;
    a[1] = sc; a[3] = sc + n0; c[1] = sc + n1; c[3] = sc + n2;
    *(LAS u32x4*)(lds + scr + lane * 32) = a;
    *(LAS u32x4*)(lds + scr + lane * 32 + 16) = c;
    CBAR();
    u32x2 wp[4];
#pragma unroll
    for (int j = 0; j < 4; ++j) wp[j] = *(LAS u32x2*)(lds + scr + ((e[j] >> 5) & 255) * 8);
#pragma unroll
    for (int j = 0; j < 4; ++j) {
      const unsigned r = wp[j][1] + __builtin_popcount(wp[j][0] & ((1u << (e[j] & 31)) - 1u));
      *(LAS unsigned*)(lds + dst + ((ok && in[j]) ? r : 4 * lane + j) * 4) = e[j] * PAGE_BYTES;
    }
    CBAR();
  };

  if (__builtin_expect(wv < 2, 1)) {
    const int half = wv, lane = lane0;
    const unsigned vland = lbase + LDS_CTL + CTL_LAND + half * 4;
    const unsigned vfree = lbase + LDS_CTL + CTL_FREE + (lane & 7) * 4;
    const unsigned ring = dst0;
    unsigned l0 = __builtin_amdgcn_ds_bpermute((b + 16 * lane) * 4, first), l1 = 0;
    const unsigned a0 = lbase + LDS_SRT + 4096 + min(2 * lane + half, nown - 1) * 4, a1 = lbase + LDS_SRT + 4096 + min(2 * lane + half + 128, nown - 1) * 4;
    const int rot = (NT >= 3 * NC) ? NT % NC : 0;
    const unsigned vperm = (lane < NC ? (lane + rot >= NC ? lane + rot - NC : lane + rot) : lane) * 4;
    const int T16 = total() * PACE_X16;
    const int step = to_sgpr((int)((float)T16 * __builtin_amdgcn_rcpf((float)max(len, 32))));
    const unsigned due0 = ((unsigned)tstart << 8) + half * (step >> 1);
    const int lrows = len - (2 * (NT - 1) + half) * PAGE;
    unsigned vf, vt, lc;
    asm volatile(
        "s_waitcnt lgkmcnt(0)\n\t"
        "s_memrealtime s[68:69]\n\t"
        "ds_read_b32 %[vf], %[vfree]\n\t"
        "s_mov_b32 s60, 1\n\t"
        "s_mov_b32 s61, 4\n\t"
        "s_mov_b32 s62, 0x1000000\n\t"
        "s_mov_b32 s66, 0\n\t"
        "s_min_u32 s67, %[nt], %[u0]\n\t"
        "s_mov_b32 s75, %[nrec]\n\t"
        "s_mov_b32 s72, 0x2000\n\t"
        "s_mov_b32 s73, 1\n\t"
        "s_mov_b32 s74, %[due1]\n\t"
        "s_mov_b64 s[76:77], %[kp]\n\t"
        "s_mov_b32 s78, %[nrec]\n\t"
        "s_mov_b32 s79, 0x20000\n\t"
        "s_and_b32 s77, s77, 0xffff\n\t"
        "v_mov_b32 %[lc], %[l0]\n\t"
        "s_getpc_b64 s[70:71]\n"
        ".LldA%=:\n\t"
        "s_add_u32 s80, s70, .LldL%=-.LldA%=\n\t"
        "s_addc_u32 s71, s71, 0\n\t"
        "s_branch .LldF%=\n"
        ".LldR%=:\n\t"
        "s_sub_u32 s62, s62, 1\n\t"
        "s_cbranch_scc1 .LldE%=\n"
        ".LldT%=:\n\t"
        "s_getreg_b32 s64, hwreg(HW_REG_IB_STS)\n\t"
        "s_and_b32 s65, s64, 15\n\t"
        "s_bfe_u32 s64, s64, 0x20016\n\t"
        "s_lshl4_add_u32 s64, s64, s65\n\t"
        "s_add_u32 s64, s64, 3\n\t"
        "s_and_b32 s64, s64, 60\n\t"
        "s_sub_u32 s65, s61, s64\n\t"
        "s_cmp_lg_u32 s65, s66\n\t"
        "s_cbranch_scc1 .LldP%=\n"
        ".LldC%=:\n\t"
        "s_waitcnt lgkmcnt(0)\n\t"
        "v_cmp_eq_u32 vcc, s60, %[vf]\n\t"
        "s_lshl_b32 s64, s68, 8\n\t"
        "s_memrealtime s[68:69]\n\t"
        "ds_read_b32 %[vf], %[vfree]\n\t"
        "s_sub_u32 s64, s64, s74\n\t"
        "s_cmp_lt_i32 s64, 0\n\t"
        "s_cbranch_scc1 .LldR%=\n\t"
        "s_cbranch_vccz .LldR%=\n\t"
        "buffer_load_dwordx4 %[o0], s[76:79], s63 offen nt lds\n\t"
        "s_add_u32 s74, s74, %[step]\n\t"
        "s_add_u32 s73, s73, 1\n\t"
        "buffer_load_dwordx4 %[o1], s[76:79], s63 offen offset:1024 nt lds\n\t"
        "s_add_u32 s60, s60, 1\n\t"
        "s_add_u32 s61, s61, 4\n\t"
        "buffer_load_dwordx4 %[o2], s[76:79], s63 offen offset:2048 nt lds\n\t"
        "s_add_u32 s72, s72, 0x2000\n\t"
        "s_and_b32 s72, s72, 0xe000\n\t"
        "buffer_load_dwordx4 %[o3], s[76:79], s63 offen offset:3072 nt lds\n"
        ".LldF%=:\n\t"
        "v_readlane_b32 s63, %[lc], s73\n\t"
        "s_add_u32 m0, s72, %[ring]\n\t"
        "s_cmp_eq_u32 s73, %[ntm]\n\t"
        "s_cselect_b32 s78, s75, %[nrec]\n\t"
        "s_cmp_lg_u32 s60, s67\n\t"
        "s_cbranch_scc1 .LldT%=\n\t"
        "s_cmp_lg_u32 s60, %[u0]\n\t"
        "s_cbranch_scc1 .LldG%=\n\t"
        "s_cmp_le_u32 %[nt], %[u0]\n\t"
        "s_cbranch_scc1 .LldG%=\n"
        ".LldS%=:\n\t"
        "ds_read_b32 %[lc], %[vland] offset:%[srt]\n\t"
        "ds_read_b32 %[l0], %[a0]\n\t"
        "ds_read_b32 %[l1], %[a1]\n\t"
        "s_sub_u32 s62, s62, 1\n\t"
        "s_cbranch_scc1 .LldE%=\n\t"
        "s_waitcnt lgkmcnt(0)\n\t"
        "v_readfirstlane_b32 s64, %[lc]\n\t"
        "s_cmp_eq_u32 s64, 0\n\t"
        "s_cbranch_scc1 .LldS%=\n\t"
        "s_max_i32 s65, %[lrows], 0\n\t"
        "s_lshl_b32 s65, s65, 12\n\t"
        "s_add_u32 s64, s64, s65\n\t"
        "s_sub_u32 s64, s64, 1\n\t"
        "s_cmp_lt_i32 %[lrows], 16\n\t"
        "s_cselect_b32 s75, s64, %[nrec]\n\t"
        "s_min_u32 s67, %[nt], 64\n\t"
        "v_mov_b32 %[lc], %[l0]\n\t"
        "s_branch .LldF%=\n"
        ".LldG%=:\n\t"
        "s_cmp_eq_u32 s73, %[nt]\n\t"
        "s_cbranch_scc1 .LldV%=\n\t"
        "v_mov_b32 %[lc], %[l1]\n\t"
        "v_readfirstlane_b32 s63, %[l1]\n\t"
        "s_add_u32 s67, s60, %[nt]\n\t"
        "s_sub_u32 s67, s67, 64\n\t"
        "s_branch .LldT%=\n"
        ".LldV%=:\n\t"
        "s_cmp_lg_u32 s60, %[nt]\n\t"
        "s_cbranch_scc1 .LldD%=\n\t"
        "s_mov_b64 s[76:77], %[vp]\n\t"
        "s_and_b32 s77, s77, 0xffff\n\t"
        "s_mov_b32 s73, 0\n\t"
        "s_min_u32 s67, %[nt], 64\n\t"
        "s_add_u32 s67, s67, s60\n\t"
        "s_mov_b32 s74, %[duev]\n\t"
        "s_cmp_eq_u32 %[ntm], 0\n\t"
        "s_cselect_b32 s78, s75, %[nrec]\n\t"
        "ds_bpermute_b32 %[lc], %[vperm], %[l0]\n\t"
        "v_xor_b32 %[o1], 16, %[o1]\n\t"
        "v_xor_b32 %[o2], 32, %[o2]\n\t"
        "v_xor_b32 %[o3], 48, %[o3]\n\t"
        "s_waitcnt lgkmcnt(0)\n\t"
        "v_readfirstlane_b32 s63, %[lc]\n\t"
        "s_branch .LldT%=\n"
        ".LldD%=:\n\t"
        "s_add_u32 s74, s74, 0x40000000\n\t"
        "s_mov_b32 s60, -1\n\t"
        "s_branch .LldT%=\n"
        ".LldP%=:\n\t"
        "s_min_u32 s64, s64, 32\n\t"
        "s_add_u32 s70, s80, s64\n\t"
        "v_mov_b32 %[vt], s65\n\t"
        "s_mov_b32 s66, s65\n\t"
        "s_setpc_b64 s[70:71]\n\t"
        ".p2align 6\n"
        ".LldL%=:\n\t"
        "s_waitcnt vmcnt(0)\n\t"
        "s_waitcnt vmcnt(4)\n\t"
        "s_waitcnt vmcnt(8)\n\t"
        "s_waitcnt vmcnt(12)\n\t"
        "s_waitcnt vmcnt(16)\n\t"
        "s_waitcnt vmcnt(20)\n\t"
        "s_waitcnt vmcnt(24)\n\t"
        "s_waitcnt vmcnt(28)\n\t"
        "s_waitcnt vmcnt(32)\n\t"
        "ds_write_b32 %[vland], %[vt]\n\t"
        "s_cmp_lg_u32 s65, %[fin]\n\t"
        "s_cbranch_scc1 .LldC%=\n"
        ".LldE%=:\n\t"
        "s_waitcnt vmcnt(0) lgkmcnt(0)\n\t"
        : [vf] "=&v"(vf), [vt] "=&v"(vt), [lc] "=&v"(lc), [l0] "+&v"(l0), [l1] "+&v"(l1), [o1] "+&v"(vo1), [o2] "+&v"(vo2), [o3] "+&v"(vo3)
        : [vperm] "v"(vperm), [a0] "v"(a0), [a1] "v"(a1), [o0] "v"(vo0), [vland] "v"(vland), [vfree] "v"(vfree),
          [kp] "s"(kp), [vp] "s"(vp), [nrec] "s"(kvbytes), [nt] "s"(NT), [ring] "s"(ring),
          [step] "s"(step), [due1] "s"(due0 + step + SOFT * 256), [duev] "s"(due0 + (to_sgpr(T16) >> 5) + (SOFT + GAP_TICKS) * 256), [lrows] "s"(lrows), [ntm] "s"(NT - 1), [fin] "s"(8 * NT), [u0] "n"(U0), [srt] "n"(CTL_SORT - CTL_LAND)
        : "memory", "scc", "vcc", "s60", "s61", "s62", "s63", "s64", "s65", "s66", "s67", "s68", "s69", "s70", "s71", "s72", "s73", "s74", "s75",
          "s76", "s77", "s78", "s79", "s80");
    asm volatile("s_endpgm" ::: "memory");
  }

  int lane = lane0;
  asm volatile("" : "+v"(lane));
  if (wv == 7) {
#pragma unroll 1
    for (int i = 0; i < 2; ++i) {
      sort_pages(lane, i ? bp : b, i ? npl : nown, LDS_SRT + i * 2048, i ? LDS_PLIST : LDS_SRT + 4096);
      if (i == 0) {
        const unsigned lo = *(LAS unsigned*)(lds + LDS_SRT + 4096 + (nown - 1) * 4) + 1;
        LGKM0();
        ctl[CTL_SORT / 4 + (lane & 1)] = lo;
      }
    }
  }
  if (wv < 4) {
    VMWAIT(0);
    ctl[CTL_QOK / 4 + (wv & 1)] = 1;
  }
  const int tot = total();
  const int w = wv - 2;
  const int r32 = lane & 31, hi = lane >> 5;
  const int nmine = (NT > w) ? (NT - w + NC - 1) / NC : 0;
  const unsigned aland = lbase + LDS_CTL + CTL_LAND + (lane & 1) * 4;
  const int tots = __builtin_amdgcn_readfirstlane(tot);
  const bool pf_on = npl * 256 * 64 > tots * PF_X64 && npl > nown && (h & 3) == 0;
  const unsigned pf_last = 2 * NT - to_sgpr((int)((float)(2 * NT * (PF_LEAD + 23 * NC)) * 4096.0f * __builtin_amdgcn_rcpf((float)(tot * PACE_X16))));
  const unsigned pf_ratio = to_sgpr((int)((float)NTL * 65536.0f * __builtin_amdgcn_rcpf((float)NT)));
  const unsigned pf_per = PF_PER + (g & 7) * PF_PSTEP, pf_inv = (65536 + pf_per - 1) / pf_per;
  auto prefetch = [&](unsigned op) {
    const unsigned d = pf_last - op;
    if (d >= 1024u) return;
    const unsigned r = d - ((d * pf_inv) >> 16) * pf_per;
    if (r >= PF_RUN) return;
    unsigned tl = 2 * NTL - 1 - NC - (((d - r) * pf_ratio) >> 16) - r;
    if (tl >= 2u * NTL) return;
    const bool isv = tl >= (unsigned)NTL;
    tl -= isv ? NTL : 0;
    const __amdgpu_buffer_rsrc_t rp = __builtin_amdgcn_make_buffer_rsrc(isv ? (void*)vp : (void*)kp, 0, (int)kvbytes, 0x00020000);
    unsigned ln = lane;
    asm volatile("" : "+v"(ln));
    const unsigned vo = (*(LAS unsigned*)(lds + LDS_PLIST + min(2 * tl + (ln >> 5), (unsigned)npl - 1) * 4) & 0x7fff0000u) + (h + 1) * (HD * 2) +
                        ((ln >> 1) & 15) * 4096 + (ln & 1) * 128;
    asm volatile("s_mov_b32 m0, %0\n\ts_nop 4\n\tbuffer_load_dword %2, %1, 0 offen lds" :: "s"(lbase + LDS_DUMMY), "s"(rp), "v"(vo) : "memory");
  };
  const int pos = (w + NC * 22 - NT) % NC;
  const unsigned palp = LDS_WREG + pos * W_SIZE + W_ALP + r32 * 4;
  const unsigned ppst = LDS_WREG + pos * W_SIZE + lane * 16;
  asm volatile("" ::: "v128", "v255");
  unsigned resc = 0;
  float m = -3.0e38f, mprev = -3.0e38f, l = 0.f;

  {
    const unsigned fk = (((unsigned)r32 & 3) << 2) | (((unsigned)r32 >> 2) & 3);
    const unsigned kaddr0 = LDS_RING + r32 * 256 + (((unsigned)hi ^ fk) << 4);
    bf16x8 qf[8];
    wait_above<1>(aland + (CTL_QOK - CTL_LAND), 0);
#pragma unroll
    for (int s = 0; s < 8; ++s) qf[s] = *(LAS bf16x8*)(lds + ((kaddr0 + LDS_Q - LDS_RING) ^ (s << 5)));
#pragma unroll 1
    for (int k = 0; k < nmine; ++k) {
      const unsigned t = w + NC * k;
      wait_above<1>(aland, 4 * t);
      bf16x8 kf[8];
#pragma unroll
      for (int s = 0; s < 8; ++s) kf[s] = *(LAS bf16x8*)(lds + ((kaddr0 + ((t & 7) << 13)) ^ (s << 5)));
      asm volatile("s_waitcnt lgkmcnt(0)" : "+v"(kf[0]), "+v"(kf[1]), "+v"(kf[2]), "+v"(kf[3]), "+v"(kf[4]), "+v"(kf[5]), "+v"(kf[6]), "+v"(kf[7]) :: "memory");
      ctl[CTL_FREE / 4 + (t & 7)] = t + 8;
      if (pf_on) prefetch(t);
      f32x16 sc;
#pragma unroll
      for (int r = 0; r < 16; ++r) sc[r] = 0.f;
#pragma unroll
      for (int s = 0; s < 8; ++s) sc = __builtin_amdgcn_mfma_f32_32x32x16_bf16(kf[s], qf[s], sc, 0, 0, 0);
      if ((int)t == NT - 1) {
        const int kb = 2 * t * PAGE + 4 * hi;
#pragma unroll
        for (int r = 0; r < 16; ++r) sc[r] = (kb + 8 * (r >> 2) + (r & 3) < len) ? sc[r] : -__builtin_inff();
      }
      float mx = sc[0];
#pragma unroll
      for (int r = 1; r < 16; ++r) mx = fmaxf(mx, sc[r]);
      mx = swap_max(mx);
      const float mn = fmaxf(m, mx);
      const float alpha = __builtin_amdgcn_exp2f((m - mn) * CS);
      mprev = m;
      m = mn;
      float ps = 0.f;
#pragma unroll
      for (int r = 0; r < 16; ++r) { sc[r] = __builtin_amdgcn_exp2f((sc[r] - mn) * CS); ps += sc[r]; }
      l = fmaf(l, alpha, ps);
      u32x4 pf[2];
#pragma unroll
      for (int s = 0; s < 2; ++s)
        pf[s] = u32x4{pack2(sc[8 * s], sc[8 * s + 1]), pack2(sc[8 * s + 2], sc[8 * s + 3]), pack2(sc[8 * s + 4], sc[8 * s + 5]), pack2(sc[8 * s + 6], sc[8 * s + 7])};
      if (__builtin_amdgcn_ballot_w64(alpha != 1.0f)) resc |= 1u << k;
      *(LAS float*)(lds + palp + k * 128) = alpha;
      if (k >= KREG || k == nmine - 1) {
        const unsigned dst = ppst + ((k == nmine - 1) ? W_LAST : (k - KREG) * 2048);
        *(LAS u32x4*)(lds + dst) = pf[0];
        *(LAS u32x4*)(lds + dst + 1024) = pf[1];
      } else {
        asm volatile("s_set_gpr_idx_on %8, gpr_idx(DST)\n\t"
                     "v_mov_b32 v128, %0\n\tv_mov_b32 v129, %1\n\tv_mov_b32 v130, %2\n\tv_mov_b32 v131, %3\n\t"
                     "v_mov_b32 v132, %4\n\tv_mov_b32 v133, %5\n\tv_mov_b32 v134, %6\n\tv_mov_b32 v135, %7\n\t"
                     "s_set_gpr_idx_off"
                     :: "v"(pf[0][0]), "v"(pf[0][1]), "v"(pf[0][2]), "v"(pf[0][3]), "v"(pf[1][0]), "v"(pf[1][1]), "v"(pf[1][2]), "v"(pf[1][3]), "s"(8 * k));
      }
    }
  }
  l = swap_sum(l);
  *(LAS f32x2*)(lds + LDS_ML + w * 256 + r32 * 8) = f32x2{m, l};
  LGKM0();
  ctl[CTL_MLOK / 4 + w] = 1;

  const unsigned tq = (lane & 15) >> 2, tp = lane & 3, tblk = (lane >> 4) & 1;
  const unsigned vaddr0 = LDS_RING + (4 * hi + tq) * 256 + ((tq ^ (pos & 3)) << 6) + ((2 * tblk + (tp >> 1)) << 4) + ((tp & 1) << 3);
  f32x16 o[4];
#pragma unroll
  for (int i = 0; i < 4; ++i)
#pragma unroll
    for (int r = 0; r < 16; ++r) o[i][r] = 0.f;
  auto weights = [&]() {
    wait_above<1>(lbase + LDS_CTL + CTL_MLOK + min(lane, NC - 1) * 4, 0);
    unsigned a = LDS_ML + r32 * 8;
    asm volatile("" : "+v"(a) :: "memory");
    float M = -3.0e38f, L = 0.f;
    f32x2 ml[NC];
#pragma unroll
    for (int i = 0; i < NC; ++i) { ml[i] = *(LAS f32x2*)(lds + a + i * 256); M = fmaxf(M, ml[i][0]); }
#pragma unroll
    for (int i = 0; i < NC; ++i) L = fmaf(__builtin_amdgcn_exp2f((ml[i][0] - M) * CS), ml[i][1], L);
    const float inv = 1.0f / L;
    *(LAS float*)(lds + (a >> 1) + (LDS_WT - LDS_ML / 2) + pos * 128) = __builtin_amdgcn_exp2f((mprev - M) * CS) * inv;
    const float ct = __builtin_amdgcn_exp2f((m - M) * CS) * inv;
#pragma unroll
    for (int s = 0; s < 2; ++s) {
      u32x4 x = *(LAS u32x4*)(lds + ppst + W_LAST + s * 1024);
#pragma unroll
      for (int e = 0; e < 4; ++e) x[e] = pack2(__uint_as_float(x[e] << 16) * ct, __uint_as_float(x[e] & 0xffff0000u) * ct);
      *(LAS u32x4*)(lds + ppst + W_LAST + s * 1024) = x;
    }
    LGKM0();
    ctl[CTL_WTOK / 4 + w] = 1;
  };
  resc &= ~1u;
  if (nmine <= 3) weights();
#pragma unroll 1
  for (int k = 0; k < nmine - 1; ++k) {
    if (k == 2) weights();
    if ((resc >> k) & 1) {
      const float alpha = *(LAS float*)(lds + palp + k * 128);
#pragma unroll
      for (int i = 0; i < 4; ++i)
#pragma unroll
        for (int r = 0; r < 16; ++r) o[i][r] *= alpha;
    }
    const unsigned u = NT + ((k == 0 && NT >= 3 * NC) ? pos : w + NC * k);
    u32x4 pf[2];
    if (k >= KREG) {
      pf[0] = *(LAS u32x4*)(lds + ppst + (k - KREG) * 2048);
      pf[1] = *(LAS u32x4*)(lds + ppst + (k - KREG) * 2048 + 1024);
    } else {
      asm volatile("s_set_gpr_idx_on %8, gpr_idx(SRC0)\n\t"
                   "v_mov_b32 %0, v128\n\tv_mov_b32 %1, v129\n\tv_mov_b32 %2, v130\n\tv_mov_b32 %3, v131\n\t"
                   "v_mov_b32 %4, v132\n\tv_mov_b32 %5, v133\n\tv_mov_b32 %6, v134\n\tv_mov_b32 %7, v135\n\t"
                   "s_set_gpr_idx_off\n\ts_nop 1"
                   : "=v"(pf[0][0]), "=v"(pf[0][1]), "=v"(pf[0][2]), "=v"(pf[0][3]), "=v"(pf[1][0]), "=v"(pf[1][1]), "=v"(pf[1][2]), "=v"(pf[1][3])
                   : "s"(8 * k));
    }
    asm volatile("" : "+v"(pf[0]), "+v"(pf[1]), "+v"(o[0]), "+v"(o[1]), "+v"(o[2]), "+v"(o[3]));
    if (k == nmine - 2) wait_above<1>(lbase + LDS_CTL + CTL_WTOK + min(lane, NC - 1) * 4, 0);
    wait_above<1>(aland, 4 * u);
    bf16x8 vf[4][2];
#pragma unroll
    for (int i = 0; i < 4; ++i) {
      const unsigned a = (vaddr0 + ((u & 7) << 13)) ^ (i << 6);
#pragma unroll
      for (int s = 0; s < 2; ++s) {
        const s16x4 lo = __builtin_amdgcn_ds_read_tr16_b64_v4i16((LAS s16x4*)(lds + a + 4096 * s));
        const s16x4 up = __builtin_amdgcn_ds_read_tr16_b64_v4i16((LAS s16x4*)(lds + a + 4096 * s + 2048));
        vf[i][s] = __builtin_bit_cast(bf16x8, __builtin_shufflevector(lo, up, 0, 1, 2, 3, 4, 5, 6, 7));
      }
    }
    asm volatile("s_waitcnt lgkmcnt(0)" : "+v"(vf[0][0]), "+v"(vf[0][1]), "+v"(vf[1][0]), "+v"(vf[1][1]), "+v"(vf[2][0]), "+v"(vf[2][1]), "+v"(vf[3][0]), "+v"(vf[3][1]) :: "memory");
    ctl[CTL_FREE / 4 + (u & 7)] = u + 8;
    if (pf_on) prefetch(u);
#pragma unroll
    for (int s = 0; s < 2; ++s)
#pragma unroll
      for (int i = 0; i < 4; ++i) o[i] = __builtin_amdgcn_mfma_f32_32x32x16_bf16(vf[i][s], __builtin_bit_cast(bf16x8, pf[s]), o[i], 0, 0, 0);
  }

  {
    unsigned l16 = lane * 16;
    asm volatile("" : "+v"(l16));
    const unsigned l4 = l16 >> 2, awt = LDS_WT + (l4 & 127);
    auto put = [&](unsigned dst, const f32x16& x) {
#pragma unroll
      for (int j = 0; j < 4; ++j) *(LAS f32x4*)(lds + dst + j * 1024) = f32x4{x[4 * j], x[4 * j + 1], x[4 * j + 2], x[4 * j + 3]};
    };
    float co[6];
    bf16x8 pl[NC][2];
    auto ld_p = [&](int p) {
      pl[p][0] = *(LAS bf16x8*)(lds + LDS_WREG + p * W_SIZE + W_LAST + l16);
      pl[p][1] = *(LAS bf16x8*)(lds + LDS_WREG + p * W_SIZE + W_LAST + l16 + 1024);
    };
#pragma unroll
    for (int i = 0; i < 4; ++i) co[i] = *(LAS float*)(lds + awt + ((pos & 3) ^ i) * 128);
    co[4] = *(LAS float*)(lds + awt + 4 * 128);
    co[5] = *(LAS float*)(lds + awt + 5 * 128);
    ld_p(0); ld_p(1);
    asm volatile("" : "+v"(l16) :: "memory");
    if (nmine > 1) {
      if (pos > 3) put(LDS_RING + ((2 * NT - 2 * NC + pos) & 7) * 8192 + l16, o[0]);
#pragma unroll
      for (int i = 1; i < 4; ++i) put(LDS_WREG + pos * W_SIZE + (i - 1) * 4096 + l16, o[i]);
      LGKM0();
      ctl[CTL_ODONE / 4 + pos] = 1;
    }
    if (pos < 4) {
      f32x16 of = o[0], ot;
#pragma unroll
      for (int r = 0; r < 16; ++r) ot[r] = 0.f;
      auto src_o = [&](int p) -> unsigned {
        const int i = pos ^ (p & 3);
        return l16 + (i == 0 ? LDS_RING + ((2 * NT - 2 * NC + p) & 7) * 8192 : LDS_WREG + p * W_SIZE + (i - 1) * 4096);
      };
      auto fma_o = [&](unsigned src, float c) {
#pragma unroll
        for (int j = 0; j < 4; ++j) {
          const f32x4 x = *(LAS f32x4*)(lds + src + j * 1024);
#pragma unroll
          for (int e = 0; e < 4; ++e) of[4 * j + e] = fmaf(x[e], c, of[4 * j + e]);
        }
      };
      auto mma_t = [&](int p, bf16x8 p0, bf16x8 p1) {
        const unsigned a = vaddr0 + (((2 * NT - NC + p) & 7) << 13);
        bf16x8 vf[2];
#pragma unroll
        for (int s = 0; s < 2; ++s) {
          const s16x4 lo = __builtin_amdgcn_ds_read_tr16_b64_v4i16((LAS s16x4*)(lds + a + 4096 * s));
          const s16x4 up = __builtin_amdgcn_ds_read_tr16_b64_v4i16((LAS s16x4*)(lds + a + 4096 * s + 2048));
          vf[s] = __builtin_bit_cast(bf16x8, __builtin_shufflevector(lo, up, 0, 1, 2, 3, 4, 5, 6, 7));
        }
        ot = __builtin_amdgcn_mfma_f32_32x32x16_bf16(vf[0], p0, ot, 0, 0, 0);
        ot = __builtin_amdgcn_mfma_f32_32x32x16_bf16(vf[1], p1, ot, 0, 0, 0);
      };
      if (NT >= 2 * NC) {
#define PIN_OF() asm volatile("" : "+v"(of), "+v"(ot))
#pragma unroll
        for (int r = 0; r < 16; ++r) of[r] *= co[0];
        const bool wl = (l4 & 24) == 0;
        const unsigned ua = 4 * (2 * NT - NC);
        wait_above_v(wl ? aland : lbase + LDS_CTL + CTL_ODONE + (l4 & 12), wl ? ua : 0u);
        mma_t(0, pl[0][0], pl[0][1]);
#pragma unroll
        for (int i = 1; i < 4; ++i) fma_o(LDS_WREG + (pos ^ i) * W_SIZE + (i - 1) * 4096 + l16, co[i]);
        PIN_OF();
        ld_p(2); ld_p(3); ld_p(4); ld_p(5);
        wait_above_v(wl ? aland : lbase + LDS_CTL + CTL_ODONE + 16 + (l4 & 4), wl ? ua + 8 : 0u);
        mma_t(1, pl[1][0], pl[1][1]);
        mma_t(2, pl[2][0], pl[2][1]);
        fma_o(src_o(4), co[4]);
        fma_o(src_o(5), co[5]);
#pragma unroll
        for (int r = 0; r < 16; ++r) { ot[r] += of[r]; of[r] = 0.f; }
        asm volatile("" : "+v"(ot));
        wait_above<0>(aland, ua + 20);
        mma_t(3, pl[3][0], pl[3][1]);
        mma_t(4, pl[4][0], pl[4][1]);
        mma_t(5, pl[5][0], pl[5][1]);
        of = ot;
      } else {
        wait_above<1>(lbase + LDS_CTL + CTL_WTOK + min(l4, (NC - 1) * 4u), 0);
        const float cw = *(LAS float*)(lds + awt + pos * 128);
#pragma unroll
        for (int r = 0; r < 16; ++r) of[r] *= cw;
#pragma unroll 1
        for (int p = max(NC - NT, 0); p < NC; ++p) {
          if (p != pos && NT - 2 * NC + p >= 0) {
            wait_above<1>(lbase + LDS_CTL + CTL_ODONE + p * 4, 0);
            fma_o(src_o(p), *(LAS float*)(lds + awt + p * 128));
          }
          wait_above<1>(aland, 4 * (2 * NT - NC + p));
          mma_t(p, *(LAS bf16x8*)(lds + LDS_WREG + p * W_SIZE + W_LAST + l16), *(LAS bf16x8*)(lds + LDS_WREG + p * W_SIZE + W_LAST + l16 + 1024));
        }
#pragma unroll
        for (int r = 0; r < 16; ++r) of[r] += ot[r];
      }
      const __amdgpu_buffer_rsrc_t ro = __builtin_amdgcn_make_buffer_rsrc(outp, 0, NB * HQ * SQ * HD * 2, 0x00020000);
      const unsigned orow = (unsigned)(b * HQ + 2 * h) * SQ * HD * 2 + pos * 64 + ((l16 & 511) << 4) + ((l16 >> 5) & 16);
#pragma unroll
      for (int j = 0; j < 2; ++j) {
        const unsigned a0 = pack2(of[8 * j], of[8 * j + 1]), a1 = pack2(of[8 * j + 2], of[8 * j + 3]);
        const unsigned b0 = pack2(of[8 * j + 4], of[8 * j + 5]), b1 = pack2(of[8 * j + 6], of[8 * j + 7]);
        auto s0 = __builtin_amdgcn_permlane32_swap(a0, b0, false, false);
        auto s1 = __builtin_amdgcn_permlane32_swap(a1, b1, false, false);
        u32x4 val = {s0[0], s1[0], s0[1], s1[1]};
        __builtin_amdgcn_raw_buffer_store_b128(val, ro, orow + j * 32, 0, 0);
      }
    }
  }
}
}

extern "C" void kernel_launch(void* const* d_in, const int* in_sizes, int n_in, void* d_out, int out_size, void* d_ws,
                              size_t ws_size, hipStream_t stream) {
  (void)n_in; (void)out_size; (void)d_ws; (void)ws_size;
  const unsigned nphys = (unsigned)(in_sizes[1] / (PAGE * HKV * HD));
  const unsigned kvbytes = nphys * PAGE_BYTES;
  (void)hipFuncSetAttribute((const void*)paged_attn, hipFuncAttributeMaxDynamicSharedMemorySize, LDS_TOTAL);
  hipLaunchKernelGGL(paged_attn, dim3(256), dim3(512), LDS_TOTAL, stream, d_in[0], d_in[1], d_in[2], (const int*)d_in[3],
                     (const int*)d_in[4], d_out, kvbytes, nphys);
}
```

```cpp
#define PACE_X16 431
#include <hip/hip_runtime.h>
#include <hip/hip_bf16.h>

#define SOFT 15
#define GAP_TICKS 10
#define WIN_TICKS 87
#define CODEPF 11520
#define PF_LEAD 180
#define PF_X64 72
#define PF_PER 4
#define PF_PSTEP 2
#define PF_RUN 3

namespace {
constexpr int NB = 16, HQ = 32, HKV = 16, SQ = 16, HD = 128, PAGE = 16, NBPS = 256, SKV = NBPS * PAGE;
constexpr unsigned PAGE_BYTES = PAGE * HKV * HD * 2;
constexpr unsigned TLBG = 0x200000u;
constexpr int U0 = 4;
constexpr int NC = 6;
constexpr int KREG = 16, KLDS = 5;
static_assert(NC * (KREG + KLDS + 1) >= NBPS / 2, "P storage");
constexpr unsigned LDS_RING = 0;
constexpr unsigned LDS_WREG = 65536;
constexpr unsigned W_ALP = KLDS * 2048;
constexpr unsigned W_LAST = W_ALP + (KREG + KLDS + 1) * 128;
constexpr unsigned W_SIZE = W_LAST + 2048;
static_assert(W_LAST >= 3 * 4096 && W_SIZE % 256 == 0, "consumer region");
constexpr unsigned LDS_TBL = LDS_WREG + W_SIZE;
constexpr unsigned LDS_SRT = LDS_WREG + 3 * W_SIZE;
constexpr unsigned LDS_Q = LDS_WREG;
constexpr unsigned LDS_ML = LDS_WREG + NC * W_SIZE;
constexpr unsigned LDS_WT = LDS_ML + NC * 256;
constexpr unsigned LDS_CTL = LDS_WT + NC * 128;
constexpr unsigned CTL_LAND = 0;
constexpr unsigned CTL_QOK = 16;
constexpr unsigned CTL_SORT = 24;
constexpr unsigned CTL_FREE = 32;
constexpr unsigned CTL_MLOK = 64;
constexpr unsigned CTL_ODONE = 96;
constexpr unsigned CTL_WTOK = 128;
constexpr unsigned LDS_DUMMY = LDS_CTL + 256;
constexpr unsigned LDS_PLIST = LDS_DUMMY + 256;
constexpr unsigned LDS_TOTAL = LDS_PLIST + 1024;
static_assert(LDS_TOTAL <= 163840, "lds");

typedef __attribute__((ext_vector_type(8))) __bf16 bf16x8;
typedef __attribute__((ext_vector_type(2))) __bf16 bf16x2;
typedef __attribute__((ext_vector_type(4))) short s16x4;
typedef __attribute__((ext_vector_type(16))) float f32x16;
typedef __attribute__((ext_vector_type(4))) float f32x4;
typedef __attribute__((ext_vector_type(2))) float f32x2;
typedef __attribute__((ext_vector_type(4))) unsigned u32x4;
typedef __attribute__((ext_vector_type(2))) unsigned u32x2;
#define LAS __attribute__((address_space(3)))

__device__ __forceinline__ float swap_max(float x) {
  auto r = __builtin_amdgcn_permlane32_swap(__float_as_uint(x), __float_as_uint(x), false, false);
  return fmaxf(__uint_as_float(r[0]), __uint_as_float(r[1]));
}
__device__ __forceinline__ float swap_sum(float x) {
  auto r = __builtin_amdgcn_permlane32_swap(__float_as_uint(x), __float_as_uint(x), false, false);
  return __uint_as_float(r[0]) + __uint_as_float(r[1]);
}
__device__ __forceinline__ unsigned pack2(float a, float b) {
  bf16x2 v = {(__bf16)a, (__bf16)b};
  return __builtin_bit_cast(unsigned, v);
}
template <int CTRL> __device__ __forceinline__ int dpp_add(int x) { return x + __builtin_amdgcn_update_dpp(0, x, CTRL, 0xf, 0xf, true); }
__device__ __forceinline__ unsigned now32() { return (unsigned)__builtin_amdgcn_s_memrealtime(); }
__device__ __forceinline__ int to_sgpr(int v) {
  int r;
  asm volatile("s_nop 1\n\tv_readfirstlane_b32 %0, %1" : "=s"(r) : "v"(v));
  return r;
}
#define LGKM0() asm volatile("s_waitcnt lgkmcnt(0)" ::: "memory")
#define VMWAIT(n) asm volatile("s_waitcnt vmcnt(" #n ")" ::: "memory")
#define CBAR() asm volatile("" ::: "memory")
template <int SLEEP> __device__ __forceinline__ void wait_above(unsigned addr, unsigned u) {
  unsigned v, cnt;
  if (SLEEP)
    asm volatile("s_mov_b32 %1, 0x800000\n\ts_branch 2f\n"
                 "1:\n\ts_sleep %4\n"
                 "2:\n\tds_read_b32 %0, %2\n\ts_sub_u32 %1, %1, 1\n\ts_waitcnt lgkmcnt(0)\n\t"
                 "v_cmp_ge_u32 vcc, %3, %0\n\ts_cbranch_scc1 3f\n\ts_nop 1\n\ts_cbranch_vccnz 1b\n3:"
                 : "=&v"(v), "=&s"(cnt) : "v"(addr), "s"(u), "n"(SLEEP) : "memory", "scc", "vcc");
  else
    asm volatile("s_mov_b32 %1, 0x800000\n"
                 "1:\n\tds_read_b32 %0, %2\n\ts_sub_u32 %1, %1, 1\n\ts_waitcnt lgkmcnt(0)\n\t"
                 "v_cmp_ge_u32 vcc, %3, %0\n\ts_cbranch_scc1 3f\n\ts_nop 1\n\ts_cbranch_vccnz 1b\n3:"
                 : "=&v"(v), "=&s"(cnt) : "v"(addr), "s"(u) : "memory", "scc", "vcc");
}

__device__ __forceinline__ void wait_above_v(unsigned addr, unsigned u) {
  unsigned v, cnt;
  asm volatile("s_mov_b32 %1, 0x800000\n"
               "1:\n\tds_read_b32 %0, %2\n\ts_sub_u32 %1, %1, 1\n\ts_waitcnt lgkmcnt(0)\n\t"
               "v_cmp_ge_u32 vcc, %3, %0\n\ts_cbranch_scc1 3f\n\ts_nop 1\n\ts_cbranch_vccnz 1b\n3:"
               : "=&v"(v), "=&s"(cnt) : "v"(addr), "v"(u) : "memory", "scc", "vcc");
}

__global__ void __launch_bounds__(512, 1) __attribute__((amdgpu_num_vgpr(64)))
paged_attn(const void* __restrict__ qp, const void* __restrict__ kp, const void* __restrict__ vp, const int* __restrict__ bt,
           const int* __restrict__ lens, void* __restrict__ outp, unsigned kvbytes, unsigned nphys) {
  extern __shared__ __attribute__((aligned(16))) char smem[];
  LAS char* lds = (LAS char*)smem;
  const int lane0 = threadIdx.x & 63;
  const int wv = __builtin_amdgcn_readfirstlane(threadIdx.x >> 6);
  const int g = blockIdx.x;
  const int h = g >> 4;
  constexpr float CS = 0.08838834764831845f * 1.4426950408889634f;
  volatile LAS unsigned* ctl = (volatile LAS unsigned*)(lds + LDS_CTL);

  const unsigned lbase = (unsigned)(size_t)lds;

  unsigned lraw, first;
  u32x4 lq;
  {
    asm volatile("global_load_dword %0, %3, %4\n\t"
                 "global_load_dwordx4 %1, %5, %4\n\t"
                 "global_load_dword %2, %6, %7"
                 : "=&v"(lraw), "=&v"(lq), "=&v"(first)
                 : "v"((lane0 & 15) * 4), "s"(lens), "v"(lane0 & 48), "v"((lane0 & 15) * (wv < 2 ? NBPS * 4 : 0) + (lane0 >> 4) * 8 + (wv & 1) * 4), "s"(bt),
                   "s"(qp), "s"(kp), "s"(vp), "s"(outp), "s"(kvbytes), "s"(nphys)
                 : "memory");
    __builtin_amdgcn_sched_barrier(0);
  }
  if (wv >= 4) {
    if (wv == 7) {
      const __amdgpu_buffer_rsrc_t rb = __builtin_amdgcn_make_buffer_rsrc((void*)bt, 0, NB * NBPS * 4, 0x00020000);
#define TBL4(SO) "buffer_load_dwordx4 %2, %1, " SO " offen lds\n\tbuffer_load_dwordx4 %2, %1, " SO " offen offset:1024 lds\n\t" \
                 "buffer_load_dwordx4 %2, %1, " SO " offen offset:2048 lds\n\tbuffer_load_dwordx4 %2, %1, " SO " offen offset:3072 lds\n\t"
      asm volatile("s_mov_b32 m0, %0\n\ts_nop 0\n\t" TBL4("0") "s_add_u32 m0, m0, 4096\n\ts_nop 0\n\t" TBL4("%3")
                   "s_add_u32 m0, m0, %6\n\ts_nop 0\n\t" TBL4("%4") "s_add_u32 m0, m0, 4096\n\ts_nop 0\n\t" TBL4("%5")
                   :: "s"(lbase + LDS_TBL), "s"(rb), "v"(lane0 * 16), "s"(4096), "s"(8192), "s"(12288), "n"(W_SIZE - 4096) : "memory", "scc");
    }
    unsigned long long pc;
    asm volatile("s_getpc_b64 %0" : "=s"(pc));
    const __amdgpu_buffer_rsrc_t rt = __builtin_amdgcn_make_buffer_rsrc(
        wv == 5 ? (void*)pc : wv == 4 ? (void*)vp : (void*)kp, 0, wv == 5 ? CODEPF : (wv != 7 && (g & 8)) ? (int)kvbytes : 0, 0x00020000);
    const unsigned mul = wv == 5 ? 512u : 16 * TLBG + 4224u, add = wv == 5 ? ((g >> 3) & 3) * 128 : (g >> 4) * (TLBG + 33024u);
    asm volatile("s_mov_b32 m0, %0\n\ts_nop 0\n\tbuffer_load_dword %1, %2, 0 offen lds"
                 :: "s"(lbase + LDS_DUMMY), "v"((lane0 & 31) * mul + add), "s"(rt) : "memory");
    *(LAS u32x4*)(lds + LDS_SRT + (threadIdx.x - 256) * 16) = u32x4{0u, 0u, 0u, 0u};
    if (threadIdx.x < 296) ctl[lane0] = ((lane0 >> 3) == CTL_FREE / 32) ? (lane0 & 7) : 0u;
    asm volatile("s_waitcnt lgkmcnt(0)\n\ts_barrier" ::: "memory");
  }
  if (wv < 2) __builtin_amdgcn_s_setprio(3);
  const unsigned rstride = wv < 2 ? 4096 : 256;
  unsigned vo0 = (lane0 >> 4) * rstride + (((unsigned)(lane0 & 15) ^ ((lane0 >> 4) << 2)) << 4) + (wv < 2 ? h * (HD * 2) : 0);
  unsigned vo1 = (vo0 + 4 * rstride - 1024) ^ 16, vo2 = (vo0 + 8 * rstride - 2048) ^ 32, vo3 = (vo0 + 12 * rstride - 3072) ^ 48;
  const unsigned want = (g & 8) ? 15 - (g & 7) : (g & 7);
  const unsigned dst0 = lbase + (wv < 2 ? LDS_RING : LDS_Q) + (wv & 1) * 4096;
  asm volatile("s_cmp_ge_u32 %8, 4\n\ts_cbranch_scc1 1f\n\ts_waitcnt vmcnt(0)\n1:\n\ts_waitcnt vmcnt(1)" : "+v"(lraw), "+v"(lq), "+v"(first), "+v"(vo0), "+v"(vo1), "+v"(vo2), "+v"(vo3) : "s"(dst0), "s"(wv) : "memory", "scc");
  unsigned lm1 = min(lraw - 1u, (unsigned)SKV - 1u);
  unsigned rank = 0;
  {
    const unsigned key = lm1 * 16 + (lane0 & 15);
#pragma unroll
    for (int j = 0; j < 4; ++j) rank += (min(lq[j] - 1u, (unsigned)SKV - 1u) * 16 + ((lane0 >> 4) * 4 + j) > key) ? 1u : 0u;
    auto r1 = __builtin_amdgcn_permlane16_swap(rank, rank, false, false);
    rank = r1[0] + r1[1];
    auto r2 = __builtin_amdgcn_permlane32_swap(rank, rank, false, false);
    rank = r2[0] + r2[1];
  }
  first = min(first, nphys - 1) * PAGE_BYTES;
  const int b = __builtin_ctz((unsigned)__builtin_amdgcn_ballot_w64(rank == want) | 0x8000u);
  unsigned long long tstart = 0;
  if (wv < 4) {
    const bool qok = (int)__builtin_amdgcn_readlane(lraw, b) > 0;
    const __amdgpu_buffer_rsrc_t r0 = __builtin_amdgcn_make_buffer_rsrc(wv < 2 ? (void*)kp : (void*)qp, 0,
                                                                        wv < 2 ? (int)kvbytes : qok ? NB * HQ * SQ * HD * 2 : 0, 0x00020000);
    const unsigned so = wv < 2 ? __builtin_amdgcn_readlane(first, b) : qok ? (unsigned)((b * HQ + 2 * h) * SQ * HD * 2) + (wv & 1) * 4096 : 0u;
    asm volatile("s_waitcnt vmcnt(0)\n\t"
                 "s_mov_b32 m0, %2\n\t"
                 "s_memrealtime %0\n\t"
                 "s_nop 2\n\t"
                 "buffer_load_dwordx4 %4, %1, %3 offen nt lds\n\t"
                 "buffer_load_dwordx4 %5, %1, %3 offen offset:1024 nt lds\n\t"
                 "buffer_load_dwordx4 %6, %1, %3 offen offset:2048 nt lds\n\t"
                 "buffer_load_dwordx4 %7, %1, %3 offen offset:3072 nt lds\n\t"
                 "s_waitcnt lgkmcnt(0)\n\t"
                 "s_barrier"
                 : "=&s"(tstart) : "s"(r0), "s"(dst0), "s"(so), "v"(vo0), "v"(vo1), "v"(vo2), "v"(vo3) : "memory");
  }
  asm volatile("" : "+v"(lm1), "+v"(rank), "+v"(first) :: "memory");
  __builtin_amdgcn_sched_barrier(0);
  const int bp = __builtin_ctz((unsigned)__builtin_amdgcn_ballot_w64(rank == 15 - want) | 0x8000u);
  const int len = __builtin_amdgcn_readlane(lm1, b) + 1, nown = (len + PAGE - 1) / PAGE, NT = (nown + 1) >> 1;
  const int npl = (__builtin_amdgcn_readlane(lm1, bp) >> 4) + 1, NTL = (npl + 1) >> 1;
  auto total = [&]() __attribute__((always_inline)) {
    int t = lm1 + 1;
    t = dpp_add<0x121>(t); t = dpp_add<0x122>(t); t = dpp_add<0x124>(t); t = dpp_add<0x128>(t);
    return t;
  };

  auto sort_pages = [&](int lane, int bb, int n, unsigned scr, unsigned dst) __attribute__((always_inline)) {
    u32x4 e = *(LAS u32x4*)(lds + LDS_TBL + (bb >> 3) * W_SIZE + (bb & 7) * 1024 + lane * 16);
    bool in[4];
#pragma unroll
    for (int j = 0; j < 4; ++j) {
      e[j] = min(e[j], nphys - 1);
      in[j] = 4 * lane + j >= 2 * U0 && 4 * lane + j < n - 1;
      __hip_atomic_fetch_or((LAS unsigned*)(lds + scr + ((e[j] >> 5) & 255) * 8), in[j] ? 1u << (e[j] & 31) : 0u, __ATOMIC_RELAXED, __HIP_MEMORY_SCOPE_WAVEFRONT);
    }
    CBAR();
    u32x4 a = *(LAS u32x4*)(lds + scr + lane * 32), c = *(LAS u32x4*)(lds + scr + lane * 32 + 16);
    const int n0 = __builtin_popcount(a[0]), n1 = n0 + __builtin_popcount(a[2]), n2 = n1 + __builtin_popcount(c[0]), n3 = n2 + __builtin_popcount(c[2]);
    int sc = n3;
    sc = dpp_add<0x111>(sc); sc = dpp_add<0x112>(sc); sc = dpp_add<0x114>(sc); sc = dpp_add<0x118>(sc);
    sc += __builtin_amdgcn_update_dpp(0, sc, 0x142, 0xa, 0xf, false);
    sc += __builtin_amdgcn_update_dpp(0, sc, 0x143, 0xc, 0xf, false);
    const bool ok = nphys <= 8192u && __builtin_amdgcn_readlane(sc, 63) == n - 1 - 2 * U0;
    sc += 2 * U0 - n3;
    a[1] = sc; a[3] = sc + n0; c[1] = sc + n1; c[3] = sc + n2;
    *(LAS u32x4*)(lds + scr + lane * 32) = a;
    *(LAS u32x4*)(lds + scr + lane * 32 + 16) = c;
    CBAR();
    u32x2 wp[4];
#pragma unroll
    for (int j = 0; j < 4; ++j) wp[j] = *(LAS u32x2*)(lds + scr + ((e[j] >> 5) & 255) * 8);
#pragma unroll
    for (int j = 0; j < 4; ++j) {
      const unsigned r = wp[j][1] + __builtin_popcount(wp[j][0] & ((1u << (e[j] & 31)) - 1u));
      *(LAS unsigned*)(lds + dst + ((ok && in[j]) ? r : 4 * lane + j) * 4) = e[j] * PAGE_BYTES;
    }
    CBAR();
  };

  if (__builtin_expect(wv < 2, 1)) {
    const int half = wv, lane = lane0;
    const unsigned vland = lbase + LDS_CTL + CTL_LAND + half * 4;
    const unsigned vfree = lbase + LDS_CTL + CTL_FREE + (lane & 7) * 4;
    const unsigned ring = dst0;
    unsigned l0 = __builtin_amdgcn_ds_bpermute((b + 16 * lane) * 4, first), l1 = 0;
    const unsigned a0 = lbase + LDS_SRT + 4096 + min(2 * lane + half, nown - 1) * 4, a1 = lbase + LDS_SRT + 4096 + min(2 * lane + half + 128, nown - 1) * 4;
    const int rot = (NT >= 3 * NC) ? NT % NC : 0;
    const unsigned vperm = (lane < NC ? (lane + rot >= NC ? lane + rot - NC : lane + rot) : lane) * 4;
    const int T16 = total() * PACE_X16;
    const int step = to_sgpr((int)((float)T16 * __builtin_amdgcn_rcpf((float)max(len, 32))));
    const int win = to_sgpr((h & 3) == 1 ? 64 : (int)((float)(WIN_TICKS * 1024) * __builtin_amdgcn_rcpf((float)step) + 0.5f));
    const unsigned due0 = ((unsigned)tstart << 8) + half * (step >> 1);
    const int lrows = len - (2 * (NT - 1) + half) * PAGE;
    unsigned vf, vt, lc;
    asm volatile(
        "s_waitcnt lgkmcnt(0)\n\t"
        "s_memrealtime s[68:69]\n\t"
        "ds_read_b32 %[vf], %[vfree]\n\t"
        "s_mov_b32 s60, 1\n\t"
        "s_mov_b32 s61, 4\n\t"
        "s_mov_b32 s62, 0x1000000\n\t"
        "s_mov_b32 s66, 0\n\t"
        "s_min_u32 s67, %[nt], %[u0]\n\t"
        "s_mov_b32 s75, %[nrec]\n\t"
        "s_mov_b32 s72, 0x2000\n\t"
        "s_mov_b32 s73, 1\n\t"
        "s_mov_b32 s74, %[due1]\n\t"
        "s_mov_b64 s[76:77], %[kp]\n\t"
        "s_mov_b32 s78, %[nrec]\n\t"
        "s_mov_b32 s79, 0x20000\n\t"
        "s_and_b32 s77, s77, 0xffff\n\t"
        "v_mov_b32 %[lc], %[l0]\n\t"
        "s_getpc_b64 s[70:71]\n"
        ".LldA%=:\n\t"
        "s_add_u32 s80, s70, .LldL%=-.LldA%=\n\t"
        "s_addc_u32 s71, s71, 0\n\t"
        "s_branch .LldF%=\n"
        ".LldR%=:\n\t"
        "s_sub_u32 s62, s62, 1\n\t"
        "s_cbranch_scc1 .LldE%=\n"
        ".LldT%=:\n\t"
        "s_getreg_b32 s64, hwreg(HW_REG_IB_STS)\n\t"
        "s_and_b32 s65, s64, 15\n\t"
        "s_bfe_u32 s64, s64, 0x20016\n\t"
        "s_lshl4_add_u32 s81, s64, s65\n\t"
        "s_add_u32 s64, s81, 3\n\t"
        "s_and_b32 s64, s64, 60\n\t"
        "s_sub_u32 s65, s61, s64\n\t"
        "s_cmp_lg_u32 s65, s66\n\t"
        "s_cbranch_scc1 .LldP%=\n"
        ".LldC%=:\n\t"
        "s_waitcnt lgkmcnt(0)\n\t"
        "v_cmp_eq_u32 vcc, s60, %[vf]\n\t"
        "s_lshl_b32 s64, s68, 8\n\t"
        "s_memrealtime s[68:69]\n\t"
        "ds_read_b32 %[vf], %[vfree]\n\t"
        "s_sub_u32 s64, s64, s74\n\t"
        "s_cmp_lt_i32 s64, 0\n\t"
        "s_cbranch_scc1 .LldR%=\n\t"
        "s_cbranch_vccz .LldR%=\n\t"
        "s_cmp_gt_u32 s81, %[win]\n\t"
        "s_cbranch_scc1 .LldR%=\n\t"
        "buffer_load_dwordx4 %[o0], s[76:79], s63 offen nt lds\n\t"
        "s_add_u32 s74, s74, %[step]\n\t"
        "s_add_u32 s73, s73, 1\n\t"
        "buffer_load_dwordx4 %[o1], s[76:79], s63 offen offset:1024 nt lds\n\t"
        "s_add_u32 s60, s60, 1\n\t"
        "s_add_u32 s61, s61, 4\n\t"
        "buffer_load_dwordx4 %[o2], s[76:79], s63 offen offset:2048 nt lds\n\t"
        "s_add_u32 s72, s72, 0x2000\n\t"
        "s_and_b32 s72, s72, 0xe000\n\t"
        "buffer_load_dwordx4 %[o3], s[76:79], s63 offen offset:3072 nt lds\n"
        ".LldF%=:\n\t"
        "v_readlane_b32 s63, %[lc], s73\n\t"
        "s_add_u32 m0, s72, %[ring]\n\t"
        "s_cmp_eq_u32 s73, %[ntm]\n\t"
        "s_cselect_b32 s78, s75, %[nrec]\n\t"
        "s_cmp_lg_u32 s60, s67\n\t"
        "s_cbranch_scc1 .LldT%=\n\t"
        "s_cmp_lg_u32 s60, %[u0]\n\t"
        "s_cbranch_scc1 .LldG%=\n\t"
        "s_cmp_le_u32 %[nt], %[u0]\n\t"
        "s_cbranch_scc1 .LldG%=\n"
        ".LldS%=:\n\t"
        "ds_read_b32 %[lc], %[vland] offset:%[srt]\n\t"
        "ds_read_b32 %[l0], %[a0]\n\t"
        "ds_read_b32 %[l1], %[a1]\n\t"
        "s_sub_u32 s62, s62, 1\n\t"
        "s_cbranch_scc1 .LldE%=\n\t"
        "s_waitcnt lgkmcnt(0)\n\t"
        "v_readfirstlane_b32 s64, %[lc]\n\t"
        "s_cmp_eq_u32 s64, 0\n\t"
        "s_cbranch_scc1 .LldS%=\n\t"
        "s_max_i32 s65, %[lrows], 0\n\t"
        "s_lshl_b32 s65, s65, 12\n\t"
        "s_add_u32 s64, s64, s65\n\t"
        "s_sub_u32 s64, s64, 1\n\t"
        "s_cmp_lt_i32 %[lrows], 16\n\t"
        "s_cselect_b32 s75, s64, %[nrec]\n\t"
        "s_min_u32 s67, %[nt], 64\n\t"
        "v_mov_b32 %[lc], %[l0]\n\t"
        "s_branch .LldF%=\n"
        ".LldG%=:\n\t"
        "s_cmp_eq_u32 s73, %[nt]\n\t"
        "s_cbranch_scc1 .LldV%=\n\t"
        "v_mov_b32 %[lc], %[l1]\n\t"
        "v_readfirstlane_b32 s63, %[l1]\n\t"
        "s_add_u32 s67, s60, %[nt]\n\t"
        "s_sub_u32 s67, s67, 64\n\t"
        "s_branch .LldT%=\n"
        ".LldV%=:\n\t"
        "s_cmp_lg_u32 s60, %[nt]\n\t"
        "s_cbranch_scc1 .LldD%=\n\t"
        "s_mov_b64 s[76:77], %[vp]\n\t"
        "s_and_b32 s77, s77, 0xffff\n\t"
        "s_mov_b32 s73, 0\n\t"
        "s_min_u32 s67, %[nt], 64\n\t"
        "s_add_u32 s67, s67, s60\n\t"
        "s_mov_b32 s74, %[duev]\n\t"
        "s_cmp_eq_u32 %[ntm], 0\n\t"
        "s_cselect_b32 s78, s75, %[nrec]\n\t"
        "ds_bpermute_b32 %[lc], %[vperm], %[l0]\n\t"
        "v_xor_b32 %[o1], 16, %[o1]\n\t"
        "v_xor_b32 %[o2], 32, %[o2]\n\t"
        "v_xor_b32 %[o3], 48, %[o3]\n\t"
        "s_waitcnt lgkmcnt(0)\n\t"
        "v_readfirstlane_b32 s63, %[lc]\n\t"
        "s_branch .LldT%=\n"
        ".LldD%=:\n\t"
        "s_add_u32 s74, s74, 0x40000000\n\t"
        "s_mov_b32 s60, -1\n\t"
        "s_branch .LldT%=\n"
        ".LldP%=:\n\t"
        "s_min_u32 s64, s64, 32\n\t"
        "s_add_u32 s70, s80, s64\n\t"
        "v_mov_b32 %[vt], s65\n\t"
        "s_mov_b32 s66, s65\n\t"
        "s_setpc_b64 s[70:71]\n\t"
        ".p2align 6\n"
        ".LldL%=:\n\t"
        "s_waitcnt vmcnt(0)\n\t"
        "s_waitcnt vmcnt(4)\n\t"
        "s_waitcnt vmcnt(8)\n\t"
        "s_waitcnt vmcnt(12)\n\t"
        "s_waitcnt vmcnt(16)\n\t"
        "s_waitcnt vmcnt(20)\n\t"
        "s_waitcnt vmcnt(24)\n\t"
        "s_waitcnt vmcnt(28)\n\t"
        "s_waitcnt vmcnt(32)\n\t"
        "ds_write_b32 %[vland], %[vt]\n\t"
        "s_cmp_lg_u32 s65, %[fin]\n\t"
        "s_cbranch_scc1 .LldC%=\n"
        ".LldE%=:\n\t"
        "s_waitcnt vmcnt(0) lgkmcnt(0)\n\t"
        : [vf] "=&v"(vf), [vt] "=&v"(vt), [lc] "=&v"(lc), [l0] "+&v"(l0), [l1] "+&v"(l1), [o1] "+&v"(vo1), [o2] "+&v"(vo2), [o3] "+&v"(vo3)
        : [vperm] "v"(vperm), [a0] "v"(a0), [a1] "v"(a1), [o0] "v"(vo0), [vland] "v"(vland), [vfree] "v"(vfree),
          [kp] "s"(kp), [vp] "s"(vp), [nrec] "s"(kvbytes), [nt] "s"(NT), [ring] "s"(ring),
          [step] "s"(step), [due1] "s"(due0 + step + SOFT * 256), [duev] "s"(due0 + (to_sgpr(T16) >> 5) + (SOFT + GAP_TICKS) * 256), [lrows] "s"(lrows), [ntm] "s"(NT - 1), [fin] "s"(8 * NT), [u0] "n"(U0), [win] "s"(win), [srt] "n"(CTL_SORT - CTL_LAND)
        : "memory", "scc", "vcc", "s60", "s61", "s62", "s63", "s64", "s65", "s66", "s67", "s68", "s69", "s70", "s71", "s72", "s73", "s74", "s75",
          "s76", "s77", "s78", "s79", "s80", "s81");
    asm volatile("s_endpgm" ::: "memory");
  }

  int lane = lane0;
  asm volatile("" : "+v"(lane));
  if (wv == 7) {
#pragma unroll 1
    for (int i = 0; i < 2; ++i) {
      sort_pages(lane, i ? bp : b, i ? npl : nown, LDS_SRT + i * 2048, i ? LDS_PLIST : LDS_SRT + 4096);
      if (i == 0) {
        const unsigned lo = *(LAS unsigned*)(lds + LDS_SRT + 4096 + (nown - 1) * 4) + 1;
        LGKM0();
        ctl[CTL_SORT / 4 + (lane & 1)] = lo;
      }
    }
  }
  if (wv < 4) {
    VMWAIT(0);
    ctl[CTL_QOK / 4 + (wv & 1)] = 1;
  }
  const int tot = total();
  const int w = wv - 2;
  const int r32 = lane & 31, hi = lane >> 5;
  const int nmine = (NT > w) ? (NT - w + NC - 1) / NC : 0;
  const unsigned aland = lbase + LDS_CTL + CTL_LAND + (lane & 1) * 4;
  const int tots = __builtin_amdgcn_readfirstlane(tot);
  const bool pf_on = npl * 256 * 64 > tots * PF_X64 && npl > nown && (h & 3) == 0;
  const unsigned pf_last = 2 * NT - to_sgpr((int)((float)(2 * NT * (PF_LEAD + 23 * NC)) * 4096.0f * __builtin_amdgcn_rcpf((float)(tot * PACE_X16))));
  const unsigned pf_ratio = to_sgpr((int)((float)NTL * 65536.0f * __builtin_amdgcn_rcpf((float)NT)));
  const unsigned pf_per = PF_PER + (g & 7) * PF_PSTEP, pf_inv = (65536 + pf_per - 1) / pf_per;
  auto prefetch = [&](unsigned op) {
    const unsigned d = pf_last - op;
    if (d >= 1024u) return;
    const unsigned r = d - ((d * pf_inv) >> 16) * pf_per;
    if (r >= PF_RUN) return;
    unsigned tl = 2 * NTL - 1 - NC - (((d - r) * pf_ratio) >> 16) - r;
    if (tl >= 2u * NTL) return;
    const bool isv = tl >= (unsigned)NTL;
    tl -= isv ? NTL : 0;
    const __amdgpu_buffer_rsrc_t rp = __builtin_amdgcn_make_buffer_rsrc(isv ? (void*)vp : (void*)kp, 0, (int)kvbytes, 0x00020000);
    unsigned ln = lane;
    asm volatile("" : "+v"(ln));
    const unsigned vo = (*(LAS unsigned*)(lds + LDS_PLIST + min(2 * tl + (ln >> 5), (unsigned)npl - 1) * 4) & 0x7fff0000u) + (h + 1) * (HD * 2) +
                        ((ln >> 1) & 15) * 4096 + (ln & 1) * 128;
    asm volatile("s_mov_b32 m0, %0\n\ts_nop 4\n\tbuffer_load_dword %2, %1, 0 offen lds" :: "s"(lbase + LDS_DUMMY), "s"(rp), "v"(vo) : "memory");
  };
  const int pos = (w + NC * 22 - NT) % NC;
  const unsigned palp = LDS_WREG + pos * W_SIZE + W_ALP + r32 * 4;
  const unsigned ppst = LDS_WREG + pos * W_SIZE + lane * 16;
  asm volatile("" ::: "v128", "v255");
  unsigned resc = 0;
  float m = -3.0e38f, mprev = -3.0e38f, l = 0.f;

  {
    const unsigned fk = (((unsigned)r32 & 3) << 2) | (((unsigned)r32 >> 2) & 3);
    const unsigned kaddr0 = LDS_RING + r32 * 256 + (((unsigned)hi ^ fk) << 4);
    bf16x8 qf[8];
    wait_above<1>(aland + (CTL_QOK - CTL_LAND), 0);
#pragma unroll
    for (int s = 0; s < 8; ++s) qf[s] = *(LAS bf16x8*)(lds + ((kaddr0 + LDS_Q - LDS_RING) ^ (s << 5)));
#pragma unroll 1
    for (int k = 0; k < nmine; ++k) {
      const unsigned t = w + NC * k;
      wait_above<1>(aland, 4 * t);
      bf16x8 kf[8];
#pragma unroll
      for (int s = 0; s < 8; ++s) kf[s] = *(LAS bf16x8*)(lds + ((kaddr0 + ((t & 7) << 13)) ^ (s << 5)));
      asm volatile("s_waitcnt lgkmcnt(0)" : "+v"(kf[0]), "+v"(kf[1]), "+v"(kf[2]), "+v"(kf[3]), "+v"(kf[4]), "+v"(kf[5]), "+v"(kf[6]), "+v"(kf[7]) :: "memory");
      ctl[CTL_FREE / 4 + (t & 7)] = t + 8;
      if (pf_on) prefetch(t);
      f32x16 sc;
#pragma unroll
      for (int r = 0; r < 16; ++r) sc[r] = 0.f;
#pragma unroll
      for (int s = 0; s < 8; ++s) sc = __builtin_amdgcn_mfma_f32_32x32x16_bf16(kf[s], qf[s], sc, 0, 0, 0);
      if ((int)t == NT - 1) {
        const int kb = 2 * t * PAGE + 4 * hi;
#pragma unroll
        for (int r = 0; r < 16; ++r) sc[r] = (kb + 8 * (r >> 2) + (r & 3) < len) ? sc[r] : -__builtin_inff();
      }
      float mx = sc[0];
#pragma unroll
      for (int r = 1; r < 16; ++r) mx = fmaxf(mx, sc[r]);
      mx = swap_max(mx);
      const float mn = fmaxf(m, mx);
      const float alpha = __builtin_amdgcn_exp2f((m - mn) * CS);
      mprev = m;
      m = mn;
      float ps = 0.f;
#pragma unroll
      for (int r = 0; r < 16; ++r) { sc[r] = __builtin_amdgcn_exp2f((sc[r] - mn) * CS); ps += sc[r]; }
      l = fmaf(l, alpha, ps);
      u32x4 pf[2];
#pragma unroll
      for (int s = 0; s < 2; ++s)
        pf[s] = u32x4{pack2(sc[8 * s], sc[8 * s + 1]), pack2(sc[8 * s + 2], sc[8 * s + 3]), pack2(sc[8 * s + 4], sc[8 * s + 5]), pack2(sc[8 * s + 6], sc[8 * s + 7])};
      if (__builtin_amdgcn_ballot_w64(alpha != 1.0f)) resc |= 1u << k;
      *(LAS float*)(lds + palp + k * 128) = alpha;
      if (k >= KREG || k == nmine - 1) {
        const unsigned dst = ppst + ((k == nmine - 1) ? W_LAST : (k - KREG) * 2048);
        *(LAS u32x4*)(lds + dst) = pf[0];
        *(LAS u32x4*)(lds + dst + 1024) = pf[1];
      } else {
        asm volatile("s_set_gpr_idx_on %8, gpr_idx(DST)\n\t"
                     "v_mov_b32 v128, %0\n\tv_mov_b32 v129, %1\n\tv_mov_b32 v130, %2\n\tv_mov_b32 v131, %3\n\t"
                     "v_mov_b32 v132, %4\n\tv_mov_b32 v133, %5\n\tv_mov_b32 v134, %6\n\tv_mov_b32 v135, %7\n\t"
                     "s_set_gpr_idx_off"
                     :: "v"(pf[0][0]), "v"(pf[0][1]), "v"(pf[0][2]), "v"(pf[0][3]), "v"(pf[1][0]), "v"(pf[1][1]), "v"(pf[1][2]), "v"(pf[1][3]), "s"(8 * k));
      }
    }
  }
  l = swap_sum(l);
  *(LAS f32x2*)(lds + LDS_ML + w * 256 + r32 * 8) = f32x2{m, l};
  LGKM0();
  ctl[CTL_MLOK / 4 + w] = 1;

  const unsigned tq = (lane & 15) >> 2, tp = lane & 3, tblk = (lane >> 4) & 1;
  const unsigned vaddr0 = LDS_RING + (4 * hi + tq) * 256 + ((tq ^ (pos & 3)) << 6) + ((2 * tblk + (tp >> 1)) << 4) + ((tp & 1) << 3);
  f32x16 o[4];
#pragma unroll
  for (int i = 0; i < 4; ++i)
#pragma unroll
    for (int r = 0; r < 16; ++r) o[i][r] = 0.f;
  auto weights = [&]() {
    wait_above<1>(lbase + LDS_CTL + CTL_MLOK + min(lane, NC - 1) * 4, 0);
    unsigned a = LDS_ML + r32 * 8;
    asm volatile("" : "+v"(a) :: "memory");
    float M = -3.0e38f, L = 0.f;
    f32x2 ml[NC];
#pragma unroll
    for (int i = 0; i < NC; ++i) { ml[i] = *(LAS f32x2*)(lds + a + i * 256); M = fmaxf(M, ml[i][0]); }
#pragma unroll
    for (int i = 0; i < NC; ++i) L = fmaf(__builtin_amdgcn_exp2f((ml[i][0] - M) * CS), ml[i][1], L);
    const float inv = 1.0f / L;
    *(LAS float*)(lds + (a >> 1) + (LDS_WT - LDS_ML / 2) + pos * 128) = __builtin_amdgcn_exp2f((mprev - M) * CS) * inv;
    const float ct = __builtin_amdgcn_exp2f((m - M) * CS) * inv;
#pragma unroll
    for (int s = 0; s < 2; ++s) {
      u32x4 x = *(LAS u32x4*)(lds + ppst + W_LAST + s * 1024);
#pragma unroll
      for (int e = 0; e < 4; ++e) x[e] = pack2(__uint_as_float(x[e] << 16) * ct, __uint_as_float(x[e] & 0xffff0000u) * ct);
      *(LAS u32x4*)(lds + ppst + W_LAST + s * 1024) = x;
    }
    LGKM0();
    ctl[CTL_WTOK / 4 + w] = 1;
  };
  resc &= ~1u;
  if (nmine <= 3) weights();
#pragma unroll 1
  for (int k = 0; k < nmine - 1; ++k) {
    if (k == 2) weights();
    if ((resc >> k) & 1) {
      const float alpha = *(LAS float*)(lds + palp + k * 128);
#pragma unroll
      for (int i = 0; i < 4; ++i)
#pragma unroll
        for (int r = 0; r < 16; ++r) o[i][r] *= alpha;
    }
    const unsigned u = NT + ((k == 0 && NT >= 3 * NC) ? pos : w + NC * k);
    u32x4 pf[2];
    if (k >= KREG) {
      pf[0] = *(LAS u32x4*)(lds + ppst + (k - KREG) * 2048);
      pf[1] = *(LAS u32x4*)(lds + ppst + (k - KREG) * 2048 + 1024);
    } else {
      asm volatile("s_set_gpr_idx_on %8, gpr_idx(SRC0)\n\t"
                   "v_mov_b32 %0, v128\n\tv_mov_b32 %1, v129\n\tv_mov_b32 %2, v130\n\tv_mov_b32 %3, v131\n\t"
                   "v_mov_b32 %4, v132\n\tv_mov_b32 %5, v133\n\tv_mov_b32 %6, v134\n\tv_mov_b32 %7, v135\n\t"
                   "s_set_gpr_idx_off\n\ts_nop 1"
                   : "=v"(pf[0][0]), "=v"(pf[0][1]), "=v"(pf[0][2]), "=v"(pf[0][3]), "=v"(pf[1][0]), "=v"(pf[1][1]), "=v"(pf[1][2]), "=v"(pf[1][3])
                   : "s"(8 * k));
    }
    asm volatile("" : "+v"(pf[0]), "+v"(pf[1]), "+v"(o[0]), "+v"(o[1]), "+v"(o[2]), "+v"(o[3]));
    if (k == nmine - 2) wait_above<1>(lbase + LDS_CTL + CTL_WTOK + min(lane, NC - 1) * 4, 0);
    wait_above<1>(aland, 4 * u);
    bf16x8 vf[4][2];
#pragma unroll
    for (int i = 0; i < 4; ++i) {
      const unsigned a = (vaddr0 + ((u & 7) << 13)) ^ (i << 6);
#pragma unroll
      for (int s = 0; s < 2; ++s) {
        const s16x4 lo = __builtin_amdgcn_ds_read_tr16_b64_v4i16((LAS s16x4*)(lds + a + 4096 * s));
        const s16x4 up = __builtin_amdgcn_ds_read_tr16_b64_v4i16((LAS s16x4*)(lds + a + 4096 * s + 2048));
        vf[i][s] = __builtin_bit_cast(bf16x8, __builtin_shufflevector(lo, up, 0, 1, 2, 3, 4, 5, 6, 7));
      }
    }
    asm volatile("s_waitcnt lgkmcnt(0)" : "+v"(vf[0][0]), "+v"(vf[0][1]), "+v"(vf[1][0]), "+v"(vf[1][1]), "+v"(vf[2][0]), "+v"(vf[2][1]), "+v"(vf[3][0]), "+v"(vf[3][1]) :: "memory");
    ctl[CTL_FREE / 4 + (u & 7)] = u + 8;
    if (pf_on) prefetch(u);
#pragma unroll
    for (int s = 0; s < 2; ++s)
#pragma unroll
      for (int i = 0; i < 4; ++i) o[i] = __builtin_amdgcn_mfma_f32_32x32x16_bf16(vf[i][s], __builtin_bit_cast(bf16x8, pf[s]), o[i], 0, 0, 0);
  }

  {
    unsigned l16 = lane * 16;
    asm volatile("" : "+v"(l16));
    const unsigned l4 = l16 >> 2, awt = LDS_WT + (l4 & 127);
    auto put = [&](unsigned dst, const f32x16& x) {
#pragma unroll
      for (int j = 0; j < 4; ++j) *(LAS f32x4*)(lds + dst + j * 1024) = f32x4{x[4 * j], x[4 * j + 1], x[4 * j + 2], x[4 * j + 3]};
    };
    float co[6];
    bf16x8 pl[NC][2];
    auto ld_p = [&](int p) {
      pl[p][0] = *(LAS bf16x8*)(lds + LDS_WREG + p * W_SIZE + W_LAST + l16);
      pl[p][1] = *(LAS bf16x8*)(lds + LDS_WREG + p * W_SIZE + W_LAST + l16 + 1024);
    };
#pragma unroll
    for (int i = 0; i < 4; ++i) co[i] = *(LAS float*)(lds + awt + ((pos & 3) ^ i) * 128);
    co[4] = *(LAS float*)(lds + awt + 4 * 128);
    co[5] = *(LAS float*)(lds + awt + 5 * 128);
    ld_p(0); ld_p(1);
    asm volatile("" : "+v"(l16) :: "memory");
    if (nmine > 1) {
      if (pos > 3) put(LDS_RING + ((2 * NT - 2 * NC + pos) & 7) * 8192 + l16, o[0]);
#pragma unroll
      for (int i = 1; i < 4; ++i) put(LDS_WREG + pos * W_SIZE + (i - 1) * 4096 + l16, o[i]);
      LGKM0();
      ctl[CTL_ODONE / 4 + pos] = 1;
    }
    if (pos < 4) {
      f32x16 of = o[0], ot;
#pragma unroll
      for (int r = 0; r < 16; ++r) ot[r] = 0.f;
      auto src_o = [&](int p) -> unsigned {
        const int i = pos ^ (p & 3);
        return l16 + (i == 0 ? LDS_RING + ((2 * NT - 2 * NC + p) & 7) * 8192 : LDS_WREG + p * W_SIZE + (i - 1) * 4096);
      };
      auto fma_o = [&](unsigned src, float c) {
#pragma unroll
        for (int j = 0; j < 4; ++j) {
          const f32x4 x = *(LAS f32x4*)(lds + src + j * 1024);
#pragma unroll
          for (int e = 0; e < 4; ++e) of[4 * j + e] = fmaf(x[e], c, of[4 * j + e]);
        }
      };
      auto mma_t = [&](int p, bf16x8 p0, bf16x8 p1) {
        const unsigned a = vaddr0 + (((2 * NT - NC + p) & 7) << 13);
        bf16x8 vf[2];
#pragma unroll
        for (int s = 0; s < 2; ++s) {
          const s16x4 lo = __builtin_amdgcn_ds_read_tr16_b64_v4i16((LAS s16x4*)(lds + a + 4096 * s));
          const s16x4 up = __builtin_amdgcn_ds_read_tr16_b64_v4i16((LAS s16x4*)(lds + a + 4096 * s + 2048));
          vf[s] = __builtin_bit_cast(bf16x8, __builtin_shufflevector(lo, up, 0, 1, 2, 3, 4, 5, 6, 7));
        }
        ot = __builtin_amdgcn_mfma_f32_32x32x16_bf16(vf[0], p0, ot, 0, 0, 0);
        ot = __builtin_amdgcn_mfma_f32_32x32x16_bf16(vf[1], p1, ot, 0, 0, 0);
      };
      if (NT >= 2 * NC) {
#define PIN_OF() asm volatile("" : "+v"(of), "+v"(ot))
#pragma unroll
        for (int r = 0; r < 16; ++r) of[r] *= co[0];
        const bool wl = (l4 & 24) == 0;
        const unsigned ua = 4 * (2 * NT - NC);
        wait_above_v(wl ? aland : lbase + LDS_CTL + CTL_ODONE + (l4 & 12), wl ? ua : 0u);
        mma_t(0, pl[0][0], pl[0][1]);
#pragma unroll
        for (int i = 1; i < 4; ++i) fma_o(LDS_WREG + (pos ^ i) * W_SIZE + (i - 1) * 4096 + l16, co[i]);
        PIN_OF();
        ld_p(2); ld_p(3); ld_p(4); ld_p(5);
        wait_above_v(wl ? aland : lbase + LDS_CTL + CTL_ODONE + 16 + (l4 & 4), wl ? ua + 8 : 0u);
        mma_t(1, pl[1][0], pl[1][1]);
        mma_t(2, pl[2][0], pl[2][1]);
        fma_o(src_o(4), co[4]);
        fma_o(src_o(5), co[5]);
#pragma unroll
        for (int r = 0; r < 16; ++r) { ot[r] += of[r]; of[r] = 0.f; }
        asm volatile("" : "+v"(ot));
        wait_above<0>(aland, ua + 20);
        mma_t(3, pl[3][0], pl[3][1]);
        mma_t(4, pl[4][0], pl[4][1]);
        mma_t(5, pl[5][0], pl[5][1]);
        of = ot;
      } else {
        wait_above<1>(lbase + LDS_CTL + CTL_WTOK + min(l4, (NC - 1) * 4u), 0);
        const float cw = *(LAS float*)(lds + awt + pos * 128);
#pragma unroll
        for (int r = 0; r < 16; ++r) of[r] *= cw;
#pragma unroll 1
        for (int p = max(NC - NT, 0); p < NC; ++p) {
          if (p != pos && NT - 2 * NC + p >= 0) {
            wait_above<1>(lbase + LDS_CTL + CTL_ODONE + p * 4, 0);
            fma_o(src_o(p), *(LAS float*)(lds + awt + p * 128));
          }
          wait_above<1>(aland, 4 * (2 * NT - NC + p));
          mma_t(p, *(LAS bf16x8*)(lds + LDS_WREG + p * W_SIZE + W_LAST + l16), *(LAS bf16x8*)(lds + LDS_WREG + p * W_SIZE + W_LAST + l16 + 1024));
        }
#pragma unroll
        for (int r = 0; r < 16; ++r) of[r] += ot[r];
      }
      const __amdgpu_buffer_rsrc_t ro = __builtin_amdgcn_make_buffer_rsrc(outp, 0, NB * HQ * SQ * HD * 2, 0x00020000);
      const unsigned orow = (unsigned)(b * HQ + 2 * h) * SQ * HD * 2 + pos * 64 + ((l16 & 511) << 4) + ((l16 >> 5) & 16);
#pragma unroll
      for (int j = 0; j < 2; ++j) {
        const unsigned a0 = pack2(of[8 * j], of[8 * j + 1]), a1 = pack2(of[8 * j + 2], of[8 * j + 3]);
        const unsigned b0 = pack2(of[8 * j + 4], of[8 * j + 5]), b1 = pack2(of[8 * j + 6], of[8 * j + 7]);
        auto s0 = __builtin_amdgcn_permlane32_swap(a0, b0, false, false);
        auto s1 = __builtin_amdgcn_permlane32_swap(a1, b1, false, false);
        u32x4 val = {s0[0], s1[0], s0[1], s1[1]};
        __builtin_amdgcn_raw_buffer_store_b128(val, ro, orow + j * 32, 0, 0);
      }
    }
  }
}
}

extern "C" void kernel_launch(void* const* d_in, const int* in_sizes, int n_in, void* d_out, int out_size, void* d_ws,
                              size_t ws_size, hipStream_t stream) {
  (void)n_in; (void)out_size; (void)d_ws; (void)ws_size;
  const unsigned nphys = (unsigned)(in_sizes[1] / (PAGE * HKV * HD));
  const unsigned kvbytes = nphys * PAGE_BYTES;
  (void)hipFuncSetAttribute((const void*)paged_attn, hipFuncAttributeMaxDynamicSharedMemorySize, LDS_TOTAL);
  hipLaunchKernelGGL(paged_attn, dim3(256), dim3(512), LDS_TOTAL, stream, d_in[0], d_in[1], d_in[2], (const int*)d_in[3],
                     (const int*)d_in[4], d_out, kvbytes, nphys);
}
```

```cpp
#define PACE_X16 432
#define SOFT 10
#include <hip/hip_runtime.h>
#include <hip/hip_bf16.h>

#define GAP_TICKS 10
#define WIN_TICKS 87
#define WIN_C1 115
#define LATE_TICKS 100
#define HALT_SHIFT 240
#define HALT_FROM 400
#define CODEPF 11520
#define PF_LEAD 180
#define PF_X64 72
#define PF_PER 4
#define PF_PSTEP 2
#define PF_RUN 3

namespace {
constexpr int NB = 16, HQ = 32, HKV = 16, SQ = 16, HD = 128, PAGE = 16, NBPS = 256, SKV = NBPS * PAGE;
constexpr unsigned PAGE_BYTES = PAGE * HKV * HD * 2;
constexpr unsigned TLBG = 0x200000u;
constexpr int U0 = 4;
constexpr int NC = 6;
constexpr int KREG = 16, KLDS = 5;
static_assert(NC * (KREG + KLDS + 1) >= NBPS / 2, "P storage");
constexpr unsigned LDS_RING = 0;
constexpr unsigned LDS_WREG = 65536;
constexpr unsigned W_ALP = KLDS * 2048;
constexpr unsigned W_LAST = W_ALP + (KREG + KLDS + 1) * 128;
constexpr unsigned W_SIZE = W_LAST + 2048;
static_assert(W_LAST >= 3 * 4096 && W_SIZE % 256 == 0, "consumer region");
constexpr unsigned LDS_TBL = LDS_WREG + W_SIZE;
constexpr unsigned LDS_SRT = LDS_WREG + 3 * W_SIZE;
constexpr unsigned LDS_Q = LDS_WREG;
constexpr unsigned LDS_ML = LDS_WREG + NC * W_SIZE;
constexpr unsigned LDS_WT = LDS_ML + NC * 256;
constexpr unsigned LDS_CTL = LDS_WT + NC * 128;
constexpr unsigned CTL_LAND = 0;
constexpr unsigned CTL_QOK = 16;
constexpr unsigned CTL_SORT = 24;
constexpr unsigned CTL_FREE = 32;
constexpr unsigned CTL_MLOK = 64;
constexpr unsigned CTL_ODONE = 96;
constexpr unsigned CTL_WTOK = 128;
constexpr unsigned LDS_DUMMY = LDS_CTL + 256;
constexpr unsigned LDS_PLIST = LDS_DUMMY + 256;
constexpr unsigned LDS_TOTAL = LDS_PLIST + 1024;
static_assert(LDS_TOTAL <= 163840, "lds");

typedef __attribute__((ext_vector_type(8))) __bf16 bf16x8;
typedef __attribute__((ext_vector_type(2))) __bf16 bf16x2;
typedef __attribute__((ext_vector_type(4))) short s16x4;
typedef __attribute__((ext_vector_type(16))) float f32x16;
typedef __attribute__((ext_vector_type(4))) float f32x4;
typedef __attribute__((ext_vector_type(2))) float f32x2;
typedef __attribute__((ext_vector_type(4))) unsigned u32x4;
typedef __attribute__((ext_vector_type(2))) unsigned u32x2;
#define LAS __attribute__((address_space(3)))

__device__ __forceinline__ float swap_max(float x) {
  auto r = __builtin_amdgcn_permlane32_swap(__float_as_uint(x), __float_as_uint(x), false, false);
  return fmaxf(__uint_as_float(r[0]), __uint_as_float(r[1]));
}
__device__ __forceinline__ float swap_sum(float x) {
  auto r = __builtin_amdgcn_permlane32_swap(__float_as_uint(x), __float_as_uint(x), false, false);
  return __uint_as_float(r[0]) + __uint_as_float(r[1]);
}
__device__ __forceinline__ unsigned pack2(float a, float b) {
  bf16x2 v = {(__bf16)a, (__bf16)b};
  return __builtin_bit_cast(unsigned, v);
}
template <int CTRL> __device__ __forceinline__ int dpp_add(int x) { return x + __builtin_amdgcn_update_dpp(0, x, CTRL, 0xf, 0xf, true); }
__device__ __forceinline__ int to_sgpr(int v) {
  int r;
  asm volatile("s_nop 1\n\tv_readfirstlane_b32 %0, %1\n\ts_nop 1" : "=s"(r) : "v"(v));
  return r;
}
#define LGKM0() asm volatile("s_waitcnt lgkmcnt(0)" ::: "memory")
#define VMWAIT(n) asm volatile("s_waitcnt vmcnt(" #n ")" ::: "memory")
#define CBAR() asm volatile("" ::: "memory")
template <int SLEEP> __device__ __forceinline__ void wait_above(unsigned addr, unsigned u) {
  unsigned v, cnt;
  if (SLEEP)
    asm volatile("s_mov_b32 %1, 0x800000\n\ts_branch 2f\n"
                 "1:\n\ts_sleep %4\n"
                 "2:\n\tds_read_b32 %0, %2\n\ts_sub_u32 %1, %1, 1\n\ts_waitcnt lgkmcnt(0)\n\t"
                 "v_cmp_ge_u32 vcc, %3, %0\n\ts_cbranch_scc1 3f\n\ts_nop 1\n\ts_cbranch_vccnz 1b\n3:"
                 : "=&v"(v), "=&s"(cnt) : "v"(addr), "s"(u), "n"(SLEEP) : "memory", "scc", "vcc");
  else
    asm volatile("s_mov_b32 %1, 0x800000\n"
                 "1:\n\tds_read_b32 %0, %2\n\ts_sub_u32 %1, %1, 1\n\ts_waitcnt lgkmcnt(0)\n\t"
                 "v_cmp_ge_u32 vcc, %3, %0\n\ts_cbranch_scc1 3f\n\ts_nop 1\n\ts_cbranch_vccnz 1b\n3:"
                 : "=&v"(v), "=&s"(cnt) : "v"(addr), "s"(u) : "memory", "scc", "vcc");
}

__device__ __forceinline__ void wait_above_v(unsigned addr, unsigned u) {
  unsigned v, cnt;
  asm volatile("s_mov_b32 %1, 0x800000\n"
               "1:\n\tds_read_b32 %0, %2\n\ts_sub_u32 %1, %1, 1\n\ts_waitcnt lgkmcnt(0)\n\t"
               "v_cmp_ge_u32 vcc, %3, %0\n\ts_cbranch_scc1 3f\n\ts_nop 1\n\ts_cbranch_vccnz 1b\n3:"
               : "=&v"(v), "=&s"(cnt) : "v"(addr), "v"(u) : "memory", "scc", "vcc");
}

__global__ void __launch_bounds__(512, 1) __attribute__((amdgpu_num_vgpr(64)))
paged_attn(const void* __restrict__ qp, const void* __restrict__ kp, const void* __restrict__ vp, const int* __restrict__ bt,
           const int* __restrict__ lens, void* __restrict__ outp, unsigned kvbytes, unsigned nphys) {
  extern __shared__ __attribute__((aligned(16))) char smem[];
  LAS char* lds = (LAS char*)smem;
  const int lane0 = threadIdx.x & 63;
  const int wv = __builtin_amdgcn_readfirstlane(threadIdx.x >> 6);
  const int g = blockIdx.x;
  const int h = g >> 4;
  constexpr float CS = 0.08838834764831845f * 1.4426950408889634f;
  volatile LAS unsigned* ctl = (volatile LAS unsigned*)(lds + LDS_CTL);

  const unsigned lbase = (unsigned)(size_t)lds;

  unsigned lraw, first;
  u32x4 lq;
  {
    asm volatile("global_load_dword %0, %3, %4\n\t"
                 "global_load_dwordx4 %1, %5, %4\n\t"
                 "global_load_dword %2, %6, %7"
                 : "=&v"(lraw), "=&v"(lq), "=&v"(first)
                 : "v"((lane0 & 15) * 4), "s"(lens), "v"(lane0 & 48), "v"((lane0 & 15) * (wv < 2 ? NBPS * 4 : 0) + (lane0 >> 4) * 8 + (wv & 1) * 4), "s"(bt),
                   "s"(qp), "s"(kp), "s"(vp), "s"(outp), "s"(kvbytes), "s"(nphys)
                 : "memory");
    __builtin_amdgcn_sched_barrier(0);
  }
  if (wv >= 4) {
    if (wv == 7) {
      const __amdgpu_buffer_rsrc_t rb = __builtin_amdgcn_make_buffer_rsrc((void*)bt, 0, NB * NBPS * 4, 0x00020000);
#define TBL4(SO) "buffer_load_dwordx4 %2, %1, " SO " offen lds\n\tbuffer_load_dwordx4 %2, %1, " SO " offen offset:1024 lds\n\t" \
                 "buffer_load_dwordx4 %2, %1, " SO " offen offset:2048 lds\n\tbuffer_load_dwordx4 %2, %1, " SO " offen offset:3072 lds\n\t"
      asm volatile("s_mov_b32 m0, %0\n\ts_nop 0\n\t" TBL4("0") "s_add_u32 m0, m0, 4096\n\ts_nop 0\n\t" TBL4("%3")
                   "s_add_u32 m0, m0, %6\n\ts_nop 0\n\t" TBL4("%4") "s_add_u32 m0, m0, 4096\n\ts_nop 0\n\t" TBL4("%5")
                   :: "s"(lbase + LDS_TBL), "s"(rb), "v"(lane0 * 16), "s"(4096), "s"(8192), "s"(12288), "n"(W_SIZE - 4096) : "memory", "scc");
    }
    unsigned long long pc;
    asm volatile("s_getpc_b64 %0" : "=s"(pc));
    const __amdgpu_buffer_rsrc_t rt = __builtin_amdgcn_make_buffer_rsrc(
        wv == 5 ? (void*)pc : wv == 4 ? (void*)vp : (void*)kp, 0, wv == 5 ? CODEPF : (wv != 7 && (g & 8)) ? (int)kvbytes : 0, 0x00020000);
    const unsigned mul = wv == 5 ? 512u : 16 * TLBG + 4224u, add = wv == 5 ? ((g >> 3) & 3) * 128 : (g >> 4) * (TLBG + 33024u);
    asm volatile("s_mov_b32 m0, %0\n\ts_nop 0\n\tbuffer_load_dword %1, %2, 0 offen lds"
                 :: "s"(lbase + LDS_DUMMY), "v"((lane0 & 31) * mul + add), "s"(rt) : "memory");
    *(LAS u32x4*)(lds + LDS_SRT + (threadIdx.x - 256) * 16) = u32x4{0u, 0u, 0u, 0u};
    if (threadIdx.x < 296) ctl[lane0] = ((lane0 >> 3) == CTL_FREE / 32) ? (lane0 & 7) : 0u;
    asm volatile("s_waitcnt lgkmcnt(0)\n\ts_barrier" ::: "memory");
  }
  if (wv < 2) __builtin_amdgcn_s_setprio(3);
  const unsigned rstride = wv < 2 ? 4096 : 256;
  unsigned vo0 = (lane0 >> 4) * rstride + (((unsigned)(lane0 & 15) ^ ((lane0 >> 4) << 2)) << 4) + (wv < 2 ? h * (HD * 2) : 0);
  unsigned vo1 = (vo0 + 4 * rstride - 1024) ^ 16, vo2 = (vo0 + 8 * rstride - 2048) ^ 32, vo3 = (vo0 + 12 * rstride - 3072) ^ 48;
  const unsigned want = (g & 8) ? 15 - (g & 7) : (g & 7);
  const unsigned dst0 = lbase + (wv < 2 ? LDS_RING : LDS_Q) + (wv & 1) * 4096;
  asm volatile("s_cmp_ge_u32 %8, 4\n\ts_cbranch_scc1 1f\n\ts_waitcnt vmcnt(0)\n1:\n\ts_waitcnt vmcnt(1)" : "+v"(lraw), "+v"(lq), "+v"(first), "+v"(vo0), "+v"(vo1), "+v"(vo2), "+v"(vo3) : "s"(dst0), "s"(wv) : "memory", "scc");
  unsigned lm1 = min(lraw - 1u, (unsigned)SKV - 1u);
  unsigned rank = 0;
  {
    const unsigned key = lm1 * 16 + (lane0 & 15);
#pragma unroll
    for (int j = 0; j < 4; ++j) rank += (min(lq[j] - 1u, (unsigned)SKV - 1u) * 16 + ((lane0 >> 4) * 4 + j) > key) ? 1u : 0u;
    auto r1 = __builtin_amdgcn_permlane16_swap(rank, rank, false, false);
    rank = r1[0] + r1[1];
    auto r2 = __builtin_amdgcn_permlane32_swap(rank, rank, false, false);
    rank = r2[0] + r2[1];
  }
  first = min(first, nphys - 1) * PAGE_BYTES;
  const int b = __builtin_ctz((unsigned)__builtin_amdgcn_ballot_w64(rank == want) | 0x8000u);
  unsigned long long tstart = 0;
  if (wv < 4) {
    const bool qok = (int)__builtin_amdgcn_readlane(lraw, b) > 0;
    const __amdgpu_buffer_rsrc_t r0 = __builtin_amdgcn_make_buffer_rsrc(wv < 2 ? (void*)kp : (void*)qp, 0,
                                                                        wv < 2 ? (int)kvbytes : qok ? NB * HQ * SQ * HD * 2 : 0, 0x00020000);
    const unsigned so = wv < 2 ? __builtin_amdgcn_readlane(first, b) : qok ? (unsigned)((b * HQ + 2 * h) * SQ * HD * 2) + (wv & 1) * 4096 : 0u;
    asm volatile("s_waitcnt vmcnt(0)\n\t"
                 "s_mov_b32 m0, %2\n\t"
                 "s_memrealtime %0\n\t"
                 "s_nop 2\n\t"
                 "buffer_load_dwordx4 %4, %1, %3 offen nt lds\n\t"
                 "buffer_load_dwordx4 %5, %1, %3 offen offset:1024 nt lds\n\t"
                 "buffer_load_dwordx4 %6, %1, %3 offen offset:2048 nt lds\n\t"
                 "buffer_load_dwordx4 %7, %1, %3 offen offset:3072 nt lds\n\t"
                 "s_waitcnt lgkmcnt(0)\n\t"
                 "s_barrier"
                 : "=&s"(tstart) : "s"(r0), "s"(dst0), "s"(so), "v"(vo0), "v"(vo1), "v"(vo2), "v"(vo3) : "memory");
  }
  asm volatile("" : "+v"(lm1), "+v"(rank), "+v"(first) :: "memory");
  __builtin_amdgcn_sched_barrier(0);
  const int bp = __builtin_ctz((unsigned)__builtin_amdgcn_ballot_w64(rank == 15 - want) | 0x8000u);
  const int len = __builtin_amdgcn_readlane(lm1, b) + 1, nown = (len + PAGE - 1) / PAGE, NT = (nown + 1) >> 1;
  const int npl = (__builtin_amdgcn_readlane(lm1, bp) >> 4) + 1, NTL = (npl + 1) >> 1;
  auto total = [&]() __attribute__((always_inline)) {
    int t = lm1 + 1;
    t = dpp_add<0x121>(t); t = dpp_add<0x122>(t); t = dpp_add<0x124>(t); t = dpp_add<0x128>(t);
    return t;
  };

  auto sort_pages = [&](int lane, int bb, int n, unsigned scr, unsigned dst) __attribute__((always_inline)) {
    u32x4 e = *(LAS u32x4*)(lds + LDS_TBL + (bb >> 3) * W_SIZE + (bb & 7) * 1024 + lane * 16);
    bool in[4];
#pragma unroll
    for (int j = 0; j < 4; ++j) {
      e[j] = min(e[j], nphys - 1);
      in[j] = 4 * lane + j >= 2 * U0 && 4 * lane + j < n - 1;
      __hip_atomic_fetch_or((LAS unsigned*)(lds + scr + ((e[j] >> 5) & 255) * 8), in[j] ? 1u << (e[j] & 31) : 0u, __ATOMIC_RELAXED, __HIP_MEMORY_SCOPE_WAVEFRONT);
    }
    CBAR();
    u32x4 a = *(LAS u32x4*)(lds + scr + lane * 32), c = *(LAS u32x4*)(lds + scr + lane * 32 + 16);
    const int n0 = __builtin_popcount(a[0]), n1 = n0 + __builtin_popcount(a[2]), n2 = n1 + __builtin_popcount(c[0]), n3 = n2 + __builtin_popcount(c[2]);
    int sc = n3;
    sc = dpp_add<0x111>(sc); sc = dpp_add<0x112>(sc); sc = dpp_add<0x114>(sc); sc = dpp_add<0x118>(sc);
    sc += __builtin_amdgcn_update_dpp(0, sc, 0x142, 0xa, 0xf, false);
    sc += __builtin_amdgcn_update_dpp(0, sc, 0x143, 0xc, 0xf, false);
    const bool ok = nphys <= 8192u && __builtin_amdgcn_readlane(sc, 63) == n - 1 - 2 * U0;
    sc += 2 * U0 - n3;
    a[1] = sc; a[3] = sc + n0; c[1] = sc + n1; c[3] = sc + n2;
    *(LAS u32x4*)(lds + scr + lane * 32) = a;
    *(LAS u32x4*)(lds + scr + lane * 32 + 16) = c;
    CBAR();
    u32x2 wp[4];
#pragma unroll
    for (int j = 0; j < 4; ++j) wp[j] = *(LAS u32x2*)(lds + scr + ((e[j] >> 5) & 255) * 8);
#pragma unroll
    for (int j = 0; j < 4; ++j) {
      const unsigned r = wp[j][1] + __builtin_popcount(wp[j][0] & ((1u << (e[j] & 31)) - 1u));
      *(LAS unsigned*)(lds + dst + ((ok && in[j]) ? r : 4 * lane + j) * 4) = e[j] * PAGE_BYTES;
    }
    CBAR();
  };

  if (__builtin_expect(wv < 2, 1)) {
    const int half = wv, lane = lane0;
    const unsigned vland = lbase + LDS_CTL + CTL_LAND + half * 4;
    const unsigned vfree = lbase + LDS_CTL + CTL_FREE + (lane & 7) * 4;
    const unsigned ring = dst0;
    unsigned l0 = __builtin_amdgcn_ds_bpermute((b + 16 * lane) * 4, first), l1 = 0;
    const unsigned a0 = lbase + LDS_SRT + 4096 + min(2 * lane + half, nown - 1) * 4, a1 = lbase + LDS_SRT + 4096 + min(2 * lane + half + 128, nown - 1) * 4;
    const int rot = (NT >= 3 * NC) ? NT % NC : 0;
    const unsigned vperm = (lane < NC ? (lane + rot >= NC ? lane + rot - NC : lane + rot) : lane) * 4;
    const int T16 = total() * PACE_X16;
    const int step = to_sgpr((int)((float)T16 * __builtin_amdgcn_rcpf((float)max(len, 32))));
    const int win = to_sgpr((int)((float)(((h & 3) == 1 ? WIN_C1 : WIN_TICKS) * 1024) * __builtin_amdgcn_rcpf((float)step) + 0.5f));
    const int hmin = to_sgpr((int)((float)(HALT_FROM * 1024) * __builtin_amdgcn_rcpf((float)step)));
    const unsigned due0 = ((unsigned)tstart << 8) + half * (step >> 1);
    const int lrows = len - (2 * (NT - 1) + half) * PAGE;
    unsigned vf, vt, lc;
    asm volatile(
        "s_waitcnt lgkmcnt(0)\n\t"
        "s_memrealtime s[68:69]\n\t"
        "ds_read_b32 %[vf], %[vfree]\n\t"
        "s_mov_b32 s60, 1\n\t"
        "s_mov_b32 s61, 4\n\t"
        "s_mov_b32 s62, 0x1000000\n\t"
        "s_mov_b32 s66, 0\n\t"
        "s_min_u32 s67, %[nt], %[u0]\n\t"
        "s_mov_b32 s75, %[nrec]\n\t"
        "s_mov_b32 s72, 0x2000\n\t"
        "s_mov_b32 s73, 1\n\t"
        "s_mov_b32 s74, %[due1]\n\t"
        "s_mov_b32 s85, %[duev]\n\t"
        "s_mov_b32 s86, %[late]\n\t"
        "s_mov_b64 s[76:77], %[kp]\n\t"
        "s_mov_b32 s78, %[nrec]\n\t"
        "s_mov_b32 s79, 0x20000\n\t"
        "s_and_b32 s77, s77, 0xffff\n\t"
        "v_mov_b32 %[lc], %[l0]\n\t"
        "s_getpc_b64 s[70:71]\n"
        ".LldA%=:\n\t"
        "s_add_u32 s80, s70, .LldL%=-.LldA%=\n\t"
        "s_addc_u32 s71, s71, 0\n\t"
        "s_branch .LldF%=\n"
        ".LldR%=:\n\t"
        "s_sub_u32 s62, s62, 1\n\t"
        "s_cbranch_scc1 .LldE%=\n"
        ".LldT%=:\n\t"
        "s_getreg_b32 s64, hwreg(HW_REG_IB_STS)\n\t"
        "s_and_b32 s65, s64, 15\n\t"
        "s_bfe_u32 s64, s64, 0x20016\n\t"
        "s_lshl4_add_u32 s81, s64, s65\n\t"
        "s_add_u32 s64, s81, 3\n\t"
        "s_and_b32 s64, s64, 60\n\t"
        "s_sub_u32 s65, s61, s64\n\t"
        "s_cmp_lg_u32 s65, s66\n\t"
        "s_cbranch_scc1 .LldP%=\n"
        ".LldC%=:\n\t"
        "s_waitcnt lgkmcnt(0)\n\t"
        "v_cmp_eq_u32 vcc, s60, %[vf]\n\t"
        "s_lshl_b32 s64, s68, 8\n\t"
        "s_memrealtime s[68:69]\n\t"
        "ds_read_b32 %[vf], %[vfree]\n\t"
        "s_sub_u32 s64, s64, s74\n\t"
        "s_cmp_lt_i32 s64, 0\n\t"
        "s_cbranch_scc1 .LldR%=\n\t"
        "s_cmp_gt_i32 s64, s86\n\t"
        "s_cbranch_scc1 .LldH%=\n"
        ".LldI%=:\n\t"
        "s_cbranch_vccz .LldR%=\n\t"
        "s_cmp_gt_u32 s81, %[win]\n\t"
        "s_cbranch_scc1 .LldR%=\n\t"
        "buffer_load_dwordx4 %[o0], s[76:79], s63 offen nt lds\n\t"
        "s_add_u32 s74, s74, %[step]\n\t"
        "s_add_u32 s73, s73, 1\n\t"
        "buffer_load_dwordx4 %[o1], s[76:79], s63 offen offset:1024 nt lds\n\t"
        "s_add_u32 s60, s60, 1\n\t"
        "s_add_u32 s61, s61, 4\n\t"
        "s_add_u32 s86, s64, %[late]\n\t"
        "buffer_load_dwordx4 %[o2], s[76:79], s63 offen offset:2048 nt lds\n\t"
        "s_add_u32 s72, s72, 0x2000\n\t"
        "s_and_b32 s72, s72, 0xe000\n\t"
        "buffer_load_dwordx4 %[o3], s[76:79], s63 offen offset:3072 nt lds\n"
        ".LldF%=:\n\t"
        "v_readlane_b32 s63, %[lc], s73\n\t"
        "s_add_u32 m0, s72, %[ring]\n\t"
        "s_cmp_eq_u32 s73, %[ntm]\n\t"
        "s_cselect_b32 s78, s75, %[nrec]\n\t"
        "s_cmp_lg_u32 s60, s67\n\t"
        "s_cbranch_scc1 .LldT%=\n\t"
        "s_cmp_lg_u32 s60, %[u0]\n\t"
        "s_cbranch_scc1 .LldG%=\n\t"
        "s_cmp_le_u32 %[nt], %[u0]\n\t"
        "s_cbranch_scc1 .LldG%=\n"
        ".LldS%=:\n\t"
        "ds_read_b32 %[lc], %[vland] offset:%[srt]\n\t"
        "ds_read_b32 %[l0], %[a0]\n\t"
        "ds_read_b32 %[l1], %[a1]\n\t"
        "s_sub_u32 s62, s62, 1\n\t"
        "s_cbranch_scc1 .LldE%=\n\t"
        "s_waitcnt lgkmcnt(0)\n\t"
        "v_readfirstlane_b32 s64, %[lc]\n\t"
        "s_cmp_eq_u32 s64, 0\n\t"
        "s_cbranch_scc1 .LldS%=\n\t"
        "s_max_i32 s65, %[lrows], 0\n\t"
        "s_lshl_b32 s65, s65, 12\n\t"
        "s_add_u32 s64, s64, s65\n\t"
        "s_sub_u32 s64, s64, 1\n\t"
        "s_cmp_lt_i32 %[lrows], 16\n\t"
        "s_cselect_b32 s75, s64, %[nrec]\n\t"
        "s_min_u32 s67, %[nt], 64\n\t"
        "v_mov_b32 %[lc], %[l0]\n\t"
        "s_branch .LldF%=\n"
        ".LldG%=:\n\t"
        "s_cmp_eq_u32 s73, %[nt]\n\t"
        "s_cbranch_scc1 .LldV%=\n\t"
        "v_mov_b32 %[lc], %[l1]\n\t"
        "v_readfirstlane_b32 s63, %[l1]\n\t"
        "s_add_u32 s67, s60, %[nt]\n\t"
        "s_sub_u32 s67, s67, 64\n\t"
        "s_branch .LldT%=\n"
        ".LldV%=:\n\t"
        "s_cmp_lg_u32 s60, %[nt]\n\t"
        "s_cbranch_scc1 .LldD%=\n\t"
        "s_mov_b64 s[76:77], %[vp]\n\t"
        "s_and_b32 s77, s77, 0xffff\n\t"
        "s_mov_b32 s73, 0\n\t"
        "s_min_u32 s67, %[nt], 64\n\t"
        "s_add_u32 s67, s67, s60\n\t"
        "s_mov_b32 s74, s85\n\t"
        "s_cmp_eq_u32 %[ntm], 0\n\t"
        "s_cselect_b32 s78, s75, %[nrec]\n\t"
        "ds_bpermute_b32 %[lc], %[vperm], %[l0]\n\t"
        "v_xor_b32 %[o1], 16, %[o1]\n\t"
        "v_xor_b32 %[o2], 32, %[o2]\n\t"
        "v_xor_b32 %[o3], 48, %[o3]\n\t"
        "s_waitcnt lgkmcnt(0)\n\t"
        "v_readfirstlane_b32 s63, %[lc]\n\t"
        "s_branch .LldT%=\n"
        ".LldH%=:\n\t"
        "s_cmp_lt_u32 s61, %[hmin]\n\t"
        "s_cbranch_scc1 .LldI%=\n\t"
        "s_add_u32 s74, s74, %[hshift]\n\t"
        "s_add_u32 s85, s85, %[hshift]\n\t"
        "s_branch .LldR%=\n"
        ".LldD%=:\n\t"
        "s_add_u32 s74, s74, 0x40000000\n\t"
        "s_mov_b32 s60, -1\n\t"
        "s_branch .LldT%=\n"
        ".LldP%=:\n\t"
        "s_min_u32 s64, s64, 32\n\t"
        "s_add_u32 s70, s80, s64\n\t"
        "v_mov_b32 %[vt], s65\n\t"
        "s_mov_b32 s66, s65\n\t"
        "s_setpc_b64 s[70:71]\n\t"
        ".p2align 6\n"
        ".LldL%=:\n\t"
        "s_waitcnt vmcnt(0)\n\t"
        "s_waitcnt vmcnt(4)\n\t"
        "s_waitcnt vmcnt(8)\n\t"
        "s_waitcnt vmcnt(12)\n\t"
        "s_waitcnt vmcnt(16)\n\t"
        "s_waitcnt vmcnt(20)\n\t"
        "s_waitcnt vmcnt(24)\n\t"
        "s_waitcnt vmcnt(28)\n\t"
        "s_waitcnt vmcnt(32)\n\t"
        "ds_write_b32 %[vland], %[vt]\n\t"
        "s_cmp_lg_u32 s65, %[fin]\n\t"
        "s_cbranch_scc1 .LldC%=\n"
        ".LldE%=:\n\t"
        "s_waitcnt vmcnt(0) lgkmcnt(0)\n\t"
        : [vf] "=&v"(vf), [vt] "=&v"(vt), [lc] "=&v"(lc), [l0] "+&v"(l0), [l1] "+&v"(l1), [o1] "+&v"(vo1), [o2] "+&v"(vo2), [o3] "+&v"(vo3)
        : [vperm] "v"(vperm), [a0] "v"(a0), [a1] "v"(a1), [o0] "v"(vo0), [vland] "v"(vland), [vfree] "v"(vfree),
          [kp] "s"(kp), [vp] "s"(vp), [nrec] "s"(kvbytes), [nt] "s"(NT), [ring] "s"(ring),
          [step] "s"(step), [due1] "s"(due0 + step + SOFT * 256), [duev] "s"(due0 + (to_sgpr(T16) >> 5) + (SOFT + GAP_TICKS) * 256), [lrows] "s"(lrows), [ntm] "s"(NT - 1), [fin] "s"(8 * NT), [u0] "n"(U0), [win] "s"(win), [late] "n"(LATE_TICKS * 256), [hshift] "n"(HALT_SHIFT * 256), [hmin] "s"(hmin), [srt] "n"(CTL_SORT - CTL_LAND)
        : "memory", "scc", "vcc", "s60", "s61", "s62", "s63", "s64", "s65", "s66", "s67", "s68", "s69", "s70", "s71", "s72", "s73", "s74", "s75",
          "s76", "s77", "s78", "s79", "s80", "s81", "s85", "s86");
    asm volatile("s_endpgm" ::: "memory");
  }

  int lane = lane0;
  asm volatile("" : "+v"(lane));
  if (wv == 7) {
#pragma unroll 1
    for (int i = 0; i < 2; ++i) {
      sort_pages(lane, i ? bp : b, i ? npl : nown, LDS_SRT + i * 2048, i ? LDS_PLIST : LDS_SRT + 4096);
      if (i == 0) {
        const unsigned lo = *(LAS unsigned*)(lds + LDS_SRT + 4096 + (nown - 1) * 4) + 1;
        LGKM0();
        ctl[CTL_SORT / 4 + (lane & 1)] = lo;
      }
    }
  }
  if (wv < 4) {
    VMWAIT(0);
    ctl[CTL_QOK / 4 + (wv & 1)] = 1;
  }
  const int tot = total();
  const int w = wv - 2;
  const int r32 = lane & 31, hi = lane >> 5;
  const int nmine = (NT > w) ? (NT - w + NC - 1) / NC : 0;
  const unsigned aland = lbase + LDS_CTL + CTL_LAND + (lane & 1) * 4;
  const int tots = __builtin_amdgcn_readfirstlane(tot);
  const bool pf_on = npl * 256 * 64 > tots * PF_X64 && npl > nown && (h & 3) == 0;
  const unsigned pf_last = 2 * NT - to_sgpr((int)((float)(2 * NT * (PF_LEAD + 23 * NC)) * 4096.0f * __builtin_amdgcn_rcpf((float)(tot * PACE_X16))));
  const unsigned pf_ratio = to_sgpr((int)((float)NTL * 65536.0f * __builtin_amdgcn_rcpf((float)NT)));
  const unsigned pf_per = PF_PER + (g & 7) * PF_PSTEP, pf_inv = (65536 + pf_per - 1) / pf_per;
  auto prefetch = [&](unsigned op) {
    const unsigned d = pf_last - op;
    if (d >= 1024u) return;
    const unsigned r = d - ((d * pf_inv) >> 16) * pf_per;
    if (r >= PF_RUN) return;
    unsigned tl = 2 * NTL - 1 - NC - (((d - r) * pf_ratio) >> 16) - r;
    if (tl >= 2u * NTL) return;
    const bool isv = tl >= (unsigned)NTL;
    tl -= isv ? NTL : 0;
    const __amdgpu_buffer_rsrc_t rp = __builtin_amdgcn_make_buffer_rsrc(isv ? (void*)vp : (void*)kp, 0, (int)kvbytes, 0x00020000);
    unsigned ln = lane;
    asm volatile("" : "+v"(ln));
    const unsigned vo = (*(LAS unsigned*)(lds + LDS_PLIST + min(2 * tl + (ln >> 5), (unsigned)npl - 1) * 4) & 0x7fff0000u) + (h + 1) * (HD * 2) +
                        ((ln >> 1) & 15) * 4096 + (ln & 1) * 128;
    asm volatile("s_mov_b32 m0, %0\n\ts_nop 4\n\tbuffer_load_dword %2, %1, 0 offen lds" :: "s"(lbase + LDS_DUMMY), "s"(rp), "v"(vo) : "memory");
  };
  const int pos = (w + NC * 22 - NT) % NC;
  const unsigned palp = LDS_WREG + pos * W_SIZE + W_ALP + r32 * 4;
  const unsigned ppst = LDS_WREG + pos * W_SIZE + lane * 16;
  asm volatile("" ::: "v128", "v255");
  unsigned resc = 0;
  float m = -3.0e38f, mprev = -3.0e38f, l = 0.f;

  {
    const unsigned fk = (((unsigned)r32 & 3) << 2) | (((unsigned)r32 >> 2) & 3);
    const unsigned kaddr0 = LDS_RING + r32 * 256 + (((unsigned)hi ^ fk) << 4);
    bf16x8 qf[8];
    wait_above<1>(aland + (CTL_QOK - CTL_LAND), 0);
#pragma unroll
    for (int s = 0; s < 8; ++s) qf[s] = *(LAS bf16x8*)(lds + ((kaddr0 + LDS_Q - LDS_RING) ^ (s << 5)));
#pragma unroll 1
    for (int k = 0; k < nmine; ++k) {
      const unsigned t = w + NC * k;
      wait_above<1>(aland, 4 * t);
      bf16x8 kf[8];
#pragma unroll
      for (int s = 0; s < 8; ++s) kf[s] = *(LAS bf16x8*)(lds + ((kaddr0 + ((t & 7) << 13)) ^ (s << 5)));
      asm volatile("s_waitcnt lgkmcnt(0)" : "+v"(kf[0]), "+v"(kf[1]), "+v"(kf[2]), "+v"(kf[3]), "+v"(kf[4]), "+v"(kf[5]), "+v"(kf[6]), "+v"(kf[7]) :: "memory");
      ctl[CTL_FREE / 4 + (t & 7)] = t + 8;
      if (pf_on) prefetch(t);
      f32x16 sc;
#pragma unroll
      for (int r = 0; r < 16; ++r) sc[r] = 0.f;
#pragma unroll
      for (int s = 0; s < 8; ++s) sc = __builtin_amdgcn_mfma_f32_32x32x16_bf16(kf[s], qf[s], sc, 0, 0, 0);
      if ((int)t == NT - 1) {
        const int kb = 2 * t * PAGE + 4 * hi;
#pragma unroll
        for (int r = 0; r < 16; ++r) sc[r] = (kb + 8 * (r >> 2) + (r & 3) < len) ? sc[r] : -__builtin_inff();
      }
      float mx = sc[0];
#pragma unroll
      for (int r = 1; r < 16; ++r) mx = fmaxf(mx, sc[r]);
      mx = swap_max(mx);
      const float mn = fmaxf(m, mx);
      const float alpha = __builtin_amdgcn_exp2f((m - mn) * CS);
      mprev = m;
      m = mn;
      float ps = 0.f;
#pragma unroll
      for (int r = 0; r < 16; ++r) { sc[r] = __builtin_amdgcn_exp2f((sc[r] - mn) * CS); ps += sc[r]; }
      l = fmaf(l, alpha, ps);
      u32x4 pf[2];
#pragma unroll
      for (int s = 0; s < 2; ++s)
        pf[s] = u32x4{pack2(sc[8 * s], sc[8 * s + 1]), pack2(sc[8 * s + 2], sc[8 * s + 3]), pack2(sc[8 * s + 4], sc[8 * s + 5]), pack2(sc[8 * s + 6], sc[8 * s + 7])};
      if (__builtin_amdgcn_ballot_w64(alpha != 1.0f)) resc |= 1u << k;
      *(LAS float*)(lds + palp + k * 128) = alpha;
      if (k >= KREG || k == nmine - 1) {
        const unsigned dst = ppst + ((k == nmine - 1) ? W_LAST : (k - KREG) * 2048);
        *(LAS u32x4*)(lds + dst) = pf[0];
        *(LAS u32x4*)(lds + dst + 1024) = pf[1];
      } else {
        asm volatile("s_set_gpr_idx_on %8, gpr_idx(DST)\n\t"
                     "v_mov_b32 v128, %0\n\tv_mov_b32 v129, %1\n\tv_mov_b32 v130, %2\n\tv_mov_b32 v131, %3\n\t"
                     "v_mov_b32 v132, %4\n\tv_mov_b32 v133, %5\n\tv_mov_b32 v134, %6\n\tv_mov_b32 v135, %7\n\t"
                     "s_set_gpr_idx_off"
                     :: "v"(pf[0][0]), "v"(pf[0][1]), "v"(pf[0][2]), "v"(pf[0][3]), "v"(pf[1][0]), "v"(pf[1][1]), "v"(pf[1][2]), "v"(pf[1][3]), "s"(8 * k));
      }
    }
  }
  l = swap_sum(l);
  *(LAS f32x2*)(lds + LDS_ML + w * 256 + r32 * 8) = f32x2{m, l};
  LGKM0();
  ctl[CTL_MLOK / 4 + w] = 1;

  const unsigned tq = (lane & 15) >> 2, tp = lane & 3, tblk = (lane >> 4) & 1;
  const unsigned vaddr0 = LDS_RING + (4 * hi + tq) * 256 + ((tq ^ (pos & 3)) << 6) + ((2 * tblk + (tp >> 1)) << 4) + ((tp & 1) << 3);
  f32x16 o[4];
#pragma unroll
  for (int i = 0; i < 4; ++i)
#pragma unroll
    for (int r = 0; r < 16; ++r) o[i][r] = 0.f;
  auto weights = [&]() {
    wait_above<1>(lbase + LDS_CTL + CTL_MLOK + min(lane, NC - 1) * 4, 0);
    unsigned a = LDS_ML + r32 * 8;
    asm volatile("" : "+v"(a) :: "memory");
    float M = -3.0e38f, L = 0.f;
    f32x2 ml[NC];
#pragma unroll
    for (int i = 0; i < NC; ++i) { ml[i] = *(LAS f32x2*)(lds + a + i * 256); M = fmaxf(M, ml[i][0]); }
#pragma unroll
    for (int i = 0; i < NC; ++i) L = fmaf(__builtin_amdgcn_exp2f((ml[i][0] - M) * CS), ml[i][1], L);
    const float inv = 1.0f / L;
    *(LAS float*)(lds + (a >> 1) + (LDS_WT - LDS_ML / 2) + pos * 128) = __builtin_amdgcn_exp2f((mprev - M) * CS) * inv;
    const float ct = __builtin_amdgcn_exp2f((m - M) * CS) * inv;
#pragma unroll
    for (int s = 0; s < 2; ++s) {
      u32x4 x = *(LAS u32x4*)(lds + ppst + W_LAST + s * 1024);
#pragma unroll
      for (int e = 0; e < 4; ++e) x[e] = pack2(__uint_as_float(x[e] << 16) * ct, __uint_as_float(x[e] & 0xffff0000u) * ct);
      *(LAS u32x4*)(lds + ppst + W_LAST + s * 1024) = x;
    }
    LGKM0();
    ctl[CTL_WTOK / 4 + w] = 1;
  };
  resc &= ~1u;
  if (nmine <= 3) weights();
#pragma unroll 1
  for (int k = 0; k < nmine - 1; ++k) {
    if (k == 2) weights();
    if ((resc >> k) & 1) {
      const float alpha = *(LAS float*)(lds + palp + k * 128);
#pragma unroll
      for (int i = 0; i < 4; ++i)
#pragma unroll
        for (int r = 0; r < 16; ++r) o[i][r] *= alpha;
    }
    const unsigned u = NT + ((k == 0 && NT >= 3 * NC) ? pos : w + NC * k);
    u32x4 pf[2];
    if (k >= KREG) {
      pf[0] = *(LAS u32x4*)(lds + ppst + (k - KREG) * 2048);
      pf[1] = *(LAS u32x4*)(lds + ppst + (k - KREG) * 2048 + 1024);
    } else {
      asm volatile("s_set_gpr_idx_on %8, gpr_idx(SRC0)\n\t"
                   "v_mov_b32 %0, v128\n\tv_mov_b32 %1, v129\n\tv_mov_b32 %2, v130\n\tv_mov_b32 %3, v131\n\t"
                   "v_mov_b32 %4, v132\n\tv_mov_b32 %5, v133\n\tv_mov_b32 %6, v134\n\tv_mov_b32 %7, v135\n\t"
                   "s_set_gpr_idx_off\n\ts_nop 1"
                   : "=v"(pf[0][0]), "=v"(pf[0][1]), "=v"(pf[0][2]), "=v"(pf[0][3]), "=v"(pf[1][0]), "=v"(pf[1][1]), "=v"(pf[1][2]), "=v"(pf[1][3])
                   : "s"(8 * k));
    }
    asm volatile("" : "+v"(pf[0]), "+v"(pf[1]), "+v"(o[0]), "+v"(o[1]), "+v"(o[2]), "+v"(o[3]));
    if (k == nmine - 2) wait_above<1>(lbase + LDS_CTL + CTL_WTOK + min(lane, NC - 1) * 4, 0);
    wait_above<1>(aland, 4 * u);
    bf16x8 vf[4][2];
#pragma unroll
    for (int i = 0; i < 4; ++i) {
      const unsigned a = (vaddr0 + ((u & 7) << 13)) ^ (i << 6);
#pragma unroll
      for (int s = 0; s < 2; ++s) {
        const s16x4 lo = __builtin_amdgcn_ds_read_tr16_b64_v4i16((LAS s16x4*)(lds + a + 4096 * s));
        const s16x4 up = __builtin_amdgcn_ds_read_tr16_b64_v4i16((LAS s16x4*)(lds + a + 4096 * s + 2048));
        vf[i][s] = __builtin_bit_cast(bf16x8, __builtin_shufflevector(lo, up, 0, 1, 2, 3, 4, 5, 6, 7));
      }
    }
    asm volatile("s_waitcnt lgkmcnt(0)" : "+v"(vf[0][0]), "+v"(vf[0][1]), "+v"(vf[1][0]), "+v"(vf[1][1]), "+v"(vf[2][0]), "+v"(vf[2][1]), "+v"(vf[3][0]), "+v"(vf[3][1]) :: "memory");
    ctl[CTL_FREE / 4 + (u & 7)] = u + 8;
    if (pf_on) prefetch(u);
#pragma unroll
    for (int s = 0; s < 2; ++s)
#pragma unroll
      for (int i = 0; i < 4; ++i) o[i] = __builtin_amdgcn_mfma_f32_32x32x16_bf16(vf[i][s], __builtin_bit_cast(bf16x8, pf[s]), o[i], 0, 0, 0);
  }

  {
    unsigned l16 = lane * 16;
    asm volatile("" : "+v"(l16));
    const unsigned l4 = l16 >> 2, awt = LDS_WT + (l4 & 127);
    auto put = [&](unsigned dst, const f32x16& x) {
#pragma unroll
      for (int j = 0; j < 4; ++j) *(LAS f32x4*)(lds + dst + j * 1024) = f32x4{x[4 * j], x[4 * j + 1], x[4 * j + 2], x[4 * j + 3]};
    };
    float co[6];
    bf16x8 pl[NC][2];
    auto ld_p = [&](int p) {
      pl[p][0] = *(LAS bf16x8*)(lds + LDS_WREG + p * W_SIZE + W_LAST + l16);
      pl[p][1] = *(LAS bf16x8*)(lds + LDS_WREG + p * W_SIZE + W_LAST + l16 + 1024);
    };
#pragma unroll
    for (int i = 0; i < 4; ++i) co[i] = *(LAS float*)(lds + awt + ((pos & 3) ^ i) * 128);
    co[4] = *(LAS float*)(lds + awt + 4 * 128);
    co[5] = *(LAS float*)(lds + awt + 5 * 128);
    ld_p(0); ld_p(1);
    asm volatile("" : "+v"(l16) :: "memory");
    if (nmine > 1) {
      if (pos > 3) put(LDS_RING + ((2 * NT - 2 * NC + pos) & 7) * 8192 + l16, o[0]);
#pragma unroll
      for (int i = 1; i < 4; ++i) put(LDS_WREG + pos * W_SIZE + (i - 1) * 4096 + l16, o[i]);
      LGKM0();
      ctl[CTL_ODONE / 4 + pos] = 1;
    }
    if (pos < 4) {
      f32x16 of = o[0], ot;
#pragma unroll
      for (int r = 0; r < 16; ++r) ot[r] = 0.f;
      auto src_o = [&](int p) -> unsigned {
        const int i = pos ^ (p & 3);
        return l16 + (i == 0 ? LDS_RING + ((2 * NT - 2 * NC + p) & 7) * 8192 : LDS_WREG + p * W_SIZE + (i - 1) * 4096);
      };
      auto fma_o = [&](unsigned src, float c) {
#pragma unroll
        for (int j = 0; j < 4; ++j) {
          const f32x4 x = *(LAS f32x4*)(lds + src + j * 1024);
#pragma unroll
          for (int e = 0; e < 4; ++e) of[4 * j + e] = fmaf(x[e], c, of[4 * j + e]);
        }
      };
      auto mma_t = [&](int p, bf16x8 p0, bf16x8 p1) {
        const unsigned a = vaddr0 + (((2 * NT - NC + p) & 7) << 13);
        bf16x8 vf[2];
#pragma unroll
        for (int s = 0; s < 2; ++s) {
          const s16x4 lo = __builtin_amdgcn_ds_read_tr16_b64_v4i16((LAS s16x4*)(lds + a + 4096 * s));
          const s16x4 up = __builtin_amdgcn_ds_read_tr16_b64_v4i16((LAS s16x4*)(lds + a + 4096 * s + 2048));
          vf[s] = __builtin_bit_cast(bf16x8, __builtin_shufflevector(lo, up, 0, 1, 2, 3, 4, 5, 6, 7));
        }
        ot = __builtin_amdgcn_mfma_f32_32x32x16_bf16(vf[0], p0, ot, 0, 0, 0);
        ot = __builtin_amdgcn_mfma_f32_32x32x16_bf16(vf[1], p1, ot, 0, 0, 0);
      };
      if (NT >= 2 * NC) {
#define PIN_OF() asm volatile("" : "+v"(of), "+v"(ot))
#pragma unroll
        for (int r = 0; r < 16; ++r) of[r] *= co[0];
        const bool wl = (l4 & 24) == 0;
        const unsigned ua = 4 * (2 * NT - NC);
        wait_above_v(wl ? aland : lbase + LDS_CTL + CTL_ODONE + (l4 & 12), wl ? ua : 0u);
        mma_t(0, pl[0][0], pl[0][1]);
#pragma unroll
        for (int i = 1; i < 4; ++i) fma_o(LDS_WREG + (pos ^ i) * W_SIZE + (i - 1) * 4096 + l16, co[i]);
        PIN_OF();
        ld_p(2); ld_p(3); ld_p(4); ld_p(5);
        wait_above_v(wl ? aland : lbase + LDS_CTL + CTL_ODONE + 16 + (l4 & 4), wl ? ua + 8 : 0u);
        mma_t(1, pl[1][0], pl[1][1]);
        mma_t(2, pl[2][0], pl[2][1]);
        fma_o(src_o(4), co[4]);
        fma_o(src_o(5), co[5]);
#pragma unroll
        for (int r = 0; r < 16; ++r) { ot[r] += of[r]; of[r] = 0.f; }
        asm volatile("" : "+v"(ot));
        wait_above<0>(aland, ua + 20);
        mma_t(3, pl[3][0], pl[3][1]);
        mma_t(4, pl[4][0], pl[4][1]);
        mma_t(5, pl[5][0], pl[5][1]);
        of = ot;
      } else {
        wait_above<1>(lbase + LDS_CTL + CTL_WTOK + min(l4, (NC - 1) * 4u), 0);
        const float cw = *(LAS float*)(lds + awt + pos * 128);
#pragma unroll
        for (int r = 0; r < 16; ++r) of[r] *= cw;
#pragma unroll 1
        for (int p = max(NC - NT, 0); p < NC; ++p) {
          if (p != pos && NT - 2 * NC + p >= 0) {
            wait_above<1>(lbase + LDS_CTL + CTL_ODONE + p * 4, 0);
            fma_o(src_o(p), *(LAS float*)(lds + awt + p * 128));
          }
          wait_above<1>(aland, 4 * (2 * NT - NC + p));
          mma_t(p, *(LAS bf16x8*)(lds + LDS_WREG + p * W_SIZE + W_LAST + l16), *(LAS bf16x8*)(lds + LDS_WREG + p * W_SIZE + W_LAST + l16 + 1024));
        }
#pragma unroll
        for (int r = 0; r < 16; ++r) of[r] += ot[r];
      }
      const __amdgpu_buffer_rsrc_t ro = __builtin_amdgcn_make_buffer_rsrc(outp, 0, NB * HQ * SQ * HD * 2, 0x00020000);
      const unsigned orow = (unsigned)(b * HQ + 2 * h) * SQ * HD * 2 + pos * 64 + ((l16 & 511) << 4) + ((l16 >> 5) & 16);
#pragma unroll
      for (int j = 0; j < 2; ++j) {
        const unsigned a0 = pack2(of[8 * j], of[8 * j + 1]), a1 = pack2(of[8 * j + 2], of[8 * j + 3]);
        const unsigned b0 = pack2(of[8 * j + 4], of[8 * j + 5]), b1 = pack2(of[8 * j + 6], of[8 * j + 7]);
        auto s0 = __builtin_amdgcn_permlane32_swap(a0, b0, false, false);
        auto s1 = __builtin_amdgcn_permlane32_swap(a1, b1, false, false);
        u32x4 val = {s0[0], s1[0], s0[1], s1[1]};
        __builtin_amdgcn_raw_buffer_store_b128(val, ro, orow + j * 32, 0, 0);
      }
    }
  }
}
}

extern "C" void kernel_launch(void* const* d_in, const int* in_sizes, int n_in, void* d_out, int out_size, void* d_ws,
                              size_t ws_size, hipStream_t stream) {
  (void)n_in; (void)out_size; (void)d_ws; (void)ws_size;
  const unsigned nphys = (unsigned)(in_sizes[1] / (PAGE * HKV * HD));
  const unsigned kvbytes = nphys * PAGE_BYTES;
  (void)hipFuncSetAttribute((const void*)paged_attn, hipFuncAttributeMaxDynamicSharedMemorySize, LDS_TOTAL);
  hipLaunchKernelGGL(paged_attn, dim3(256), dim3(512), LDS_TOTAL, stream, d_in[0], d_in[1], d_in[2], (const int*)d_in[3],
                     (const int*)d_in[4], d_out, kvbytes, nphys);
}
```

```cpp
#include <hip/hip_runtime.h>
#include <hip/hip_bf16.h>

#define PACE_X16 433
#define SOFT 15
#define GAP_TICKS 10
#define END_TICKS 160
#define WIN_TICKS 87
#define WIN_C1 115
#define LATE_TICKS 100
#define HALT_SHIFT 240
#define HALT_FROM 400
#define CODEPF 11520
#define PF_LEAD 180
#define PF_X64 72
#define PF_PER 4
#define PF_PSTEP 2
#define PF_RUN 3

namespace {
constexpr int NB = 16, HQ = 32, HKV = 16, SQ = 16, HD = 128, PAGE = 16, NBPS = 256, SKV = NBPS * PAGE;
constexpr unsigned PAGE_BYTES = PAGE * HKV * HD * 2;
constexpr unsigned TLBG = 0x200000u;
constexpr int U0 = 4;
constexpr int NC = 6;
constexpr int KREG = 16, KLDS = 5;
static_assert(NC * (KREG + KLDS + 1) >= NBPS / 2, "P storage");
constexpr unsigned LDS_RING = 0;
constexpr unsigned LDS_WREG = 65536;
constexpr unsigned W_ALP = KLDS * 2048;
constexpr unsigned W_LAST = W_ALP + (KREG + KLDS + 1) * 128;
constexpr unsigned W_SIZE = W_LAST + 2048;
static_assert(W_LAST >= 3 * 4096 && W_SIZE % 256 == 0, "consumer region");
constexpr unsigned LDS_TBL = LDS_WREG + W_SIZE;
constexpr unsigned LDS_SRT = LDS_WREG + 3 * W_SIZE;
constexpr unsigned LDS_Q = LDS_WREG;
constexpr unsigned LDS_ML = LDS_WREG + NC * W_SIZE;
constexpr unsigned LDS_WT = LDS_ML + NC * 256;
constexpr unsigned LDS_CTL = LDS_WT + NC * 128;
constexpr unsigned CTL_LAND = 0;
constexpr unsigned CTL_QOK = 16;
constexpr unsigned CTL_SORT = 24;
constexpr unsigned CTL_FREE = 32;
constexpr unsigned CTL_MLOK = 64;
constexpr unsigned CTL_ODONE = 96;
constexpr unsigned CTL_WTOK = 128;
constexpr unsigned LDS_DUMMY = LDS_CTL + 256;
constexpr unsigned LDS_PLIST = LDS_DUMMY + 256;
constexpr unsigned LDS_TOTAL = LDS_PLIST + 1024;
static_assert(LDS_TOTAL <= 163840, "lds");

typedef __attribute__((ext_vector_type(8))) __bf16 bf16x8;
typedef __attribute__((ext_vector_type(2))) __bf16 bf16x2;
typedef __attribute__((ext_vector_type(4))) short s16x4;
typedef __attribute__((ext_vector_type(16))) float f32x16;
typedef __attribute__((ext_vector_type(4))) float f32x4;
typedef __attribute__((ext_vector_type(2))) float f32x2;
typedef __attribute__((ext_vector_type(4))) unsigned u32x4;
typedef __attribute__((ext_vector_type(2))) unsigned u32x2;
#define LAS __attribute__((address_space(3)))

__device__ __forceinline__ float swap_max(float x) {
  auto r = __builtin_amdgcn_permlane32_swap(__float_as_uint(x), __float_as_uint(x), false, false);
  return fmaxf(__uint_as_float(r[0]), __uint_as_float(r[1]));
}
__device__ __forceinline__ float swap_sum(float x) {
  auto r = __builtin_amdgcn_permlane32_swap(__float_as_uint(x), __float_as_uint(x), false, false);
  return __uint_as_float(r[0]) + __uint_as_float(r[1]);
}
__device__ __forceinline__ unsigned pack2(float a, float b) {
  bf16x2 v = {(__bf16)a, (__bf16)b};
  return __builtin_bit_cast(unsigned, v);
}
template <int CTRL> __device__ __forceinline__ int dpp_add(int x) { return x + __builtin_amdgcn_update_dpp(0, x, CTRL, 0xf, 0xf, true); }
__device__ __forceinline__ int to_sgpr(int v) {
  int r;
  asm volatile("s_nop 1\n\tv_readfirstlane_b32 %0, %1\n\ts_nop 1" : "=s"(r) : "v"(v));
  return r;
}
#define LGKM0() asm volatile("s_waitcnt lgkmcnt(0)" ::: "memory")
#define VMWAIT(n) asm volatile("s_waitcnt vmcnt(" #n ")" ::: "memory")
#define CBAR() asm volatile("" ::: "memory")
template <int SLEEP> __device__ __forceinline__ void wait_above(unsigned addr, unsigned u) {
  unsigned v, cnt;
  if (SLEEP)
    asm volatile("s_mov_b32 %1, 0x800000\n\ts_branch 2f\n"
                 "1:\n\ts_sleep %4\n"
                 "2:\n\tds_read_b32 %0, %2\n\ts_sub_u32 %1, %1, 1\n\ts_waitcnt lgkmcnt(0)\n\t"
                 "v_cmp_ge_u32 vcc, %3, %0\n\ts_cbranch_scc1 3f\n\ts_nop 1\n\ts_cbranch_vccnz 1b\n3:"
                 : "=&v"(v), "=&s"(cnt) : "v"(addr), "s"(u), "n"(SLEEP) : "memory", "scc", "vcc");
  else
    asm volatile("s_mov_b32 %1, 0x800000\n"
                 "1:\n\tds_read_b32 %0, %2\n\ts_sub_u32 %1, %1, 1\n\ts_waitcnt lgkmcnt(0)\n\t"
                 "v_cmp_ge_u32 vcc, %3, %0\n\ts_cbranch_scc1 3f\n\ts_nop 1\n\ts_cbranch_vccnz 1b\n3:"
                 : "=&v"(v), "=&s"(cnt) : "v"(addr), "s"(u) : "memory", "scc", "vcc");
}

__device__ __forceinline__ void wait_above_v(unsigned addr, unsigned u) {
  unsigned v, cnt;
  asm volatile("s_mov_b32 %1, 0x800000\n"
               "1:\n\tds_read_b32 %0, %2\n\ts_sub_u32 %1, %1, 1\n\ts_waitcnt lgkmcnt(0)\n\t"
               "v_cmp_ge_u32 vcc, %3, %0\n\ts_cbranch_scc1 3f\n\ts_nop 1\n\ts_cbranch_vccnz 1b\n3:"
               : "=&v"(v), "=&s"(cnt) : "v"(addr), "v"(u) : "memory", "scc", "vcc");
}

__global__ void __launch_bounds__(512, 1) __attribute__((amdgpu_num_vgpr(64)))
paged_attn(const void* __restrict__ qp, const void* __restrict__ kp, const void* __restrict__ vp, const int* __restrict__ bt,
           const int* __restrict__ lens, void* __restrict__ outp, unsigned kvbytes, unsigned nphys) {
  extern __shared__ __attribute__((aligned(16))) char smem[];
  LAS char* lds = (LAS char*)smem;
  const int lane0 = threadIdx.x & 63;
  const int wv = __builtin_amdgcn_readfirstlane(threadIdx.x >> 6);
  const int g = blockIdx.x;
  const int h = g >> 4;
  constexpr float CS = 0.08838834764831845f * 1.4426950408889634f;
  volatile LAS unsigned* ctl = (volatile LAS unsigned*)(lds + LDS_CTL);

  const unsigned lbase = (unsigned)(size_t)lds;

  unsigned lraw, first;
  u32x4 lq;
  {
    asm volatile("global_load_dword %0, %3, %4\n\t"
                 "global_load_dwordx4 %1, %5, %4\n\t"
                 "global_load_dword %2, %6, %7"
                 : "=&v"(lraw), "=&v"(lq), "=&v"(first)
                 : "v"((lane0 & 15) * 4), "s"(lens), "v"(lane0 & 48), "v"((lane0 & 15) * (wv < 2 ? NBPS * 4 : 0) + (lane0 >> 4) * 8 + (wv & 1) * 4), "s"(bt),
                   "s"(qp), "s"(kp), "s"(vp), "s"(outp), "s"(kvbytes), "s"(nphys)
                 : "memory");
    __builtin_amdgcn_sched_barrier(0);
  }
  if (wv >= 4) {
    if (wv == 7) {
      const __amdgpu_buffer_rsrc_t rb = __builtin_amdgcn_make_buffer_rsrc((void*)bt, 0, NB * NBPS * 4, 0x00020000);
#define TBL4(SO) "buffer_load_dwordx4 %2, %1, " SO " offen lds\n\tbuffer_load_dwordx4 %2, %1, " SO " offen offset:1024 lds\n\t" \
                 "buffer_load_dwordx4 %2, %1, " SO " offen offset:2048 lds\n\tbuffer_load_dwordx4 %2, %1, " SO " offen offset:3072 lds\n\t"
      asm volatile("s_mov_b32 m0, %0\n\ts_nop 0\n\t" TBL4("0") "s_add_u32 m0, m0, 4096\n\ts_nop 0\n\t" TBL4("%3")
                   "s_add_u32 m0, m0, %6\n\ts_nop 0\n\t" TBL4("%4") "s_add_u32 m0, m0, 4096\n\ts_nop 0\n\t" TBL4("%5")
                   :: "s"(lbase + LDS_TBL), "s"(rb), "v"(lane0 * 16), "s"(4096), "s"(8192), "s"(12288), "n"(W_SIZE - 4096) : "memory", "scc");
    }
    unsigned long long pc;
    asm volatile("s_getpc_b64 %0" : "=s"(pc));
    const __amdgpu_buffer_rsrc_t rt = __builtin_amdgcn_make_buffer_rsrc(
        wv == 5 ? (void*)pc : wv == 4 ? (void*)vp : (void*)kp, 0, wv == 5 ? CODEPF : (wv != 7 && (g & 8)) ? (int)kvbytes : 0, 0x00020000);
    const unsigned mul = wv == 5 ? 512u : 16 * TLBG + 4224u, add = wv == 5 ? ((g >> 3) & 3) * 128 : (g >> 4) * (TLBG + 33024u);
    asm volatile("s_mov_b32 m0, %0\n\ts_nop 0\n\tbuffer_load_dword %1, %2, 0 offen lds"
                 :: "s"(lbase + LDS_DUMMY), "v"((lane0 & 31) * mul + add), "s"(rt) : "memory");
    *(LAS u32x4*)(lds + LDS_SRT + (threadIdx.x - 256) * 16) = u32x4{0u, 0u, 0u, 0u};
    if (threadIdx.x < 296) ctl[lane0] = ((lane0 >> 3) == CTL_FREE / 32) ? (lane0 & 7) : 0u;
    asm volatile("s_waitcnt lgkmcnt(0)\n\ts_barrier" ::: "memory");
  }
  if (wv < 2) __builtin_amdgcn_s_setprio(3);
  const unsigned rstride = wv < 2 ? 4096 : 256;
  unsigned vo0 = (lane0 >> 4) * rstride + (((unsigned)(lane0 & 15) ^ ((lane0 >> 4) << 2)) << 4) + (wv < 2 ? h * (HD * 2) : 0);
  unsigned vo1 = (vo0 + 4 * rstride - 1024) ^ 16, vo2 = (vo0 + 8 * rstride - 2048) ^ 32, vo3 = (vo0 + 12 * rstride - 3072) ^ 48;
  const unsigned want = (g & 8) ? 15 - (g & 7) : (g & 7);
  const unsigned dst0 = lbase + (wv < 2 ? LDS_RING : LDS_Q) + (wv & 1) * 4096;
  asm volatile("s_cmp_ge_u32 %8, 4\n\ts_cbranch_scc1 1f\n\ts_waitcnt vmcnt(0)\n1:\n\ts_waitcnt vmcnt(1)" : "+v"(lraw), "+v"(lq), "+v"(first), "+v"(vo0), "+v"(vo1), "+v"(vo2), "+v"(vo3) : "s"(dst0), "s"(wv) : "memory", "scc");
  unsigned lm1 = min(lraw - 1u, (unsigned)SKV - 1u);
  unsigned rank = 0;
  {
    const unsigned key = lm1 * 16 + (lane0 & 15);
#pragma unroll
    for (int j = 0; j < 4; ++j) rank += (min(lq[j] - 1u, (unsigned)SKV - 1u) * 16 + ((lane0 >> 4) * 4 + j) > key) ? 1u : 0u;
    auto r1 = __builtin_amdgcn_permlane16_swap(rank, rank, false, false);
    rank = r1[0] + r1[1];
    auto r2 = __builtin_amdgcn_permlane32_swap(rank, rank, false, false);
    rank = r2[0] + r2[1];
  }
  first = min(first, nphys - 1) * PAGE_BYTES;
  const int b = __builtin_ctz((unsigned)__builtin_amdgcn_ballot_w64(rank == want) | 0x8000u);
  unsigned long long tstart = 0;
  if (wv < 4) {
    const bool qok = (int)__builtin_amdgcn_readlane(lraw, b) > 0;
    const __amdgpu_buffer_rsrc_t r0 = __builtin_amdgcn_make_buffer_rsrc(wv < 2 ? (void*)kp : (void*)qp, 0,
                                                                        wv < 2 ? (int)kvbytes : qok ? NB * HQ * SQ * HD * 2 : 0, 0x00020000);
    const unsigned so = wv < 2 ? __builtin_amdgcn_readlane(first, b) : qok ? (unsigned)((b * HQ + 2 * h) * SQ * HD * 2) + (wv & 1) * 4096 : 0u;
    asm volatile("s_waitcnt vmcnt(0)\n\t"
                 "s_mov_b32 m0, %2\n\t"
                 "s_memrealtime %0\n\t"
                 "s_nop 2\n\t"
                 "buffer_load_dwordx4 %4, %1, %3 offen nt lds\n\t"
                 "buffer_load_dwordx4 %5, %1, %3 offen offset:1024 nt lds\n\t"
                 "buffer_load_dwordx4 %6, %1, %3 offen offset:2048 nt lds\n\t"
                 "buffer_load_dwordx4 %7, %1, %3 offen offset:3072 nt lds\n\t"
                 "s_waitcnt lgkmcnt(0)\n\t"
                 "s_barrier"
                 : "=&s"(tstart) : "s"(r0), "s"(dst0), "s"(so), "v"(vo0), "v"(vo1), "v"(vo2), "v"(vo3) : "memory");
  }
  asm volatile("" : "+v"(lm1), "+v"(rank), "+v"(first) :: "memory");
  __builtin_amdgcn_sched_barrier(0);
  const int bp = __builtin_ctz((unsigned)__builtin_amdgcn_ballot_w64(rank == 15 - want) | 0x8000u);
  const int len = __builtin_amdgcn_readlane(lm1, b) + 1, nown = (len + PAGE - 1) / PAGE, NT = (nown + 1) >> 1;
  const int npl = (__builtin_amdgcn_readlane(lm1, bp) >> 4) + 1, NTL = (npl + 1) >> 1;
  auto total = [&]() __attribute__((always_inline)) {
    int t = lm1 + 1;
    t = dpp_add<0x121>(t); t = dpp_add<0x122>(t); t = dpp_add<0x124>(t); t = dpp_add<0x128>(t);
    return t;
  };

  auto sort_pages = [&](int lane, int bb, int n, unsigned scr, unsigned dst) __attribute__((always_inline)) {
    u32x4 e = *(LAS u32x4*)(lds + LDS_TBL + (bb >> 3) * W_SIZE + (bb & 7) * 1024 + lane * 16);
    bool in[4];
#pragma unroll
    for (int j = 0; j < 4; ++j) {
      e[j] = min(e[j], nphys - 1);
      in[j] = 4 * lane + j >= 2 * U0 && 4 * lane + j < n - 1;
      __hip_atomic_fetch_or((LAS unsigned*)(lds + scr + ((e[j] >> 5) & 255) * 8), in[j] ? 1u << (e[j] & 31) : 0u, __ATOMIC_RELAXED, __HIP_MEMORY_SCOPE_WAVEFRONT);
    }
    CBAR();
    u32x4 a = *(LAS u32x4*)(lds + scr + lane * 32), c = *(LAS u32x4*)(lds + scr + lane * 32 + 16);
    const int n0 = __builtin_popcount(a[0]), n1 = n0 + __builtin_popcount(a[2]), n2 = n1 + __builtin_popcount(c[0]), n3 = n2 + __builtin_popcount(c[2]);
    int sc = n3;
    sc = dpp_add<0x111>(sc); sc = dpp_add<0x112>(sc); sc = dpp_add<0x114>(sc); sc = dpp_add<0x118>(sc);
    sc += __builtin_amdgcn_update_dpp(0, sc, 0x142, 0xa, 0xf, false);
    sc += __builtin_amdgcn_update_dpp(0, sc, 0x143, 0xc, 0xf, false);
    const bool ok = nphys <= 8192u && __builtin_amdgcn_readlane(sc, 63) == n - 1 - 2 * U0;
    sc += 2 * U0 - n3;
    a[1] = sc; a[3] = sc + n0; c[1] = sc + n1; c[3] = sc + n2;
    *(LAS u32x4*)(lds + scr + lane * 32) = a;
    *(LAS u32x4*)(lds + scr + lane * 32 + 16) = c;
    CBAR();
    u32x2 wp[4];
#pragma unroll
    for (int j = 0; j < 4; ++j) wp[j] = *(LAS u32x2*)(lds + scr + ((e[j] >> 5) & 255) * 8);
#pragma unroll
    for (int j = 0; j < 4; ++j) {
      const unsigned r = wp[j][1] + __builtin_popcount(wp[j][0] & ((1u << (e[j] & 31)) - 1u));
      *(LAS unsigned*)(lds + dst + ((ok && in[j]) ? r : 4 * lane + j) * 4) = e[j] * PAGE_BYTES;
    }
    CBAR();
  };

  if (__builtin_expect(wv < 2, 1)) {
    const int half = wv, lane = lane0;
    const unsigned vland = lbase + LDS_CTL + CTL_LAND + half * 4;
    const unsigned vfree = lbase + LDS_CTL + CTL_FREE + (lane & 7) * 4;
    const unsigned ring = dst0;
    unsigned l0 = __builtin_amdgcn_ds_bpermute((b + 16 * lane) * 4, first), l1 = 0;
    const unsigned a0 = lbase + LDS_SRT + 4096 + min(2 * lane + half, nown - 1) * 4, a1 = lbase + LDS_SRT + 4096 + min(2 * lane + half + 128, nown - 1) * 4;
    const int rot = (NT >= 3 * NC) ? NT % NC : 0;
    const unsigned vperm = (lane < NC ? (lane + rot >= NC ? lane + rot - NC : lane + rot) : lane) * 4;
    const int T16 = total() * PACE_X16;
    const int step = to_sgpr((int)((float)T16 * __builtin_amdgcn_rcpf((float)max(len, 32))));
    const int stepv = min(step, to_sgpr((int)((float)((T16 >> 5) - END_TICKS * 256) * __builtin_amdgcn_rcpf((float)max(NT - NC - 1, 1)))));
    const int win = to_sgpr((int)((float)(((h & 3) == 1 ? WIN_C1 : WIN_TICKS) * 1024) * __builtin_amdgcn_rcpf((float)step) + 0.5f));
    const int hmin = to_sgpr((int)((float)(HALT_FROM * 1024) * __builtin_amdgcn_rcpf((float)step)));
    const unsigned due0 = ((unsigned)tstart << 8) + half * (step >> 1);
    const int lrows = len - (2 * (NT - 1) + half) * PAGE;
    unsigned vf, vt, lc;
    asm volatile(
        "s_waitcnt lgkmcnt(0)\n\t"
        "s_memrealtime s[68:69]\n\t"
        "ds_read_b32 %[vf], %[vfree]\n\t"
        "s_mov_b32 s60, 1\n\t"
        "s_mov_b32 s61, 4\n\t"
        "s_mov_b32 s62, 0x1000000\n\t"
        "s_mov_b32 s66, 0\n\t"
        "s_min_u32 s67, %[nt], %[u0]\n\t"
        "s_mov_b32 s75, %[nrec]\n\t"
        "s_mov_b32 s72, 0x2000\n\t"
        "s_mov_b32 s73, 1\n\t"
        "s_mov_b32 s74, %[due1]\n\t"
        "s_mov_b32 s85, %[duev]\n\t"
        "s_mov_b32 s86, %[late]\n\t"
        "s_mov_b32 s87, %[step]\n\t"
        "s_mov_b64 s[76:77], %[kp]\n\t"
        "s_mov_b32 s78, %[nrec]\n\t"
        "s_mov_b32 s79, 0x20000\n\t"
        "s_and_b32 s77, s77, 0xffff\n\t"
        "v_mov_b32 %[lc], %[l0]\n\t"
        "s_getpc_b64 s[70:71]\n"
        ".LldA%=:\n\t"
        "s_add_u32 s80, s70, .LldL%=-.LldA%=\n\t"
        "s_addc_u32 s71, s71, 0\n\t"
        "s_branch .LldF%=\n"
        ".LldR%=:\n\t"
        "s_sub_u32 s62, s62, 1\n\t"
        "s_cbranch_scc1 .LldE%=\n"
        ".LldT%=:\n\t"
        "s_getreg_b32 s64, hwreg(HW_REG_IB_STS)\n\t"
        "s_and_b32 s65, s64, 15\n\t"
        "s_bfe_u32 s64, s64, 0x20016\n\t"
        "s_lshl4_add_u32 s81, s64, s65\n\t"
        "s_add_u32 s64, s81, 3\n\t"
        "s_and_b32 s64, s64, 60\n\t"
        "s_sub_u32 s65, s61, s64\n\t"
        "s_cmp_lg_u32 s65, s66\n\t"
        "s_cbranch_scc1 .LldP%=\n"
        ".LldC%=:\n\t"
        "s_waitcnt lgkmcnt(0)\n\t"
        "v_cmp_eq_u32 vcc, s60, %[vf]\n\t"
        "s_lshl_b32 s64, s68, 8\n\t"
        "s_memrealtime s[68:69]\n\t"
        "ds_read_b32 %[vf], %[vfree]\n\t"
        "s_sub_u32 s64, s64, s74\n\t"
        "s_cmp_lt_i32 s64, 0\n\t"
        "s_cbranch_scc1 .LldR%=\n\t"
        "s_cmp_gt_i32 s64, s86\n\t"
        "s_cbranch_scc1 .LldH%=\n"
        ".LldI%=:\n\t"
        "s_cbranch_vccz .LldR%=\n\t"
        "s_cmp_gt_u32 s81, %[win]\n\t"
        "s_cbranch_scc1 .LldR%=\n\t"
        "buffer_load_dwordx4 %[o0], s[76:79], s63 offen nt lds\n\t"
        "s_add_u32 s74, s74, s87\n\t"
        "s_add_u32 s73, s73, 1\n\t"
        "buffer_load_dwordx4 %[o1], s[76:79], s63 offen offset:1024 nt lds\n\t"
        "s_add_u32 s60, s60, 1\n\t"
        "s_add_u32 s61, s61, 4\n\t"
        "s_add_u32 s86, s64, %[late]\n\t"
        "buffer_load_dwordx4 %[o2], s[76:79], s63 offen offset:2048 nt lds\n\t"
        "s_add_u32 s72, s72, 0x2000\n\t"
        "s_and_b32 s72, s72, 0xe000\n\t"
        "buffer_load_dwordx4 %[o3], s[76:79], s63 offen offset:3072 nt lds\n"
        ".LldF%=:\n\t"
        "v_readlane_b32 s63, %[lc], s73\n\t"
        "s_add_u32 m0, s72, %[ring]\n\t"
        "s_cmp_eq_u32 s73, %[ntm]\n\t"
        "s_cselect_b32 s78, s75, %[nrec]\n\t"
        "s_cmp_lg_u32 s60, s67\n\t"
        "s_cbranch_scc1 .LldT%=\n\t"
        "s_cmp_lg_u32 s60, %[u0]\n\t"
        "s_cbranch_scc1 .LldG%=\n\t"
        "s_cmp_le_u32 %[nt], %[u0]\n\t"
        "s_cbranch_scc1 .LldG%=\n"
        ".LldS%=:\n\t"
        "ds_read_b32 %[lc], %[vland] offset:%[srt]\n\t"
        "ds_read_b32 %[l0], %[a0]\n\t"
        "ds_read_b32 %[l1], %[a1]\n\t"
        "s_sub_u32 s62, s62, 1\n\t"
        "s_cbranch_scc1 .LldE%=\n\t"
        "s_waitcnt lgkmcnt(0)\n\t"
        "v_readfirstlane_b32 s64, %[lc]\n\t"
        "s_cmp_eq_u32 s64, 0\n\t"
        "s_cbranch_scc1 .LldS%=\n\t"
        "s_max_i32 s65, %[lrows], 0\n\t"
        "s_lshl_b32 s65, s65, 12\n\t"
        "s_add_u32 s64, s64, s65\n\t"
        "s_sub_u32 s64, s64, 1\n\t"
        "s_cmp_lt_i32 %[lrows], 16\n\t"
        "s_cselect_b32 s75, s64, %[nrec]\n\t"
        "s_min_u32 s67, %[nt], 64\n\t"
        "v_mov_b32 %[lc], %[l0]\n\t"
        "s_branch .LldF%=\n"
        ".LldG%=:\n\t"
        "s_cmp_eq_u32 s73, %[nt]\n\t"
        "s_cbranch_scc1 .LldV%=\n\t"
        "v_mov_b32 %[lc], %[l1]\n\t"
        "v_readfirstlane_b32 s63, %[l1]\n\t"
        "s_add_u32 s67, s60, %[nt]\n\t"
        "s_sub_u32 s67, s67, 64\n\t"
        "s_branch .LldT%=\n"
        ".LldV%=:\n\t"
        "s_cmp_lg_u32 s60, %[nt]\n\t"
        "s_cbranch_scc1 .LldD%=\n\t"
        "s_mov_b64 s[76:77], %[vp]\n\t"
        "s_and_b32 s77, s77, 0xffff\n\t"
        "s_mov_b32 s73, 0\n\t"
        "s_min_u32 s67, %[nt], 64\n\t"
        "s_add_u32 s67, s67, s60\n\t"
        "s_mov_b32 s74, s85\n\t"
        "s_max_i32 s87, %[stepv], 0\n\t"
        "s_cmp_eq_u32 %[ntm], 0\n\t"
        "s_cselect_b32 s78, s75, %[nrec]\n\t"
        "ds_bpermute_b32 %[lc], %[vperm], %[l0]\n\t"
        "v_xor_b32 %[o1], 16, %[o1]\n\t"
        "v_xor_b32 %[o2], 32, %[o2]\n\t"
        "v_xor_b32 %[o3], 48, %[o3]\n\t"
        "s_waitcnt lgkmcnt(0)\n\t"
        "v_readfirstlane_b32 s63, %[lc]\n\t"
        "s_branch .LldT%=\n"
        ".LldH%=:\n\t"
        "s_cmp_lt_u32 s61, %[hmin]\n\t"
        "s_cbranch_scc1 .LldI%=\n\t"
        "s_add_u32 s74, s74, %[hshift]\n\t"
        "s_add_u32 s85, s85, %[hshift]\n\t"
        "s_branch .LldR%=\n"
        ".LldD%=:\n\t"
        "s_add_u32 s74, s74, 0x40000000\n\t"
        "s_mov_b32 s60, -1\n\t"
        "s_branch .LldT%=\n"
        ".LldP%=:\n\t"
        "s_min_u32 s64, s64, 32\n\t"
        "s_add_u32 s70, s80, s64\n\t"
        "v_mov_b32 %[vt], s65\n\t"
        "s_mov_b32 s66, s65\n\t"
        "s_setpc_b64 s[70:71]\n\t"
        ".p2align 6\n"
        ".LldL%=:\n\t"
        "s_waitcnt vmcnt(0)\n\t"
        "s_waitcnt vmcnt(4)\n\t"
        "s_waitcnt vmcnt(8)\n\t"
        "s_waitcnt vmcnt(12)\n\t"
        "s_waitcnt vmcnt(16)\n\t"
        "s_waitcnt vmcnt(20)\n\t"
        "s_waitcnt vmcnt(24)\n\t"
        "s_waitcnt vmcnt(28)\n\t"
        "s_waitcnt vmcnt(32)\n\t"
        "ds_write_b32 %[vland], %[vt]\n\t"
        "s_cmp_lg_u32 s65, %[fin]\n\t"
        "s_cbranch_scc1 .LldC%=\n"
        ".LldE%=:\n\t"
        "s_waitcnt vmcnt(0) lgkmcnt(0)\n\t"
        : [vf] "=&v"(vf), [vt] "=&v"(vt), [lc] "=&v"(lc), [l0] "+&v"(l0), [l1] "+&v"(l1), [o1] "+&v"(vo1), [o2] "+&v"(vo2), [o3] "+&v"(vo3)
        : [vperm] "v"(vperm), [a0] "v"(a0), [a1] "v"(a1), [o0] "v"(vo0), [vland] "v"(vland), [vfree] "v"(vfree),
          [kp] "s"(kp), [vp] "s"(vp), [nrec] "s"(kvbytes), [nt] "s"(NT), [ring] "s"(ring),
          [step] "s"(step), [stepv] "s"(stepv), [due1] "s"(due0 + step + SOFT * 256), [duev] "s"(due0 + (to_sgpr(T16) >> 5) + (SOFT + GAP_TICKS) * 256), [lrows] "s"(lrows), [ntm] "s"(NT - 1), [fin] "s"(8 * NT), [u0] "n"(U0), [win] "s"(win), [late] "n"(LATE_TICKS * 256), [hshift] "n"(HALT_SHIFT * 256), [hmin] "s"(hmin), [srt] "n"(CTL_SORT - CTL_LAND)
        : "memory", "scc", "vcc", "s60", "s61", "s62", "s63", "s64", "s65", "s66", "s67", "s68", "s69", "s70", "s71", "s72", "s73", "s74", "s75",
          "s76", "s77", "s78", "s79", "s80", "s81", "s85", "s86", "s87");
    asm volatile("s_endpgm" ::: "memory");
  }

  int lane = lane0;
  asm volatile("" : "+v"(lane));
  if (wv == 7) {
#pragma unroll 1
    for (int i = 0; i < 2; ++i) {
      sort_pages(lane, i ? bp : b, i ? npl : nown, LDS_SRT + i * 2048, i ? LDS_PLIST : LDS_SRT + 4096);
      if (i == 0) {
        const unsigned lo = *(LAS unsigned*)(lds + LDS_SRT + 4096 + (nown - 1) * 4) + 1;
        LGKM0();
        ctl[CTL_SORT / 4 + (lane & 1)] = lo;
      }
    }
  }
  if (wv < 4) {
    VMWAIT(0);
    ctl[CTL_QOK / 4 + (wv & 1)] = 1;
  }
  const int tot = total();
  const int w = wv - 2;
  const int r32 = lane & 31, hi = lane >> 5;
  const int nmine = (NT > w) ? (NT - w + NC - 1) / NC : 0;
  const unsigned aland = lbase + LDS_CTL + CTL_LAND + (lane & 1) * 4;
  const int tots = __builtin_amdgcn_readfirstlane(tot);
  const bool pf_on = npl * 256 * 64 > tots * PF_X64 && npl > nown && (h & 3) == 0;
  const unsigned pf_last = 2 * NT - to_sgpr((int)((float)(2 * NT * (PF_LEAD + 23 * NC)) * 4096.0f * __builtin_amdgcn_rcpf((float)(tot * PACE_X16))));
  const unsigned pf_ratio = to_sgpr((int)((float)NTL * 65536.0f * __builtin_amdgcn_rcpf((float)NT)));
  const unsigned pf_per = PF_PER + (g & 7) * PF_PSTEP, pf_inv = (65536 + pf_per - 1) / pf_per;
  auto prefetch = [&](unsigned op) {
    const unsigned d = pf_last - op;
    if (d >= 1024u) return;
    const unsigned r = d - ((d * pf_inv) >> 16) * pf_per;
    if (r >= PF_RUN) return;
    unsigned tl = 2 * NTL - 1 - NC - (((d - r) * pf_ratio) >> 16) - r;
    if (tl >= 2u * NTL) return;
    const bool isv = tl >= (unsigned)NTL;
    tl -= isv ? NTL : 0;
    const __amdgpu_buffer_rsrc_t rp = __builtin_amdgcn_make_buffer_rsrc(isv ? (void*)vp : (void*)kp, 0, (int)kvbytes, 0x00020000);
    unsigned ln = lane;
    asm volatile("" : "+v"(ln));
    const unsigned vo = (*(LAS unsigned*)(lds + LDS_PLIST + min(2 * tl + (ln >> 5), (unsigned)npl - 1) * 4) & 0x7fff0000u) + (h + 1) * (HD * 2) +
                        ((ln >> 1) & 15) * 4096 + (ln & 1) * 128;
    asm volatile("s_mov_b32 m0, %0\n\ts_nop 4\n\tbuffer_load_dword %2, %1, 0 offen lds" :: "s"(lbase + LDS_DUMMY), "s"(rp), "v"(vo) : "memory");
  };
  const int pos = (w + NC * 22 - NT) % NC;
  const unsigned palp = LDS_WREG + pos * W_SIZE + W_ALP + r32 * 4;
  const unsigned ppst = LDS_WREG + pos * W_SIZE + lane * 16;
  asm volatile("" ::: "v128", "v255");
  unsigned resc = 0;
  float m = -3.0e38f, mprev = -3.0e38f, l = 0.f;

  {
    const unsigned fk = (((unsigned)r32 & 3) << 2) | (((unsigned)r32 >> 2) & 3);
    const unsigned kaddr0 = LDS_RING + r32 * 256 + (((unsigned)hi ^ fk) << 4);
    bf16x8 qf[8];
    wait_above<1>(aland + (CTL_QOK - CTL_LAND), 0);
#pragma unroll
    for (int s = 0; s < 8; ++s) qf[s] = *(LAS bf16x8*)(lds + ((kaddr0 + LDS_Q - LDS_RING) ^ (s << 5)));
#pragma unroll 1
    for (int k = 0; k < nmine; ++k) {
      const unsigned t = w + NC * k;
      wait_above<1>(aland, 4 * t);
      bf16x8 kf[8];
#pragma unroll
      for (int s = 0; s < 8; ++s) kf[s] = *(LAS bf16x8*)(lds + ((kaddr0 + ((t & 7) << 13)) ^ (s << 5)));
      asm volatile("s_waitcnt lgkmcnt(0)" : "+v"(kf[0]), "+v"(kf[1]), "+v"(kf[2]), "+v"(kf[3]), "+v"(kf[4]), "+v"(kf[5]), "+v"(kf[6]), "+v"(kf[7]) :: "memory");
      ctl[CTL_FREE / 4 + (t & 7)] = t + 8;
      if (pf_on) prefetch(t);
      f32x16 sc;
#pragma unroll
      for (int r = 0; r < 16; ++r) sc[r] = 0.f;
#pragma unroll
      for (int s = 0; s < 8; ++s) sc = __builtin_amdgcn_mfma_f32_32x32x16_bf16(kf[s], qf[s], sc, 0, 0, 0);
      if ((int)t == NT - 1) {
        const int kb = 2 * t * PAGE + 4 * hi;
#pragma unroll
        for (int r = 0; r < 16; ++r) sc[r] = (kb + 8 * (r >> 2) + (r & 3) < len) ? sc[r] : -__builtin_inff();
      }
      float mx = sc[0];
#pragma unroll
      for (int r = 1; r < 16; ++r) mx = fmaxf(mx, sc[r]);
      mx = swap_max(mx);
      const float mn = fmaxf(m, mx);
      const float alpha = __builtin_amdgcn_exp2f((m - mn) * CS);
      mprev = m;
      m = mn;
      float ps = 0.f;
#pragma unroll
      for (int r = 0; r < 16; ++r) { sc[r] = __builtin_amdgcn_exp2f((sc[r] - mn) * CS); ps += sc[r]; }
      l = fmaf(l, alpha, ps);
      u32x4 pf[2];
#pragma unroll
      for (int s = 0; s < 2; ++s)
        pf[s] = u32x4{pack2(sc[8 * s], sc[8 * s + 1]), pack2(sc[8 * s + 2], sc[8 * s + 3]), pack2(sc[8 * s + 4], sc[8 * s + 5]), pack2(sc[8 * s + 6], sc[8 * s + 7])};
      if (__builtin_amdgcn_ballot_w64(alpha != 1.0f)) resc |= 1u << k;
      *(LAS float*)(lds + palp + k * 128) = alpha;
      if (k >= KREG || k == nmine - 1) {
        const unsigned dst = ppst + ((k == nmine - 1) ? W_LAST : (k - KREG) * 2048);
        *(LAS u32x4*)(lds + dst) = pf[0];
        *(LAS u32x4*)(lds + dst + 1024) = pf[1];
      } else {
        asm volatile("s_set_gpr_idx_on %8, gpr_idx(DST)\n\t"
                     "v_mov_b32 v128, %0\n\tv_mov_b32 v129, %1\n\tv_mov_b32 v130, %2\n\tv_mov_b32 v131, %3\n\t"
                     "v_mov_b32 v132, %4\n\tv_mov_b32 v133, %5\n\tv_mov_b32 v134, %6\n\tv_mov_b32 v135, %7\n\t"
                     "s_set_gpr_idx_off"
                     :: "v"(pf[0][0]), "v"(pf[0][1]), "v"(pf[0][2]), "v"(pf[0][3]), "v"(pf[1][0]), "v"(pf[1][1]), "v"(pf[1][2]), "v"(pf[1][3]), "s"(8 * k));
      }
    }
  }
  l = swap_sum(l);
  *(LAS f32x2*)(lds + LDS_ML + w * 256 + r32 * 8) = f32x2{m, l};
  LGKM0();
  ctl[CTL_MLOK / 4 + w] = 1;

  const unsigned tq = (lane & 15) >> 2, tp = lane & 3, tblk = (lane >> 4) & 1;
  const unsigned vaddr0 = LDS_RING + (4 * hi + tq) * 256 + ((tq ^ (pos & 3)) << 6) + ((2 * tblk + (tp >> 1)) << 4) + ((tp & 1) << 3);
  f32x16 o[4];
#pragma unroll
  for (int i = 0; i < 4; ++i)
#pragma unroll
    for (int r = 0; r < 16; ++r) o[i][r] = 0.f;
  auto weights = [&]() {
    wait_above<1>(lbase + LDS_CTL + CTL_MLOK + min(lane, NC - 1) * 4, 0);
    unsigned a = LDS_ML + r32 * 8;
    asm volatile("" : "+v"(a) :: "memory");
    float M = -3.0e38f, L = 0.f;
    f32x2 ml[NC];
#pragma unroll
    for (int i = 0; i < NC; ++i) { ml[i] = *(LAS f32x2*)(lds + a + i * 256); M = fmaxf(M, ml[i][0]); }
#pragma unroll
    for (int i = 0; i < NC; ++i) L = fmaf(__builtin_amdgcn_exp2f((ml[i][0] - M) * CS), ml[i][1], L);
    const float inv = 1.0f / L;
    *(LAS float*)(lds + (a >> 1) + (LDS_WT - LDS_ML / 2) + pos * 128) = __builtin_amdgcn_exp2f((mprev - M) * CS) * inv;
    const float ct = __builtin_amdgcn_exp2f((m - M) * CS) * inv;
#pragma unroll
    for (int s = 0; s < 2; ++s) {
      u32x4 x = *(LAS u32x4*)(lds + ppst + W_LAST + s * 1024);
#pragma unroll
      for (int e = 0; e < 4; ++e) x[e] = pack2(__uint_as_float(x[e] << 16) * ct, __uint_as_float(x[e] & 0xffff0000u) * ct);
      *(LAS u32x4*)(lds + ppst + W_LAST + s * 1024) = x;
    }
    LGKM0();
    ctl[CTL_WTOK / 4 + w] = 1;
  };
  resc &= ~1u;
  if (nmine <= 3) weights();
#pragma unroll 1
  for (int k = 0; k < nmine - 1; ++k) {
    if (k == 2) weights();
    if ((resc >> k) & 1) {
      const float alpha = *(LAS float*)(lds + palp + k * 128);
#pragma unroll
      for (int i = 0; i < 4; ++i)
#pragma unroll
        for (int r = 0; r < 16; ++r) o[i][r] *= alpha;
    }
    const unsigned u = NT + ((k == 0 && NT >= 3 * NC) ? pos : w + NC * k);
    u32x4 pf[2];
    if (k >= KREG) {
      pf[0] = *(LAS u32x4*)(lds + ppst + (k - KREG) * 2048);
      pf[1] = *(LAS u32x4*)(lds + ppst + (k - KREG) * 2048 + 1024);
    } else {
      asm volatile("s_set_gpr_idx_on %8, gpr_idx(SRC0)\n\t"
                   "v_mov_b32 %0, v128\n\tv_mov_b32 %1, v129\n\tv_mov_b32 %2, v130\n\tv_mov_b32 %3, v131\n\t"
                   "v_mov_b32 %4, v132\n\tv_mov_b32 %5, v133\n\tv_mov_b32 %6, v134\n\tv_mov_b32 %7, v135\n\t"
                   "s_set_gpr_idx_off\n\ts_nop 1"
                   : "=v"(pf[0][0]), "=v"(pf[0][1]), "=v"(pf[0][2]), "=v"(pf[0][3]), "=v"(pf[1][0]), "=v"(pf[1][1]), "=v"(pf[1][2]), "=v"(pf[1][3])
                   : "s"(8 * k));
    }
    asm volatile("" : "+v"(pf[0]), "+v"(pf[1]), "+v"(o[0]), "+v"(o[1]), "+v"(o[2]), "+v"(o[3]));
    if (k == nmine - 2) wait_above<1>(lbase + LDS_CTL + CTL_WTOK + min(lane, NC - 1) * 4, 0);
    wait_above<1>(aland, 4 * u);
    bf16x8 vf[4][2];
#pragma unroll
    for (int i = 0; i < 4; ++i) {
      const unsigned a = (vaddr0 + ((u & 7) << 13)) ^ (i << 6);
#pragma unroll
      for (int s = 0; s < 2; ++s) {
        const s16x4 lo = __builtin_amdgcn_ds_read_tr16_b64_v4i16((LAS s16x4*)(lds + a + 4096 * s));
        const s16x4 up = __builtin_amdgcn_ds_read_tr16_b64_v4i16((LAS s16x4*)(lds + a + 4096 * s + 2048));
        vf[i][s] = __builtin_bit_cast(bf16x8, __builtin_shufflevector(lo, up, 0, 1, 2, 3, 4, 5, 6, 7));
      }
    }
    asm volatile("s_waitcnt lgkmcnt(0)" : "+v"(vf[0][0]), "+v"(vf[0][1]), "+v"(vf[1][0]), "+v"(vf[1][1]), "+v"(vf[2][0]), "+v"(vf[2][1]), "+v"(vf[3][0]), "+v"(vf[3][1]) :: "memory");
    ctl[CTL_FREE / 4 + (u & 7)] = u + 8;
    if (pf_on) prefetch(u);
#pragma unroll
    for (int s = 0; s < 2; ++s)
#pragma unroll
      for (int i = 0; i < 4; ++i) o[i] = __builtin_amdgcn_mfma_f32_32x32x16_bf16(vf[i][s], __builtin_bit_cast(bf16x8, pf[s]), o[i], 0, 0, 0);
  }

  {
    unsigned l16 = lane * 16;
    asm volatile("" : "+v"(l16));
    const unsigned l4 = l16 >> 2, awt = LDS_WT + (l4 & 127);
    auto put = [&](unsigned dst, const f32x16& x) {
#pragma unroll
      for (int j = 0; j < 4; ++j) *(LAS f32x4*)(lds + dst + j * 1024) = f32x4{x[4 * j], x[4 * j + 1], x[4 * j + 2], x[4 * j + 3]};
    };
    float co[6];
    bf16x8 pl[NC][2];
    auto ld_p = [&](int p) {
      pl[p][0] = *(LAS bf16x8*)(lds + LDS_WREG + p * W_SIZE + W_LAST + l16);
      pl[p][1] = *(LAS bf16x8*)(lds + LDS_WREG + p * W_SIZE + W_LAST + l16 + 1024);
    };
#pragma unroll
    for (int i = 0; i < 4; ++i) co[i] = *(LAS float*)(lds + awt + ((pos & 3) ^ i) * 128);
    co[4] = *(LAS float*)(lds + awt + 4 * 128);
    co[5] = *(LAS float*)(lds + awt + 5 * 128);
    ld_p(0); ld_p(1);
    asm volatile("" : "+v"(l16) :: "memory");
    if (nmine > 1) {
      if (pos > 3) put(LDS_RING + ((2 * NT - 2 * NC + pos) & 7) * 8192 + l16, o[0]);
#pragma unroll
      for (int i = 1; i < 4; ++i) put(LDS_WREG + pos * W_SIZE + (i - 1) * 4096 + l16, o[i]);
      LGKM0();
      ctl[CTL_ODONE / 4 + pos] = 1;
    }
    if (pos < 4) {
      f32x16 of = o[0], ot;
#pragma unroll
      for (int r = 0; r < 16; ++r) ot[r] = 0.f;
      auto src_o = [&](int p) -> unsigned {
        const int i = pos ^ (p & 3);
        return l16 + (i == 0 ? LDS_RING + ((2 * NT - 2 * NC + p) & 7) * 8192 : LDS_WREG + p * W_SIZE + (i - 1) * 4096);
      };
      auto fma_o = [&](unsigned src, float c) {
#pragma unroll
        for (int j = 0; j < 4; ++j) {
          const f32x4 x = *(LAS f32x4*)(lds + src + j * 1024);
#pragma unroll
          for (int e = 0; e < 4; ++e) of[4 * j + e] = fmaf(x[e], c, of[4 * j + e]);
        }
      };
      auto mma_t = [&](int p, bf16x8 p0, bf16x8 p1) {
        const unsigned a = vaddr0 + (((2 * NT - NC + p) & 7) << 13);
        bf16x8 vf[2];
#pragma unroll
        for (int s = 0; s < 2; ++s) {
          const s16x4 lo = __builtin_amdgcn_ds_read_tr16_b64_v4i16((LAS s16x4*)(lds + a + 4096 * s));
          const s16x4 up = __builtin_amdgcn_ds_read_tr16_b64_v4i16((LAS s16x4*)(lds + a + 4096 * s + 2048));
          vf[s] = __builtin_bit_cast(bf16x8, __builtin_shufflevector(lo, up, 0, 1, 2, 3, 4, 5, 6, 7));
        }
        ot = __builtin_amdgcn_mfma_f32_32x32x16_bf16(vf[0], p0, ot, 0, 0, 0);
        ot = __builtin_amdgcn_mfma_f32_32x32x16_bf16(vf[1], p1, ot, 0, 0, 0);
      };
      if (NT >= 2 * NC) {
#define PIN_OF() asm volatile("" : "+v"(of), "+v"(ot))
#pragma unroll
        for (int r = 0; r < 16; ++r) of[r] *= co[0];
        const bool wl = (l4 & 24) == 0;
        const unsigned ua = 4 * (2 * NT - NC);
        wait_above_v(wl ? aland : lbase + LDS_CTL + CTL_ODONE + (l4 & 12), wl ? ua : 0u);
        mma_t(0, pl[0][0], pl[0][1]);
#pragma unroll
        for (int i = 1; i < 4; ++i) fma_o(LDS_WREG + (pos ^ i) * W_SIZE + (i - 1) * 4096 + l16, co[i]);
        PIN_OF();
        ld_p(2); ld_p(3); ld_p(4); ld_p(5);
        wait_above_v(wl ? aland : lbase + LDS_CTL + CTL_ODONE + 16 + (l4 & 4), wl ? ua + 8 : 0u);
        mma_t(1, pl[1][0], pl[1][1]);
        mma_t(2, pl[2][0], pl[2][1]);
        fma_o(src_o(4), co[4]);
        fma_o(src_o(5), co[5]);
#pragma unroll
        for (int r = 0; r < 16; ++r) { ot[r] += of[r]; of[r] = 0.f; }
        asm volatile("" : "+v"(ot));
        wait_above<0>(aland, ua + 20);
        mma_t(3, pl[3][0], pl[3][1]);
        mma_t(4, pl[4][0], pl[4][1]);
        mma_t(5, pl[5][0], pl[5][1]);
        of = ot;
      } else {
        wait_above<1>(lbase + LDS_CTL + CTL_WTOK + min(l4, (NC - 1) * 4u), 0);
        const float cw = *(LAS float*)(lds + awt + pos * 128);
#pragma unroll
        for (int r = 0; r < 16; ++r) of[r] *= cw;
#pragma unroll 1
        for (int p = max(NC - NT, 0); p < NC; ++p) {
          if (p != pos && NT - 2 * NC + p >= 0) {
            wait_above<1>(lbase + LDS_CTL + CTL_ODONE + p * 4, 0);
            fma_o(src_o(p), *(LAS float*)(lds + awt + p * 128));
          }
          wait_above<1>(aland, 4 * (2 * NT - NC + p));
          mma_t(p, *(LAS bf16x8*)(lds + LDS_WREG + p * W_SIZE + W_LAST + l16), *(LAS bf16x8*)(lds + LDS_WREG + p * W_SIZE + W_LAST + l16 + 1024));
        }
#pragma unroll
        for (int r = 0; r < 16; ++r) of[r] += ot[r];
      }
      const __amdgpu_buffer_rsrc_t ro = __builtin_amdgcn_make_buffer_rsrc(outp, 0, NB * HQ * SQ * HD * 2, 0x00020000);
      const unsigned orow = (unsigned)(b * HQ + 2 * h) * SQ * HD * 2 + pos * 64 + ((l16 & 511) << 4) + ((l16 >> 5) & 16);
#pragma unroll
      for (int j = 0; j < 2; ++j) {
        const unsigned a0 = pack2(of[8 * j], of[8 * j + 1]), a1 = pack2(of[8 * j + 2], of[8 * j + 3]);
        const unsigned b0 = pack2(of[8 * j + 4], of[8 * j + 5]), b1 = pack2(of[8 * j + 6], of[8 * j + 7]);
        auto s0 = __builtin_amdgcn_permlane32_swap(a0, b0, false, false);
        auto s1 = __builtin_amdgcn_permlane32_swap(a1, b1, false, false);
        u32x4 val = {s0[0], s1[0], s0[1], s1[1]};
        __builtin_amdgcn_raw_buffer_store_b128(val, ro, orow + j * 32, 0, 0);
      }
    }
  }
}
}

extern "C" void kernel_launch(void* const* d_in, const int* in_sizes, int n_in, void* d_out, int out_size, void* d_ws,
                              size_t ws_size, hipStream_t stream) {
  (void)n_in; (void)out_size; (void)d_ws; (void)ws_size;
  const unsigned nphys = (unsigned)(in_sizes[1] / (PAGE * HKV * HD));
  const unsigned kvbytes = nphys * PAGE_BYTES;
  (void)hipFuncSetAttribute((const void*)paged_attn, hipFuncAttributeMaxDynamicSharedMemorySize, LDS_TOTAL);
  hipLaunchKernelGGL(paged_attn, dim3(256), dim3(512), LDS_TOTAL, stream, d_in[0], d_in[1], d_in[2], (const int*)d_in[3],
                     (const int*)d_in[4], d_out, kvbytes, nphys);
}
```

```cpp
#include <hip/hip_runtime.h>
#include <hip/hip_bf16.h>

#define PACE_X16 431
#define SOFT 15
#define GAP_TICKS 10
#define WIN_TICKS 80
#define WIN_C1 110
#define LATE_TICKS 100
#define HALT_SHIFT 240
#define HALT_FROM 400
#define CODEPF 11520
#define PF_LEAD 180
#define PF_X64 72
#define PF_PER 4
#define PF_PSTEP 2
#define PF_RUN 3

namespace {
constexpr int NB = 16, HQ = 32, HKV = 16, SQ = 16, HD = 128, PAGE = 16, NBPS = 256, SKV = NBPS * PAGE;
constexpr unsigned PAGE_BYTES = PAGE * HKV * HD * 2;
constexpr unsigned TLBG = 0x200000u;
constexpr int U0 = 4;
constexpr int NC = 6;
constexpr int KREG = 16, KLDS = 5;
static_assert(NC * (KREG + KLDS + 1) >= NBPS / 2, "P storage");
constexpr unsigned LDS_RING = 0;
constexpr unsigned LDS_WREG = 65536;
constexpr unsigned W_ALP = KLDS * 2048;
constexpr unsigned W_LAST = W_ALP + (KREG + KLDS + 1) * 128;
constexpr unsigned W_SIZE = W_LAST + 2048;
static_assert(W_LAST >= 3 * 4096 && W_SIZE % 256 == 0, "consumer region");
constexpr unsigned LDS_TBL = LDS_WREG + W_SIZE;
constexpr unsigned LDS_SRT = LDS_WREG + 3 * W_SIZE;
constexpr unsigned LDS_Q = LDS_WREG;
constexpr unsigned LDS_ML = LDS_WREG + NC * W_SIZE;
constexpr unsigned LDS_WT = LDS_ML + NC * 256;
constexpr unsigned LDS_CTL = LDS_WT + NC * 128;
constexpr unsigned CTL_LAND = 0;
constexpr unsigned CTL_QOK = 16;
constexpr unsigned CTL_SORT = 24;
constexpr unsigned CTL_FREE = 32;
constexpr unsigned CTL_MLOK = 64;
constexpr unsigned CTL_ODONE = 96;
constexpr unsigned CTL_WTOK = 128;
constexpr unsigned LDS_DUMMY = LDS_CTL + 256;
constexpr unsigned LDS_PLIST = LDS_DUMMY + 256;
constexpr unsigned LDS_TOTAL = LDS_PLIST + 1024;
static_assert(LDS_TOTAL <= 163840, "lds");

typedef __attribute__((ext_vector_type(8))) __bf16 bf16x8;
typedef __attribute__((ext_vector_type(2))) __bf16 bf16x2;
typedef __attribute__((ext_vector_type(4))) short s16x4;
typedef __attribute__((ext_vector_type(16))) float f32x16;
typedef __attribute__((ext_vector_type(4))) float f32x4;
typedef __attribute__((ext_vector_type(2))) float f32x2;
typedef __attribute__((ext_vector_type(4))) unsigned u32x4;
typedef __attribute__((ext_vector_type(2))) unsigned u32x2;
#define LAS __attribute__((address_space(3)))

__device__ __forceinline__ float swap_max(float x) {
  auto r = __builtin_amdgcn_permlane32_swap(__float_as_uint(x), __float_as_uint(x), false, false);
  return fmaxf(__uint_as_float(r[0]), __uint_as_float(r[1]));
}
__device__ __forceinline__ float swap_sum(float x) {
  auto r = __builtin_amdgcn_permlane32_swap(__float_as_uint(x), __float_as_uint(x), false, false);
  return __uint_as_float(r[0]) + __uint_as_float(r[1]);
}
__device__ __forceinline__ unsigned pack2(float a, float b) {
  bf16x2 v = {(__bf16)a, (__bf16)b};
  return __builtin_bit_cast(unsigned, v);
}
template <int CTRL> __device__ __forceinline__ int dpp_add(int x) { return x + __builtin_amdgcn_update_dpp(0, x, CTRL, 0xf, 0xf, true); }
__device__ __forceinline__ int to_sgpr(int v) {
  int r;
  asm volatile("s_nop 1\n\tv_readfirstlane_b32 %0, %1\n\ts_nop 1" : "=s"(r) : "v"(v));
  return r;
}
#define LGKM0() asm volatile("s_waitcnt lgkmcnt(0)" ::: "memory")
#define VMWAIT(n) asm volatile("s_waitcnt vmcnt(" #n ")" ::: "memory")
#define CBAR() asm volatile("" ::: "memory")
template <int SLEEP> __device__ __forceinline__ void wait_above(unsigned addr, unsigned u) {
  unsigned v, cnt;
  if (SLEEP)
    asm volatile("s_mov_b32 %1, 0x800000\n\ts_branch 2f\n"
                 "1:\n\ts_sleep %4\n"
                 "2:\n\tds_read_b32 %0, %2\n\ts_sub_u32 %1, %1, 1\n\ts_waitcnt lgkmcnt(0)\n\t"
                 "v_cmp_ge_u32 vcc, %3, %0\n\ts_cbranch_scc1 3f\n\ts_nop 1\n\ts_cbranch_vccnz 1b\n3:"
                 : "=&v"(v), "=&s"(cnt) : "v"(addr), "s"(u), "n"(SLEEP) : "memory", "scc", "vcc");
  else
    asm volatile("s_mov_b32 %1, 0x800000\n"
                 "1:\n\tds_read_b32 %0, %2\n\ts_sub_u32 %1, %1, 1\n\ts_waitcnt lgkmcnt(0)\n\t"
                 "v_cmp_ge_u32 vcc, %3, %0\n\ts_cbranch_scc1 3f\n\ts_nop 1\n\ts_cbranch_vccnz 1b\n3:"
                 : "=&v"(v), "=&s"(cnt) : "v"(addr), "s"(u) : "memory", "scc", "vcc");
}

__device__ __forceinline__ void wait_above_v(unsigned addr, unsigned u) {
  unsigned v, cnt;
  asm volatile("s_mov_b32 %1, 0x800000\n"
               "1:\n\tds_read_b32 %0, %2\n\ts_sub_u32 %1, %1, 1\n\ts_waitcnt lgkmcnt(0)\n\t"
               "v_cmp_ge_u32 vcc, %3, %0\n\ts_cbranch_scc1 3f\n\ts_nop 1\n\ts_cbranch_vccnz 1b\n3:"
               : "=&v"(v), "=&s"(cnt) : "v"(addr), "v"(u) : "memory", "scc", "vcc");
}

__global__ void __launch_bounds__(512, 1) __attribute__((amdgpu_num_vgpr(64)))
paged_attn(const void* __restrict__ qp, const void* __restrict__ kp, const void* __restrict__ vp, const int* __restrict__ bt,
           const int* __restrict__ lens, void* __restrict__ outp, unsigned kvbytes, unsigned nphys) {
  extern __shared__ __attribute__((aligned(16))) char smem[];
  LAS char* lds = (LAS char*)smem;
  const int lane0 = threadIdx.x & 63;
  const int wv = __builtin_amdgcn_readfirstlane(threadIdx.x >> 6);
  const int g = blockIdx.x;
  const int h = g >> 4;
  constexpr float CS = 0.08838834764831845f * 1.4426950408889634f;
  volatile LAS unsigned* ctl = (volatile LAS unsigned*)(lds + LDS_CTL);

  const unsigned lbase = (unsigned)(size_t)lds;

  unsigned lraw, first;
  u32x4 lq;
  {
    asm volatile("global_load_dword %0, %3, %4\n\t"
                 "global_load_dwordx4 %1, %5, %4\n\t"
                 "global_load_dword %2, %6, %7"
                 : "=&v"(lraw), "=&v"(lq), "=&v"(first)
                 : "v"((lane0 & 15) * 4), "s"(lens), "v"(lane0 & 48), "v"((lane0 & 15) * (wv < 2 ? NBPS * 4 : 0) + (lane0 >> 4) * 8 + (wv & 1) * 4), "s"(bt),
                   "s"(qp), "s"(kp), "s"(vp), "s"(outp), "s"(kvbytes), "s"(nphys)
                 : "memory");
    __builtin_amdgcn_sched_barrier(0);
  }
  if (wv >= 4) {
    if (wv == 7) {
      const __amdgpu_buffer_rsrc_t rb = __builtin_amdgcn_make_buffer_rsrc((void*)bt, 0, NB * NBPS * 4, 0x00020000);
#define TBL4(SO) "buffer_load_dwordx4 %2, %1, " SO " offen lds\n\tbuffer_load_dwordx4 %2, %1, " SO " offen offset:1024 lds\n\t" \
                 "buffer_load_dwordx4 %2, %1, " SO " offen offset:2048 lds\n\tbuffer_load_dwordx4 %2, %1, " SO " offen offset:3072 lds\n\t"
      asm volatile("s_mov_b32 m0, %0\n\ts_nop 0\n\t" TBL4("0") "s_add_u32 m0, m0, 4096\n\ts_nop 0\n\t" TBL4("%3")
                   "s_add_u32 m0, m0, %6\n\ts_nop 0\n\t" TBL4("%4") "s_add_u32 m0, m0, 4096\n\ts_nop 0\n\t" TBL4("%5")
                   :: "s"(lbase + LDS_TBL), "s"(rb), "v"(lane0 * 16), "s"(4096), "s"(8192), "s"(12288), "n"(W_SIZE - 4096) : "memory", "scc");
    }
    unsigned long long pc;
    asm volatile("s_getpc_b64 %0" : "=s"(pc));
    const __amdgpu_buffer_rsrc_t rt = __builtin_amdgcn_make_buffer_rsrc(
        wv == 5 ? (void*)pc : wv == 4 ? (void*)vp : (void*)kp, 0, wv == 5 ? CODEPF : (wv != 7 && (g & 8)) ? (int)kvbytes : 0, 0x00020000);
    const unsigned mul = wv == 5 ? 512u : 16 * TLBG + 4224u, add = wv == 5 ? ((g >> 3) & 3) * 128 : (g >> 4) * (TLBG + 33024u);
    asm volatile("s_mov_b32 m0, %0\n\ts_nop 0\n\tbuffer_load_dword %1, %2, 0 offen lds"
                 :: "s"(lbase + LDS_DUMMY), "v"((lane0 & 31) * mul + add), "s"(rt) : "memory");
    *(LAS u32x4*)(lds + LDS_SRT + (threadIdx.x - 256) * 16) = u32x4{0u, 0u, 0u, 0u};
    if (threadIdx.x < 296) ctl[lane0] = ((lane0 >> 3) == CTL_FREE / 32) ? (lane0 & 7) : 0u;
    asm volatile("s_waitcnt lgkmcnt(0)\n\ts_barrier" ::: "memory");
  }
  if (wv < 2) __builtin_amdgcn_s_setprio(3);
  const unsigned rstride = wv < 2 ? 4096 : 256;
  unsigned vo0 = (lane0 >> 4) * rstride + (((unsigned)(lane0 & 15) ^ ((lane0 >> 4) << 2)) << 4) + (wv < 2 ? h * (HD * 2) : 0);
  unsigned vo1 = (vo0 + 4 * rstride - 1024) ^ 16, vo2 = (vo0 + 8 * rstride - 2048) ^ 32, vo3 = (vo0 + 12 * rstride - 3072) ^ 48;
  const unsigned want = (g & 8) ? 15 - (g & 7) : (g & 7);
  const unsigned dst0 = lbase + (wv < 2 ? LDS_RING : LDS_Q) + (wv & 1) * 4096;
  asm volatile("s_cmp_ge_u32 %8, 4\n\ts_cbranch_scc1 1f\n\ts_waitcnt vmcnt(0)\n1:\n\ts_waitcnt vmcnt(1)" : "+v"(lraw), "+v"(lq), "+v"(first), "+v"(vo0), "+v"(vo1), "+v"(vo2), "+v"(vo3) : "s"(dst0), "s"(wv) : "memory", "scc");
  unsigned lm1 = min(lraw - 1u, (unsigned)SKV - 1u);
  unsigned rank = 0;
  {
    const unsigned key = lm1 * 16 + (lane0 & 15);
#pragma unroll
    for (int j = 0; j < 4; ++j) rank += (min(lq[j] - 1u, (unsigned)SKV - 1u) * 16 + ((lane0 >> 4) * 4 + j) > key) ? 1u : 0u;
    auto r1 = __builtin_amdgcn_permlane16_swap(rank, rank, false, false);
    rank = r1[0] + r1[1];
    auto r2 = __builtin_amdgcn_permlane32_swap(rank, rank, false, false);
    rank = r2[0] + r2[1];
  }
  first = min(first, nphys - 1) * PAGE_BYTES;
  const int b = __builtin_ctz((unsigned)__builtin_amdgcn_ballot_w64(rank == want) | 0x8000u);
  unsigned long long tstart = 0;
  if (wv < 4) {
    const bool qok = (int)__builtin_amdgcn_readlane(lraw, b) > 0;
    const __amdgpu_buffer_rsrc_t r0 = __builtin_amdgcn_make_buffer_rsrc(wv < 2 ? (void*)kp : (void*)qp, 0,
                                                                        wv < 2 ? (int)kvbytes : qok ? NB * HQ * SQ * HD * 2 : 0, 0x00020000);
    const unsigned so = wv < 2 ? __builtin_amdgcn_readlane(first, b) : qok ? (unsigned)((b * HQ + 2 * h) * SQ * HD * 2) + (wv & 1) * 4096 : 0u;
    asm volatile("s_waitcnt vmcnt(0)\n\t"
                 "s_mov_b32 m0, %2\n\t"
                 "s_memrealtime %0\n\t"
                 "s_nop 2\n\t"
                 "buffer_load_dwordx4 %4, %1, %3 offen nt lds\n\t"
                 "buffer_load_dwordx4 %5, %1, %3 offen offset:1024 nt lds\n\t"
                 "buffer_load_dwordx4 %6, %1, %3 offen offset:2048 nt lds\n\t"
                 "buffer_load_dwordx4 %7, %1, %3 offen offset:3072 nt lds\n\t"
                 "s_waitcnt lgkmcnt(0)\n\t"
                 "s_barrier"
                 : "=&s"(tstart) : "s"(r0), "s"(dst0), "s"(so), "v"(vo0), "v"(vo1), "v"(vo2), "v"(vo3) : "memory");
  }
  asm volatile("" : "+v"(lm1), "+v"(rank), "+v"(first) :: "memory");
  __builtin_amdgcn_sched_barrier(0);
  const int bp = __builtin_ctz((unsigned)__builtin_amdgcn_ballot_w64(rank == 15 - want) | 0x8000u);
  const int len = __builtin_amdgcn_readlane(lm1, b) + 1, nown = (len + PAGE - 1) / PAGE, NT = (nown + 1) >> 1;
  const int npl = (__builtin_amdgcn_readlane(lm1, bp) >> 4) + 1, NTL = (npl + 1) >> 1;
  auto total = [&]() __attribute__((always_inline)) {
    int t = lm1 + 1;
    t = dpp_add<0x121>(t); t = dpp_add<0x122>(t); t = dpp_add<0x124>(t); t = dpp_add<0x128>(t);
    return t;
  };

  auto sort_pages = [&](int lane, int bb, int n, unsigned scr, unsigned dst) __attribute__((always_inline)) {
    u32x4 e = *(LAS u32x4*)(lds + LDS_TBL + (bb >> 3) * W_SIZE + (bb & 7) * 1024 + lane * 16);
    bool in[4];
#pragma unroll
    for (int j = 0; j < 4; ++j) {
      e[j] = min(e[j], nphys - 1);
      in[j] = 4 * lane + j >= 2 * U0 && 4 * lane + j < n - 1;
      __hip_atomic_fetch_or((LAS unsigned*)(lds + scr + ((e[j] >> 5) & 255) * 8), in[j] ? 1u << (e[j] & 31) : 0u, __ATOMIC_RELAXED, __HIP_MEMORY_SCOPE_WAVEFRONT);
    }
    CBAR();
    u32x4 a = *(LAS u32x4*)(lds + scr + lane * 32), c = *(LAS u32x4*)(lds + scr + lane * 32 + 16);
    const int n0 = __builtin_popcount(a[0]), n1 = n0 + __builtin_popcount(a[2]), n2 = n1 + __builtin_popcount(c[0]), n3 = n2 + __builtin_popcount(c[2]);
    int sc = n3;
    sc = dpp_add<0x111>(sc); sc = dpp_add<0x112>(sc); sc = dpp_add<0x114>(sc); sc = dpp_add<0x118>(sc);
    sc += __builtin_amdgcn_update_dpp(0, sc, 0x142, 0xa, 0xf, false);
    sc += __builtin_amdgcn_update_dpp(0, sc, 0x143, 0xc, 0xf, false);
    const bool ok = nphys <= 8192u && __builtin_amdgcn_readlane(sc, 63) == n - 1 - 2 * U0;
    sc += 2 * U0 - n3;
    a[1] = sc; a[3] = sc + n0; c[1] = sc + n1; c[3] = sc + n2;
    *(LAS u32x4*)(lds + scr + lane * 32) = a;
    *(LAS u32x4*)(lds + scr + lane * 32 + 16) = c;
    CBAR();
    u32x2 wp[4];
#pragma unroll
    for (int j = 0; j < 4; ++j) wp[j] = *(LAS u32x2*)(lds + scr + ((e[j] >> 5) & 255) * 8);
#pragma unroll
    for (int j = 0; j < 4; ++j) {
      const unsigned r = wp[j][1] + __builtin_popcount(wp[j][0] & ((1u << (e[j] & 31)) - 1u));
      *(LAS unsigned*)(lds + dst + ((ok && in[j]) ? r : 4 * lane + j) * 4) = e[j] * PAGE_BYTES;
    }
    CBAR();
  };

  if (__builtin_expect(wv < 2, 1)) {
    const int half = wv, lane = lane0;
    const unsigned vland = lbase + LDS_CTL + CTL_LAND + half * 4;
    const unsigned vfree = lbase + LDS_CTL + CTL_FREE + (lane & 7) * 4;
    const unsigned ring = dst0;
    unsigned l0 = __builtin_amdgcn_ds_bpermute((b + 16 * lane) * 4, first), l1 = 0;
    const unsigned a0 = lbase + LDS_SRT + 4096 + min(2 * lane + half, nown - 1) * 4, a1 = lbase + LDS_SRT + 4096 + min(2 * lane + half + 128, nown - 1) * 4;
    const int rot = (NT >= 3 * NC) ? NT % NC : 0;
    const unsigned vperm = (lane < NC ? (lane + rot >= NC ? lane + rot - NC : lane + rot) : lane) * 4;
    const int T16 = total() * PACE_X16;
    const int step = to_sgpr((int)((float)T16 * __builtin_amdgcn_rcpf((float)max(len, 32))));
    const int win = to_sgpr((int)((float)(((h & 3) == 1 ? WIN_C1 : WIN_TICKS) * 1024) * __builtin_amdgcn_rcpf((float)step) + 0.5f));
    const int hmin = to_sgpr((int)((float)(HALT_FROM * 256) * __builtin_amdgcn_rcpf((float)step)));
    const unsigned due0 = ((unsigned)tstart << 8) + half * (step >> 1);
    const int lrows = len - (2 * (NT - 1) + half) * PAGE;
    unsigned vf, vt, lc;
    asm volatile(
        "s_waitcnt lgkmcnt(0)\n\t"
        "s_memrealtime s[68:69]\n\t"
        "ds_read_b32 %[vf], %[vfree]\n\t"
        "s_mov_b32 s60, 1\n\t"
        "s_mov_b32 s61, 0\n\t"
        "s_mov_b32 s62, 0x1000000\n\t"
        "s_mov_b32 s66, 0\n\t"
        "s_min_u32 s67, %[nt], %[u0]\n\t"
        "s_mov_b32 s75, %[nrec]\n\t"
        "s_mov_b32 s72, 0x2000\n\t"
        "s_mov_b32 s73, 1\n\t"
        "s_mov_b32 s74, %[due1]\n\t"
        "s_mov_b32 s85, %[duev]\n\t"
        "s_mov_b32 s86, %[late]\n\t"
        "s_mov_b64 s[76:77], %[kp]\n\t"
        "s_mov_b32 s78, %[nrec]\n\t"
        "s_mov_b32 s79, 0x20000\n\t"
        "s_and_b32 s77, s77, 0xffff\n\t"
        "v_mov_b32 %[lc], %[l0]\n\t"
        "s_getpc_b64 s[70:71]\n"
        ".LldA%=:\n\t"
        "s_add_u32 s80, s70, .LldL%=-.LldA%=\n\t"
        "s_addc_u32 s71, s71, 0\n\t"
        "s_branch .LldF%=\n"
        ".LldX%=:\n\t"
        "s_cmp_lt_i32 s64, 0\n\t"
        "s_cbranch_scc0 .LldH%=\n"
        ".LldR%=:\n\t"
        "s_sub_u32 s62, s62, 1\n\t"
        "s_cbranch_scc1 .LldE%=\n"
        ".LldT%=:\n\t"
        "s_waitcnt lgkmcnt(0)\n\t"
        "s_getreg_b32 s64, hwreg(HW_REG_IB_STS)\n\t"
        "s_and_b32 s65, s64, 15\n\t"
        "s_bfe_u32 s64, s64, 0x20016\n\t"
        "s_lshl4_add_u32 s81, s64, s65\n\t"
        "s_cmp_gt_i32 s81, s61\n\t"
        "s_cbranch_scc1 .LldC%=\n\t"
        "s_add_u32 s64, s81, 3\n\t"
        "s_and_b32 s64, s64, 28\n\t"
        "s_add_u32 s70, s80, s64\n\t"
        "s_add_u32 s66, s66, s61\n\t"
        "s_sub_u32 s61, s64, 4\n\t"
        "s_sub_u32 s66, s66, s61\n\t"
        "v_mov_b32 %[vt], s66\n\t"
        "s_setpc_b64 s[70:71]\n\t"
        ".p2align 5\n"
        ".LldL%=:\n\t"
        "s_waitcnt vmcnt(0)\n\t"
        "s_waitcnt vmcnt(4)\n\t"
        "s_waitcnt vmcnt(8)\n\t"
        "s_waitcnt vmcnt(12)\n\t"
        "s_waitcnt vmcnt(16)\n\t"
        "s_waitcnt vmcnt(20)\n\t"
        "s_waitcnt vmcnt(24)\n\t"
        "s_waitcnt vmcnt(28)\n\t"
        "ds_write_b32 %[vland], %[vt]\n"
        ".LldC%=:\n\t"
        "v_cmp_eq_u32 vcc, s60, %[vf]\n\t"
        "s_lshl_b32 s64, s68, 8\n\t"
        "s_memrealtime s[68:69]\n\t"
        "ds_read_b32 %[vf], %[vfree]\n\t"
        "s_sub_u32 s64, s64, s74\n\t"
        "s_cmp_gt_u32 s64, s86\n\t"
        "s_cbranch_scc1 .LldX%=\n"
        ".LldI%=:\n\t"
        "s_cbranch_vccz .LldR%=\n\t"
        "s_cmp_gt_u32 s81, %[win]\n\t"
        "s_cbranch_scc1 .LldR%=\n\t"
        "buffer_load_dwordx4 %[o0], s[76:79], s63 offen nt lds\n\t"
        "s_add_u32 s74, s74, %[step]\n\t"
        "s_add_u32 s73, s73, 1\n\t"
        "buffer_load_dwordx4 %[o1], s[76:79], s63 offen offset:1024 nt lds\n\t"
        "s_add_u32 s60, s60, 1\n\t"
        "s_add_u32 s61, s61, 4\n\t"
        "s_add_u32 s86, s64, %[late]\n\t"
        "buffer_load_dwordx4 %[o2], s[76:79], s63 offen offset:2048 nt lds\n\t"
        "s_add_u32 s72, s72, 0x2000\n\t"
        "s_and_b32 s72, s72, 0xe000\n\t"
        "buffer_load_dwordx4 %[o3], s[76:79], s63 offen offset:3072 nt lds\n"
        ".LldF%=:\n\t"
        "v_readlane_b32 s63, %[lc], s73\n\t"
        "s_add_u32 m0, s72, %[ring]\n\t"
        "s_cmp_lg_u32 s60, s67\n\t"
        "s_cbranch_scc1 .LldT%=\n\t"
        "s_cmp_eq_u32 s73, %[nt]\n\t"
        "s_cbranch_scc1 .LldV%=\n\t"
        "s_cmp_lg_u32 s60, %[u0]\n\t"
        "s_cbranch_scc1 .LldG%=\n\t"
        "s_cmp_le_u32 %[nt], %[u0]\n\t"
        "s_cbranch_scc1 .LldG%=\n"
        ".LldS%=:\n\t"
        "ds_read_b32 %[lc], %[vland] offset:%[srt]\n\t"
        "ds_read_b32 %[l0], %[a0]\n\t"
        "ds_read_b32 %[l1], %[a1]\n\t"
        "s_sub_u32 s62, s62, 1\n\t"
        "s_cbranch_scc1 .LldE%=\n\t"
        "s_waitcnt lgkmcnt(0)\n\t"
        "v_readfirstlane_b32 s64, %[lc]\n\t"
        "s_cmp_eq_u32 s64, 0\n\t"
        "s_cbranch_scc1 .LldS%=\n\t"
        "s_max_i32 s65, %[lrows], 0\n\t"
        "s_lshl_b32 s65, s65, 12\n\t"
        "s_add_u32 s64, s64, s65\n\t"
        "s_sub_u32 s64, s64, 1\n\t"
        "s_cmp_lt_i32 %[lrows], 16\n\t"
        "s_cselect_b32 s75, s64, %[nrec]\n\t"
        "v_mov_b32 %[lc], %[l0]\n"
        ".LldG%=:\n\t"
        "s_cmp_lg_u32 s73, 64\n\t"
        "s_cbranch_scc1 .LldN%=\n\t"
        "v_mov_b32 %[lc], %[l1]\n"
        ".LldN%=:\n\t"
        "s_cmp_eq_u32 s73, %[ntm]\n\t"
        "s_cselect_b32 s78, s75, %[nrec]\n\t"
        "s_cselect_b32 s64, %[nt], %[ntm]\n\t"
        "s_min_u32 s65, s64, 64\n\t"
        "s_cmp_lt_u32 s73, 64\n\t"
        "s_cselect_b32 s64, s65, s64\n\t"
        "s_sub_u32 s67, s60, s73\n\t"
        "s_add_u32 s67, s67, s64\n\t"
        "s_branch .LldF%=\n"
        ".LldV%=:\n\t"
        "s_cmp_lg_u32 s60, %[nt]\n\t"
        "s_cbranch_scc1 .LldD%=\n\t"
        "s_mov_b64 s[76:77], %[vp]\n\t"
        "s_and_b32 s77, s77, 0xffff\n\t"
        "s_mov_b32 s73, 0\n\t"
        "s_mov_b32 s74, s85\n\t"
        "ds_bpermute_b32 %[lc], %[vperm], %[l0]\n\t"
        "v_xor_b32 %[o1], 16, %[o1]\n\t"
        "v_xor_b32 %[o2], 32, %[o2]\n\t"
        "v_xor_b32 %[o3], 48, %[o3]\n\t"
        "s_waitcnt lgkmcnt(0)\n\t"
        "s_branch .LldN%=\n"
        ".LldH%=:\n\t"
        "s_cmp_eq_u32 s66, %[fin]\n\t"
        "s_cbranch_scc1 .LldE%=\n\t"
        "s_cmp_lt_i32 s60, %[hmin]\n\t"
        "s_cbranch_scc1 .LldI%=\n\t"
        "s_add_u32 s74, s74, %[hshift]\n\t"
        "s_add_u32 s85, s85, %[hshift]\n\t"
        "s_branch .LldR%=\n"
        ".LldD%=:\n\t"
        "s_sub_u32 s74, s74, 0x10000000\n\t"
        "s_mov_b32 s86, 0\n\t"
        "s_mov_b32 s60, -1\n\t"
        "s_branch .LldT%=\n"
        ".LldE%=:\n\t"
        "s_waitcnt vmcnt(0) lgkmcnt(0)\n\t"
        : [vf] "=&v"(vf), [vt] "=&v"(vt), [lc] "=&v"(lc), [l0] "+&v"(l0), [l1] "+&v"(l1), [o1] "+&v"(vo1), [o2] "+&v"(vo2), [o3] "+&v"(vo3)
        : [vperm] "v"(vperm), [a0] "v"(a0), [a1] "v"(a1), [o0] "v"(vo0), [vland] "v"(vland), [vfree] "v"(vfree),
          [kp] "s"(kp), [vp] "s"(vp), [nrec] "s"(kvbytes), [nt] "s"(NT), [ring] "s"(ring),
          [step] "s"(step), [due1] "s"(due0 + step + SOFT * 256), [duev] "s"(due0 + (to_sgpr(T16) >> 5) + (SOFT + GAP_TICKS) * 256), [lrows] "s"(lrows), [ntm] "s"(NT - 1), [fin] "s"(8 * NT), [u0] "n"(U0), [win] "s"(win), [late] "n"(LATE_TICKS * 256), [hshift] "n"(HALT_SHIFT * 256), [hmin] "s"(hmin), [srt] "n"(CTL_SORT - CTL_LAND)
        : "memory", "scc", "vcc", "s60", "s61", "s62", "s63", "s64", "s65", "s66", "s67", "s68", "s69", "s70", "s71", "s72", "s73", "s74", "s75",
          "s76", "s77", "s78", "s79", "s80", "s81", "s85", "s86");
    asm volatile("s_endpgm" ::: "memory");
  }

  int lane = lane0;
  asm volatile("" : "+v"(lane));
  if (wv == 7) {
#pragma unroll 1
    for (int i = 0; i < 2; ++i) {
      sort_pages(lane, i ? bp : b, i ? npl : nown, LDS_SRT + i * 2048, i ? LDS_PLIST : LDS_SRT + 4096);
      if (i == 0) {
        const unsigned lo = *(LAS unsigned*)(lds + LDS_SRT + 4096 + (nown - 1) * 4) + 1;
        LGKM0();
        ctl[CTL_SORT / 4 + (lane & 1)] = lo;
      }
    }
  }
  if (wv < 4) {
    VMWAIT(0);
    ctl[CTL_QOK / 4 + (wv & 1)] = 1;
  }
  const int tot = total();
  const int w = wv - 2;
  const int r32 = lane & 31, hi = lane >> 5;
  const int nmine = (NT > w) ? (NT - w + NC - 1) / NC : 0;
  const unsigned aland = lbase + LDS_CTL + CTL_LAND + (lane & 1) * 4;
  const int tots = __builtin_amdgcn_readfirstlane(tot);
  const bool pf_on = npl * 256 * 64 > tots * PF_X64 && npl > nown && (h & 3) == 0;
  const unsigned pf_last = 2 * NT - to_sgpr((int)((float)(2 * NT * (PF_LEAD + 23 * NC)) * 4096.0f * __builtin_amdgcn_rcpf((float)(tot * PACE_X16))));
  const unsigned pf_ratio = to_sgpr((int)((float)NTL * 65536.0f * __builtin_amdgcn_rcpf((float)NT)));
  const unsigned pf_per = PF_PER + (g & 7) * PF_PSTEP, pf_inv = (65536 + pf_per - 1) / pf_per;
  auto prefetch = [&](unsigned op) {
    const unsigned d = pf_last - op;
    if (d >= 1024u) return;
    const unsigned r = d - ((d * pf_inv) >> 16) * pf_per;
    if (r >= PF_RUN) return;
    unsigned tl = 2 * NTL - 1 - NC - (((d - r) * pf_ratio) >> 16) - r;
    if (tl >= 2u * NTL) return;
    const bool isv = tl >= (unsigned)NTL;
    tl -= isv ? NTL : 0;
    const __amdgpu_buffer_rsrc_t rp = __builtin_amdgcn_make_buffer_rsrc(isv ? (void*)vp : (void*)kp, 0, (int)kvbytes, 0x00020000);
    unsigned ln = lane;
    asm volatile("" : "+v"(ln));
    const unsigned vo = (*(LAS unsigned*)(lds + LDS_PLIST + min(2 * tl + (ln >> 5), (unsigned)npl - 1) * 4) & 0x7fff0000u) + (h + 1) * (HD * 2) +
                        ((ln >> 1) & 15) * 4096 + (ln & 1) * 128;
    asm volatile("s_mov_b32 m0, %0\n\ts_nop 4\n\tbuffer_load_dword %2, %1, 0 offen lds" :: "s"(lbase + LDS_DUMMY), "s"(rp), "v"(vo) : "memory");
  };
  const int pos = (w + NC * 22 - NT) % NC;
  const unsigned palp = LDS_WREG + pos * W_SIZE + W_ALP + r32 * 4;
  const unsigned ppst = LDS_WREG + pos * W_SIZE + lane * 16;
  asm volatile("" ::: "v128", "v255");
  unsigned resc = 0;
  float m = -3.0e38f, mprev = -3.0e38f, l = 0.f;

  {
    const unsigned fk = (((unsigned)r32 & 3) << 2) | (((unsigned)r32 >> 2) & 3);
    const unsigned kaddr0 = LDS_RING + r32 * 256 + (((unsigned)hi ^ fk) << 4);
    bf16x8 qf[8];
    wait_above<1>(aland + (CTL_QOK - CTL_LAND), 0);
#pragma unroll
    for (int s = 0; s < 8; ++s) qf[s] = *(LAS bf16x8*)(lds + ((kaddr0 + LDS_Q - LDS_RING) ^ (s << 5)));
#pragma unroll 1
    for (int k = 0; k < nmine; ++k) {
      const unsigned t = w + NC * k;
      wait_above<1>(aland, 4 * t);
      bf16x8 kf[8];
#pragma unroll
      for (int s = 0; s < 8; ++s) kf[s] = *(LAS bf16x8*)(lds + ((kaddr0 + ((t & 7) << 13)) ^ (s << 5)));
      asm volatile("s_waitcnt lgkmcnt(0)" : "+v"(kf[0]), "+v"(kf[1]), "+v"(kf[2]), "+v"(kf[3]), "+v"(kf[4]), "+v"(kf[5]), "+v"(kf[6]), "+v"(kf[7]) :: "memory");
      ctl[CTL_FREE / 4 + (t & 7)] = t + 8;
      if (pf_on) prefetch(t);
      f32x16 sc;
#pragma unroll
      for (int r = 0; r < 16; ++r) sc[r] = 0.f;
#pragma unroll
      for (int s = 0; s < 8; ++s) sc = __builtin_amdgcn_mfma_f32_32x32x16_bf16(kf[s], qf[s], sc, 0, 0, 0);
      if ((int)t == NT - 1) {
        const int kb = 2 * t * PAGE + 4 * hi;
#pragma unroll
        for (int r = 0; r < 16; ++r) sc[r] = (kb + 8 * (r >> 2) + (r & 3) < len) ? sc[r] : -__builtin_inff();
      }
      float mx = sc[0];
#pragma unroll
      for (int r = 1; r < 16; ++r) mx = fmaxf(mx, sc[r]);
      mx = swap_max(mx);
      const float mn = fmaxf(m, mx);
      const float alpha = __builtin_amdgcn_exp2f((m - mn) * CS);
      mprev = m;
      m = mn;
      float ps = 0.f;
#pragma unroll
      for (int r = 0; r < 16; ++r) { sc[r] = __builtin_amdgcn_exp2f((sc[r] - mn) * CS); ps += sc[r]; }
      l = fmaf(l, alpha, ps);
      u32x4 pf[2];
#pragma unroll
      for (int s = 0; s < 2; ++s)
        pf[s] = u32x4{pack2(sc[8 * s], sc[8 * s + 1]), pack2(sc[8 * s + 2], sc[8 * s + 3]), pack2(sc[8 * s + 4], sc[8 * s + 5]), pack2(sc[8 * s + 6], sc[8 * s + 7])};
      if (__builtin_amdgcn_ballot_w64(alpha != 1.0f)) resc |= 1u << k;
      *(LAS float*)(lds + palp + k * 128) = alpha;
      if (k >= KREG || k == nmine - 1) {
        const unsigned dst = ppst + ((k == nmine - 1) ? W_LAST : (k - KREG) * 2048);
        *(LAS u32x4*)(lds + dst) = pf[0];
        *(LAS u32x4*)(lds + dst + 1024) = pf[1];
      } else {
        asm volatile("s_set_gpr_idx_on %8, gpr_idx(DST)\n\t"
                     "v_mov_b32 v128, %0\n\tv_mov_b32 v129, %1\n\tv_mov_b32 v130, %2\n\tv_mov_b32 v131, %3\n\t"
                     "v_mov_b32 v132, %4\n\tv_mov_b32 v133, %5\n\tv_mov_b32 v134, %6\n\tv_mov_b32 v135, %7\n\t"
                     "s_set_gpr_idx_off"
                     :: "v"(pf[0][0]), "v"(pf[0][1]), "v"(pf[0][2]), "v"(pf[0][3]), "v"(pf[1][0]), "v"(pf[1][1]), "v"(pf[1][2]), "v"(pf[1][3]), "s"(8 * k));
      }
    }
  }
  l = swap_sum(l);
  *(LAS f32x2*)(lds + LDS_ML + w * 256 + r32 * 8) = f32x2{m, l};
  LGKM0();
  ctl[CTL_MLOK / 4 + w] = 1;

  const unsigned tq = (lane & 15) >> 2, tp = lane & 3, tblk = (lane >> 4) & 1;
  const unsigned vaddr0 = LDS_RING + (4 * hi + tq) * 256 + ((tq ^ (pos & 3)) << 6) + ((2 * tblk + (tp >> 1)) << 4) + ((tp & 1) << 3);
  f32x16 o[4];
#pragma unroll
  for (int i = 0; i < 4; ++i)
#pragma unroll
    for (int r = 0; r < 16; ++r) o[i][r] = 0.f;
  auto weights = [&]() {
    wait_above<1>(lbase + LDS_CTL + CTL_MLOK + min(lane, NC - 1) * 4, 0);
    unsigned a = LDS_ML + r32 * 8;
    asm volatile("" : "+v"(a) :: "memory");
    float M = -3.0e38f, L = 0.f;
    f32x2 ml[NC];
#pragma unroll
    for (int i = 0; i < NC; ++i) { ml[i] = *(LAS f32x2*)(lds + a + i * 256); M = fmaxf(M, ml[i][0]); }
#pragma unroll
    for (int i = 0; i < NC; ++i) L = fmaf(__builtin_amdgcn_exp2f((ml[i][0] - M) * CS), ml[i][1], L);
    const float inv = 1.0f / L;
    *(LAS float*)(lds + (a >> 1) + (LDS_WT - LDS_ML / 2) + pos * 128) = __builtin_amdgcn_exp2f((mprev - M) * CS) * inv;
    const float ct = __builtin_amdgcn_exp2f((m - M) * CS) * inv;
#pragma unroll
    for (int s = 0; s < 2; ++s) {
      u32x4 x = *(LAS u32x4*)(lds + ppst + W_LAST + s * 1024);
#pragma unroll
      for (int e = 0; e < 4; ++e) x[e] = pack2(__uint_as_float(x[e] << 16) * ct, __uint_as_float(x[e] & 0xffff0000u) * ct);
      *(LAS u32x4*)(lds + ppst + W_LAST + s * 1024) = x;
    }
    LGKM0();
    ctl[CTL_WTOK / 4 + w] = 1;
  };
  resc &= ~1u;
  if (nmine <= 3) weights();
#pragma unroll 1
  for (int k = 0; k < nmine - 1; ++k) {
    if (k == 2) weights();
    if ((resc >> k) & 1) {
      const float alpha = *(LAS float*)(lds + palp + k * 128);
#pragma unroll
      for (int i = 0; i < 4; ++i)
#pragma unroll
        for (int r = 0; r < 16; ++r) o[i][r] *= alpha;
    }
    const unsigned u = NT + ((k == 0 && NT >= 3 * NC) ? pos : w + NC * k);
    u32x4 pf[2];
    if (k >= KREG) {
      pf[0] = *(LAS u32x4*)(lds + ppst + (k - KREG) * 2048);
      pf[1] = *(LAS u32x4*)(lds + ppst + (k - KREG) * 2048 + 1024);
    } else {
      asm volatile("s_set_gpr_idx_on %8, gpr_idx(SRC0)\n\t"
                   "v_mov_b32 %0, v128\n\tv_mov_b32 %1, v129\n\tv_mov_b32 %2, v130\n\tv_mov_b32 %3, v131\n\t"
                   "v_mov_b32 %4, v132\n\tv_mov_b32 %5, v133\n\tv_mov_b32 %6, v134\n\tv_mov_b32 %7, v135\n\t"
                   "s_set_gpr_idx_off\n\ts_nop 1"
                   : "=v"(pf[0][0]), "=v"(pf[0][1]), "=v"(pf[0][2]), "=v"(pf[0][3]), "=v"(pf[1][0]), "=v"(pf[1][1]), "=v"(pf[1][2]), "=v"(pf[1][3])
                   : "s"(8 * k));
    }
    asm volatile("" : "+v"(pf[0]), "+v"(pf[1]), "+v"(o[0]), "+v"(o[1]), "+v"(o[2]), "+v"(o[3]));
    if (k == nmine - 2) wait_above<1>(lbase + LDS_CTL + CTL_WTOK + min(lane, NC - 1) * 4, 0);
    wait_above<1>(aland, 4 * u);
    bf16x8 vf[4][2];
#pragma unroll
    for (int i = 0; i < 4; ++i) {
      const unsigned a = (vaddr0 + ((u & 7) << 13)) ^ (i << 6);
#pragma unroll
      for (int s = 0; s < 2; ++s) {
        const s16x4 lo = __builtin_amdgcn_ds_read_tr16_b64_v4i16((LAS s16x4*)(lds + a + 4096 * s));
        const s16x4 up = __builtin_amdgcn_ds_read_tr16_b64_v4i16((LAS s16x4*)(lds + a + 4096 * s + 2048));
        vf[i][s] = __builtin_bit_cast(bf16x8, __builtin_shufflevector(lo, up, 0, 1, 2, 3, 4, 5, 6, 7));
      }
    }
    asm volatile("s_waitcnt lgkmcnt(0)" : "+v"(vf[0][0]), "+v"(vf[0][1]), "+v"(vf[1][0]), "+v"(vf[1][1]), "+v"(vf[2][0]), "+v"(vf[2][1]), "+v"(vf[3][0]), "+v"(vf[3][1]) :: "memory");
    ctl[CTL_FREE / 4 + (u & 7)] = u + 8;
    if (pf_on) prefetch(u);
#pragma unroll
    for (int s = 0; s < 2; ++s)
#pragma unroll
      for (int i = 0; i < 4; ++i) o[i] = __builtin_amdgcn_mfma_f32_32x32x16_bf16(vf[i][s], __builtin_bit_cast(bf16x8, pf[s]), o[i], 0, 0, 0);
  }

  {
    unsigned l16 = lane * 16;
    asm volatile("" : "+v"(l16));
    const unsigned l4 = l16 >> 2, awt = LDS_WT + (l4 & 127);
    auto put = [&](unsigned dst, const f32x16& x) {
#pragma unroll
      for (int j = 0; j < 4; ++j) *(LAS f32x4*)(lds + dst + j * 1024) = f32x4{x[4 * j], x[4 * j + 1], x[4 * j + 2], x[4 * j + 3]};
    };
    float co[6];
    bf16x8 pl[NC][2];
    auto ld_p = [&](int p) {
      pl[p][0] = *(LAS bf16x8*)(lds + LDS_WREG + p * W_SIZE + W_LAST + l16);
      pl[p][1] = *(LAS bf16x8*)(lds + LDS_WREG + p * W_SIZE + W_LAST + l16 + 1024);
    };
#pragma unroll
    for (int i = 0; i < 4; ++i) co[i] = *(LAS float*)(lds + awt + ((pos & 3) ^ i) * 128);
    co[4] = *(LAS float*)(lds + awt + 4 * 128);
    co[5] = *(LAS float*)(lds + awt + 5 * 128);
    ld_p(0); ld_p(1);
    asm volatile("" : "+v"(l16) :: "memory");
    if (nmine > 1) {
      if (pos > 3) put(LDS_RING + ((2 * NT - 2 * NC + pos) & 7) * 8192 + l16, o[0]);
#pragma unroll
      for (int i = 1; i < 4; ++i) put(LDS_WREG + pos * W_SIZE + (i - 1) * 4096 + l16, o[i]);
      LGKM0();
      ctl[CTL_ODONE / 4 + pos] = 1;
    }
    if (pos < 4) {
      f32x16 of = o[0], ot;
#pragma unroll
      for (int r = 0; r < 16; ++r) ot[r] = 0.f;
      auto src_o = [&](int p) -> unsigned {
        const int i = pos ^ (p & 3);
        return l16 + (i == 0 ? LDS_RING + ((2 * NT - 2 * NC + p) & 7) * 8192 : LDS_WREG + p * W_SIZE + (i - 1) * 4096);
      };
      auto fma_o = [&](unsigned src, float c) {
#pragma unroll
        for (int j = 0; j < 4; ++j) {
          const f32x4 x = *(LAS f32x4*)(lds + src + j * 1024);
#pragma unroll
          for (int e = 0; e < 4; ++e) of[4 * j + e] = fmaf(x[e], c, of[4 * j + e]);
        }
      };
      auto mma_t = [&](int p, bf16x8 p0, bf16x8 p1) {
        const unsigned a = vaddr0 + (((2 * NT - NC + p) & 7) << 13);
        bf16x8 vf[2];
#pragma unroll
        for (int s = 0; s < 2; ++s) {
          const s16x4 lo = __builtin_amdgcn_ds_read_tr16_b64_v4i16((LAS s16x4*)(lds + a + 4096 * s));
          const s16x4 up = __builtin_amdgcn_ds_read_tr16_b64_v4i16((LAS s16x4*)(lds + a + 4096 * s + 2048));
          vf[s] = __builtin_bit_cast(bf16x8, __builtin_shufflevector(lo, up, 0, 1, 2, 3, 4, 5, 6, 7));
        }
        ot = __builtin_amdgcn_mfma_f32_32x32x16_bf16(vf[0], p0, ot, 0, 0, 0);
        ot = __builtin_amdgcn_mfma_f32_32x32x16_bf16(vf[1], p1, ot, 0, 0, 0);
      };
      if (NT >= 2 * NC) {
#define PIN_OF() asm volatile("" : "+v"(of), "+v"(ot))
#pragma unroll
        for (int r = 0; r < 16; ++r) of[r] *= co[0];
        const bool wl = (l4 & 24) == 0;
        const unsigned ua = 4 * (2 * NT - NC);
        wait_above_v(wl ? aland : lbase + LDS_CTL + CTL_ODONE + (l4 & 12), wl ? ua : 0u);
        mma_t(0, pl[0][0], pl[0][1]);
#pragma unroll
        for (int i = 1; i < 4; ++i) fma_o(LDS_WREG + (pos ^ i) * W_SIZE + (i - 1) * 4096 + l16, co[i]);
        PIN_OF();
        ld_p(2); ld_p(3); ld_p(4); ld_p(5);
        wait_above_v(wl ? aland : lbase + LDS_CTL + CTL_ODONE + 16 + (l4 & 4), wl ? ua + 8 : 0u);
        mma_t(1, pl[1][0], pl[1][1]);
        mma_t(2, pl[2][0], pl[2][1]);
        fma_o(src_o(4), co[4]);
        fma_o(src_o(5), co[5]);
#pragma unroll
        for (int r = 0; r < 16; ++r) { ot[r] += of[r]; of[r] = 0.f; }
        asm volatile("" : "+v"(ot));
        wait_above<0>(aland, ua + 20);
        mma_t(3, pl[3][0], pl[3][1]);
        mma_t(4, pl[4][0], pl[4][1]);
        mma_t(5, pl[5][0], pl[5][1]);
        of = ot;
      } else {
        wait_above<1>(lbase + LDS_CTL + CTL_WTOK + min(l4, (NC - 1) * 4u), 0);
        const float cw = *(LAS float*)(lds + awt + pos * 128);
#pragma unroll
        for (int r = 0; r < 16; ++r) of[r] *= cw;
#pragma unroll 1
        for (int p = max(NC - NT, 0); p < NC; ++p) {
          if (p != pos && NT - 2 * NC + p >= 0) {
            wait_above<1>(lbase + LDS_CTL + CTL_ODONE + p * 4, 0);
            fma_o(src_o(p), *(LAS float*)(lds + awt + p * 128));
          }
          wait_above<1>(aland, 4 * (2 * NT - NC + p));
          mma_t(p, *(LAS bf16x8*)(lds + LDS_WREG + p * W_SIZE + W_LAST + l16), *(LAS bf16x8*)(lds + LDS_WREG + p * W_SIZE + W_LAST + l16 + 1024));
        }
#pragma unroll
        for (int r = 0; r < 16; ++r) of[r] += ot[r];
      }
      const __amdgpu_buffer_rsrc_t ro = __builtin_amdgcn_make_buffer_rsrc(outp, 0, NB * HQ * SQ * HD * 2, 0x00020000);
      const unsigned orow = (unsigned)(b * HQ + 2 * h) * SQ * HD * 2 + pos * 64 + ((l16 & 511) << 4) + ((l16 >> 5) & 16);
#pragma unroll
      for (int j = 0; j < 2; ++j) {
        const unsigned a0 = pack2(of[8 * j], of[8 * j + 1]), a1 = pack2(of[8 * j + 2], of[8 * j + 3]);
        const unsigned b0 = pack2(of[8 * j + 4], of[8 * j + 5]), b1 = pack2(of[8 * j + 6], of[8 * j + 7]);
        auto s0 = __builtin_amdgcn_permlane32_swap(a0, b0, false, false);
        auto s1 = __builtin_amdgcn_permlane32_swap(a1, b1, false, false);
        u32x4 val = {s0[0], s1[0], s0[1], s1[1]};
        __builtin_amdgcn_raw_buffer_store_b128(val, ro, orow + j * 32, 0, 0);
      }
    }
  }
}
}

extern "C" void kernel_launch(void* const* d_in, const int* in_sizes, int n_in, void* d_out, int out_size, void* d_ws,
                              size_t ws_size, hipStream_t stream) {
  (void)n_in; (void)out_size; (void)d_ws; (void)ws_size;
  const unsigned nphys = (unsigned)(in_sizes[1] / (PAGE * HKV * HD));
  const unsigned kvbytes = nphys * PAGE_BYTES;
  (void)hipFuncSetAttribute((const void*)paged_attn, hipFuncAttributeMaxDynamicSharedMemorySize, LDS_TOTAL);
  hipLaunchKernelGGL(paged_attn, dim3(256), dim3(512), LDS_TOTAL, stream, d_in[0], d_in[1], d_in[2], (const int*)d_in[3],
                     (const int*)d_in[4], d_out, kvbytes, nphys);
}
```

```cpp
#include <hip/hip_runtime.h>
#include <hip/hip_bf16.h>

#define PACE_X16 433
#define SOFT 15
#define GAP_TICKS 10
#define WIN_TICKS 87
#define WIN_C1 115
#define LATE_TICKS 100
#define HALT_SHIFT 240
#define HALT_FROM 400
#define CODEPF 11520
#define PF_LEAD 180
#define PF_X64 72
#define PF_PER 4
#define PF_PSTEP 2
#define PF_RUN 3

namespace {
constexpr int NB = 16, HQ = 32, HKV = 16, SQ = 16, HD = 128, PAGE = 16, NBPS = 256, SKV = NBPS * PAGE;
constexpr unsigned PAGE_BYTES = PAGE * HKV * HD * 2;
constexpr unsigned TLBG = 0x200000u;
constexpr int U0 = 4;
constexpr int NC = 6;
constexpr int KREG = 16, KLDS = 5;
static_assert(NC * (KREG + KLDS + 1) >= NBPS / 2, "P storage");
constexpr unsigned LDS_RING = 0;
constexpr unsigned LDS_WREG = 65536;
constexpr unsigned W_ALP = KLDS * 2048;
constexpr unsigned W_LAST = W_ALP + (KREG + KLDS + 1) * 128;
constexpr unsigned W_SIZE = W_LAST + 2048;
static_assert(W_LAST >= 3 * 4096 && W_SIZE % 256 == 0, "consumer region");
constexpr unsigned LDS_TBL = LDS_WREG + W_SIZE;
constexpr unsigned LDS_SRT = LDS_WREG + 3 * W_SIZE;
constexpr unsigned LDS_Q = LDS_WREG;
constexpr unsigned LDS_ML = LDS_WREG + NC * W_SIZE;
constexpr unsigned LDS_WT = LDS_ML + NC * 256;
constexpr unsigned LDS_CTL = LDS_WT + NC * 128;
constexpr unsigned CTL_LAND = 0;
constexpr unsigned CTL_QOK = 16;
constexpr unsigned CTL_SORT = 24;
constexpr unsigned CTL_FREE = 32;
constexpr unsigned CTL_MLOK = 64;
constexpr unsigned CTL_ODONE = 96;
constexpr unsigned CTL_WTOK = 128;
constexpr unsigned LDS_DUMMY = LDS_CTL + 256;
constexpr unsigned LDS_PLIST = LDS_DUMMY + 256;
constexpr unsigned LDS_TOTAL = LDS_PLIST + 1024;
static_assert(LDS_TOTAL <= 163840, "lds");

typedef __attribute__((ext_vector_type(8))) __bf16 bf16x8;
typedef __attribute__((ext_vector_type(2))) __bf16 bf16x2;
typedef __attribute__((ext_vector_type(4))) short s16x4;
typedef __attribute__((ext_vector_type(16))) float f32x16;
typedef __attribute__((ext_vector_type(4))) float f32x4;
typedef __attribute__((ext_vector_type(2))) float f32x2;
typedef __attribute__((ext_vector_type(4))) unsigned u32x4;
typedef __attribute__((ext_vector_type(2))) unsigned u32x2;
#define LAS __attribute__((address_space(3)))

__device__ __forceinline__ float swap_max(float x) {
  auto r = __builtin_amdgcn_permlane32_swap(__float_as_uint(x), __float_as_uint(x), false, false);
  return fmaxf(__uint_as_float(r[0]), __uint_as_float(r[1]));
}
__device__ __forceinline__ float swap_sum(float x) {
  auto r = __builtin_amdgcn_permlane32_swap(__float_as_uint(x), __float_as_uint(x), false, false);
  return __uint_as_float(r[0]) + __uint_as_float(r[1]);
}
__device__ __forceinline__ unsigned pack2(float a, float b) {
  bf16x2 v = {(__bf16)a, (__bf16)b};
  return __builtin_bit_cast(unsigned, v);
}
template <int CTRL> __device__ __forceinline__ int dpp_add(int x) { return x + __builtin_amdgcn_update_dpp(0, x, CTRL, 0xf, 0xf, true); }
__device__ __forceinline__ int to_sgpr(int v) {
  int r;
  asm volatile("s_nop 1\n\tv_readfirstlane_b32 %0, %1\n\ts_nop 1" : "=s"(r) : "v"(v));
  return r;
}
#define LGKM0() asm volatile("s_waitcnt lgkmcnt(0)" ::: "memory")
#define VMWAIT(n) asm volatile("s_waitcnt vmcnt(" #n ")" ::: "memory")
#define CBAR() asm volatile("" ::: "memory")
template <int SLEEP> __device__ __forceinline__ void wait_above(unsigned addr, unsigned u) {
  unsigned v, cnt;
  if (SLEEP)
    asm volatile("s_mov_b32 %1, 0x800000\n\ts_branch 2f\n"
                 "1:\n\ts_sleep %4\n"
                 "2:\n\tds_read_b32 %0, %2\n\ts_sub_u32 %1, %1, 1\n\ts_waitcnt lgkmcnt(0)\n\t"
                 "v_cmp_ge_u32 vcc, %3, %0\n\ts_cbranch_scc1 3f\n\ts_nop 1\n\ts_cbranch_vccnz 1b\n3:"
                 : "=&v"(v), "=&s"(cnt) : "v"(addr), "s"(u), "n"(SLEEP) : "memory", "scc", "vcc");
  else
    asm volatile("s_mov_b32 %1, 0x800000\n"
                 "1:\n\tds_read_b32 %0, %2\n\ts_sub_u32 %1, %1, 1\n\ts_waitcnt lgkmcnt(0)\n\t"
                 "v_cmp_ge_u32 vcc, %3, %0\n\ts_cbranch_scc1 3f\n\ts_nop 1\n\ts_cbranch_vccnz 1b\n3:"
                 : "=&v"(v), "=&s"(cnt) : "v"(addr), "s"(u) : "memory", "scc", "vcc");
}

__device__ __forceinline__ void wait_above_v(unsigned addr, unsigned u) {
  unsigned v, cnt;
  asm volatile("s_mov_b32 %1, 0x800000\n"
               "1:\n\tds_read_b32 %0, %2\n\ts_sub_u32 %1, %1, 1\n\ts_waitcnt lgkmcnt(0)\n\t"
               "v_cmp_ge_u32 vcc, %3, %0\n\ts_cbranch_scc1 3f\n\ts_nop 1\n\ts_cbranch_vccnz 1b\n3:"
               : "=&v"(v), "=&s"(cnt) : "v"(addr), "v"(u) : "memory", "scc", "vcc");
}

__global__ void __launch_bounds__(512, 1) __attribute__((amdgpu_num_vgpr(64)))
paged_attn(const void* __restrict__ qp, const void* __restrict__ kp, const void* __restrict__ vp, const int* __restrict__ bt,
           const int* __restrict__ lens, void* __restrict__ outp, unsigned kvbytes, unsigned nphys) {
  extern __shared__ __attribute__((aligned(16))) char smem[];
  LAS char* lds = (LAS char*)smem;
  const int lane0 = threadIdx.x & 63;
  const int wv = __builtin_amdgcn_readfirstlane(threadIdx.x >> 6);
  const int g = blockIdx.x;
  const int h = g >> 4;
  constexpr float CS = 0.08838834764831845f * 1.4426950408889634f;
  volatile LAS unsigned* ctl = (volatile LAS unsigned*)(lds + LDS_CTL);

  const unsigned lbase = (unsigned)(size_t)lds;

  unsigned lraw, first;
  u32x4 lq;
  {
    asm volatile("global_load_dword %0, %3, %4\n\t"
                 "global_load_dwordx4 %1, %5, %4\n\t"
                 "global_load_dword %2, %6, %7"
                 : "=&v"(lraw), "=&v"(lq), "=&v"(first)
                 : "v"((lane0 & 15) * 4), "s"(lens), "v"(lane0 & 48), "v"((lane0 & 15) * (wv < 2 ? NBPS * 4 : 0) + (lane0 >> 4) * 8 + (wv & 1) * 4), "s"(bt),
                   "s"(qp), "s"(kp), "s"(vp), "s"(outp), "s"(kvbytes), "s"(nphys)
                 : "memory");
    __builtin_amdgcn_sched_barrier(0);
  }
  if (wv >= 4) {
    if (wv == 7) {
      const __amdgpu_buffer_rsrc_t rb = __builtin_amdgcn_make_buffer_rsrc((void*)bt, 0, NB * NBPS * 4, 0x00020000);
#define TBL4(SO) "buffer_load_dwordx4 %2, %1, " SO " offen lds\n\tbuffer_load_dwordx4 %2, %1, " SO " offen offset:1024 lds\n\t" \
                 "buffer_load_dwordx4 %2, %1, " SO " offen offset:2048 lds\n\tbuffer_load_dwordx4 %2, %1, " SO " offen offset:3072 lds\n\t"
      asm volatile("s_mov_b32 m0, %0\n\ts_nop 0\n\t" TBL4("0") "s_add_u32 m0, m0, 4096\n\ts_nop 0\n\t" TBL4("%3")
                   "s_add_u32 m0, m0, %6\n\ts_nop 0\n\t" TBL4("%4") "s_add_u32 m0, m0, 4096\n\ts_nop 0\n\t" TBL4("%5")
                   :: "s"(lbase + LDS_TBL), "s"(rb), "v"(lane0 * 16), "s"(4096), "s"(8192), "s"(12288), "n"(W_SIZE - 4096) : "memory", "scc");
    }
    unsigned long long pc;
    asm volatile("s_getpc_b64 %0" : "=s"(pc));
    const __amdgpu_buffer_rsrc_t rt = __builtin_amdgcn_make_buffer_rsrc(
        wv == 5 ? (void*)pc : wv == 4 ? (void*)vp : (void*)kp, 0, wv == 5 ? CODEPF : (wv != 7 && (g & 8)) ? (int)kvbytes : 0, 0x00020000);
    const unsigned mul = wv == 5 ? 512u : 16 * TLBG + 4224u, add = wv == 5 ? ((g >> 3) & 3) * 128 : (g >> 4) * (TLBG + 33024u);
    asm volatile("s_mov_b32 m0, %0\n\ts_nop 0\n\tbuffer_load_dword %1, %2, 0 offen lds"
                 :: "s"(lbase + LDS_DUMMY), "v"((lane0 & 31) * mul + add), "s"(rt) : "memory");
    *(LAS u32x4*)(lds + LDS_SRT + (threadIdx.x - 256) * 16) = u32x4{0u, 0u, 0u, 0u};
    if (threadIdx.x < 296) ctl[lane0] = ((lane0 >> 3) == CTL_FREE / 32) ? (lane0 & 7) : 0u;
    asm volatile("s_waitcnt lgkmcnt(0)\n\ts_barrier" ::: "memory");
  }
  if (wv < 2) __builtin_amdgcn_s_setprio(3);
  const unsigned rstride = wv < 2 ? 4096 : 256;
  unsigned vo0 = (lane0 >> 4) * rstride + (((unsigned)(lane0 & 15) ^ ((lane0 >> 4) << 2)) << 4) + (wv < 2 ? h * (HD * 2) : 0);
  unsigned vo1 = (vo0 + 4 * rstride - 1024) ^ 16, vo2 = (vo0 + 8 * rstride - 2048) ^ 32, vo3 = (vo0 + 12 * rstride - 3072) ^ 48;
  const unsigned want = (g & 8) ? 15 - (g & 7) : (g & 7);
  const unsigned dst0 = lbase + (wv < 2 ? LDS_RING : LDS_Q) + (wv & 1) * 4096;
  asm volatile("s_cmp_ge_u32 %8, 4\n\ts_cbranch_scc1 1f\n\ts_waitcnt vmcnt(0)\n1:\n\ts_waitcnt vmcnt(1)" : "+v"(lraw), "+v"(lq), "+v"(first), "+v"(vo0), "+v"(vo1), "+v"(vo2), "+v"(vo3) : "s"(dst0), "s"(wv) : "memory", "scc");
  unsigned lm1 = min(lraw - 1u, (unsigned)SKV - 1u);
  unsigned rank = 0;
  {
    const unsigned key = lm1 * 16 + (lane0 & 15);
#pragma unroll
    for (int j = 0; j < 4; ++j) rank += (min(lq[j] - 1u, (unsigned)SKV - 1u) * 16 + ((lane0 >> 4) * 4 + j) > key) ? 1u : 0u;
    auto r1 = __builtin_amdgcn_permlane16_swap(rank, rank, false, false);
    rank = r1[0] + r1[1];
    auto r2 = __builtin_amdgcn_permlane32_swap(rank, rank, false, false);
    rank = r2[0] + r2[1];
  }
  first = min(first, nphys - 1) * PAGE_BYTES;
  const int b = __builtin_ctz((unsigned)__builtin_amdgcn_ballot_w64(rank == want) | 0x8000u);
  unsigned long long tstart = 0;
  if (wv < 4) {
    const bool qok = (int)__builtin_amdgcn_readlane(lraw, b) > 0;
    const __amdgpu_buffer_rsrc_t r0 = __builtin_amdgcn_make_buffer_rsrc(wv < 2 ? (void*)kp : (void*)qp, 0,
                                                                        wv < 2 ? (int)kvbytes : qok ? NB * HQ * SQ * HD * 2 : 0, 0x00020000);
    const unsigned so = wv < 2 ? __builtin_amdgcn_readlane(first, b) : qok ? (unsigned)((b * HQ + 2 * h) * SQ * HD * 2) + (wv & 1) * 4096 : 0u;
    asm volatile("s_waitcnt vmcnt(0)\n\t"
                 "s_mov_b32 m0, %2\n\t"
                 "s_memrealtime %0\n\t"
                 "s_nop 2\n\t"
                 "buffer_load_dwordx4 %4, %1, %3 offen nt lds\n\t"
                 "buffer_load_dwordx4 %5, %1, %3 offen offset:1024 nt lds\n\t"
                 "buffer_load_dwordx4 %6, %1, %3 offen offset:2048 nt lds\n\t"
                 "buffer_load_dwordx4 %7, %1, %3 offen offset:3072 nt lds\n\t"
                 "s_waitcnt lgkmcnt(0)\n\t"
                 "s_barrier"
                 : "=&s"(tstart) : "s"(r0), "s"(dst0), "s"(so), "v"(vo0), "v"(vo1), "v"(vo2), "v"(vo3) : "memory");
  }
  asm volatile("" : "+v"(lm1), "+v"(rank), "+v"(first) :: "memory");
  __builtin_amdgcn_sched_barrier(0);
  const int bp = __builtin_ctz((unsigned)__builtin_amdgcn_ballot_w64(rank == 15 - want) | 0x8000u);
  const int len = __builtin_amdgcn_readlane(lm1, b) + 1, nown = (len + PAGE - 1) / PAGE, NT = (nown + 1) >> 1;
  const int npl = (__builtin_amdgcn_readlane(lm1, bp) >> 4) + 1, NTL = (npl + 1) >> 1;
  auto total = [&]() __attribute__((always_inline)) {
    int t = lm1 + 1;
    t = dpp_add<0x121>(t); t = dpp_add<0x122>(t); t = dpp_add<0x124>(t); t = dpp_add<0x128>(t);
    return t;
  };

  auto sort_pages = [&](int lane, int bb, int n, unsigned scr, unsigned dst) __attribute__((always_inline)) {
    u32x4 e = *(LAS u32x4*)(lds + LDS_TBL + (bb >> 3) * W_SIZE + (bb & 7) * 1024 + lane * 16);
    bool in[4];
#pragma unroll
    for (int j = 0; j < 4; ++j) {
      e[j] = min(e[j], nphys - 1);
      in[j] = 4 * lane + j >= 2 * U0 && 4 * lane + j < n - 1;
      __hip_atomic_fetch_or((LAS unsigned*)(lds + scr + ((e[j] >> 5) & 255) * 8), in[j] ? 1u << (e[j] & 31) : 0u, __ATOMIC_RELAXED, __HIP_MEMORY_SCOPE_WAVEFRONT);
    }
    CBAR();
    u32x4 a = *(LAS u32x4*)(lds + scr + lane * 32), c = *(LAS u32x4*)(lds + scr + lane * 32 + 16);
    const int n0 = __builtin_popcount(a[0]), n1 = n0 + __builtin_popcount(a[2]), n2 = n1 + __builtin_popcount(c[0]), n3 = n2 + __builtin_popcount(c[2]);
    int sc = n3;
    sc = dpp_add<0x111>(sc); sc = dpp_add<0x112>(sc); sc = dpp_add<0x114>(sc); sc = dpp_add<0x118>(sc);
    sc += __builtin_amdgcn_update_dpp(0, sc, 0x142, 0xa, 0xf, false);
    sc += __builtin_amdgcn_update_dpp(0, sc, 0x143, 0xc, 0xf, false);
    const bool ok = nphys <= 8192u && __builtin_amdgcn_readlane(sc, 63) == n - 1 - 2 * U0;
    sc += 2 * U0 - n3;
    a[1] = sc; a[3] = sc + n0; c[1] = sc + n1; c[3] = sc + n2;
    *(LAS u32x4*)(lds + scr + lane * 32) = a;
    *(LAS u32x4*)(lds + scr + lane * 32 + 16) = c;
    CBAR();
    u32x2 wp[4];
#pragma unroll
    for (int j = 0; j < 4; ++j) wp[j] = *(LAS u32x2*)(lds + scr + ((e[j] >> 5) & 255) * 8);
#pragma unroll
    for (int j = 0; j < 4; ++j) {
      const unsigned r = wp[j][1] + __builtin_popcount(wp[j][0] & ((1u << (e[j] & 31)) - 1u));
      *(LAS unsigned*)(lds + dst + ((ok && in[j]) ? r : 4 * lane + j) * 4) = e[j] * PAGE_BYTES;
    }
    CBAR();
  };

  if (__builtin_expect(wv < 2, 1)) {
    const int half = wv, lane = lane0;
    const unsigned vland = lbase + LDS_CTL + CTL_LAND + half * 4;
    const unsigned vfree = lbase + LDS_CTL + CTL_FREE + (lane & 7) * 4;
    const unsigned ring = dst0;
    unsigned l0 = __builtin_amdgcn_ds_bpermute((b + 16 * lane) * 4, first), l1 = 0;
    const unsigned a0 = lbase + LDS_SRT + 4096 + min(2 * lane + half, nown - 1) * 4, a1 = lbase + LDS_SRT + 4096 + min(2 * lane + half + 128, nown - 1) * 4;
    const int rot = (NT >= 3 * NC) ? NT % NC : 0;
    const unsigned vperm = (lane < NC ? (lane + rot >= NC ? lane + rot - NC : lane + rot) : lane) * 4;
    const int T16 = total() * PACE_X16;
    const int step = to_sgpr((int)((float)T16 * __builtin_amdgcn_rcpf((float)max(len, 32))));
    const int win = to_sgpr((int)((float)(((h & 3) == 1 ? WIN_C1 : WIN_TICKS) * 1024) * __builtin_amdgcn_rcpf((float)step) + 0.5f));
    const int hmin = to_sgpr((int)((float)(HALT_FROM * 1024) * __builtin_amdgcn_rcpf((float)step)));
    const unsigned due0 = ((unsigned)tstart << 8) + half * (step >> 1);
    const int lrows = len - (2 * (NT - 1) + half) * PAGE;
    unsigned vf, vt, lc;
    asm volatile(
        "s_waitcnt lgkmcnt(0)\n\t"
        "s_memrealtime s[68:69]\n\t"
        "ds_read_b32 %[vf], %[vfree]\n\t"
        "s_mov_b32 s60, 1\n\t"
        "s_mov_b32 s61, 4\n\t"
        "s_mov_b32 s62, 0x1000000\n\t"
        "s_mov_b32 s66, 0\n\t"
        "s_min_u32 s67, %[nt], %[u0]\n\t"
        "s_mov_b32 s75, %[nrec]\n\t"
        "s_mov_b32 s72, 0x2000\n\t"
        "s_mov_b32 s73, 1\n\t"
        "s_mov_b32 s74, %[due1]\n\t"
        "s_mov_b32 s85, %[duev]\n\t"
        "s_mov_b32 s86, %[late]\n\t"
        "s_mov_b64 s[76:77], %[kp]\n\t"
        "s_mov_b32 s78, %[nrec]\n\t"
        "s_mov_b32 s79, 0x20000\n\t"
        "s_and_b32 s77, s77, 0xffff\n\t"
        "v_mov_b32 %[lc], %[l0]\n\t"
        "s_getpc_b64 s[70:71]\n"
        ".LldA%=:\n\t"
        "s_add_u32 s80, s70, .LldL%=-.LldA%=\n\t"
        "s_addc_u32 s71, s71, 0\n\t"
        "s_branch .LldF%=\n"
        ".LldR%=:\n\t"
        "s_sub_u32 s62, s62, 1\n\t"
        "s_cbranch_scc1 .LldE%=\n"
        ".LldT%=:\n\t"
        "s_getreg_b32 s64, hwreg(HW_REG_IB_STS)\n\t"
        "s_and_b32 s65, s64, 15\n\t"
        "s_bfe_u32 s64, s64, 0x20016\n\t"
        "s_lshl4_add_u32 s81, s64, s65\n\t"
        "s_add_u32 s64, s81, 3\n\t"
        "s_and_b32 s64, s64, 60\n\t"
        "s_sub_u32 s65, s61, s64\n\t"
        "s_cmp_lg_u32 s65, s66\n\t"
        "s_cbranch_scc1 .LldP%=\n"
        ".LldC%=:\n\t"
        "s_waitcnt lgkmcnt(0)\n\t"
        "v_cmp_eq_u32 vcc, s60, %[vf]\n\t"
        "s_lshl_b32 s64, s68, 8\n\t"
        "s_memrealtime s[68:69]\n\t"
        "ds_read_b32 %[vf], %[vfree]\n\t"
        "s_sub_u32 s64, s64, s74\n\t"
        "s_cmp_lt_i32 s64, 0\n\t"
        "s_cbranch_scc1 .LldR%=\n\t"
        "s_cmp_gt_i32 s64, s86\n\t"
        "s_cbranch_scc1 .LldH%=\n"
        ".LldI%=:\n\t"
        "s_cbranch_vccz .LldR%=\n\t"
        "s_cmp_gt_u32 s81, %[win]\n\t"
        "s_cbranch_scc1 .LldR%=\n\t"
        "buffer_load_dwordx4 %[o0], s[76:79], s63 offen nt lds\n\t"
        "s_add_u32 s74, s74, %[step]\n\t"
        "s_add_u32 s73, s73, 1\n\t"
        "buffer_load_dwordx4 %[o1], s[76:79], s63 offen offset:1024 nt lds\n\t"
        "s_add_u32 s60, s60, 1\n\t"
        "s_add_u32 s61, s61, 4\n\t"
        "s_add_u32 s86, s64, %[late]\n\t"
        "buffer_load_dwordx4 %[o2], s[76:79], s63 offen offset:2048 nt lds\n\t"
        "s_add_u32 s72, s72, 0x2000\n\t"
        "s_and_b32 s72, s72, 0xe000\n\t"
        "buffer_load_dwordx4 %[o3], s[76:79], s63 offen offset:3072 nt lds\n"
        ".LldF%=:\n\t"
        "v_readlane_b32 s63, %[lc], s73\n\t"
        "s_add_u32 m0, s72, %[ring]\n\t"
        "s_cmp_eq_u32 s73, %[ntm]\n\t"
        "s_cselect_b32 s78, s75, %[nrec]\n\t"
        "s_cmp_lg_u32 s60, s67\n\t"
        "s_cbranch_scc1 .LldT%=\n\t"
        "s_cmp_lg_u32 s60, %[u0]\n\t"
        "s_cbranch_scc1 .LldG%=\n\t"
        "s_cmp_le_u32 %[nt], %[u0]\n\t"
        "s_cbranch_scc1 .LldG%=\n"
        ".LldS%=:\n\t"
        "ds_read_b32 %[lc], %[vland] offset:%[srt]\n\t"
        "ds_read_b32 %[l0], %[a0]\n\t"
        "ds_read_b32 %[l1], %[a1]\n\t"
        "s_sub_u32 s62, s62, 1\n\t"
        "s_cbranch_scc1 .LldE%=\n\t"
        "s_waitcnt lgkmcnt(0)\n\t"
        "v_readfirstlane_b32 s64, %[lc]\n\t"
        "s_cmp_eq_u32 s64, 0\n\t"
        "s_cbranch_scc1 .LldS%=\n\t"
        "s_max_i32 s65, %[lrows], 0\n\t"
        "s_lshl_b32 s65, s65, 12\n\t"
        "s_add_u32 s64, s64, s65\n\t"
        "s_sub_u32 s64, s64, 1\n\t"
        "s_cmp_lt_i32 %[lrows], 16\n\t"
        "s_cselect_b32 s75, s64, %[nrec]\n\t"
        "s_min_u32 s67, %[nt], 64\n\t"
        "v_mov_b32 %[lc], %[l0]\n\t"
        "s_branch .LldF%=\n"
        ".LldG%=:\n\t"
        "s_cmp_eq_u32 s73, %[nt]\n\t"
        "s_cbranch_scc1 .LldV%=\n\t"
        "v_mov_b32 %[lc], %[l1]\n\t"
        "v_readfirstlane_b32 s63, %[l1]\n\t"
        "s_add_u32 s67, s60, %[nt]\n\t"
        "s_sub_u32 s67, s67, 64\n\t"
        "s_branch .LldT%=\n"
        ".LldV%=:\n\t"
        "s_cmp_lg_u32 s60, %[nt]\n\t"
        "s_cbranch_scc1 .LldD%=\n\t"
        "s_mov_b64 s[76:77], %[vp]\n\t"
        "s_and_b32 s77, s77, 0xffff\n\t"
        "s_mov_b32 s73, 0\n\t"
        "s_min_u32 s67, %[nt], 64\n\t"
        "s_add_u32 s67, s67, s60\n\t"
        "s_mov_b32 s74, s85\n\t"
        "s_cmp_eq_u32 %[ntm], 0\n\t"
        "s_cselect_b32 s78, s75, %[nrec]\n\t"
        "ds_bpermute_b32 %[lc], %[vperm], %[l0]\n\t"
        "v_xor_b32 %[o1], 16, %[o1]\n\t"
        "v_xor_b32 %[o2], 32, %[o2]\n\t"
        "v_xor_b32 %[o3], 48, %[o3]\n\t"
        "s_waitcnt lgkmcnt(0)\n\t"
        "v_readfirstlane_b32 s63, %[lc]\n\t"
        "s_branch .LldT%=\n"
        ".LldH%=:\n\t"
        "s_cmp_lt_u32 s61, %[hmin]\n\t"
        "s_cbranch_scc1 .LldI%=\n\t"
        "s_add_u32 s74, s74, %[hshift]\n\t"
        "s_add_u32 s85, s85, %[hshift]\n\t"
        "s_branch .LldR%=\n"
        ".LldD%=:\n\t"
        "s_add_u32 s74, s74, 0x40000000\n\t"
        "s_mov_b32 s60, -1\n\t"
        "s_branch .LldT%=\n"
        ".LldP%=:\n\t"
        "s_min_u32 s64, s64, 32\n\t"
        "s_add_u32 s70, s80, s64\n\t"
        "v_mov_b32 %[vt], s65\n\t"
        "s_mov_b32 s66, s65\n\t"
        "s_setpc_b64 s[70:71]\n\t"
        ".p2align 6\n"
        ".LldL%=:\n\t"
        "s_waitcnt vmcnt(0)\n\t"
        "s_waitcnt vmcnt(4)\n\t"
        "s_waitcnt vmcnt(8)\n\t"
        "s_waitcnt vmcnt(12)\n\t"
        "s_waitcnt vmcnt(16)\n\t"
        "s_waitcnt vmcnt(20)\n\t"
        "s_waitcnt vmcnt(24)\n\t"
        "s_waitcnt vmcnt(28)\n\t"
        "s_waitcnt vmcnt(32)\n\t"
        "ds_write_b32 %[vland], %[vt]\n\t"
        "s_cmp_lg_u32 s65, %[fin]\n\t"
        "s_cbranch_scc1 .LldC%=\n"
        ".LldE%=:\n\t"
        "s_waitcnt vmcnt(0) lgkmcnt(0)\n\t"
        : [vf] "=&v"(vf), [vt] "=&v"(vt), [lc] "=&v"(lc), [l0] "+&v"(l0), [l1] "+&v"(l1), [o1] "+&v"(vo1), [o2] "+&v"(vo2), [o3] "+&v"(vo3)
        : [vperm] "v"(vperm), [a0] "v"(a0), [a1] "v"(a1), [o0] "v"(vo0), [vland] "v"(vland), [vfree] "v"(vfree),
          [kp] "s"(kp), [vp] "s"(vp), [nrec] "s"(kvbytes), [nt] "s"(NT), [ring] "s"(ring),
          [step] "s"(step), [due1] "s"(due0 + step + SOFT * 256), [duev] "s"(due0 + (to_sgpr(T16) >> 5) + (SOFT + GAP_TICKS) * 256), [lrows] "s"(lrows), [ntm] "s"(NT - 1), [fin] "s"(8 * NT), [u0] "n"(U0), [win] "s"(win), [late] "n"(LATE_TICKS * 256), [hshift] "n"(HALT_SHIFT * 256), [hmin] "s"(hmin), [srt] "n"(CTL_SORT - CTL_LAND)
        : "memory", "scc", "vcc", "s60", "s61", "s62", "s63", "s64", "s65", "s66", "s67", "s68", "s69", "s70", "s71", "s72", "s73", "s74", "s75",
          "s76", "s77", "s78", "s79", "s80", "s81", "s85", "s86");
    asm volatile("s_endpgm" ::: "memory");
  }

  int lane = lane0;
  asm volatile("" : "+v"(lane));
  if (wv == 7) {
#pragma unroll 1
    for (int i = 0; i < 2; ++i) {
      sort_pages(lane, i ? bp : b, i ? npl : nown, LDS_SRT + i * 2048, i ? LDS_PLIST : LDS_SRT + 4096);
      if (i == 0) {
        const unsigned lo = *(LAS unsigned*)(lds + LDS_SRT + 4096 + (nown - 1) * 4) + 1;
        LGKM0();
        ctl[CTL_SORT / 4 + (lane & 1)] = lo;
      }
    }
  }
  if (wv < 4) {
    VMWAIT(0);
    ctl[CTL_QOK / 4 + (wv & 1)] = 1;
  }
  const int tot = total();
  const int w = wv - 2;
  const int r32 = lane & 31, hi = lane >> 5;
  const int nmine = (NT > w) ? (NT - w + NC - 1) / NC : 0;
  const unsigned aland = lbase + LDS_CTL + CTL_LAND + (lane & 1) * 4;
  const int tots = __builtin_amdgcn_readfirstlane(tot);
  const bool pf_on = npl * 256 * 64 > tots * PF_X64 && npl > nown && (h & 3) == 0;
  const unsigned pf_last = 2 * NT - to_sgpr((int)((float)(2 * NT * (PF_LEAD + 23 * NC)) * 4096.0f * __builtin_amdgcn_rcpf((float)(tot * PACE_X16))));
  const unsigned pf_ratio = to_sgpr((int)((float)NTL * 65536.0f * __builtin_amdgcn_rcpf((float)NT)));
  const unsigned pf_per = PF_PER + (g & 7) * PF_PSTEP, pf_inv = (65536 + pf_per - 1) / pf_per;
  auto prefetch = [&](unsigned op) {
    const unsigned d = pf_last - op;
    if (d >= 1024u) return;
    const unsigned r = d - ((d * pf_inv) >> 16) * pf_per;
    if (r >= PF_RUN) return;
    unsigned tl = 2 * NTL - 1 - NC - (((d - r) * pf_ratio) >> 16) - r;
    if (tl >= 2u * NTL) return;
    const bool isv = tl >= (unsigned)NTL;
    tl -= isv ? NTL : 0;
    const __amdgpu_buffer_rsrc_t rp = __builtin_amdgcn_make_buffer_rsrc(isv ? (void*)vp : (void*)kp, 0, (int)kvbytes, 0x00020000);
    unsigned ln = lane;
    asm volatile("" : "+v"(ln));
    const unsigned vo = (*(LAS unsigned*)(lds + LDS_PLIST + min(2 * tl + (ln >> 5), (unsigned)npl - 1) * 4) & 0x7fff0000u) + (h + 1) * (HD * 2) +
                        ((ln >> 1) & 15) * 4096 + (ln & 1) * 128;
    asm volatile("s_mov_b32 m0, %0\n\ts_nop 4\n\tbuffer_load_dword %2, %1, 0 offen lds" :: "s"(lbase + LDS_DUMMY), "s"(rp), "v"(vo) : "memory");
  };
  const int pos = (w + NC * 22 - NT) % NC;
  const unsigned palp = LDS_WREG + pos * W_SIZE + W_ALP + r32 * 4;
  const unsigned ppst = LDS_WREG + pos * W_SIZE + lane * 16;
  asm volatile("" ::: "v128", "v255");
  unsigned resc = 0;
  float m = -3.0e38f, mprev = -3.0e38f, l = 0.f;

  {
    const unsigned fk = (((unsigned)r32 & 3) << 2) | (((unsigned)r32 >> 2) & 3);
    const unsigned kaddr0 = LDS_RING + r32 * 256 + (((unsigned)hi ^ fk) << 4);
    bf16x8 qf[8];
    wait_above<1>(aland + (CTL_QOK - CTL_LAND), 0);
#pragma unroll
    for (int s = 0; s < 8; ++s) qf[s] = *(LAS bf16x8*)(lds + ((kaddr0 + LDS_Q - LDS_RING) ^ (s << 5)));
#pragma unroll 1
    for (int k = 0; k < nmine; ++k) {
      const unsigned t = w + NC * k;
      wait_above<1>(aland, 4 * t);
      bf16x8 kf[8];
#pragma unroll
      for (int s = 0; s < 8; ++s) kf[s] = *(LAS bf16x8*)(lds + ((kaddr0 + ((t & 7) << 13)) ^ (s << 5)));
      asm volatile("s_waitcnt lgkmcnt(0)" : "+v"(kf[0]), "+v"(kf[1]), "+v"(kf[2]), "+v"(kf[3]), "+v"(kf[4]), "+v"(kf[5]), "+v"(kf[6]), "+v"(kf[7]) :: "memory");
      ctl[CTL_FREE / 4 + (t & 7)] = t + 8;
      if (pf_on) prefetch(t);
      f32x16 sc;
#pragma unroll
      for (int r = 0; r < 16; ++r) sc[r] = 0.f;
#pragma unroll
      for (int s = 0; s < 8; ++s) sc = __builtin_amdgcn_mfma_f32_32x32x16_bf16(kf[s], qf[s], sc, 0, 0, 0);
      if ((int)t == NT - 1) {
        const int kb = 2 * t * PAGE + 4 * hi;
#pragma unroll
        for (int r = 0; r < 16; ++r) sc[r] = (kb + 8 * (r >> 2) + (r & 3) < len) ? sc[r] : -__builtin_inff();
      }
      float mx = sc[0];
#pragma unroll
      for (int r = 1; r < 16; ++r) mx = fmaxf(mx, sc[r]);
      mx = swap_max(mx);
      const float mn = fmaxf(m, mx);
      const float alpha = __builtin_amdgcn_exp2f((m - mn) * CS);
      mprev = m;
      m = mn;
      float ps = 0.f;
#pragma unroll
      for (int r = 0; r < 16; ++r) { sc[r] = __builtin_amdgcn_exp2f((sc[r] - mn) * CS); ps += sc[r]; }
      l = fmaf(l, alpha, ps);
      u32x4 pf[2];
#pragma unroll
      for (int s = 0; s < 2; ++s)
        pf[s] = u32x4{pack2(sc[8 * s], sc[8 * s + 1]), pack2(sc[8 * s + 2], sc[8 * s + 3]), pack2(sc[8 * s + 4], sc[8 * s + 5]), pack2(sc[8 * s + 6], sc[8 * s + 7])};
      if (__builtin_amdgcn_ballot_w64(alpha != 1.0f)) resc |= 1u << k;
      *(LAS float*)(lds + palp + k * 128) = alpha;
      if (k >= KREG || k == nmine - 1) {
        const unsigned dst = ppst + ((k == nmine - 1) ? W_LAST : (k - KREG) * 2048);
        *(LAS u32x4*)(lds + dst) = pf[0];
        *(LAS u32x4*)(lds + dst + 1024) = pf[1];
      } else {
        asm volatile("s_set_gpr_idx_on %8, gpr_idx(DST)\n\t"
                     "v_mov_b32 v128, %0\n\tv_mov_b32 v129, %1\n\tv_mov_b32 v130, %2\n\tv_mov_b32 v131, %3\n\t"
                     "v_mov_b32 v132, %4\n\tv_mov_b32 v133, %5\n\tv_mov_b32 v134, %6\n\tv_mov_b32 v135, %7\n\t"
                     "s_set_gpr_idx_off"
                     :: "v"(pf[0][0]), "v"(pf[0][1]), "v"(pf[0][2]), "v"(pf[0][3]), "v"(pf[1][0]), "v"(pf[1][1]), "v"(pf[1][2]), "v"(pf[1][3]), "s"(8 * k));
      }
    }
  }
  l = swap_sum(l);
  *(LAS f32x2*)(lds + LDS_ML + w * 256 + r32 * 8) = f32x2{m, l};
  LGKM0();
  ctl[CTL_MLOK / 4 + w] = 1;

  const unsigned tq = (lane & 15) >> 2, tp = lane & 3, tblk = (lane >> 4) & 1;
  const unsigned vaddr0 = LDS_RING + (4 * hi + tq) * 256 + ((tq ^ (pos & 3)) << 6) + ((2 * tblk + (tp >> 1)) << 4) + ((tp & 1) << 3);
  f32x16 o[4];
#pragma unroll
  for (int i = 0; i < 4; ++i)
#pragma unroll
    for (int r = 0; r < 16; ++r) o[i][r] = 0.f;
  auto weights = [&]() {
    wait_above<1>(lbase + LDS_CTL + CTL_MLOK + min(lane, NC - 1) * 4, 0);
    unsigned a = LDS_ML + r32 * 8;
    asm volatile("" : "+v"(a) :: "memory");
    float M = -3.0e38f, L = 0.f;
    f32x2 ml[NC];
#pragma unroll
    for (int i = 0; i < NC; ++i) { ml[i] = *(LAS f32x2*)(lds + a + i * 256); M = fmaxf(M, ml[i][0]); }
#pragma unroll
    for (int i = 0; i < NC; ++i) L = fmaf(__builtin_amdgcn_exp2f((ml[i][0] - M) * CS), ml[i][1], L);
    const float inv = 1.0f / L;
    *(LAS float*)(lds + (a >> 1) + (LDS_WT - LDS_ML / 2) + pos * 128) = __builtin_amdgcn_exp2f((mprev - M) * CS) * inv;
    const float ct = __builtin_amdgcn_exp2f((m - M) * CS) * inv;
#pragma unroll
    for (int s = 0; s < 2; ++s) {
      u32x4 x = *(LAS u32x4*)(lds + ppst + W_LAST + s * 1024);
#pragma unroll
      for (int e = 0; e < 4; ++e) x[e] = pack2(__uint_as_float(x[e] << 16) * ct, __uint_as_float(x[e] & 0xffff0000u) * ct);
      *(LAS u32x4*)(lds + ppst + W_LAST + s * 1024) = x;
    }
    LGKM0();
    ctl[CTL_WTOK / 4 + w] = 1;
  };
  resc &= ~1u;
  if (nmine <= 3) weights();
#pragma unroll 1
  for (int k = 0; k < nmine - 1; ++k) {
    if (k == 2) weights();
    if ((resc >> k) & 1) {
      const float alpha = *(LAS float*)(lds + palp + k * 128);
#pragma unroll
      for (int i = 0; i < 4; ++i)
#pragma unroll
        for (int r = 0; r < 16; ++r) o[i][r] *= alpha;
    }
    const unsigned u = NT + ((k == 0 && NT >= 3 * NC) ? pos : w + NC * k);
    u32x4 pf[2];
    if (k >= KREG) {
      pf[0] = *(LAS u32x4*)(lds + ppst + (k - KREG) * 2048);
      pf[1] = *(LAS u32x4*)(lds + ppst + (k - KREG) * 2048 + 1024);
    } else {
      asm volatile("s_set_gpr_idx_on %8, gpr_idx(SRC0)\n\t"
                   "v_mov_b32 %0, v128\n\tv_mov_b32 %1, v129\n\tv_mov_b32 %2, v130\n\tv_mov_b32 %3, v131\n\t"
                   "v_mov_b32 %4, v132\n\tv_mov_b32 %5, v133\n\tv_mov_b32 %6, v134\n\tv_mov_b32 %7, v135\n\t"
                   "s_set_gpr_idx_off\n\ts_nop 1"
                   : "=v"(pf[0][0]), "=v"(pf[0][1]), "=v"(pf[0][2]), "=v"(pf[0][3]), "=v"(pf[1][0]), "=v"(pf[1][1]), "=v"(pf[1][2]), "=v"(pf[1][3])
                   : "s"(8 * k));
    }
    asm volatile("" : "+v"(pf[0]), "+v"(pf[1]), "+v"(o[0]), "+v"(o[1]), "+v"(o[2]), "+v"(o[3]));
    if (k == nmine - 2) wait_above<1>(lbase + LDS_CTL + CTL_WTOK + min(lane, NC - 1) * 4, 0);
    wait_above<1>(aland, 4 * u);
    bf16x8 vf[4][2];
#pragma unroll
    for (int i = 0; i < 4; ++i) {
      const unsigned a = (vaddr0 + ((u & 7) << 13)) ^ (i << 6);
#pragma unroll
      for (int s = 0; s < 2; ++s) {
        const s16x4 lo = __builtin_amdgcn_ds_read_tr16_b64_v4i16((LAS s16x4*)(lds + a + 4096 * s));
        const s16x4 up = __builtin_amdgcn_ds_read_tr16_b64_v4i16((LAS s16x4*)(lds + a + 4096 * s + 2048));
        vf[i][s] = __builtin_bit_cast(bf16x8, __builtin_shufflevector(lo, up, 0, 1, 2, 3, 4, 5, 6, 7));
      }
    }
    asm volatile("s_waitcnt lgkmcnt(0)" : "+v"(vf[0][0]), "+v"(vf[0][1]), "+v"(vf[1][0]), "+v"(vf[1][1]), "+v"(vf[2][0]), "+v"(vf[2][1]), "+v"(vf[3][0]), "+v"(vf[3][1]) :: "memory");
    ctl[CTL_FREE / 4 + (u & 7)] = u + 8;
    if (pf_on) prefetch(u);
#pragma unroll
    for (int s = 0; s < 2; ++s)
#pragma unroll
      for (int i = 0; i < 4; ++i) o[i] = __builtin_amdgcn_mfma_f32_32x32x16_bf16(vf[i][s], __builtin_bit_cast(bf16x8, pf[s]), o[i], 0, 0, 0);
  }

  {
    unsigned l16 = lane * 16;
    asm volatile("" : "+v"(l16));
    const unsigned l4 = l16 >> 2, awt = LDS_WT + (l4 & 127);
    auto put = [&](unsigned dst, const f32x16& x) {
#pragma unroll
      for (int j = 0; j < 4; ++j) *(LAS f32x4*)(lds + dst + j * 1024) = f32x4{x[4 * j], x[4 * j + 1], x[4 * j + 2], x[4 * j + 3]};
    };
    float co[6];
    bf16x8 pl[NC][2];
    auto ld_p = [&](int p) {
      pl[p][0] = *(LAS bf16x8*)(lds + LDS_WREG + p * W_SIZE + W_LAST + l16);
      pl[p][1] = *(LAS bf16x8*)(lds + LDS_WREG + p * W_SIZE + W_LAST + l16 + 1024);
    };
#pragma unroll
    for (int i = 0; i < 4; ++i) co[i] = *(LAS float*)(lds + awt + ((pos & 3) ^ i) * 128);
    co[4] = *(LAS float*)(lds + awt + 4 * 128);
    co[5] = *(LAS float*)(lds + awt + 5 * 128);
    ld_p(0); ld_p(1);
    asm volatile("" : "+v"(l16) :: "memory");
    if (nmine > 1) {
      if (pos > 3) put(LDS_RING + ((2 * NT - 2 * NC + pos) & 7) * 8192 + l16, o[0]);
#pragma unroll
      for (int i = 1; i < 4; ++i) put(LDS_WREG + pos * W_SIZE + (i - 1) * 4096 + l16, o[i]);
      LGKM0();
      ctl[CTL_ODONE / 4 + pos] = 1;
    }
    if (pos < 4) {
      f32x16 of = o[0], ot;
#pragma unroll
      for (int r = 0; r < 16; ++r) ot[r] = 0.f;
      auto src_o = [&](int p) -> unsigned {
        const int i = pos ^ (p & 3);
        return l16 + (i == 0 ? LDS_RING + ((2 * NT - 2 * NC + p) & 7) * 8192 : LDS_WREG + p * W_SIZE + (i - 1) * 4096);
      };
      auto fma_o = [&](unsigned src, float c) {
#pragma unroll
        for (int j = 0; j < 4; ++j) {
          const f32x4 x = *(LAS f32x4*)(lds + src + j * 1024);
#pragma unroll
          for (int e = 0; e < 4; ++e) of[4 * j + e] = fmaf(x[e], c, of[4 * j + e]);
        }
      };
      auto mma_t = [&](int p, bf16x8 p0, bf16x8 p1) {
        const unsigned a = vaddr0 + (((2 * NT - NC + p) & 7) << 13);
        bf16x8 vf[2];
#pragma unroll
        for (int s = 0; s < 2; ++s) {
          const s16x4 lo = __builtin_amdgcn_ds_read_tr16_b64_v4i16((LAS s16x4*)(lds + a + 4096 * s));
          const s16x4 up = __builtin_amdgcn_ds_read_tr16_b64_v4i16((LAS s16x4*)(lds + a + 4096 * s + 2048));
          vf[s] = __builtin_bit_cast(bf16x8, __builtin_shufflevector(lo, up, 0, 1, 2, 3, 4, 5, 6, 7));
        }
        ot = __builtin_amdgcn_mfma_f32_32x32x16_bf16(vf[0], p0, ot, 0, 0, 0);
        ot = __builtin_amdgcn_mfma_f32_32x32x16_bf16(vf[1], p1, ot, 0, 0, 0);
      };
      if (NT >= 2 * NC) {
#define PIN_OF() asm volatile("" : "+v"(of), "+v"(ot))
#pragma unroll
        for (int r = 0; r < 16; ++r) of[r] *= co[0];
        const bool wl = (l4 & 24) == 0;
        const unsigned ua = 4 * (2 * NT - NC);
        wait_above_v(wl ? aland : lbase + LDS_CTL + CTL_ODONE + (l4 & 12), wl ? ua : 0u);
        mma_t(0, pl[0][0], pl[0][1]);
#pragma unroll
        for (int i = 1; i < 4; ++i) fma_o(LDS_WREG + (pos ^ i) * W_SIZE + (i - 1) * 4096 + l16, co[i]);
        PIN_OF();
        ld_p(2); ld_p(3); ld_p(4); ld_p(5);
        wait_above_v(wl ? aland : lbase + LDS_CTL + CTL_ODONE + 16 + (l4 & 4), wl ? ua + 8 : 0u);
        mma_t(1, pl[1][0], pl[1][1]);
        mma_t(2, pl[2][0], pl[2][1]);
        fma_o(src_o(4), co[4]);
        fma_o(src_o(5), co[5]);
#pragma unroll
        for (int r = 0; r < 16; ++r) { ot[r] += of[r]; of[r] = 0.f; }
        asm volatile("" : "+v"(ot));
        wait_above<0>(aland, ua + 20);
        mma_t(3, pl[3][0], pl[3][1]);
        mma_t(4, pl[4][0], pl[4][1]);
        mma_t(5, pl[5][0], pl[5][1]);
        of = ot;
      } else {
        wait_above<1>(lbase + LDS_CTL + CTL_WTOK + min(l4, (NC - 1) * 4u), 0);
        const float cw = *(LAS float*)(lds + awt + pos * 128);
#pragma unroll
        for (int r = 0; r < 16; ++r) of[r] *= cw;
#pragma unroll 1
        for (int p = max(NC - NT, 0); p < NC; ++p) {
          if (p != pos && NT - 2 * NC + p >= 0) {
            wait_above<1>(lbase + LDS_CTL + CTL_ODONE + p * 4, 0);
            fma_o(src_o(p), *(LAS float*)(lds + awt + p * 128));
          }
          wait_above<1>(aland, 4 * (2 * NT - NC + p));
          mma_t(p, *(LAS bf16x8*)(lds + LDS_WREG + p * W_SIZE + W_LAST + l16), *(LAS bf16x8*)(lds + LDS_WREG + p * W_SIZE + W_LAST + l16 + 1024));
        }
#pragma unroll
        for (int r = 0; r < 16; ++r) of[r] += ot[r];
      }
      const __amdgpu_buffer_rsrc_t ro = __builtin_amdgcn_make_buffer_rsrc(outp, 0, NB * HQ * SQ * HD * 2, 0x00020000);
      const unsigned orow = (unsigned)(b * HQ + 2 * h) * SQ * HD * 2 + pos * 64 + ((l16 & 511) << 4) + ((l16 >> 5) & 16);
#pragma unroll
      for (int j = 0; j < 2; ++j) {
        const unsigned a0 = pack2(of[8 * j], of[8 * j + 1]), a1 = pack2(of[8 * j + 2], of[8 * j + 3]);
        const unsigned b0 = pack2(of[8 * j + 4], of[8 * j + 5]), b1 = pack2(of[8 * j + 6], of[8 * j + 7]);
        auto s0 = __builtin_amdgcn_permlane32_swap(a0, b0, false, false);
        auto s1 = __builtin_amdgcn_permlane32_swap(a1, b1, false, false);
        u32x4 val = {s0[0], s1[0], s0[1], s1[1]};
        __builtin_amdgcn_raw_buffer_store_b128(val, ro, orow + j * 32, 0, 0);
      }
    }
  }
}
}

extern "C" void kernel_launch(void* const* d_in, const int* in_sizes, int n_in, void* d_out, int out_size, void* d_ws,
                              size_t ws_size, hipStream_t stream) {
  (void)n_in; (void)out_size; (void)d_ws; (void)ws_size;
  const unsigned nphys = (unsigned)(in_sizes[1] / (PAGE * HKV * HD));
  const unsigned kvbytes = nphys * PAGE_BYTES;
  (void)hipFuncSetAttribute((const void*)paged_attn, hipFuncAttributeMaxDynamicSharedMemorySize, LDS_TOTAL);
  hipLaunchKernelGGL(paged_attn, dim3(256), dim3(512), LDS_TOTAL, stream, d_in[0], d_in[1], d_in[2], (const int*)d_in[3],
                     (const int*)d_in[4], d_out, kvbytes, nphys);
}
```

```cpp
#define PACE_X16 431
#define SOFT 30
#include <hip/hip_runtime.h>
#include <hip/hip_bf16.h>

#define GAP_TICKS 10
#define WIN_TICKS 82
#define WIN_C1 110
#define LATE_TICKS 100
#define HALT_SHIFT 240
#define HALT_FROM 400
#define CODEPF 11776
#define PF_LEAD 180
#define PF_X64 70
#define PF_PER 4
#define PF_PSTEP 2
#define PF_RUN 3

namespace {
constexpr int NB = 16, HQ = 32, HKV = 16, SQ = 16, HD = 128, PAGE = 16, NBPS = 256, SKV = NBPS * PAGE;
constexpr unsigned PAGE_BYTES = PAGE * HKV * HD * 2;
constexpr unsigned TLBG = 0x200000u;
constexpr int U0 = 4;
constexpr int NC = 6;
constexpr int KREG = 16, KLDS = 5;
static_assert(NC * (KREG + KLDS + 1) >= NBPS / 2, "P storage");
constexpr unsigned LDS_RING = 0;
constexpr unsigned LDS_WREG = 65536;
constexpr unsigned W_ALP = KLDS * 2048;
constexpr unsigned W_LAST = W_ALP + (KREG + KLDS + 1) * 128;
constexpr unsigned W_SIZE = W_LAST + 2048;
static_assert(W_LAST >= 3 * 4096 && W_SIZE % 256 == 0, "consumer region");
constexpr unsigned LDS_TBL = LDS_WREG + W_SIZE;
constexpr unsigned LDS_SRT = LDS_WREG + 3 * W_SIZE;
constexpr unsigned LDS_Q = LDS_WREG;
constexpr unsigned LDS_ML = LDS_WREG + NC * W_SIZE;
constexpr unsigned LDS_WT = LDS_ML + NC * 256;
constexpr unsigned LDS_CTL = LDS_WT + NC * 128;
constexpr unsigned CTL_LAND = 0;
constexpr unsigned CTL_QOK = 16;
constexpr unsigned CTL_SORT = 24;
constexpr unsigned CTL_FREE = 32;
constexpr unsigned CTL_MLOK = 64;
constexpr unsigned CTL_ODONE = 96;
constexpr unsigned CTL_WTOK = 128;
constexpr unsigned LDS_DUMMY = LDS_CTL + 256;
constexpr unsigned LDS_PLIST = LDS_DUMMY + 256;
constexpr unsigned LDS_TOTAL = LDS_PLIST + 1024;
static_assert(LDS_TOTAL <= 163840, "lds");

typedef __attribute__((ext_vector_type(8))) __bf16 bf16x8;
typedef __attribute__((ext_vector_type(2))) __bf16 bf16x2;
typedef __attribute__((ext_vector_type(4))) short s16x4;
typedef __attribute__((ext_vector_type(16))) float f32x16;
typedef __attribute__((ext_vector_type(4))) float f32x4;
typedef __attribute__((ext_vector_type(2))) float f32x2;
typedef __attribute__((ext_vector_type(4))) unsigned u32x4;
typedef __attribute__((ext_vector_type(2))) unsigned u32x2;
#define LAS __attribute__((address_space(3)))

__device__ __forceinline__ float swap_max(float x) {
  auto r = __builtin_amdgcn_permlane32_swap(__float_as_uint(x), __float_as_uint(x), false, false);
  return fmaxf(__uint_as_float(r[0]), __uint_as_float(r[1]));
}
__device__ __forceinline__ float swap_sum(float x) {
  auto r = __builtin_amdgcn_permlane32_swap(__float_as_uint(x), __float_as_uint(x), false, false);
  return __uint_as_float(r[0]) + __uint_as_float(r[1]);
}
__device__ __forceinline__ unsigned pack2(float a, float b) {
  bf16x2 v = {(__bf16)a, (__bf16)b};
  return __builtin_bit_cast(unsigned, v);
}
template <int CTRL> __device__ __forceinline__ int dpp_add(int x) { return x + __builtin_amdgcn_update_dpp(0, x, CTRL, 0xf, 0xf, true); }
__device__ __forceinline__ int to_sgpr(int v) {
  int r;
  asm volatile("s_nop 1\n\tv_readfirstlane_b32 %0, %1\n\ts_nop 1" : "=s"(r) : "v"(v));
  return r;
}
#define LGKM0() asm volatile("s_waitcnt lgkmcnt(0)" ::: "memory")
#define VMWAIT(n) asm volatile("s_waitcnt vmcnt(" #n ")" ::: "memory")
#define CBAR() asm volatile("" ::: "memory")
template <int SLEEP> __device__ __forceinline__ void wait_above(unsigned addr, unsigned u) {
  unsigned v, cnt;
  if (SLEEP)
    asm volatile("s_mov_b32 %1, 0x800000\n\ts_branch 2f\n"
                 "1:\n\ts_sleep %4\n"
                 "2:\n\tds_read_b32 %0, %2\n\ts_sub_u32 %1, %1, 1\n\ts_waitcnt lgkmcnt(0)\n\t"
                 "v_cmp_ge_u32 vcc, %3, %0\n\ts_cbranch_scc1 3f\n\ts_nop 1\n\ts_cbranch_vccnz 1b\n3:"
                 : "=&v"(v), "=&s"(cnt) : "v"(addr), "s"(u), "n"(SLEEP) : "memory", "scc", "vcc");
  else
    asm volatile("s_mov_b32 %1, 0x800000\n"
                 "1:\n\tds_read_b32 %0, %2\n\ts_sub_u32 %1, %1, 1\n\ts_waitcnt lgkmcnt(0)\n\t"
                 "v_cmp_ge_u32 vcc, %3, %0\n\ts_cbranch_scc1 3f\n\ts_nop 1\n\ts_cbranch_vccnz 1b\n3:"
                 : "=&v"(v), "=&s"(cnt) : "v"(addr), "s"(u) : "memory", "scc", "vcc");
}

__device__ __forceinline__ void wait_above_v(unsigned addr, unsigned u) {
  unsigned v, cnt;
  asm volatile("s_mov_b32 %1, 0x800000\n"
               "1:\n\tds_read_b32 %0, %2\n\ts_sub_u32 %1, %1, 1\n\ts_waitcnt lgkmcnt(0)\n\t"
               "v_cmp_ge_u32 vcc, %3, %0\n\ts_cbranch_scc1 3f\n\ts_nop 1\n\ts_cbranch_vccnz 1b\n3:"
               : "=&v"(v), "=&s"(cnt) : "v"(addr), "v"(u) : "memory", "scc", "vcc");
}

__global__ void __launch_bounds__(512, 1) __attribute__((amdgpu_num_vgpr(64)))
paged_attn(const void* __restrict__ qp, const void* __restrict__ kp, const void* __restrict__ vp, const int* __restrict__ bt,
           const int* __restrict__ lens, void* __restrict__ outp, unsigned kvbytes, unsigned nphys) {
  extern __shared__ __attribute__((aligned(16))) char smem[];
  LAS char* lds = (LAS char*)smem;
  const int lane0 = threadIdx.x & 63;
  const int wv = __builtin_amdgcn_readfirstlane(threadIdx.x >> 6);
  const int g = blockIdx.x;
  const int h = g >> 4;
  constexpr float CS = 0.08838834764831845f * 1.4426950408889634f;
  volatile LAS unsigned* ctl = (volatile LAS unsigned*)(lds + LDS_CTL);

  const unsigned lbase = (unsigned)(size_t)lds;

  unsigned lraw, first;
  u32x4 lq;
  {
    asm volatile("global_load_dword %0, %3, %4\n\t"
                 "global_load_dwordx4 %1, %5, %4\n\t"
                 "global_load_dword %2, %6, %7"
                 : "=&v"(lraw), "=&v"(lq), "=&v"(first)
                 : "v"((lane0 & 15) * 4), "s"(lens), "v"(lane0 & 48), "v"((lane0 & 15) * (wv < 2 ? NBPS * 4 : 0) + (lane0 >> 4) * 8 + (wv & 1) * 4), "s"(bt),
                   "s"(qp), "s"(kp), "s"(vp), "s"(outp), "s"(kvbytes), "s"(nphys)
                 : "memory");
    __builtin_amdgcn_sched_barrier(0);
  }
  if (wv >= 4) {
    if (wv == 7) {
      const __amdgpu_buffer_rsrc_t rb = __builtin_amdgcn_make_buffer_rsrc((void*)bt, 0, NB * NBPS * 4, 0x00020000);
#define TBL4(SO) "buffer_load_dwordx4 %2, %1, " SO " offen lds\n\tbuffer_load_dwordx4 %2, %1, " SO " offen offset:1024 lds\n\t" \
                 "buffer_load_dwordx4 %2, %1, " SO " offen offset:2048 lds\n\tbuffer_load_dwordx4 %2, %1, " SO " offen offset:3072 lds\n\t"
      asm volatile("s_mov_b32 m0, %0\n\ts_nop 0\n\t" TBL4("0") "s_add_u32 m0, m0, 4096\n\ts_nop 0\n\t" TBL4("%3")
                   "s_add_u32 m0, m0, %6\n\ts_nop 0\n\t" TBL4("%4") "s_add_u32 m0, m0, 4096\n\ts_nop 0\n\t" TBL4("%5")
                   :: "s"(lbase + LDS_TBL), "s"(rb), "v"(lane0 * 16), "s"(4096), "s"(8192), "s"(12288), "n"(W_SIZE - 4096) : "memory", "scc");
    }
    unsigned long long pc;
    asm volatile("s_getpc_b64 %0" : "=s"(pc));
    const __amdgpu_buffer_rsrc_t rt = __builtin_amdgcn_make_buffer_rsrc(
        wv == 5 ? (void*)pc : wv == 4 ? (void*)vp : (void*)kp, 0, wv == 5 ? CODEPF : (wv != 7 && (g & 8)) ? (int)kvbytes : 0, 0x00020000);
    const unsigned mul = wv == 5 ? 512u : 16 * TLBG + 4224u, add = wv == 5 ? ((g >> 3) & 3) * 128 : (g >> 4) * (TLBG + 33024u);
    asm volatile("s_mov_b32 m0, %0\n\ts_nop 0\n\tbuffer_load_dword %1, %2, 0 offen lds"
                 :: "s"(lbase + LDS_DUMMY), "v"((lane0 & 31) * mul + add), "s"(rt) : "memory");
    *(LAS u32x4*)(lds + LDS_SRT + (threadIdx.x - 256) * 16) = u32x4{0u, 0u, 0u, 0u};
    if (threadIdx.x < 296) ctl[lane0] = ((lane0 >> 3) == CTL_FREE / 32) ? (lane0 & 7) : 0u;
    asm volatile("s_waitcnt lgkmcnt(0)\n\ts_barrier" ::: "memory");
  }
  if (wv < 2) __builtin_amdgcn_s_setprio(3);
  const unsigned rstride = wv < 2 ? 4096 : 256;
  unsigned vo0 = (lane0 >> 4) * rstride + (((unsigned)(lane0 & 15) ^ ((lane0 >> 4) << 2)) << 4) + (wv < 2 ? h * (HD * 2) : 0);
  unsigned vo1 = (vo0 + 4 * rstride - 1024) ^ 16, vo2 = (vo0 + 8 * rstride - 2048) ^ 32, vo3 = (vo0 + 12 * rstride - 3072) ^ 48;
  const unsigned want = (g & 8) ? 15 - (g & 7) : (g & 7);
  const unsigned dst0 = lbase + (wv < 2 ? LDS_RING : LDS_Q) + (wv & 1) * 4096;
  asm volatile("s_cmp_ge_u32 %8, 4\n\ts_cbranch_scc1 1f\n\ts_waitcnt vmcnt(0)\n1:\n\ts_waitcnt vmcnt(1)" : "+v"(lraw), "+v"(lq), "+v"(first), "+v"(vo0), "+v"(vo1), "+v"(vo2), "+v"(vo3) : "s"(dst0), "s"(wv) : "memory", "scc");
  unsigned lm1 = min(lraw - 1u, (unsigned)SKV - 1u);
  unsigned rank = 0;
  {
    const unsigned key = lm1 * 16 + (lane0 & 15);
#pragma unroll
    for (int j = 0; j < 4; ++j) rank += (min(lq[j] - 1u, (unsigned)SKV - 1u) * 16 + ((lane0 >> 4) * 4 + j) > key) ? 1u : 0u;
    auto r1 = __builtin_amdgcn_permlane16_swap(rank, rank, false, false);
    rank = r1[0] + r1[1];
    auto r2 = __builtin_amdgcn_permlane32_swap(rank, rank, false, false);
    rank = r2[0] + r2[1];
  }
  first = min(first, nphys - 1) * PAGE_BYTES;
  const int b = __builtin_ctz((unsigned)__builtin_amdgcn_ballot_w64(rank == want) | 0x8000u);
  unsigned long long tstart = 0;
  if (wv < 4) {
    const bool qok = (int)__builtin_amdgcn_readlane(lraw, b) > 0;
    const __amdgpu_buffer_rsrc_t r0 = __builtin_amdgcn_make_buffer_rsrc(wv < 2 ? (void*)kp : (void*)qp, 0,
                                                                        wv < 2 ? (int)kvbytes : qok ? NB * HQ * SQ * HD * 2 : 0, 0x00020000);
    const unsigned so = wv < 2 ? __builtin_amdgcn_readlane(first, b) : qok ? (unsigned)((b * HQ + 2 * h) * SQ * HD * 2) + (wv & 1) * 4096 : 0u;
    asm volatile("s_waitcnt vmcnt(0)\n\t"
                 "s_mov_b32 m0, %2\n\t"
                 "s_memrealtime %0\n\t"
                 "s_nop 2\n\t"
                 "buffer_load_dwordx4 %4, %1, %3 offen nt lds\n\t"
                 "buffer_load_dwordx4 %5, %1, %3 offen offset:1024 nt lds\n\t"
                 "buffer_load_dwordx4 %6, %1, %3 offen offset:2048 nt lds\n\t"
                 "buffer_load_dwordx4 %7, %1, %3 offen offset:3072 nt lds\n\t"
                 "s_waitcnt lgkmcnt(0)\n\t"
                 "s_barrier"
                 : "=&s"(tstart) : "s"(r0), "s"(dst0), "s"(so), "v"(vo0), "v"(vo1), "v"(vo2), "v"(vo3) : "memory");
  }
  asm volatile("" : "+v"(lm1), "+v"(rank), "+v"(first) :: "memory");
  __builtin_amdgcn_sched_barrier(0);
  const int bp = __builtin_ctz((unsigned)__builtin_amdgcn_ballot_w64(rank == 15 - want) | 0x8000u);
  const int len = __builtin_amdgcn_readlane(lm1, b) + 1, nown = (len + PAGE - 1) / PAGE, NT = (nown + 1) >> 1;
  const int npl = (__builtin_amdgcn_readlane(lm1, bp) >> 4) + 1, NTL = (npl + 1) >> 1;
  auto total = [&]() __attribute__((always_inline)) {
    int t = lm1 + 1;
    t = dpp_add<0x121>(t); t = dpp_add<0x122>(t); t = dpp_add<0x124>(t); t = dpp_add<0x128>(t);
    return t;
  };

  auto sort_pages = [&](int lane, int bb, int n, unsigned scr, unsigned dst) __attribute__((always_inline)) {
    u32x4 e = *(LAS u32x4*)(lds + LDS_TBL + (bb >> 3) * W_SIZE + (bb & 7) * 1024 + lane * 16);
    bool in[4];
#pragma unroll
    for (int j = 0; j < 4; ++j) {
      e[j] = min(e[j], nphys - 1);
      in[j] = 4 * lane + j >= 2 * U0 && 4 * lane + j < n - 1;
      __hip_atomic_fetch_or((LAS unsigned*)(lds + scr + ((e[j] >> 5) & 255) * 8), in[j] ? 1u << (e[j] & 31) : 0u, __ATOMIC_RELAXED, __HIP_MEMORY_SCOPE_WAVEFRONT);
    }
    CBAR();
    u32x4 a = *(LAS u32x4*)(lds + scr + lane * 32), c = *(LAS u32x4*)(lds + scr + lane * 32 + 16);
    const int n0 = __builtin_popcount(a[0]), n1 = n0 + __builtin_popcount(a[2]), n2 = n1 + __builtin_popcount(c[0]), n3 = n2 + __builtin_popcount(c[2]);
    int sc = n3;
    sc = dpp_add<0x111>(sc); sc = dpp_add<0x112>(sc); sc = dpp_add<0x114>(sc); sc = dpp_add<0x118>(sc);
    sc += __builtin_amdgcn_update_dpp(0, sc, 0x142, 0xa, 0xf, false);
    sc += __builtin_amdgcn_update_dpp(0, sc, 0x143, 0xc, 0xf, false);
    const bool ok = nphys <= 8192u && __builtin_amdgcn_readlane(sc, 63) == n - 1 - 2 * U0;
    sc += 2 * U0 - n3;
    a[1] = sc; a[3] = sc + n0; c[1] = sc + n1; c[3] = sc + n2;
    *(LAS u32x4*)(lds + scr + lane * 32) = a;
    *(LAS u32x4*)(lds + scr + lane * 32 + 16) = c;
    CBAR();
    u32x2 wp[4];
#pragma unroll
    for (int j = 0; j < 4; ++j) wp[j] = *(LAS u32x2*)(lds + scr + ((e[j] >> 5) & 255) * 8);
#pragma unroll
    for (int j = 0; j < 4; ++j) {
      const unsigned r = wp[j][1] + __builtin_popcount(wp[j][0] & ((1u << (e[j] & 31)) - 1u));
      *(LAS unsigned*)(lds + dst + ((ok && in[j]) ? r : 4 * lane + j) * 4) = e[j] * PAGE_BYTES;
    }
    CBAR();
  };

  if (__builtin_expect(wv < 2, 1)) {
    const int half = wv, lane = lane0;
    const unsigned vland = lbase + LDS_CTL + CTL_LAND + half * 4;
    const unsigned vfree = lbase + LDS_CTL + CTL_FREE + (lane & 7) * 4;
    const unsigned ring = dst0;
    unsigned l0 = __builtin_amdgcn_ds_bpermute((b + 16 * lane) * 4, first), l1 = 0;
    const unsigned a0 = lbase + LDS_SRT + 4096 + min(2 * lane + half, nown - 1) * 4, a1 = lbase + LDS_SRT + 4096 + min(2 * lane + half + 128, nown - 1) * 4;
    const int rot = (NT >= 3 * NC) ? NT % NC : 0;
    const unsigned vperm = (lane < NC ? (lane + rot >= NC ? lane + rot - NC : lane + rot) : lane) * 4;
    const int T16 = total() * PACE_X16;
    const int step = to_sgpr((int)((float)T16 * __builtin_amdgcn_rcpf((float)max(len, 32))));
    const int win = to_sgpr((int)((float)(((h & 3) == 1 ? WIN_C1 : WIN_TICKS) * 1024) * __builtin_amdgcn_rcpf((float)step) + 0.5f));
    const int hmin = to_sgpr((int)((float)(HALT_FROM * 256) * __builtin_amdgcn_rcpf((float)step)));
    const unsigned due0 = ((unsigned)tstart << 8) + half * (step >> 1);
    const int lrows = len - (2 * (NT - 1) + half) * PAGE;
    unsigned vf, vt, lc;
    asm volatile(
        "s_waitcnt lgkmcnt(0)\n\t"
        "s_memrealtime s[68:69]\n\t"
        "ds_read_b32 %[vf], %[vfree]\n\t"
        "s_mov_b32 s60, 1\n\t"
        "s_mov_b32 s61, 0\n\t"
        "s_mov_b32 s62, 0x1000000\n\t"
        "s_mov_b32 s66, 0\n\t"
        "s_min_u32 s67, %[nt], %[u0]\n\t"
        "s_mov_b32 s75, %[nrec]\n\t"
        "s_mov_b32 s72, 0x2000\n\t"
        "s_mov_b32 s73, 1\n\t"
        "s_mov_b32 s74, %[due1]\n\t"
        "s_mov_b32 s85, %[duev]\n\t"
        "s_mov_b32 s86, %[late]\n\t"
        "s_mov_b64 s[76:77], %[kp]\n\t"
        "s_mov_b32 s78, %[nrec]\n\t"
        "s_mov_b32 s79, 0x20000\n\t"
        "s_and_b32 s77, s77, 0xffff\n\t"
        "v_mov_b32 %[lc], %[l0]\n\t"
        "s_getpc_b64 s[70:71]\n"
        ".LldA%=:\n\t"
        "s_add_u32 s80, s70, .LldL%=-.LldA%=\n\t"
        "s_addc_u32 s71, s71, 0\n\t"
        "s_branch .LldF%=\n"
        ".LldX%=:\n\t"
        "s_cmp_lt_i32 s64, 0\n\t"
        "s_cbranch_scc0 .LldH%=\n"
        ".LldR%=:\n\t"
        "s_sub_u32 s62, s62, 1\n\t"
        "s_cbranch_scc1 .LldE%=\n"
        ".LldT%=:\n\t"
        "s_waitcnt lgkmcnt(0)\n\t"
        "s_getreg_b32 s64, hwreg(HW_REG_IB_STS)\n\t"
        "s_and_b32 s65, s64, 15\n\t"
        "s_bfe_u32 s64, s64, 0x20016\n\t"
        "s_lshl4_add_u32 s81, s64, s65\n\t"
        "s_cmp_gt_i32 s81, s61\n\t"
        "s_cbranch_scc1 .LldC%=\n\t"
        "s_add_u32 s64, s81, 3\n\t"
        "s_and_b32 s64, s64, 28\n\t"
        "s_add_u32 s70, s80, s64\n\t"
        "s_add_u32 s66, s66, s61\n\t"
        "s_sub_u32 s61, s64, 4\n\t"
        "s_sub_u32 s66, s66, s61\n\t"
        "v_mov_b32 %[vt], s66\n\t"
        "s_setpc_b64 s[70:71]\n\t"
        ".p2align 5\n"
        ".LldL%=:\n\t"
        "s_waitcnt vmcnt(0)\n\t"
        "s_waitcnt vmcnt(4)\n\t"
        "s_waitcnt vmcnt(8)\n\t"
        "s_waitcnt vmcnt(12)\n\t"
        "s_waitcnt vmcnt(16)\n\t"
        "s_waitcnt vmcnt(20)\n\t"
        "s_waitcnt vmcnt(24)\n\t"
        "s_waitcnt vmcnt(28)\n\t"
        "ds_write_b32 %[vland], %[vt]\n"
        ".LldC%=:\n\t"
        "v_cmp_eq_u32 vcc, s60, %[vf]\n\t"
        "s_lshl_b32 s64, s68, 8\n\t"
        "s_memrealtime s[68:69]\n\t"
        "ds_read_b32 %[vf], %[vfree]\n\t"
        "s_sub_u32 s64, s64, s74\n\t"
        "s_cmp_gt_u32 s64, s86\n\t"
        "s_cbranch_scc1 .LldX%=\n"
        ".LldI%=:\n\t"
        "s_cbranch_vccz .LldR%=\n\t"
        "s_cmp_gt_u32 s81, %[win]\n\t"
        "s_cbranch_scc1 .LldR%=\n\t"
        "buffer_load_dwordx4 %[o0], s[76:79], s63 offen nt lds\n\t"
        "s_add_u32 s74, s74, %[step]\n\t"
        "s_add_u32 s73, s73, 1\n\t"
        "buffer_load_dwordx4 %[o1], s[76:79], s63 offen offset:1024 nt lds\n\t"
        "s_add_u32 s60, s60, 1\n\t"
        "s_add_u32 s61, s61, 4\n\t"
        "s_add_u32 s86, s64, %[late]\n\t"
        "buffer_load_dwordx4 %[o2], s[76:79], s63 offen offset:2048 nt lds\n\t"
        "s_add_u32 s72, s72, 0x2000\n\t"
        "s_and_b32 s72, s72, 0xe000\n\t"
        "buffer_load_dwordx4 %[o3], s[76:79], s63 offen offset:3072 nt lds\n"
        ".LldF%=:\n\t"
        "v_readlane_b32 s63, %[lc], s73\n\t"
        "s_add_u32 m0, s72, %[ring]\n\t"
        "s_cmp_lg_u32 s60, s67\n\t"
        "s_cbranch_scc1 .LldT%=\n\t"
        "s_cmp_eq_u32 s73, %[nt]\n\t"
        "s_cbranch_scc1 .LldV%=\n\t"
        "s_cmp_lg_u32 s60, %[u0]\n\t"
        "s_cbranch_scc1 .LldG%=\n\t"
        "s_cmp_le_u32 %[nt], %[u0]\n\t"
        "s_cbranch_scc1 .LldG%=\n"
        ".LldS%=:\n\t"
        "ds_read_b32 %[lc], %[vland] offset:%[srt]\n\t"
        "ds_read_b32 %[l0], %[a0]\n\t"
        "ds_read_b32 %[l1], %[a1]\n\t"
        "s_sub_u32 s62, s62, 1\n\t"
        "s_cbranch_scc1 .LldE%=\n\t"
        "s_waitcnt lgkmcnt(0)\n\t"
        "v_readfirstlane_b32 s64, %[lc]\n\t"
        "s_cmp_eq_u32 s64, 0\n\t"
        "s_cbranch_scc1 .LldS%=\n\t"
        "s_max_i32 s65, %[lrows], 0\n\t"
        "s_lshl_b32 s65, s65, 12\n\t"
        "s_add_u32 s64, s64, s65\n\t"
        "s_sub_u32 s64, s64, 1\n\t"
        "s_cmp_lt_i32 %[lrows], 16\n\t"
        "s_cselect_b32 s75, s64, %[nrec]\n\t"
        "v_mov_b32 %[lc], %[l0]\n"
        ".LldG%=:\n\t"
        "s_cmp_lg_u32 s73, 64\n\t"
        "s_cbranch_scc1 .LldN%=\n\t"
        "v_mov_b32 %[lc], %[l1]\n"
        ".LldN%=:\n\t"
        "s_cmp_eq_u32 s73, %[ntm]\n\t"
        "s_cselect_b32 s78, s75, %[nrec]\n\t"
        "s_cselect_b32 s64, %[nt], %[ntm]\n\t"
        "s_min_u32 s65, s64, 64\n\t"
        "s_cmp_lt_u32 s73, 64\n\t"
        "s_cselect_b32 s64, s65, s64\n\t"
        "s_sub_u32 s67, s60, s73\n\t"
        "s_add_u32 s67, s67, s64\n\t"
        "s_branch .LldF%=\n"
        ".LldV%=:\n\t"
        "s_cmp_lg_u32 s60, %[nt]\n\t"
        "s_cbranch_scc1 .LldD%=\n\t"
        "s_mov_b64 s[76:77], %[vp]\n\t"
        "s_and_b32 s77, s77, 0xffff\n\t"
        "s_mov_b32 s73, 0\n\t"
        "s_mov_b32 s74, s85\n\t"
        "ds_bpermute_b32 %[lc], %[vperm], %[l0]\n\t"
        "v_xor_b32 %[o1], 16, %[o1]\n\t"
        "v_xor_b32 %[o2], 32, %[o2]\n\t"
        "v_xor_b32 %[o3], 48, %[o3]\n\t"
        "s_waitcnt lgkmcnt(0)\n\t"
        "s_branch .LldN%=\n"
        ".LldH%=:\n\t"
        "s_cmp_eq_u32 s66, %[fin]\n\t"
        "s_cbranch_scc1 .LldE%=\n\t"
        "s_cmp_lt_i32 s60, %[hmin]\n\t"
        "s_cbranch_scc1 .LldI%=\n\t"
        "s_add_u32 s74, s74, %[hshift]\n\t"
        "s_add_u32 s85, s85, %[hshift]\n\t"
        "s_branch .LldR%=\n"
        ".LldD%=:\n\t"
        "s_sub_u32 s74, s74, 0x10000000\n\t"
        "s_mov_b32 s86, 0\n\t"
        "s_mov_b32 s60, -1\n\t"
        "s_branch .LldT%=\n"
        ".LldE%=:\n\t"
        "s_waitcnt vmcnt(0) lgkmcnt(0)\n\t"
        : [vf] "=&v"(vf), [vt] "=&v"(vt), [lc] "=&v"(lc), [l0] "+&v"(l0), [l1] "+&v"(l1), [o1] "+&v"(vo1), [o2] "+&v"(vo2), [o3] "+&v"(vo3)
        : [vperm] "v"(vperm), [a0] "v"(a0), [a1] "v"(a1), [o0] "v"(vo0), [vland] "v"(vland), [vfree] "v"(vfree),
          [kp] "s"(kp), [vp] "s"(vp), [nrec] "s"(kvbytes), [nt] "s"(NT), [ring] "s"(ring),
          [step] "s"(step), [due1] "s"(due0 + step + SOFT * 256), [duev] "s"(due0 + (to_sgpr(T16) >> 5) + (SOFT + GAP_TICKS) * 256), [lrows] "s"(lrows), [ntm] "s"(NT - 1), [fin] "s"(8 * NT), [u0] "n"(U0), [win] "s"(win), [late] "n"(LATE_TICKS * 256), [hshift] "n"(HALT_SHIFT * 256), [hmin] "s"(hmin), [srt] "n"(CTL_SORT - CTL_LAND)
        : "memory", "scc", "vcc", "s60", "s61", "s62", "s63", "s64", "s65", "s66", "s67", "s68", "s69", "s70", "s71", "s72", "s73", "s74", "s75",
          "s76", "s77", "s78", "s79", "s80", "s81", "s85", "s86");
    asm volatile("s_endpgm" ::: "memory");
  }

  int lane = lane0;
  asm volatile("" : "+v"(lane));
  if (wv == 7) {
#pragma unroll 1
    for (int i = 0; i < 2; ++i) {
      sort_pages(lane, i ? bp : b, i ? npl : nown, LDS_SRT + i * 2048, i ? LDS_PLIST : LDS_SRT + 4096);
      if (i == 0) {
        const unsigned lo = *(LAS unsigned*)(lds + LDS_SRT + 4096 + (nown - 1) * 4) + 1;
        LGKM0();
        ctl[CTL_SORT / 4 + (lane & 1)] = lo;
      }
    }
  }
  if (wv < 4) {
    VMWAIT(0);
    ctl[CTL_QOK / 4 + (wv & 1)] = 1;
  }
  const int tot = total();
  const int w = wv - 2;
  const int r32 = lane & 31, hi = lane >> 5;
  const int nmine = (NT > w) ? (NT - w + NC - 1) / NC : 0;
  const unsigned aland = lbase + LDS_CTL + CTL_LAND + (lane & 1) * 4;
  const int tots = __builtin_amdgcn_readfirstlane(tot);
  const bool pf_on = npl * 256 * 64 > tots * PF_X64 && npl > nown && (h & 3) == 0;
  const unsigned pf_last = 2 * NT - to_sgpr((int)((float)(2 * NT * (PF_LEAD + 23 * NC)) * 4096.0f * __builtin_amdgcn_rcpf((float)(tot * PACE_X16))));
  const unsigned pf_ratio = to_sgpr((int)((float)NTL * 65536.0f * __builtin_amdgcn_rcpf((float)NT)));
  const unsigned pf_per = PF_PER + (g & 7) * PF_PSTEP, pf_inv = (65536 + pf_per - 1) / pf_per;
  auto prefetch = [&](unsigned op) {
    const unsigned d = pf_last - op;
    if (d >= 1024u) return;
    const unsigned r = d - ((d * pf_inv) >> 16) * pf_per;
    if (r >= PF_RUN) return;
    unsigned tl = 2 * NTL - 1 - NC - (((d - r) * pf_ratio) >> 16) - r;
    if (tl >= 2u * NTL) return;
    const bool isv = tl >= (unsigned)NTL;
    tl -= isv ? NTL : 0;
    const __amdgpu_buffer_rsrc_t rp = __builtin_amdgcn_make_buffer_rsrc(isv ? (void*)vp : (void*)kp, 0, (int)kvbytes, 0x00020000);
    unsigned ln = lane;
    asm volatile("" : "+v"(ln));
    const unsigned vo = (*(LAS unsigned*)(lds + LDS_PLIST + min(2 * tl + (ln >> 5), (unsigned)npl - 1) * 4) & 0x7fff0000u) + (h + 1) * (HD * 2) +
                        ((ln >> 1) & 15) * 4096 + (ln & 1) * 128;
    asm volatile("s_mov_b32 m0, %0\n\ts_nop 4\n\tbuffer_load_dword %2, %1, 0 offen lds" :: "s"(lbase + LDS_DUMMY), "s"(rp), "v"(vo) : "memory");
  };
  const int pos = (w + NC * 22 - NT) % NC;
  const unsigned palp = LDS_WREG + pos * W_SIZE + W_ALP + r32 * 4;
  const unsigned ppst = LDS_WREG + pos * W_SIZE + lane * 16;
  asm volatile("" ::: "v128", "v255");
  unsigned resc = 0;
  float m = -3.0e38f, mprev = -3.0e38f, l = 0.f;

  {
    const unsigned fk = (((unsigned)r32 & 3) << 2) | (((unsigned)r32 >> 2) & 3);
    const unsigned kaddr0 = LDS_RING + r32 * 256 + (((unsigned)hi ^ fk) << 4);
    bf16x8 qf[8];
    wait_above<1>(aland + (CTL_QOK - CTL_LAND), 0);
#pragma unroll
    for (int s = 0; s < 8; ++s) qf[s] = *(LAS bf16x8*)(lds + ((kaddr0 + LDS_Q - LDS_RING) ^ (s << 5)));
#pragma unroll 1
    for (int k = 0; k < nmine; ++k) {
      const unsigned t = w + NC * k;
      wait_above<1>(aland, 4 * t);
      bf16x8 kf[8];
#pragma unroll
      for (int s = 0; s < 8; ++s) kf[s] = *(LAS bf16x8*)(lds + ((kaddr0 + ((t & 7) << 13)) ^ (s << 5)));
      asm volatile("s_waitcnt lgkmcnt(0)" : "+v"(kf[0]), "+v"(kf[1]), "+v"(kf[2]), "+v"(kf[3]), "+v"(kf[4]), "+v"(kf[5]), "+v"(kf[6]), "+v"(kf[7]) :: "memory");
      ctl[CTL_FREE / 4 + (t & 7)] = t + 8;
      if (pf_on) prefetch(t);
      f32x16 sc;
#pragma unroll
      for (int r = 0; r < 16; ++r) sc[r] = 0.f;
#pragma unroll
      for (int s = 0; s < 8; ++s) sc = __builtin_amdgcn_mfma_f32_32x32x16_bf16(kf[s], qf[s], sc, 0, 0, 0);
      if ((int)t == NT - 1) {
        const int kb = 2 * t * PAGE + 4 * hi;
#pragma unroll
        for (int r = 0; r < 16; ++r) sc[r] = (kb + 8 * (r >> 2) + (r & 3) < len) ? sc[r] : -__builtin_inff();
      }
      float mx = sc[0];
#pragma unroll
      for (int r = 1; r < 16; ++r) mx = fmaxf(mx, sc[r]);
      mx = swap_max(mx);
      const float mn = fmaxf(m, mx);
      const float alpha = __builtin_amdgcn_exp2f((m - mn) * CS);
      mprev = m;
      m = mn;
      float ps = 0.f;
#pragma unroll
      for (int r = 0; r < 16; ++r) { sc[r] = __builtin_amdgcn_exp2f((sc[r] - mn) * CS); ps += sc[r]; }
      l = fmaf(l, alpha, ps);
      u32x4 pf[2];
#pragma unroll
      for (int s = 0; s < 2; ++s)
        pf[s] = u32x4{pack2(sc[8 * s], sc[8 * s + 1]), pack2(sc[8 * s + 2], sc[8 * s + 3]), pack2(sc[8 * s + 4], sc[8 * s + 5]), pack2(sc[8 * s + 6], sc[8 * s + 7])};
      if (__builtin_amdgcn_ballot_w64(alpha != 1.0f)) resc |= 1u << k;
      *(LAS float*)(lds + palp + k * 128) = alpha;
      if (k >= KREG || k == nmine - 1) {
        const unsigned dst = ppst + ((k == nmine - 1) ? W_LAST : (k - KREG) * 2048);
        *(LAS u32x4*)(lds + dst) = pf[0];
        *(LAS u32x4*)(lds + dst + 1024) = pf[1];
      } else {
        asm volatile("s_set_gpr_idx_on %8, gpr_idx(DST)\n\t"
                     "v_mov_b32 v128, %0\n\tv_mov_b32 v129, %1\n\tv_mov_b32 v130, %2\n\tv_mov_b32 v131, %3\n\t"
                     "v_mov_b32 v132, %4\n\tv_mov_b32 v133, %5\n\tv_mov_b32 v134, %6\n\tv_mov_b32 v135, %7\n\t"
                     "s_set_gpr_idx_off"
                     :: "v"(pf[0][0]), "v"(pf[0][1]), "v"(pf[0][2]), "v"(pf[0][3]), "v"(pf[1][0]), "v"(pf[1][1]), "v"(pf[1][2]), "v"(pf[1][3]), "s"(8 * k));
      }
    }
  }
  l = swap_sum(l);
  *(LAS f32x2*)(lds + LDS_ML + w * 256 + r32 * 8) = f32x2{m, l};
  LGKM0();
  ctl[CTL_MLOK / 4 + w] = 1;

  const unsigned tq = (lane & 15) >> 2, tp = lane & 3, tblk = (lane >> 4) & 1;
  const unsigned vaddr0 = LDS_RING + (4 * hi + tq) * 256 + ((tq ^ (pos & 3)) << 6) + ((2 * tblk + (tp >> 1)) << 4) + ((tp & 1) << 3);
  f32x16 o[4];
#pragma unroll
  for (int i = 0; i < 4; ++i)
#pragma unroll
    for (int r = 0; r < 16; ++r) o[i][r] = 0.f;
  auto weights = [&]() {
    wait_above<1>(lbase + LDS_CTL + CTL_MLOK + min(lane, NC - 1) * 4, 0);
    unsigned a = LDS_ML + r32 * 8;
    asm volatile("" : "+v"(a) :: "memory");
    float M = -3.0e38f, L = 0.f;
    f32x2 ml[NC];
#pragma unroll
    for (int i = 0; i < NC; ++i) { ml[i] = *(LAS f32x2*)(lds + a + i * 256); M = fmaxf(M, ml[i][0]); }
#pragma unroll
    for (int i = 0; i < NC; ++i) L = fmaf(__builtin_amdgcn_exp2f((ml[i][0] - M) * CS), ml[i][1], L);
    const float inv = 1.0f / L;
    *(LAS float*)(lds + (a >> 1) + (LDS_WT - LDS_ML / 2) + pos * 128) = __builtin_amdgcn_exp2f((mprev - M) * CS) * inv;
    const float ct = __builtin_amdgcn_exp2f((m - M) * CS) * inv;
#pragma unroll
    for (int s = 0; s < 2; ++s) {
      u32x4 x = *(LAS u32x4*)(lds + ppst + W_LAST + s * 1024);
#pragma unroll
      for (int e = 0; e < 4; ++e) x[e] = pack2(__uint_as_float(x[e] << 16) * ct, __uint_as_float(x[e] & 0xffff0000u) * ct);
      *(LAS u32x4*)(lds + ppst + W_LAST + s * 1024) = x;
    }
    LGKM0();
    ctl[CTL_WTOK / 4 + w] = 1;
  };
  resc &= ~1u;
  if (nmine <= 3) weights();
#pragma unroll 1
  for (int k = 0; k < nmine - 1; ++k) {
    if (k == 2) weights();
    if ((resc >> k) & 1) {
      const float alpha = *(LAS float*)(lds + palp + k * 128);
#pragma unroll
      for (int i = 0; i < 4; ++i)
#pragma unroll
        for (int r = 0; r < 16; ++r) o[i][r] *= alpha;
    }
    const unsigned u = NT + ((k == 0 && NT >= 3 * NC) ? pos : w + NC * k);
    u32x4 pf[2];
    if (k >= KREG) {
      pf[0] = *(LAS u32x4*)(lds + ppst + (k - KREG) * 2048);
      pf[1] = *(LAS u32x4*)(lds + ppst + (k - KREG) * 2048 + 1024);
    } else {
      asm volatile("s_set_gpr_idx_on %8, gpr_idx(SRC0)\n\t"
                   "v_mov_b32 %0, v128\n\tv_mov_b32 %1, v129\n\tv_mov_b32 %2, v130\n\tv_mov_b32 %3, v131\n\t"
                   "v_mov_b32 %4, v132\n\tv_mov_b32 %5, v133\n\tv_mov_b32 %6, v134\n\tv_mov_b32 %7, v135\n\t"
                   "s_set_gpr_idx_off\n\ts_nop 1"
                   : "=v"(pf[0][0]), "=v"(pf[0][1]), "=v"(pf[0][2]), "=v"(pf[0][3]), "=v"(pf[1][0]), "=v"(pf[1][1]), "=v"(pf[1][2]), "=v"(pf[1][3])
                   : "s"(8 * k));
    }
    asm volatile("" : "+v"(pf[0]), "+v"(pf[1]), "+v"(o[0]), "+v"(o[1]), "+v"(o[2]), "+v"(o[3]));
    if (k == nmine - 2) wait_above<1>(lbase + LDS_CTL + CTL_WTOK + min(lane, NC - 1) * 4, 0);
    wait_above<1>(aland, 4 * u);
    bf16x8 vf[4][2];
#pragma unroll
    for (int i = 0; i < 4; ++i) {
      const unsigned a = (vaddr0 + ((u & 7) << 13)) ^ (i << 6);
#pragma unroll
      for (int s = 0; s < 2; ++s) {
        const s16x4 lo = __builtin_amdgcn_ds_read_tr16_b64_v4i16((LAS s16x4*)(lds + a + 4096 * s));
        const s16x4 up = __builtin_amdgcn_ds_read_tr16_b64_v4i16((LAS s16x4*)(lds + a + 4096 * s + 2048));
        vf[i][s] = __builtin_bit_cast(bf16x8, __builtin_shufflevector(lo, up, 0, 1, 2, 3, 4, 5, 6, 7));
      }
    }
    asm volatile("s_waitcnt lgkmcnt(0)" : "+v"(vf[0][0]), "+v"(vf[0][1]), "+v"(vf[1][0]), "+v"(vf[1][1]), "+v"(vf[2][0]), "+v"(vf[2][1]), "+v"(vf[3][0]), "+v"(vf[3][1]) :: "memory");
    ctl[CTL_FREE / 4 + (u & 7)] = u + 8;
    if (pf_on) prefetch(u);
#pragma unroll
    for (int s = 0; s < 2; ++s)
#pragma unroll
      for (int i = 0; i < 4; ++i) o[i] = __builtin_amdgcn_mfma_f32_32x32x16_bf16(vf[i][s], __builtin_bit_cast(bf16x8, pf[s]), o[i], 0, 0, 0);
  }

  {
    unsigned l16 = lane * 16;
    asm volatile("" : "+v"(l16));
    const unsigned l4 = l16 >> 2, awt = LDS_WT + (l4 & 127);
    auto put = [&](unsigned dst, const f32x16& x) {
#pragma unroll
      for (int j = 0; j < 4; ++j) *(LAS f32x4*)(lds + dst + j * 1024) = f32x4{x[4 * j], x[4 * j + 1], x[4 * j + 2], x[4 * j + 3]};
    };
    float co[6];
    bf16x8 pl[NC][2];
    auto ld_p = [&](int p) {
      pl[p][0] = *(LAS bf16x8*)(lds + LDS_WREG + p * W_SIZE + W_LAST + l16);
      pl[p][1] = *(LAS bf16x8*)(lds + LDS_WREG + p * W_SIZE + W_LAST + l16 + 1024);
    };
#pragma unroll
    for (int i = 0; i < 4; ++i) co[i] = *(LAS float*)(lds + awt + ((pos & 3) ^ i) * 128);
    co[4] = *(LAS float*)(lds + awt + 4 * 128);
    co[5] = *(LAS float*)(lds + awt + 5 * 128);
    ld_p(0); ld_p(1);
    asm volatile("" : "+v"(l16) :: "memory");
    if (nmine > 1) {
      if (pos > 3) put(LDS_RING + ((2 * NT - 2 * NC + pos) & 7) * 8192 + l16, o[0]);
#pragma unroll
      for (int i = 1; i < 4; ++i) put(LDS_WREG + pos * W_SIZE + (i - 1) * 4096 + l16, o[i]);
      LGKM0();
      ctl[CTL_ODONE / 4 + pos] = 1;
    }
    if (pos < 4) {
      f32x16 of = o[0], ot;
#pragma unroll
      for (int r = 0; r < 16; ++r) ot[r] = 0.f;
      auto src_o = [&](int p) -> unsigned {
        const int i = pos ^ (p & 3);
        return l16 + (i == 0 ? LDS_RING + ((2 * NT - 2 * NC + p) & 7) * 8192 : LDS_WREG + p * W_SIZE + (i - 1) * 4096);
      };
      auto fma_o = [&](unsigned src, float c) {
#pragma unroll
        for (int j = 0; j < 4; ++j) {
          const f32x4 x = *(LAS f32x4*)(lds + src + j * 1024);
#pragma unroll
          for (int e = 0; e < 4; ++e) of[4 * j + e] = fmaf(x[e], c, of[4 * j + e]);
        }
      };
      auto mma_t = [&](int p, bf16x8 p0, bf16x8 p1) {
        const unsigned a = vaddr0 + (((2 * NT - NC + p) & 7) << 13);
        bf16x8 vf[2];
#pragma unroll
        for (int s = 0; s < 2; ++s) {
          const s16x4 lo = __builtin_amdgcn_ds_read_tr16_b64_v4i16((LAS s16x4*)(lds + a + 4096 * s));
          const s16x4 up = __builtin_amdgcn_ds_read_tr16_b64_v4i16((LAS s16x4*)(lds + a + 4096 * s + 2048));
          vf[s] = __builtin_bit_cast(bf16x8, __builtin_shufflevector(lo, up, 0, 1, 2, 3, 4, 5, 6, 7));
        }
        ot = __builtin_amdgcn_mfma_f32_32x32x16_bf16(vf[0], p0, ot, 0, 0, 0);
        ot = __builtin_amdgcn_mfma_f32_32x32x16_bf16(vf[1], p1, ot, 0, 0, 0);
      };
      if (NT >= 2 * NC) {
#define PIN_OF() asm volatile("" : "+v"(of), "+v"(ot))
#pragma unroll
        for (int r = 0; r < 16; ++r) of[r] *= co[0];
        const bool wl = (l4 & 24) == 0;
        const unsigned ua = 4 * (2 * NT - NC);
        wait_above_v(wl ? aland : lbase + LDS_CTL + CTL_ODONE + (l4 & 12), wl ? ua : 0u);
        mma_t(0, pl[0][0], pl[0][1]);
#pragma unroll
        for (int i = 1; i < 4; ++i) fma_o(LDS_WREG + (pos ^ i) * W_SIZE + (i - 1) * 4096 + l16, co[i]);
        PIN_OF();
        ld_p(2); ld_p(3); ld_p(4); ld_p(5);
        wait_above_v(wl ? aland : lbase + LDS_CTL + CTL_ODONE + 16 + (l4 & 4), wl ? ua + 8 : 0u);
        mma_t(1, pl[1][0], pl[1][1]);
        mma_t(2, pl[2][0], pl[2][1]);
        fma_o(src_o(4), co[4]);
        fma_o(src_o(5), co[5]);
#pragma unroll
        for (int r = 0; r < 16; ++r) { ot[r] += of[r]; of[r] = 0.f; }
        asm volatile("" : "+v"(ot));
        wait_above<0>(aland, ua + 20);
        mma_t(3, pl[3][0], pl[3][1]);
        mma_t(4, pl[4][0], pl[4][1]);
        mma_t(5, pl[5][0], pl[5][1]);
        of = ot;
      } else {
        wait_above<1>(lbase + LDS_CTL + CTL_WTOK + min(l4, (NC - 1) * 4u), 0);
        const float cw = *(LAS float*)(lds + awt + pos * 128);
#pragma unroll
        for (int r = 0; r < 16; ++r) of[r] *= cw;
#pragma unroll 1
        for (int p = max(NC - NT, 0); p < NC; ++p) {
          if (p != pos && NT - 2 * NC + p >= 0) {
            wait_above<1>(lbase + LDS_CTL + CTL_ODONE + p * 4, 0);
            fma_o(src_o(p), *(LAS float*)(lds + awt + p * 128));
          }
          wait_above<1>(aland, 4 * (2 * NT - NC + p));
          mma_t(p, *(LAS bf16x8*)(lds + LDS_WREG + p * W_SIZE + W_LAST + l16), *(LAS bf16x8*)(lds + LDS_WREG + p * W_SIZE + W_LAST + l16 + 1024));
        }
#pragma unroll
        for (int r = 0; r < 16; ++r) of[r] += ot[r];
      }
      const __amdgpu_buffer_rsrc_t ro = __builtin_amdgcn_make_buffer_rsrc(outp, 0, NB * HQ * SQ * HD * 2, 0x00020000);
      const unsigned orow = (unsigned)(b * HQ + 2 * h) * SQ * HD * 2 + pos * 64 + ((l16 & 511) << 4) + ((l16 >> 5) & 16);
#pragma unroll
      for (int j = 0; j < 2; ++j) {
        const unsigned a0 = pack2(of[8 * j], of[8 * j + 1]), a1 = pack2(of[8 * j + 2], of[8 * j + 3]);
        const unsigned b0 = pack2(of[8 * j + 4], of[8 * j + 5]), b1 = pack2(of[8 * j + 6], of[8 * j + 7]);
        auto s0 = __builtin_amdgcn_permlane32_swap(a0, b0, false, false);
        auto s1 = __builtin_amdgcn_permlane32_swap(a1, b1, false, false);
        u32x4 val = {s0[0], s1[0], s0[1], s1[1]};
        __builtin_amdgcn_raw_buffer_store_b128(val, ro, orow + j * 32, 0, 0);
      }
    }
  }
}
}

extern "C" void kernel_launch(void* const* d_in, const int* in_sizes, int n_in, void* d_out, int out_size, void* d_ws,
                              size_t ws_size, hipStream_t stream) {
  (void)n_in; (void)out_size; (void)d_ws; (void)ws_size;
  const unsigned nphys = (unsigned)(in_sizes[1] / (PAGE * HKV * HD));
  const unsigned kvbytes = nphys * PAGE_BYTES;
  (void)hipFuncSetAttribute((const void*)paged_attn, hipFuncAttributeMaxDynamicSharedMemorySize, LDS_TOTAL);
  hipLaunchKernelGGL(paged_attn, dim3(256), dim3(512), LDS_TOTAL, stream, d_in[0], d_in[1], d_in[2], (const int*)d_in[3],
                     (const int*)d_in[4], d_out, kvbytes, nphys);
}
```

```cpp
#include <hip/hip_runtime.h>
#include <hip/hip_bf16.h>

#define PACE_X16 434
#define SOFT 15
#define GAP_TICKS 10
#define END_TICKS 160
#define WIN_TICKS 82
#define WIN_C1 110
#define LATE_TICKS 100
#define HALT_SHIFT 240
#define HALT_FROM 400
#define CODEPF 11776
#define PF_LEAD 180
#define PF_X64 70
#define PF_PER 4
#define PF_PSTEP 2
#define PF_RUN 3

namespace {
constexpr int NB = 16, HQ = 32, HKV = 16, SQ = 16, HD = 128, PAGE = 16, NBPS = 256, SKV = NBPS * PAGE;
constexpr unsigned PAGE_BYTES = PAGE * HKV * HD * 2;
constexpr unsigned TLBG = 0x200000u;
constexpr int U0 = 4;
constexpr int NC = 6;
constexpr int KREG = 16, KLDS = 5;
static_assert(NC * (KREG + KLDS + 1) >= NBPS / 2, "P storage");
constexpr unsigned LDS_RING = 0;
constexpr unsigned LDS_WREG = 65536;
constexpr unsigned W_ALP = KLDS * 2048;
constexpr unsigned W_LAST = W_ALP + (KREG + KLDS + 1) * 128;
constexpr unsigned W_SIZE = W_LAST + 2048;
static_assert(W_LAST >= 3 * 4096 && W_SIZE % 256 == 0, "consumer region");
constexpr unsigned LDS_TBL = LDS_WREG + W_SIZE;
constexpr unsigned LDS_SRT = LDS_WREG + 3 * W_SIZE;
constexpr unsigned LDS_Q = LDS_WREG;
constexpr unsigned LDS_ML = LDS_WREG + NC * W_SIZE;
constexpr unsigned LDS_WT = LDS_ML + NC * 256;
constexpr unsigned LDS_CTL = LDS_WT + NC * 128;
constexpr unsigned CTL_LAND = 0;
constexpr unsigned CTL_QOK = 16;
constexpr unsigned CTL_SORT = 24;
constexpr unsigned CTL_FREE = 32;
constexpr unsigned CTL_MLOK = 64;
constexpr unsigned CTL_ODONE = 96;
constexpr unsigned CTL_WTOK = 128;
constexpr unsigned LDS_DUMMY = LDS_CTL + 256;
constexpr unsigned LDS_PLIST = LDS_DUMMY + 256;
constexpr unsigned LDS_TOTAL = LDS_PLIST + 1024;
static_assert(LDS_TOTAL <= 163840, "lds");

typedef __attribute__((ext_vector_type(8))) __bf16 bf16x8;
typedef __attribute__((ext_vector_type(2))) __bf16 bf16x2;
typedef __attribute__((ext_vector_type(4))) short s16x4;
typedef __attribute__((ext_vector_type(16))) float f32x16;
typedef __attribute__((ext_vector_type(4))) float f32x4;
typedef __attribute__((ext_vector_type(2))) float f32x2;
typedef __attribute__((ext_vector_type(4))) unsigned u32x4;
typedef __attribute__((ext_vector_type(2))) unsigned u32x2;
#define LAS __attribute__((address_space(3)))

__device__ __forceinline__ float swap_max(float x) {
  auto r = __builtin_amdgcn_permlane32_swap(__float_as_uint(x), __float_as_uint(x), false, false);
  return fmaxf(__uint_as_float(r[0]), __uint_as_float(r[1]));
}
__device__ __forceinline__ float swap_sum(float x) {
  auto r = __builtin_amdgcn_permlane32_swap(__float_as_uint(x), __float_as_uint(x), false, false);
  return __uint_as_float(r[0]) + __uint_as_float(r[1]);
}
__device__ __forceinline__ unsigned pack2(float a, float b) {
  bf16x2 v = {(__bf16)a, (__bf16)b};
  return __builtin_bit_cast(unsigned, v);
}
template <int CTRL> __device__ __forceinline__ int dpp_add(int x) { return x + __builtin_amdgcn_update_dpp(0, x, CTRL, 0xf, 0xf, true); }
__device__ __forceinline__ int to_sgpr(int v) {
  int r;
  asm volatile("s_nop 1\n\tv_readfirstlane_b32 %0, %1\n\ts_nop 1" : "=s"(r) : "v"(v));
  return r;
}
#define LGKM0() asm volatile("s_waitcnt lgkmcnt(0)" ::: "memory")
#define VMWAIT(n) asm volatile("s_waitcnt vmcnt(" #n ")" ::: "memory")
#define CBAR() asm volatile("" ::: "memory")
template <int SLEEP> __device__ __forceinline__ void wait_above(unsigned addr, unsigned u) {
  unsigned v, cnt;
  if (SLEEP)
    asm volatile("s_mov_b32 %1, 0x800000\n\ts_branch 2f\n"
                 "1:\n\ts_sleep %4\n"
                 "2:\n\tds_read_b32 %0, %2\n\ts_sub_u32 %1, %1, 1\n\ts_waitcnt lgkmcnt(0)\n\t"
                 "v_cmp_ge_u32 vcc, %3, %0\n\ts_cbranch_scc1 3f\n\ts_nop 1\n\ts_cbranch_vccnz 1b\n3:"
                 : "=&v"(v), "=&s"(cnt) : "v"(addr), "s"(u), "n"(SLEEP) : "memory", "scc", "vcc");
  else
    asm volatile("s_mov_b32 %1, 0x800000\n"
                 "1:\n\tds_read_b32 %0, %2\n\ts_sub_u32 %1, %1, 1\n\ts_waitcnt lgkmcnt(0)\n\t"
                 "v_cmp_ge_u32 vcc, %3, %0\n\ts_cbranch_scc1 3f\n\ts_nop 1\n\ts_cbranch_vccnz 1b\n3:"
                 : "=&v"(v), "=&s"(cnt) : "v"(addr), "s"(u) : "memory", "scc", "vcc");
}

__device__ __forceinline__ void wait_above_v(unsigned addr, unsigned u) {
  unsigned v, cnt;
  asm volatile("s_mov_b32 %1, 0x800000\n"
               "1:\n\tds_read_b32 %0, %2\n\ts_sub_u32 %1, %1, 1\n\ts_waitcnt lgkmcnt(0)\n\t"
               "v_cmp_ge_u32 vcc, %3, %0\n\ts_cbranch_scc1 3f\n\ts_nop 1\n\ts_cbranch_vccnz 1b\n3:"
               : "=&v"(v), "=&s"(cnt) : "v"(addr), "v"(u) : "memory", "scc", "vcc");
}

__global__ void __launch_bounds__(512, 1) __attribute__((amdgpu_num_vgpr(64)))
paged_attn(const void* __restrict__ qp, const void* __restrict__ kp, const void* __restrict__ vp, const int* __restrict__ bt,
           const int* __restrict__ lens, void* __restrict__ outp, unsigned kvbytes, unsigned nphys) {
  extern __shared__ __attribute__((aligned(16))) char smem[];
  LAS char* lds = (LAS char*)smem;
  const int lane0 = threadIdx.x & 63;
  const int wv = __builtin_amdgcn_readfirstlane(threadIdx.x >> 6);
  const int g = blockIdx.x;
  const int h = g >> 4;
  constexpr float CS = 0.08838834764831845f * 1.4426950408889634f;
  volatile LAS unsigned* ctl = (volatile LAS unsigned*)(lds + LDS_CTL);

  const unsigned lbase = (unsigned)(size_t)lds;

  unsigned lraw, first;
  u32x4 lq;
  {
    asm volatile("global_load_dword %0, %3, %4\n\t"
                 "global_load_dwordx4 %1, %5, %4\n\t"
                 "global_load_dword %2, %6, %7"
                 : "=&v"(lraw), "=&v"(lq), "=&v"(first)
                 : "v"((lane0 & 15) * 4), "s"(lens), "v"(lane0 & 48), "v"((lane0 & 15) * (wv < 2 ? NBPS * 4 : 0) + (lane0 >> 4) * 8 + (wv & 1) * 4), "s"(bt),
                   "s"(qp), "s"(kp), "s"(vp), "s"(outp), "s"(kvbytes), "s"(nphys)
                 : "memory");
    __builtin_amdgcn_sched_barrier(0);
  }
  if (wv >= 4) {
    if (wv == 7) {
      const __amdgpu_buffer_rsrc_t rb = __builtin_amdgcn_make_buffer_rsrc((void*)bt, 0, NB * NBPS * 4, 0x00020000);
#define TBL4(SO) "buffer_load_dwordx4 %2, %1, " SO " offen lds\n\tbuffer_load_dwordx4 %2, %1, " SO " offen offset:1024 lds\n\t" \
                 "buffer_load_dwordx4 %2, %1, " SO " offen offset:2048 lds\n\tbuffer_load_dwordx4 %2, %1, " SO " offen offset:3072 lds\n\t"
      asm volatile("s_mov_b32 m0, %0\n\ts_nop 0\n\t" TBL4("0") "s_add_u32 m0, m0, 4096\n\ts_nop 0\n\t" TBL4("%3")
                   "s_add_u32 m0, m0, %6\n\ts_nop 0\n\t" TBL4("%4") "s_add_u32 m0, m0, 4096\n\ts_nop 0\n\t" TBL4("%5")
                   :: "s"(lbase + LDS_TBL), "s"(rb), "v"(lane0 * 16), "s"(4096), "s"(8192), "s"(12288), "n"(W_SIZE - 4096) : "memory", "scc");
    }
    unsigned long long pc;
    asm volatile("s_getpc_b64 %0" : "=s"(pc));
    const __amdgpu_buffer_rsrc_t rt = __builtin_amdgcn_make_buffer_rsrc(
        wv == 5 ? (void*)pc : wv == 4 ? (void*)vp : (void*)kp, 0, wv == 5 ? CODEPF : (wv != 7 && (g & 8)) ? (int)kvbytes : 0, 0x00020000);
    const unsigned mul = wv == 5 ? 512u : 16 * TLBG + 4224u, add = wv == 5 ? ((g >> 3) & 3) * 128 : (g >> 4) * (TLBG + 33024u);
    asm volatile("s_mov_b32 m0, %0\n\ts_nop 0\n\tbuffer_load_dword %1, %2, 0 offen lds"
                 :: "s"(lbase + LDS_DUMMY), "v"((lane0 & 31) * mul + add), "s"(rt) : "memory");
    *(LAS u32x4*)(lds + LDS_SRT + (threadIdx.x - 256) * 16) = u32x4{0u, 0u, 0u, 0u};
    if (threadIdx.x < 296) ctl[lane0] = ((lane0 >> 3) == CTL_FREE / 32) ? (lane0 & 7) : 0u;
    asm volatile("s_waitcnt lgkmcnt(0)\n\ts_barrier" ::: "memory");
  }
  if (wv < 2) __builtin_amdgcn_s_setprio(3);
  const unsigned rstride = wv < 2 ? 4096 : 256;
  unsigned vo0 = (lane0 >> 4) * rstride + (((unsigned)(lane0 & 15) ^ ((lane0 >> 4) << 2)) << 4) + (wv < 2 ? h * (HD * 2) : 0);
  unsigned vo1 = (vo0 + 4 * rstride - 1024) ^ 16, vo2 = (vo0 + 8 * rstride - 2048) ^ 32, vo3 = (vo0 + 12 * rstride - 3072) ^ 48;
  const unsigned want = (g & 8) ? 15 - (g & 7) : (g & 7);
  const unsigned dst0 = lbase + (wv < 2 ? LDS_RING : LDS_Q) + (wv & 1) * 4096;
  asm volatile("s_cmp_ge_u32 %8, 4\n\ts_cbranch_scc1 1f\n\ts_waitcnt vmcnt(0)\n1:\n\ts_waitcnt vmcnt(1)" : "+v"(lraw), "+v"(lq), "+v"(first), "+v"(vo0), "+v"(vo1), "+v"(vo2), "+v"(vo3) : "s"(dst0), "s"(wv) : "memory", "scc");
  unsigned lm1 = min(lraw - 1u, (unsigned)SKV - 1u);
  unsigned rank = 0;
  {
    const unsigned key = lm1 * 16 + (lane0 & 15);
#pragma unroll
    for (int j = 0; j < 4; ++j) rank += (min(lq[j] - 1u, (unsigned)SKV - 1u) * 16 + ((lane0 >> 4) * 4 + j) > key) ? 1u : 0u;
    auto r1 = __builtin_amdgcn_permlane16_swap(rank, rank, false, false);
    rank = r1[0] + r1[1];
    auto r2 = __builtin_amdgcn_permlane32_swap(rank, rank, false, false);
    rank = r2[0] + r2[1];
  }
  first = min(first, nphys - 1) * PAGE_BYTES;
  const int b = __builtin_ctz((unsigned)__builtin_amdgcn_ballot_w64(rank == want) | 0x8000u);
  unsigned long long tstart = 0;
  if (wv < 4) {
    const bool qok = (int)__builtin_amdgcn_readlane(lraw, b) > 0;
    const __amdgpu_buffer_rsrc_t r0 = __builtin_amdgcn_make_buffer_rsrc(wv < 2 ? (void*)kp : (void*)qp, 0,
                                                                        wv < 2 ? (int)kvbytes : qok ? NB * HQ * SQ * HD * 2 : 0, 0x00020000);
    const unsigned so = wv < 2 ? __builtin_amdgcn_readlane(first, b) : qok ? (unsigned)((b * HQ + 2 * h) * SQ * HD * 2) + (wv & 1) * 4096 : 0u;
    asm volatile("s_waitcnt vmcnt(0)\n\t"
                 "s_mov_b32 m0, %2\n\t"
                 "s_memrealtime %0\n\t"
                 "s_nop 2\n\t"
                 "buffer_load_dwordx4 %4, %1, %3 offen nt lds\n\t"
                 "buffer_load_dwordx4 %5, %1, %3 offen offset:1024 nt lds\n\t"
                 "buffer_load_dwordx4 %6, %1, %3 offen offset:2048 nt lds\n\t"
                 "buffer_load_dwordx4 %7, %1, %3 offen offset:3072 nt lds\n\t"
                 "s_waitcnt lgkmcnt(0)\n\t"
                 "s_barrier"
                 : "=&s"(tstart) : "s"(r0), "s"(dst0), "s"(so), "v"(vo0), "v"(vo1), "v"(vo2), "v"(vo3) : "memory");
  }
  asm volatile("" : "+v"(lm1), "+v"(rank), "+v"(first) :: "memory");
  __builtin_amdgcn_sched_barrier(0);
  const int bp = __builtin_ctz((unsigned)__builtin_amdgcn_ballot_w64(rank == 15 - want) | 0x8000u);
  const int len = __builtin_amdgcn_readlane(lm1, b) + 1, nown = (len + PAGE - 1) / PAGE, NT = (nown + 1) >> 1;
  const int npl = (__builtin_amdgcn_readlane(lm1, bp) >> 4) + 1, NTL = (npl + 1) >> 1;
  auto total = [&]() __attribute__((always_inline)) {
    int t = lm1 + 1;
    t = dpp_add<0x121>(t); t = dpp_add<0x122>(t); t = dpp_add<0x124>(t); t = dpp_add<0x128>(t);
    return t;
  };

  auto sort_pages = [&](int lane, int bb, int n, unsigned scr, unsigned dst) __attribute__((always_inline)) {
    u32x4 e = *(LAS u32x4*)(lds + LDS_TBL + (bb >> 3) * W_SIZE + (bb & 7) * 1024 + lane * 16);
    bool in[4];
#pragma unroll
    for (int j = 0; j < 4; ++j) {
      e[j] = min(e[j], nphys - 1);
      in[j] = 4 * lane + j >= 2 * U0 && 4 * lane + j < n - 1;
      __hip_atomic_fetch_or((LAS unsigned*)(lds + scr + ((e[j] >> 5) & 255) * 8), in[j] ? 1u << (e[j] & 31) : 0u, __ATOMIC_RELAXED, __HIP_MEMORY_SCOPE_WAVEFRONT);
    }
    CBAR();
    u32x4 a = *(LAS u32x4*)(lds + scr + lane * 32), c = *(LAS u32x4*)(lds + scr + lane * 32 + 16);
    const int n0 = __builtin_popcount(a[0]), n1 = n0 + __builtin_popcount(a[2]), n2 = n1 + __builtin_popcount(c[0]), n3 = n2 + __builtin_popcount(c[2]);
    int sc = n3;
    sc = dpp_add<0x111>(sc); sc = dpp_add<0x112>(sc); sc = dpp_add<0x114>(sc); sc = dpp_add<0x118>(sc);
    sc += __builtin_amdgcn_update_dpp(0, sc, 0x142, 0xa, 0xf, false);
    sc += __builtin_amdgcn_update_dpp(0, sc, 0x143, 0xc, 0xf, false);
    const bool ok = nphys <= 8192u && __builtin_amdgcn_readlane(sc, 63) == n - 1 - 2 * U0;
    sc += 2 * U0 - n3;
    a[1] = sc; a[3] = sc + n0; c[1] = sc + n1; c[3] = sc + n2;
    *(LAS u32x4*)(lds + scr + lane * 32) = a;
    *(LAS u32x4*)(lds + scr + lane * 32 + 16) = c;
    CBAR();
    u32x2 wp[4];
#pragma unroll
    for (int j = 0; j < 4; ++j) wp[j] = *(LAS u32x2*)(lds + scr + ((e[j] >> 5) & 255) * 8);
#pragma unroll
    for (int j = 0; j < 4; ++j) {
      const unsigned r = wp[j][1] + __builtin_popcount(wp[j][0] & ((1u << (e[j] & 31)) - 1u));
      *(LAS unsigned*)(lds + dst + ((ok && in[j]) ? r : 4 * lane + j) * 4) = e[j] * PAGE_BYTES;
    }
    CBAR();
  };

  if (__builtin_expect(wv < 2, 1)) {
    const int half = wv, lane = lane0;
    const unsigned vland = lbase + LDS_CTL + CTL_LAND + half * 4;
    const unsigned vfree = lbase + LDS_CTL + CTL_FREE + (lane & 7) * 4;
    const unsigned ring = dst0;
    unsigned l0 = __builtin_amdgcn_ds_bpermute((b + 16 * lane) * 4, first), l1 = 0;
    const unsigned a0 = lbase + LDS_SRT + 4096 + min(2 * lane + half, nown - 1) * 4, a1 = lbase + LDS_SRT + 4096 + min(2 * lane + half + 128, nown - 1) * 4;
    const int rot = (NT >= 3 * NC) ? NT % NC : 0;
    const unsigned vperm = (lane < NC ? (lane + rot >= NC ? lane + rot - NC : lane + rot) : lane) * 4;
    const int T16 = total() * PACE_X16;
    const int step = to_sgpr((int)((float)T16 * __builtin_amdgcn_rcpf((float)max(len, 32))));
    const int stepv = min(step, to_sgpr((int)((float)((T16 >> 5) - END_TICKS * 256) * __builtin_amdgcn_rcpf((float)max(NT - NC - 1, 1)))));
    const int win = to_sgpr((int)((float)(((h & 3) == 1 ? WIN_C1 : WIN_TICKS) * 1024) * __builtin_amdgcn_rcpf((float)step) + 0.5f));
    const int hmin = to_sgpr((int)((float)(HALT_FROM * 256) * __builtin_amdgcn_rcpf((float)step)));
    const unsigned due0 = ((unsigned)tstart << 8) + half * (step >> 1);
    const int lrows = len - (2 * (NT - 1) + half) * PAGE;
    unsigned vf, vt, lc;
    asm volatile(
        "s_waitcnt lgkmcnt(0)\n\t"
        "s_memrealtime s[68:69]\n\t"
        "ds_read_b32 %[vf], %[vfree]\n\t"
        "s_mov_b32 s60, 1\n\t"
        "s_mov_b32 s61, 0\n\t"
        "s_mov_b32 s62, 0x1000000\n\t"
        "s_mov_b32 s66, 0\n\t"
        "s_min_u32 s67, %[nt], %[u0]\n\t"
        "s_mov_b32 s75, %[nrec]\n\t"
        "s_mov_b32 s72, 0x2000\n\t"
        "s_mov_b32 s73, 1\n\t"
        "s_mov_b32 s74, %[due1]\n\t"
        "s_mov_b32 s85, %[duev]\n\t"
        "s_mov_b32 s86, %[late]\n\t"
        "s_mov_b32 s87, %[step]\n\t"
        "s_mov_b64 s[76:77], %[kp]\n\t"
        "s_mov_b32 s78, %[nrec]\n\t"
        "s_mov_b32 s79, 0x20000\n\t"
        "s_and_b32 s77, s77, 0xffff\n\t"
        "v_mov_b32 %[lc], %[l0]\n\t"
        "s_getpc_b64 s[70:71]\n"
        ".LldA%=:\n\t"
        "s_add_u32 s80, s70, .LldL%=-.LldA%=\n\t"
        "s_addc_u32 s71, s71, 0\n\t"
        "s_branch .LldF%=\n"
        ".LldX%=:\n\t"
        "s_cmp_lt_i32 s64, 0\n\t"
        "s_cbranch_scc0 .LldH%=\n"
        ".LldR%=:\n\t"
        "s_sub_u32 s62, s62, 1\n\t"
        "s_cbranch_scc1 .LldE%=\n"
        ".LldT%=:\n\t"
        "s_waitcnt lgkmcnt(0)\n\t"
        "s_getreg_b32 s64, hwreg(HW_REG_IB_STS)\n\t"
        "s_and_b32 s65, s64, 15\n\t"
        "s_bfe_u32 s64, s64, 0x20016\n\t"
        "s_lshl4_add_u32 s81, s64, s65\n\t"
        "s_cmp_gt_i32 s81, s61\n\t"
        "s_cbranch_scc1 .LldC%=\n\t"
        "s_add_u32 s64, s81, 3\n\t"
        "s_and_b32 s64, s64, 28\n\t"
        "s_add_u32 s70, s80, s64\n\t"
        "s_add_u32 s66, s66, s61\n\t"
        "s_sub_u32 s61, s64, 4\n\t"
        "s_sub_u32 s66, s66, s61\n\t"
        "v_mov_b32 %[vt], s66\n\t"
        "s_setpc_b64 s[70:71]\n\t"
        ".p2align 5\n"
        ".LldL%=:\n\t"
        "s_waitcnt vmcnt(0)\n\t"
        "s_waitcnt vmcnt(4)\n\t"
        "s_waitcnt vmcnt(8)\n\t"
        "s_waitcnt vmcnt(12)\n\t"
        "s_waitcnt vmcnt(16)\n\t"
        "s_waitcnt vmcnt(20)\n\t"
        "s_waitcnt vmcnt(24)\n\t"
        "s_waitcnt vmcnt(28)\n\t"
        "ds_write_b32 %[vland], %[vt]\n"
        ".LldC%=:\n\t"
        "v_cmp_eq_u32 vcc, s60, %[vf]\n\t"
        "s_lshl_b32 s64, s68, 8\n\t"
        "s_memrealtime s[68:69]\n\t"
        "ds_read_b32 %[vf], %[vfree]\n\t"
        "s_sub_u32 s64, s64, s74\n\t"
        "s_cmp_gt_u32 s64, s86\n\t"
        "s_cbranch_scc1 .LldX%=\n"
        ".LldI%=:\n\t"
        "s_cbranch_vccz .LldR%=\n\t"
        "s_cmp_gt_u32 s81, %[win]\n\t"
        "s_cbranch_scc1 .LldR%=\n\t"
        "buffer_load_dwordx4 %[o0], s[76:79], s63 offen nt lds\n\t"
        "s_add_u32 s74, s74, s87\n\t"
        "s_add_u32 s73, s73, 1\n\t"
        "buffer_load_dwordx4 %[o1], s[76:79], s63 offen offset:1024 nt lds\n\t"
        "s_add_u32 s60, s60, 1\n\t"
        "s_add_u32 s61, s61, 4\n\t"
        "s_add_u32 s86, s64, %[late]\n\t"
        "buffer_load_dwordx4 %[o2], s[76:79], s63 offen offset:2048 nt lds\n\t"
        "s_add_u32 s72, s72, 0x2000\n\t"
        "s_and_b32 s72, s72, 0xe000\n\t"
        "buffer_load_dwordx4 %[o3], s[76:79], s63 offen offset:3072 nt lds\n"
        ".LldF%=:\n\t"
        "v_readlane_b32 s63, %[lc], s73\n\t"
        "s_add_u32 m0, s72, %[ring]\n\t"
        "s_cmp_lg_u32 s60, s67\n\t"
        "s_cbranch_scc1 .LldT%=\n\t"
        "s_cmp_eq_u32 s73, %[nt]\n\t"
        "s_cbranch_scc1 .LldV%=\n\t"
        "s_cmp_lg_u32 s60, %[u0]\n\t"
        "s_cbranch_scc1 .LldG%=\n\t"
        "s_cmp_le_u32 %[nt], %[u0]\n\t"
        "s_cbranch_scc1 .LldG%=\n"
        ".LldS%=:\n\t"
        "ds_read_b32 %[lc], %[vland] offset:%[srt]\n\t"
        "ds_read_b32 %[l0], %[a0]\n\t"
        "ds_read_b32 %[l1], %[a1]\n\t"
        "s_sub_u32 s62, s62, 1\n\t"
        "s_cbranch_scc1 .LldE%=\n\t"
        "s_waitcnt lgkmcnt(0)\n\t"
        "v_readfirstlane_b32 s64, %[lc]\n\t"
        "s_cmp_eq_u32 s64, 0\n\t"
        "s_cbranch_scc1 .LldS%=\n\t"
        "s_max_i32 s65, %[lrows], 0\n\t"
        "s_lshl_b32 s65, s65, 12\n\t"
        "s_add_u32 s64, s64, s65\n\t"
        "s_sub_u32 s64, s64, 1\n\t"
        "s_cmp_lt_i32 %[lrows], 16\n\t"
        "s_cselect_b32 s75, s64, %[nrec]\n\t"
        "v_mov_b32 %[lc], %[l0]\n"
        ".LldG%=:\n\t"
        "s_cmp_lg_u32 s73, 64\n\t"
        "s_cbranch_scc1 .LldN%=\n\t"
        "v_mov_b32 %[lc], %[l1]\n"
        ".LldN%=:\n\t"
        "s_cmp_eq_u32 s73, %[ntm]\n\t"
        "s_cselect_b32 s78, s75, %[nrec]\n\t"
        "s_cselect_b32 s64, %[nt], %[ntm]\n\t"
        "s_min_u32 s65, s64, 64\n\t"
        "s_cmp_lt_u32 s73, 64\n\t"
        "s_cselect_b32 s64, s65, s64\n\t"
        "s_sub_u32 s67, s60, s73\n\t"
        "s_add_u32 s67, s67, s64\n\t"
        "s_branch .LldF%=\n"
        ".LldV%=:\n\t"
        "s_cmp_lg_u32 s60, %[nt]\n\t"
        "s_cbranch_scc1 .LldD%=\n\t"
        "s_mov_b64 s[76:77], %[vp]\n\t"
        "s_and_b32 s77, s77, 0xffff\n\t"
        "s_mov_b32 s73, 0\n\t"
        "s_mov_b32 s74, s85\n\t"
        "s_max_i32 s87, %[stepv], 0\n\t"
        "ds_bpermute_b32 %[lc], %[vperm], %[l0]\n\t"
        "v_xor_b32 %[o1], 16, %[o1]\n\t"
        "v_xor_b32 %[o2], 32, %[o2]\n\t"
        "v_xor_b32 %[o3], 48, %[o3]\n\t"
        "s_waitcnt lgkmcnt(0)\n\t"
        "s_branch .LldN%=\n"
        ".LldH%=:\n\t"
        "s_cmp_eq_u32 s66, %[fin]\n\t"
        "s_cbranch_scc1 .LldE%=\n\t"
        "s_cmp_lt_i32 s60, %[hmin]\n\t"
        "s_cbranch_scc1 .LldI%=\n\t"
        "s_add_u32 s74, s74, %[hshift]\n\t"
        "s_add_u32 s85, s85, %[hshift]\n\t"
        "s_branch .LldR%=\n"
        ".LldD%=:\n\t"
        "s_sub_u32 s74, s74, 0x10000000\n\t"
        "s_mov_b32 s86, 0\n\t"
        "s_mov_b32 s60, -1\n\t"
        "s_branch .LldT%=\n"
        ".LldE%=:\n\t"
        "s_waitcnt vmcnt(0) lgkmcnt(0)\n\t"
        : [vf] "=&v"(vf), [vt] "=&v"(vt), [lc] "=&v"(lc), [l0] "+&v"(l0), [l1] "+&v"(l1), [o1] "+&v"(vo1), [o2] "+&v"(vo2), [o3] "+&v"(vo3)
        : [vperm] "v"(vperm), [a0] "v"(a0), [a1] "v"(a1), [o0] "v"(vo0), [vland] "v"(vland), [vfree] "v"(vfree),
          [kp] "s"(kp), [vp] "s"(vp), [nrec] "s"(kvbytes), [nt] "s"(NT), [ring] "s"(ring),
          [step] "s"(step), [stepv] "s"(stepv), [due1] "s"(due0 + step + SOFT * 256), [duev] "s"(due0 + (to_sgpr(T16) >> 5) + (SOFT + GAP_TICKS) * 256), [lrows] "s"(lrows), [ntm] "s"(NT - 1), [fin] "s"(8 * NT), [u0] "n"(U0), [win] "s"(win), [late] "n"(LATE_TICKS * 256), [hshift] "n"(HALT_SHIFT * 256), [hmin] "s"(hmin), [srt] "n"(CTL_SORT - CTL_LAND)
        : "memory", "scc", "vcc", "s60", "s61", "s62", "s63", "s64", "s65", "s66", "s67", "s68", "s69", "s70", "s71", "s72", "s73", "s74", "s75",
          "s76", "s77", "s78", "s79", "s80", "s81", "s85", "s86", "s87");
    asm volatile("s_endpgm" ::: "memory");
  }

  int lane = lane0;
  asm volatile("" : "+v"(lane));
  if (wv == 7) {
#pragma unroll 1
    for (int i = 0; i < 2; ++i) {
      sort_pages(lane, i ? bp : b, i ? npl : nown, LDS_SRT + i * 2048, i ? LDS_PLIST : LDS_SRT + 4096);
      if (i == 0) {
        const unsigned lo = *(LAS unsigned*)(lds + LDS_SRT + 4096 + (nown - 1) * 4) + 1;
        LGKM0();
        ctl[CTL_SORT / 4 + (lane & 1)] = lo;
      }
    }
  }
  if (wv < 4) {
    VMWAIT(0);
    ctl[CTL_QOK / 4 + (wv & 1)] = 1;
  }
  const int tot = total();
  const int w = wv - 2;
  const int r32 = lane & 31, hi = lane >> 5;
  const int nmine = (NT > w) ? (NT - w + NC - 1) / NC : 0;
  const unsigned aland = lbase + LDS_CTL + CTL_LAND + (lane & 1) * 4;
  const int tots = __builtin_amdgcn_readfirstlane(tot);
  const bool pf_on = npl * 256 * 64 > tots * PF_X64 && npl > nown && (h & 3) == 0;
  const unsigned pf_last = 2 * NT - to_sgpr((int)((float)(2 * NT * (PF_LEAD + 23 * NC)) * 4096.0f * __builtin_amdgcn_rcpf((float)(tot * PACE_X16))));
  const unsigned pf_ratio = to_sgpr((int)((float)NTL * 65536.0f * __builtin_amdgcn_rcpf((float)NT)));
  const unsigned pf_per = PF_PER + (g & 7) * PF_PSTEP, pf_inv = (65536 + pf_per - 1) / pf_per;
  auto prefetch = [&](unsigned op) {
    const unsigned d = pf_last - op;
    if (d >= 1024u) return;
    const unsigned r = d - ((d * pf_inv) >> 16) * pf_per;
    if (r >= PF_RUN) return;
    unsigned tl = 2 * NTL - 1 - NC - (((d - r) * pf_ratio) >> 16) - r;
    if (tl >= 2u * NTL) return;
    const bool isv = tl >= (unsigned)NTL;
    tl -= isv ? NTL : 0;
    const __amdgpu_buffer_rsrc_t rp = __builtin_amdgcn_make_buffer_rsrc(isv ? (void*)vp : (void*)kp, 0, (int)kvbytes, 0x00020000);
    unsigned ln = lane;
    asm volatile("" : "+v"(ln));
    const unsigned vo = (*(LAS unsigned*)(lds + LDS_PLIST + min(2 * tl + (ln >> 5), (unsigned)npl - 1) * 4) & 0x7fff0000u) + (h + 1) * (HD * 2) +
                        ((ln >> 1) & 15) * 4096 + (ln & 1) * 128;
    asm volatile("s_mov_b32 m0, %0\n\ts_nop 4\n\tbuffer_load_dword %2, %1, 0 offen lds" :: "s"(lbase + LDS_DUMMY), "s"(rp), "v"(vo) : "memory");
  };
  const int pos = (w + NC * 22 - NT) % NC;
  const unsigned palp = LDS_WREG + pos * W_SIZE + W_ALP + r32 * 4;
  const unsigned ppst = LDS_WREG + pos * W_SIZE + lane * 16;
  asm volatile("" ::: "v128", "v255");
  unsigned resc = 0;
  float m = -3.0e38f, mprev = -3.0e38f, l = 0.f;

  {
    const unsigned fk = (((unsigned)r32 & 3) << 2) | (((unsigned)r32 >> 2) & 3);
    const unsigned kaddr0 = LDS_RING + r32 * 256 + (((unsigned)hi ^ fk) << 4);
    bf16x8 qf[8];
    wait_above<1>(aland + (CTL_QOK - CTL_LAND), 0);
#pragma unroll
    for (int s = 0; s < 8; ++s) qf[s] = *(LAS bf16x8*)(lds + ((kaddr0 + LDS_Q - LDS_RING) ^ (s << 5)));
#pragma unroll 1
    for (int k = 0; k < nmine; ++k) {
      const unsigned t = w + NC * k;
      wait_above<1>(aland, 4 * t);
      bf16x8 kf[8];
#pragma unroll
      for (int s = 0; s < 8; ++s) kf[s] = *(LAS bf16x8*)(lds + ((kaddr0 + ((t & 7) << 13)) ^ (s << 5)));
      asm volatile("s_waitcnt lgkmcnt(0)" : "+v"(kf[0]), "+v"(kf[1]), "+v"(kf[2]), "+v"(kf[3]), "+v"(kf[4]), "+v"(kf[5]), "+v"(kf[6]), "+v"(kf[7]) :: "memory");
      ctl[CTL_FREE / 4 + (t & 7)] = t + 8;
      if (pf_on) prefetch(t);
      f32x16 sc;
#pragma unroll
      for (int r = 0; r < 16; ++r) sc[r] = 0.f;
#pragma unroll
      for (int s = 0; s < 8; ++s) sc = __builtin_amdgcn_mfma_f32_32x32x16_bf16(kf[s], qf[s], sc, 0, 0, 0);
      if ((int)t == NT - 1) {
        const int kb = 2 * t * PAGE + 4 * hi;
#pragma unroll
        for (int r = 0; r < 16; ++r) sc[r] = (kb + 8 * (r >> 2) + (r & 3) < len) ? sc[r] : -__builtin_inff();
      }
      float mx = sc[0];
#pragma unroll
      for (int r = 1; r < 16; ++r) mx = fmaxf(mx, sc[r]);
      mx = swap_max(mx);
      const float mn = fmaxf(m, mx);
      const float alpha = __builtin_amdgcn_exp2f((m - mn) * CS);
      mprev = m;
      m = mn;
      float ps = 0.f;
#pragma unroll
      for (int r = 0; r < 16; ++r) { sc[r] = __builtin_amdgcn_exp2f((sc[r] - mn) * CS); ps += sc[r]; }
      l = fmaf(l, alpha, ps);
      u32x4 pf[2];
#pragma unroll
      for (int s = 0; s < 2; ++s)
        pf[s] = u32x4{pack2(sc[8 * s], sc[8 * s + 1]), pack2(sc[8 * s + 2], sc[8 * s + 3]), pack2(sc[8 * s + 4], sc[8 * s + 5]), pack2(sc[8 * s + 6], sc[8 * s + 7])};
      if (__builtin_amdgcn_ballot_w64(alpha != 1.0f)) resc |= 1u << k;
      *(LAS float*)(lds + palp + k * 128) = alpha;
      if (k >= KREG || k == nmine - 1) {
        const unsigned dst = ppst + ((k == nmine - 1) ? W_LAST : (k - KREG) * 2048);
        *(LAS u32x4*)(lds + dst) = pf[0];
        *(LAS u32x4*)(lds + dst + 1024) = pf[1];
      } else {
        asm volatile("s_set_gpr_idx_on %8, gpr_idx(DST)\n\t"
                     "v_mov_b32 v128, %0\n\tv_mov_b32 v129, %1\n\tv_mov_b32 v130, %2\n\tv_mov_b32 v131, %3\n\t"
                     "v_mov_b32 v132, %4\n\tv_mov_b32 v133, %5\n\tv_mov_b32 v134, %6\n\tv_mov_b32 v135, %7\n\t"
                     "s_set_gpr_idx_off"
                     :: "v"(pf[0][0]), "v"(pf[0][1]), "v"(pf[0][2]), "v"(pf[0][3]), "v"(pf[1][0]), "v"(pf[1][1]), "v"(pf[1][2]), "v"(pf[1][3]), "s"(8 * k));
      }
    }
  }
  l = swap_sum(l);
  *(LAS f32x2*)(lds + LDS_ML + w * 256 + r32 * 8) = f32x2{m, l};
  LGKM0();
  ctl[CTL_MLOK / 4 + w] = 1;

  const unsigned tq = (lane & 15) >> 2, tp = lane & 3, tblk = (lane >> 4) & 1;
  const unsigned vaddr0 = LDS_RING + (4 * hi + tq) * 256 + ((tq ^ (pos & 3)) << 6) + ((2 * tblk + (tp >> 1)) << 4) + ((tp & 1) << 3);
  f32x16 o[4];
#pragma unroll
  for (int i = 0; i < 4; ++i)
#pragma unroll
    for (int r = 0; r < 16; ++r) o[i][r] = 0.f;
  auto weights = [&]() {
    wait_above<1>(lbase + LDS_CTL + CTL_MLOK + min(lane, NC - 1) * 4, 0);
    unsigned a = LDS_ML + r32 * 8;
    asm volatile("" : "+v"(a) :: "memory");
    float M = -3.0e38f, L = 0.f;
    f32x2 ml[NC];
#pragma unroll
    for (int i = 0; i < NC; ++i) { ml[i] = *(LAS f32x2*)(lds + a + i * 256); M = fmaxf(M, ml[i][0]); }
#pragma unroll
    for (int i = 0; i < NC; ++i) L = fmaf(__builtin_amdgcn_exp2f((ml[i][0] - M) * CS), ml[i][1], L);
    const float inv = 1.0f / L;
    *(LAS float*)(lds + (a >> 1) + (LDS_WT - LDS_ML / 2) + pos * 128) = __builtin_amdgcn_exp2f((mprev - M) * CS) * inv;
    const float ct = __builtin_amdgcn_exp2f((m - M) * CS) * inv;
#pragma unroll
    for (int s = 0; s < 2; ++s) {
      u32x4 x = *(LAS u32x4*)(lds + ppst + W_LAST + s * 1024);
#pragma unroll
      for (int e = 0; e < 4; ++e) x[e] = pack2(__uint_as_float(x[e] << 16) * ct, __uint_as_float(x[e] & 0xffff0000u) * ct);
      *(LAS u32x4*)(lds + ppst + W_LAST + s * 1024) = x;
    }
    LGKM0();
    ctl[CTL_WTOK / 4 + w] = 1;
  };
  resc &= ~1u;
  if (nmine <= 3) weights();
#pragma unroll 1
  for (int k = 0; k < nmine - 1; ++k) {
    if (k == 2) weights();
    if ((resc >> k) & 1) {
      const float alpha = *(LAS float*)(lds + palp + k * 128);
#pragma unroll
      for (int i = 0; i < 4; ++i)
#pragma unroll
        for (int r = 0; r < 16; ++r) o[i][r] *= alpha;
    }
    const unsigned u = NT + ((k == 0 && NT >= 3 * NC) ? pos : w + NC * k);
    u32x4 pf[2];
    if (k >= KREG) {
      pf[0] = *(LAS u32x4*)(lds + ppst + (k - KREG) * 2048);
      pf[1] = *(LAS u32x4*)(lds + ppst + (k - KREG) * 2048 + 1024);
    } else {
      asm volatile("s_set_gpr_idx_on %8, gpr_idx(SRC0)\n\t"
                   "v_mov_b32 %0, v128\n\tv_mov_b32 %1, v129\n\tv_mov_b32 %2, v130\n\tv_mov_b32 %3, v131\n\t"
                   "v_mov_b32 %4, v132\n\tv_mov_b32 %5, v133\n\tv_mov_b32 %6, v134\n\tv_mov_b32 %7, v135\n\t"
                   "s_set_gpr_idx_off\n\ts_nop 1"
                   : "=v"(pf[0][0]), "=v"(pf[0][1]), "=v"(pf[0][2]), "=v"(pf[0][3]), "=v"(pf[1][0]), "=v"(pf[1][1]), "=v"(pf[1][2]), "=v"(pf[1][3])
                   : "s"(8 * k));
    }
    asm volatile("" : "+v"(pf[0]), "+v"(pf[1]), "+v"(o[0]), "+v"(o[1]), "+v"(o[2]), "+v"(o[3]));
    if (k == nmine - 2) wait_above<1>(lbase + LDS_CTL + CTL_WTOK + min(lane, NC - 1) * 4, 0);
    wait_above<1>(aland, 4 * u);
    bf16x8 vf[4][2];
#pragma unroll
    for (int i = 0; i < 4; ++i) {
      const unsigned a = (vaddr0 + ((u & 7) << 13)) ^ (i << 6);
#pragma unroll
      for (int s = 0; s < 2; ++s) {
        const s16x4 lo = __builtin_amdgcn_ds_read_tr16_b64_v4i16((LAS s16x4*)(lds + a + 4096 * s));
        const s16x4 up = __builtin_amdgcn_ds_read_tr16_b64_v4i16((LAS s16x4*)(lds + a + 4096 * s + 2048));
        vf[i][s] = __builtin_bit_cast(bf16x8, __builtin_shufflevector(lo, up, 0, 1, 2, 3, 4, 5, 6, 7));
      }
    }
    asm volatile("s_waitcnt lgkmcnt(0)" : "+v"(vf[0][0]), "+v"(vf[0][1]), "+v"(vf[1][0]), "+v"(vf[1][1]), "+v"(vf[2][0]), "+v"(vf[2][1]), "+v"(vf[3][0]), "+v"(vf[3][1]) :: "memory");
    ctl[CTL_FREE / 4 + (u & 7)] = u + 8;
    if (pf_on) prefetch(u);
#pragma unroll
    for (int s = 0; s < 2; ++s)
#pragma unroll
      for (int i = 0; i < 4; ++i) o[i] = __builtin_amdgcn_mfma_f32_32x32x16_bf16(vf[i][s], __builtin_bit_cast(bf16x8, pf[s]), o[i], 0, 0, 0);
  }

  {
    unsigned l16 = lane * 16;
    asm volatile("" : "+v"(l16));
    const unsigned l4 = l16 >> 2, awt = LDS_WT + (l4 & 127);
    auto put = [&](unsigned dst, const f32x16& x) {
#pragma unroll
      for (int j = 0; j < 4; ++j) *(LAS f32x4*)(lds + dst + j * 1024) = f32x4{x[4 * j], x[4 * j + 1], x[4 * j + 2], x[4 * j + 3]};
    };
    float co[6];
    bf16x8 pl[NC][2];
    auto ld_p = [&](int p) {
      pl[p][0] = *(LAS bf16x8*)(lds + LDS_WREG + p * W_SIZE + W_LAST + l16);
      pl[p][1] = *(LAS bf16x8*)(lds + LDS_WREG + p * W_SIZE + W_LAST + l16 + 1024);
    };
#pragma unroll
    for (int i = 0; i < 4; ++i) co[i] = *(LAS float*)(lds + awt + ((pos & 3) ^ i) * 128);
    co[4] = *(LAS float*)(lds + awt + 4 * 128);
    co[5] = *(LAS float*)(lds + awt + 5 * 128);
    ld_p(0); ld_p(1);
    asm volatile("" : "+v"(l16) :: "memory");
    if (nmine > 1) {
      if (pos > 3) put(LDS_RING + ((2 * NT - 2 * NC + pos) & 7) * 8192 + l16, o[0]);
#pragma unroll
      for (int i = 1; i < 4; ++i) put(LDS_WREG + pos * W_SIZE + (i - 1) * 4096 + l16, o[i]);
      LGKM0();
      ctl[CTL_ODONE / 4 + pos] = 1;
    }
    if (pos < 4) {
      f32x16 of = o[0], ot;
#pragma unroll
      for (int r = 0; r < 16; ++r) ot[r] = 0.f;
      auto src_o = [&](int p) -> unsigned {
        const int i = pos ^ (p & 3);
        return l16 + (i == 0 ? LDS_RING + ((2 * NT - 2 * NC + p) & 7) * 8192 : LDS_WREG + p * W_SIZE + (i - 1) * 4096);
      };
      auto fma_o = [&](unsigned src, float c) {
#pragma unroll
        for (int j = 0; j < 4; ++j) {
          const f32x4 x = *(LAS f32x4*)(lds + src + j * 1024);
#pragma unroll
          for (int e = 0; e < 4; ++e) of[4 * j + e] = fmaf(x[e], c, of[4 * j + e]);
        }
      };
      auto mma_t = [&](int p, bf16x8 p0, bf16x8 p1) {
        const unsigned a = vaddr0 + (((2 * NT - NC + p) & 7) << 13);
        bf16x8 vf[2];
#pragma unroll
        for (int s = 0; s < 2; ++s) {
          const s16x4 lo = __builtin_amdgcn_ds_read_tr16_b64_v4i16((LAS s16x4*)(lds + a + 4096 * s));
          const s16x4 up = __builtin_amdgcn_ds_read_tr16_b64_v4i16((LAS s16x4*)(lds + a + 4096 * s + 2048));
          vf[s] = __builtin_bit_cast(bf16x8, __builtin_shufflevector(lo, up, 0, 1, 2, 3, 4, 5, 6, 7));
        }
        ot = __builtin_amdgcn_mfma_f32_32x32x16_bf16(vf[0], p0, ot, 0, 0, 0);
        ot = __builtin_amdgcn_mfma_f32_32x32x16_bf16(vf[1], p1, ot, 0, 0, 0);
      };
      if (NT >= 2 * NC) {
#define PIN_OF() asm volatile("" : "+v"(of), "+v"(ot))
#pragma unroll
        for (int r = 0; r < 16; ++r) of[r] *= co[0];
        const bool wl = (l4 & 24) == 0;
        const unsigned ua = 4 * (2 * NT - NC);
        wait_above_v(wl ? aland : lbase + LDS_CTL + CTL_ODONE + (l4 & 12), wl ? ua : 0u);
        mma_t(0, pl[0][0], pl[0][1]);
#pragma unroll
        for (int i = 1; i < 4; ++i) fma_o(LDS_WREG + (pos ^ i) * W_SIZE + (i - 1) * 4096 + l16, co[i]);
        PIN_OF();
        ld_p(2); ld_p(3); ld_p(4); ld_p(5);
        wait_above_v(wl ? aland : lbase + LDS_CTL + CTL_ODONE + 16 + (l4 & 4), wl ? ua + 8 : 0u);
        mma_t(1, pl[1][0], pl[1][1]);
        mma_t(2, pl[2][0], pl[2][1]);
        fma_o(src_o(4), co[4]);
        fma_o(src_o(5), co[5]);
#pragma unroll
        for (int r = 0; r < 16; ++r) { ot[r] += of[r]; of[r] = 0.f; }
        asm volatile("" : "+v"(ot));
        wait_above<0>(aland, ua + 20);
        mma_t(3, pl[3][0], pl[3][1]);
        mma_t(4, pl[4][0], pl[4][1]);
        mma_t(5, pl[5][0], pl[5][1]);
        of = ot;
      } else {
        wait_above<1>(lbase + LDS_CTL + CTL_WTOK + min(l4, (NC - 1) * 4u), 0);
        const float cw = *(LAS float*)(lds + awt + pos * 128);
#pragma unroll
        for (int r = 0; r < 16; ++r) of[r] *= cw;
#pragma unroll 1
        for (int p = max(NC - NT, 0); p < NC; ++p) {
          if (p != pos && NT - 2 * NC + p >= 0) {
            wait_above<1>(lbase + LDS_CTL + CTL_ODONE + p * 4, 0);
            fma_o(src_o(p), *(LAS float*)(lds + awt + p * 128));
          }
          wait_above<1>(aland, 4 * (2 * NT - NC + p));
          mma_t(p, *(LAS bf16x8*)(lds + LDS_WREG + p * W_SIZE + W_LAST + l16), *(LAS bf16x8*)(lds + LDS_WREG + p * W_SIZE + W_LAST + l16 + 1024));
        }
#pragma unroll
        for (int r = 0; r < 16; ++r) of[r] += ot[r];
      }
      const __amdgpu_buffer_rsrc_t ro = __builtin_amdgcn_make_buffer_rsrc(outp, 0, NB * HQ * SQ * HD * 2, 0x00020000);
      const unsigned orow = (unsigned)(b * HQ + 2 * h) * SQ * HD * 2 + pos * 64 + ((l16 & 511) << 4) + ((l16 >> 5) & 16);
#pragma unroll
      for (int j = 0; j < 2; ++j) {
        const unsigned a0 = pack2(of[8 * j], of[8 * j + 1]), a1 = pack2(of[8 * j + 2], of[8 * j + 3]);
        const unsigned b0 = pack2(of[8 * j + 4], of[8 * j + 5]), b1 = pack2(of[8 * j + 6], of[8 * j + 7]);
        auto s0 = __builtin_amdgcn_permlane32_swap(a0, b0, false, false);
        auto s1 = __builtin_amdgcn_permlane32_swap(a1, b1, false, false);
        u32x4 val = {s0[0], s1[0], s0[1], s1[1]};
        __builtin_amdgcn_raw_buffer_store_b128(val, ro, orow + j * 32, 0, 0);
      }
    }
  }
}
}

extern "C" void kernel_launch(void* const* d_in, const int* in_sizes, int n_in, void* d_out, int out_size, void* d_ws,
                              size_t ws_size, hipStream_t stream) {
  (void)n_in; (void)out_size; (void)d_ws; (void)ws_size;
  const unsigned nphys = (unsigned)(in_sizes[1] / (PAGE * HKV * HD));
  const unsigned kvbytes = nphys * PAGE_BYTES;
  (void)hipFuncSetAttribute((const void*)paged_attn, hipFuncAttributeMaxDynamicSharedMemorySize, LDS_TOTAL);
  hipLaunchKernelGGL(paged_attn, dim3(256), dim3(512), LDS_TOTAL, stream, d_in[0], d_in[1], d_in[2], (const int*)d_in[3],
                     (const int*)d_in[4], d_out, kvbytes, nphys);
}
```

```cpp
#include <hip/hip_runtime.h>
#include <hip/hip_bf16.h>

#define PACE_X16 433
#define SOFT 15
#define GAP_TICKS 10
#define WIN_TICKS 82
#define WIN_C1 110
#define LATE_TICKS 100
#define HALT_SHIFT 240
#define HALT_FROM 400
#define CODEPF 11776
#define PF_LEAD 180
#define PF_X64 70
#define PF_PER 4
#define PF_PSTEP 2
#define PF_RUN 3

namespace {
constexpr int NB = 16, HQ = 32, HKV = 16, SQ = 16, HD = 128, PAGE = 16, NBPS = 256, SKV = NBPS * PAGE;
constexpr unsigned PAGE_BYTES = PAGE * HKV * HD * 2;
constexpr unsigned TLBG = 0x200000u;
constexpr int U0 = 4;
constexpr int NC = 6;
constexpr int KREG = 16, KLDS = 5;
static_assert(NC * (KREG + KLDS + 1) >= NBPS / 2, "P storage");
constexpr unsigned LDS_RING = 0;
constexpr unsigned LDS_WREG = 65536;
constexpr unsigned W_ALP = KLDS * 2048;
constexpr unsigned W_LAST = W_ALP + (KREG + KLDS + 1) * 128;
constexpr unsigned W_SIZE = W_LAST + 2048;
static_assert(W_LAST >= 3 * 4096 && W_SIZE % 256 == 0, "consumer region");
constexpr unsigned LDS_TBL = LDS_WREG + W_SIZE;
constexpr unsigned LDS_SRT = LDS_WREG + 3 * W_SIZE;
constexpr unsigned LDS_Q = LDS_WREG;
constexpr unsigned LDS_ML = LDS_WREG + NC * W_SIZE;
constexpr unsigned LDS_WT = LDS_ML + NC * 256;
constexpr unsigned LDS_CTL = LDS_WT + NC * 128;
constexpr unsigned CTL_LAND = 0;
constexpr unsigned CTL_QOK = 16;
constexpr unsigned CTL_SORT = 24;
constexpr unsigned CTL_FREE = 32;
constexpr unsigned CTL_MLOK = 64;
constexpr unsigned CTL_ODONE = 96;
constexpr unsigned CTL_WTOK = 128;
constexpr unsigned LDS_DUMMY = LDS_CTL + 256;
constexpr unsigned LDS_PLIST = LDS_DUMMY + 256;
constexpr unsigned LDS_TOTAL = LDS_PLIST + 1024;
static_assert(LDS_TOTAL <= 163840, "lds");

typedef __attribute__((ext_vector_type(8))) __bf16 bf16x8;
typedef __attribute__((ext_vector_type(2))) __bf16 bf16x2;
typedef __attribute__((ext_vector_type(4))) short s16x4;
typedef __attribute__((ext_vector_type(16))) float f32x16;
typedef __attribute__((ext_vector_type(4))) float f32x4;
typedef __attribute__((ext_vector_type(2))) float f32x2;
typedef __attribute__((ext_vector_type(4))) unsigned u32x4;
typedef __attribute__((ext_vector_type(2))) unsigned u32x2;
#define LAS __attribute__((address_space(3)))

__device__ __forceinline__ float swap_max(float x) {
  auto r = __builtin_amdgcn_permlane32_swap(__float_as_uint(x), __float_as_uint(x), false, false);
  return fmaxf(__uint_as_float(r[0]), __uint_as_float(r[1]));
}
__device__ __forceinline__ float swap_sum(float x) {
  auto r = __builtin_amdgcn_permlane32_swap(__float_as_uint(x), __float_as_uint(x), false, false);
  return __uint_as_float(r[0]) + __uint_as_float(r[1]);
}
__device__ __forceinline__ unsigned pack2(float a, float b) {
  bf16x2 v = {(__bf16)a, (__bf16)b};
  return __builtin_bit_cast(unsigned, v);
}
template <int CTRL> __device__ __forceinline__ int dpp_add(int x) { return x + __builtin_amdgcn_update_dpp(0, x, CTRL, 0xf, 0xf, true); }
__device__ __forceinline__ int to_sgpr(int v) {
  int r;
  asm volatile("s_nop 1\n\tv_readfirstlane_b32 %0, %1\n\ts_nop 1" : "=s"(r) : "v"(v));
  return r;
}
#define LGKM0() asm volatile("s_waitcnt lgkmcnt(0)" ::: "memory")
#define VMWAIT(n) asm volatile("s_waitcnt vmcnt(" #n ")" ::: "memory")
#define CBAR() asm volatile("" ::: "memory")
template <int SLEEP> __device__ __forceinline__ void wait_above(unsigned addr, unsigned u) {
  unsigned v, cnt;
  if (SLEEP)
    asm volatile("s_mov_b32 %1, 0x800000\n\ts_branch 2f\n"
                 "1:\n\ts_sleep %4\n"
                 "2:\n\tds_read_b32 %0, %2\n\ts_sub_u32 %1, %1, 1\n\ts_waitcnt lgkmcnt(0)\n\t"
                 "v_cmp_ge_u32 vcc, %3, %0\n\ts_cbranch_scc1 3f\n\ts_nop 1\n\ts_cbranch_vccnz 1b\n3:"
                 : "=&v"(v), "=&s"(cnt) : "v"(addr), "s"(u), "n"(SLEEP) : "memory", "scc", "vcc");
  else
    asm volatile("s_mov_b32 %1, 0x800000\n"
                 "1:\n\tds_read_b32 %0, %2\n\ts_sub_u32 %1, %1, 1\n\ts_waitcnt lgkmcnt(0)\n\t"
                 "v_cmp_ge_u32 vcc, %3, %0\n\ts_cbranch_scc1 3f\n\ts_nop 1\n\ts_cbranch_vccnz 1b\n3:"
                 : "=&v"(v), "=&s"(cnt) : "v"(addr), "s"(u) : "memory", "scc", "vcc");
}

__device__ __forceinline__ void wait_above_v(unsigned addr, unsigned u) {
  unsigned v, cnt;
  asm volatile("s_mov_b32 %1, 0x800000\n"
               "1:\n\tds_read_b32 %0, %2\n\ts_sub_u32 %1, %1, 1\n\ts_waitcnt lgkmcnt(0)\n\t"
               "v_cmp_ge_u32 vcc, %3, %0\n\ts_cbranch_scc1 3f\n\ts_nop 1\n\ts_cbranch_vccnz 1b\n3:"
               : "=&v"(v), "=&s"(cnt) : "v"(addr), "v"(u) : "memory", "scc", "vcc");
}

__global__ void __launch_bounds__(512, 1) __attribute__((amdgpu_num_vgpr(64)))
paged_attn(const void* __restrict__ qp, const void* __restrict__ kp, const void* __restrict__ vp, const int* __restrict__ bt,
           const int* __restrict__ lens, void* __restrict__ outp, unsigned kvbytes, unsigned nphys) {
  extern __shared__ __attribute__((aligned(16))) char smem[];
  LAS char* lds = (LAS char*)smem;
  const int lane0 = threadIdx.x & 63;
  const int wv = __builtin_amdgcn_readfirstlane(threadIdx.x >> 6);
  const int g = blockIdx.x;
  const int h = g >> 4;
  constexpr float CS = 0.08838834764831845f * 1.4426950408889634f;
  volatile LAS unsigned* ctl = (volatile LAS unsigned*)(lds + LDS_CTL);

  const unsigned lbase = (unsigned)(size_t)lds;

  unsigned lraw, first;
  u32x4 lq;
  {
    asm volatile("global_load_dword %0, %3, %4\n\t"
                 "global_load_dwordx4 %1, %5, %4\n\t"
                 "global_load_dword %2, %6, %7"
                 : "=&v"(lraw), "=&v"(lq), "=&v"(first)
                 : "v"((lane0 & 15) * 4), "s"(lens), "v"(lane0 & 48), "v"((lane0 & 15) * (wv < 2 ? NBPS * 4 : 0) + (lane0 >> 4) * 8 + (wv & 1) * 4), "s"(bt),
                   "s"(qp), "s"(kp), "s"(vp), "s"(outp), "s"(kvbytes), "s"(nphys)
                 : "memory");
    __builtin_amdgcn_sched_barrier(0);
  }
  if (wv >= 4) {
    if (wv == 7) {
      const __amdgpu_buffer_rsrc_t rb = __builtin_amdgcn_make_buffer_rsrc((void*)bt, 0, NB * NBPS * 4, 0x00020000);
#define TBL4(SO) "buffer_load_dwordx4 %2, %1, " SO " offen lds\n\tbuffer_load_dwordx4 %2, %1, " SO " offen offset:1024 lds\n\t" \
                 "buffer_load_dwordx4 %2, %1, " SO " offen offset:2048 lds\n\tbuffer_load_dwordx4 %2, %1, " SO " offen offset:3072 lds\n\t"
      asm volatile("s_mov_b32 m0, %0\n\ts_nop 0\n\t" TBL4("0") "s_add_u32 m0, m0, 4096\n\ts_nop 0\n\t" TBL4("%3")
                   "s_add_u32 m0, m0, %6\n\ts_nop 0\n\t" TBL4("%4") "s_add_u32 m0, m0, 4096\n\ts_nop 0\n\t" TBL4("%5")
                   :: "s"(lbase + LDS_TBL), "s"(rb), "v"(lane0 * 16), "s"(4096), "s"(8192), "s"(12288), "n"(W_SIZE - 4096) : "memory", "scc");
    }
    unsigned long long pc;
    asm volatile("s_getpc_b64 %0" : "=s"(pc));
    const __amdgpu_buffer_rsrc_t rt = __builtin_amdgcn_make_buffer_rsrc(
        wv == 5 ? (void*)pc : wv == 4 ? (void*)vp : (void*)kp, 0, wv == 5 ? CODEPF : (wv != 7 && (g & 8)) ? (int)kvbytes : 0, 0x00020000);
    const unsigned mul = wv == 5 ? 512u : 16 * TLBG + 4224u, add = wv == 5 ? ((g >> 3) & 3) * 128 : (g >> 4) * (TLBG + 33024u);
    asm volatile("s_mov_b32 m0, %0\n\ts_nop 0\n\tbuffer_load_dword %1, %2, 0 offen lds"
                 :: "s"(lbase + LDS_DUMMY), "v"((lane0 & 31) * mul + add), "s"(rt) : "memory");
    *(LAS u32x4*)(lds + LDS_SRT + (threadIdx.x - 256) * 16) = u32x4{0u, 0u, 0u, 0u};
    if (threadIdx.x < 296) ctl[lane0] = ((lane0 >> 3) == CTL_FREE / 32) ? (lane0 & 7) : 0u;
    asm volatile("s_waitcnt lgkmcnt(0)\n\ts_barrier" ::: "memory");
  }
  if (wv < 2) __builtin_amdgcn_s_setprio(3);
  const unsigned rstride = wv < 2 ? 4096 : 256;
  unsigned vo0 = (lane0 >> 4) * rstride + (((unsigned)(lane0 & 15) ^ ((lane0 >> 4) << 2)) << 4) + (wv < 2 ? h * (HD * 2) : 0);
  unsigned vo1 = (vo0 + 4 * rstride - 1024) ^ 16, vo2 = (vo0 + 8 * rstride - 2048) ^ 32, vo3 = (vo0 + 12 * rstride - 3072) ^ 48;
  const unsigned want = (g & 8) ? 15 - (g & 7) : (g & 7);
  const unsigned dst0 = lbase + (wv < 2 ? LDS_RING : LDS_Q) + (wv & 1) * 4096;
  asm volatile("s_cmp_ge_u32 %8, 4\n\ts_cbranch_scc1 1f\n\ts_waitcnt vmcnt(0)\n1:\n\ts_waitcnt vmcnt(1)" : "+v"(lraw), "+v"(lq), "+v"(first), "+v"(vo0), "+v"(vo1), "+v"(vo2), "+v"(vo3) : "s"(dst0), "s"(wv) : "memory", "scc");
  unsigned lm1 = min(lraw - 1u, (unsigned)SKV - 1u);
  unsigned rank = 0;
  {
    const unsigned key = lm1 * 16 + (lane0 & 15);
#pragma unroll
    for (int j = 0; j < 4; ++j) rank += (min(lq[j] - 1u, (unsigned)SKV - 1u) * 16 + ((lane0 >> 4) * 4 + j) > key) ? 1u : 0u;
    auto r1 = __builtin_amdgcn_permlane16_swap(rank, rank, false, false);
    rank = r1[0] + r1[1];
    auto r2 = __builtin_amdgcn_permlane32_swap(rank, rank, false, false);
    rank = r2[0] + r2[1];
  }
  first = min(first, nphys - 1) * PAGE_BYTES;
  const int b = __builtin_ctz((unsigned)__builtin_amdgcn_ballot_w64(rank == want) | 0x8000u);
  unsigned long long tstart = 0;
  if (wv < 4) {
    const bool qok = (int)__builtin_amdgcn_readlane(lraw, b) > 0;
    const __amdgpu_buffer_rsrc_t r0 = __builtin_amdgcn_make_buffer_rsrc(wv < 2 ? (void*)kp : (void*)qp, 0,
                                                                        wv < 2 ? (int)kvbytes : qok ? NB * HQ * SQ * HD * 2 : 0, 0x00020000);
    const unsigned so = wv < 2 ? __builtin_amdgcn_readlane(first, b) : qok ? (unsigned)((b * HQ + 2 * h) * SQ * HD * 2) + (wv & 1) * 4096 : 0u;
    asm volatile("s_waitcnt vmcnt(0)\n\t"
                 "s_mov_b32 m0, %2\n\t"
                 "s_memrealtime %0\n\t"
                 "s_nop 2\n\t"
                 "buffer_load_dwordx4 %4, %1, %3 offen nt lds\n\t"
                 "buffer_load_dwordx4 %5, %1, %3 offen offset:1024 nt lds\n\t"
                 "buffer_load_dwordx4 %6, %1, %3 offen offset:2048 nt lds\n\t"
                 "buffer_load_dwordx4 %7, %1, %3 offen offset:3072 nt lds\n\t"
                 "s_waitcnt lgkmcnt(0)\n\t"
                 "s_barrier"
                 : "=&s"(tstart) : "s"(r0), "s"(dst0), "s"(so), "v"(vo0), "v"(vo1), "v"(vo2), "v"(vo3) : "memory");
  }
  asm volatile("" : "+v"(lm1), "+v"(rank), "+v"(first) :: "memory");
  __builtin_amdgcn_sched_barrier(0);
  const int bp = __builtin_ctz((unsigned)__builtin_amdgcn_ballot_w64(rank == 15 - want) | 0x8000u);
  const int len = __builtin_amdgcn_readlane(lm1, b) + 1, nown = (len + PAGE - 1) / PAGE, NT = (nown + 1) >> 1;
  const int npl = (__builtin_amdgcn_readlane(lm1, bp) >> 4) + 1, NTL = (npl + 1) >> 1;
  auto total = [&]() __attribute__((always_inline)) {
    int t = lm1 + 1;
    t = dpp_add<0x121>(t); t = dpp_add<0x122>(t); t = dpp_add<0x124>(t); t = dpp_add<0x128>(t);
    return t;
  };

  auto sort_pages = [&](int lane, int bb, int n, unsigned scr, unsigned dst) __attribute__((always_inline)) {
    u32x4 e = *(LAS u32x4*)(lds + LDS_TBL + (bb >> 3) * W_SIZE + (bb & 7) * 1024 + lane * 16);
    bool in[4];
#pragma unroll
    for (int j = 0; j < 4; ++j) {
      e[j] = min(e[j], nphys - 1);
      in[j] = 4 * lane + j >= 2 * U0 && 4 * lane + j < n - 1;
      __hip_atomic_fetch_or((LAS unsigned*)(lds + scr + ((e[j] >> 5) & 255) * 8), in[j] ? 1u << (e[j] & 31) : 0u, __ATOMIC_RELAXED, __HIP_MEMORY_SCOPE_WAVEFRONT);
    }
    CBAR();
    u32x4 a = *(LAS u32x4*)(lds + scr + lane * 32), c = *(LAS u32x4*)(lds + scr + lane * 32 + 16);
    const int n0 = __builtin_popcount(a[0]), n1 = n0 + __builtin_popcount(a[2]), n2 = n1 + __builtin_popcount(c[0]), n3 = n2 + __builtin_popcount(c[2]);
    int sc = n3;
    sc = dpp_add<0x111>(sc); sc = dpp_add<0x112>(sc); sc = dpp_add<0x114>(sc); sc = dpp_add<0x118>(sc);
    sc += __builtin_amdgcn_update_dpp(0, sc, 0x142, 0xa, 0xf, false);
    sc += __builtin_amdgcn_update_dpp(0, sc, 0x143, 0xc, 0xf, false);
    const bool ok = nphys <= 8192u && __builtin_amdgcn_readlane(sc, 63) == n - 1 - 2 * U0;
    sc += 2 * U0 - n3;
    a[1] = sc; a[3] = sc + n0; c[1] = sc + n1; c[3] = sc + n2;
    *(LAS u32x4*)(lds + scr + lane * 32) = a;
    *(LAS u32x4*)(lds + scr + lane * 32 + 16) = c;
    CBAR();
    u32x2 wp[4];
#pragma unroll
    for (int j = 0; j < 4; ++j) wp[j] = *(LAS u32x2*)(lds + scr + ((e[j] >> 5) & 255) * 8);
#pragma unroll
    for (int j = 0; j < 4; ++j) {
      const unsigned r = wp[j][1] + __builtin_popcount(wp[j][0] & ((1u << (e[j] & 31)) - 1u));
      *(LAS unsigned*)(lds + dst + ((ok && in[j]) ? r : 4 * lane + j) * 4) = e[j] * PAGE_BYTES;
    }
    CBAR();
  };

  if (__builtin_expect(wv < 2, 1)) {
    const int half = wv, lane = lane0;
    const unsigned vland = lbase + LDS_CTL + CTL_LAND + half * 4;
    const unsigned vfree = lbase + LDS_CTL + CTL_FREE + (lane & 7) * 4;
    const unsigned ring = dst0;
    unsigned l0 = __builtin_amdgcn_ds_bpermute((b + 16 * lane) * 4, first), l1 = 0;
    const unsigned a0 = lbase + LDS_SRT + 4096 + min(2 * lane + half, nown - 1) * 4, a1 = lbase + LDS_SRT + 4096 + min(2 * lane + half + 128, nown - 1) * 4;
    const int rot = (NT >= 3 * NC) ? NT % NC : 0;
    const unsigned vperm = (lane < NC ? (lane + rot >= NC ? lane + rot - NC : lane + rot) : lane) * 4;
    const int T16 = total() * PACE_X16;
    const int step = to_sgpr((int)((float)T16 * __builtin_amdgcn_rcpf((float)max(len, 32))));
    const int win = to_sgpr((int)((float)(((h & 3) == 1 ? WIN_C1 : WIN_TICKS) * 1024) * __builtin_amdgcn_rcpf((float)step) + 0.5f));
    const int hmin = to_sgpr((int)((float)(HALT_FROM * 256) * __builtin_amdgcn_rcpf((float)step)));
    const unsigned due0 = ((unsigned)tstart << 8) + half * (step >> 1);
    const int lrows = len - (2 * (NT - 1) + half) * PAGE;
    unsigned vf, vt, lc;
    asm volatile(
        "s_waitcnt lgkmcnt(0)\n\t"
        "s_memrealtime s[68:69]\n\t"
        "ds_read_b32 %[vf], %[vfree]\n\t"
        "s_mov_b32 s60, 1\n\t"
        "s_mov_b32 s61, 0\n\t"
        "s_mov_b32 s62, 0x1000000\n\t"
        "s_mov_b32 s66, 0\n\t"
        "s_min_u32 s67, %[nt], %[u0]\n\t"
        "s_mov_b32 s75, %[nrec]\n\t"
        "s_mov_b32 s72, 0x2000\n\t"
        "s_mov_b32 s73, 1\n\t"
        "s_mov_b32 s74, %[due1]\n\t"
        "s_mov_b32 s85, %[duev]\n\t"
        "s_mov_b32 s86, %[late]\n\t"
        "s_mov_b64 s[76:77], %[kp]\n\t"
        "s_mov_b32 s78, %[nrec]\n\t"
        "s_mov_b32 s79, 0x20000\n\t"
        "s_and_b32 s77, s77, 0xffff\n\t"
        "v_mov_b32 %[lc], %[l0]\n\t"
        "s_getpc_b64 s[70:71]\n"
        ".LldA%=:\n\t"
        "s_add_u32 s80, s70, .LldL%=-.LldA%=\n\t"
        "s_addc_u32 s71, s71, 0\n\t"
        "s_branch .LldF%=\n"
        ".LldX%=:\n\t"
        "s_cmp_lt_i32 s64, 0\n\t"
        "s_cbranch_scc0 .LldH%=\n"
        ".LldR%=:\n\t"
        "s_sub_u32 s62, s62, 1\n\t"
        "s_cbranch_scc1 .LldE%=\n"
        ".LldT%=:\n\t"
        "s_waitcnt lgkmcnt(0)\n\t"
        "s_getreg_b32 s64, hwreg(HW_REG_IB_STS)\n\t"
        "s_and_b32 s65, s64, 15\n\t"
        "s_bfe_u32 s64, s64, 0x20016\n\t"
        "s_lshl4_add_u32 s81, s64, s65\n\t"
        "s_cmp_gt_i32 s81, s61\n\t"
        "s_cbranch_scc1 .LldC%=\n\t"
        "s_add_u32 s64, s81, 3\n\t"
        "s_and_b32 s64, s64, 28\n\t"
        "s_add_u32 s70, s80, s64\n\t"
        "s_add_u32 s66, s66, s61\n\t"
        "s_sub_u32 s61, s64, 4\n\t"
        "s_sub_u32 s66, s66, s61\n\t"
        "v_mov_b32 %[vt], s66\n\t"
        "s_setpc_b64 s[70:71]\n\t"
        ".p2align 5\n"
        ".LldL%=:\n\t"
        "s_waitcnt vmcnt(0)\n\t"
        "s_waitcnt vmcnt(4)\n\t"
        "s_waitcnt vmcnt(8)\n\t"
        "s_waitcnt vmcnt(12)\n\t"
        "s_waitcnt vmcnt(16)\n\t"
        "s_waitcnt vmcnt(20)\n\t"
        "s_waitcnt vmcnt(24)\n\t"
        "s_waitcnt vmcnt(28)\n\t"
        "ds_write_b32 %[vland], %[vt]\n"
        ".LldC%=:\n\t"
        "v_cmp_eq_u32 vcc, s60, %[vf]\n\t"
        "s_lshl_b32 s64, s68, 8\n\t"
        "s_memrealtime s[68:69]\n\t"
        "ds_read_b32 %[vf], %[vfree]\n\t"
        "s_sub_u32 s64, s64, s74\n\t"
        "s_cmp_gt_u32 s64, s86\n\t"
        "s_cbranch_scc1 .LldX%=\n"
        ".LldI%=:\n\t"
        "s_cbranch_vccz .LldR%=\n\t"
        "s_cmp_gt_u32 s81, %[win]\n\t"
        "s_cbranch_scc1 .LldR%=\n\t"
        "buffer_load_dwordx4 %[o0], s[76:79], s63 offen nt lds\n\t"
        "s_add_u32 s74, s74, %[step]\n\t"
        "s_add_u32 s73, s73, 1\n\t"
        "buffer_load_dwordx4 %[o1], s[76:79], s63 offen offset:1024 nt lds\n\t"
        "s_add_u32 s60, s60, 1\n\t"
        "s_add_u32 s61, s61, 4\n\t"
        "s_add_u32 s86, s64, %[late]\n\t"
        "buffer_load_dwordx4 %[o2], s[76:79], s63 offen offset:2048 nt lds\n\t"
        "s_add_u32 s72, s72, 0x2000\n\t"
        "s_and_b32 s72, s72, 0xe000\n\t"
        "buffer_load_dwordx4 %[o3], s[76:79], s63 offen offset:3072 nt lds\n"
        ".LldF%=:\n\t"
        "v_readlane_b32 s63, %[lc], s73\n\t"
        "s_add_u32 m0, s72, %[ring]\n\t"
        "s_cmp_lg_u32 s60, s67\n\t"
        "s_cbranch_scc1 .LldT%=\n\t"
        "s_cmp_eq_u32 s73, %[nt]\n\t"
        "s_cbranch_scc1 .LldV%=\n\t"
        "s_cmp_lg_u32 s60, %[u0]\n\t"
        "s_cbranch_scc1 .LldG%=\n\t"
        "s_cmp_le_u32 %[nt], %[u0]\n\t"
        "s_cbranch_scc1 .LldG%=\n"
        ".LldS%=:\n\t"
        "ds_read_b32 %[lc], %[vland] offset:%[srt]\n\t"
        "ds_read_b32 %[l0], %[a0]\n\t"
        "ds_read_b32 %[l1], %[a1]\n\t"
        "s_sub_u32 s62, s62, 1\n\t"
        "s_cbranch_scc1 .LldE%=\n\t"
        "s_waitcnt lgkmcnt(0)\n\t"
        "v_readfirstlane_b32 s64, %[lc]\n\t"
        "s_cmp_eq_u32 s64, 0\n\t"
        "s_cbranch_scc1 .LldS%=\n\t"
        "s_max_i32 s65, %[lrows], 0\n\t"
        "s_lshl_b32 s65, s65, 12\n\t"
        "s_add_u32 s64, s64, s65\n\t"
        "s_sub_u32 s64, s64, 1\n\t"
        "s_cmp_lt_i32 %[lrows], 16\n\t"
        "s_cselect_b32 s75, s64, %[nrec]\n\t"
        "v_mov_b32 %[lc], %[l0]\n"
        ".LldG%=:\n\t"
        "s_cmp_lg_u32 s73, 64\n\t"
        "s_cbranch_scc1 .LldN%=\n\t"
        "v_mov_b32 %[lc], %[l1]\n"
        ".LldN%=:\n\t"
        "s_cmp_eq_u32 s73, %[ntm]\n\t"
        "s_cselect_b32 s78, s75, %[nrec]\n\t"
        "s_cselect_b32 s64, %[nt], %[ntm]\n\t"
        "s_min_u32 s65, s64, 64\n\t"
        "s_cmp_lt_u32 s73, 64\n\t"
        "s_cselect_b32 s64, s65, s64\n\t"
        "s_sub_u32 s67, s60, s73\n\t"
        "s_add_u32 s67, s67, s64\n\t"
        "s_branch .LldF%=\n"
        ".LldV%=:\n\t"
        "s_cmp_lg_u32 s60, %[nt]\n\t"
        "s_cbranch_scc1 .LldD%=\n\t"
        "s_mov_b64 s[76:77], %[vp]\n\t"
        "s_and_b32 s77, s77, 0xffff\n\t"
        "s_mov_b32 s73, 0\n\t"
        "s_mov_b32 s74, s85\n\t"
        "ds_bpermute_b32 %[lc], %[vperm], %[l0]\n\t"
        "v_xor_b32 %[o1], 16, %[o1]\n\t"
        "v_xor_b32 %[o2], 32, %[o2]\n\t"
        "v_xor_b32 %[o3], 48, %[o3]\n\t"
        "s_waitcnt lgkmcnt(0)\n\t"
        "s_branch .LldN%=\n"
        ".LldH%=:\n\t"
        "s_cmp_eq_u32 s66, %[fin]\n\t"
        "s_cbranch_scc1 .LldE%=\n\t"
        "s_cmp_lt_i32 s60, %[hmin]\n\t"
        "s_cbranch_scc1 .LldI%=\n\t"
        "s_add_u32 s74, s74, %[hshift]\n\t"
        "s_add_u32 s85, s85, %[hshift]\n\t"
        "s_branch .LldR%=\n"
        ".LldD%=:\n\t"
        "s_sub_u32 s74, s74, 0x10000000\n\t"
        "s_mov_b32 s86, 0\n\t"
        "s_mov_b32 s60, -1\n\t"
        "s_branch .LldT%=\n"
        ".LldE%=:\n\t"
        "s_waitcnt vmcnt(0) lgkmcnt(0)\n\t"
        : [vf] "=&v"(vf), [vt] "=&v"(vt), [lc] "=&v"(lc), [l0] "+&v"(l0), [l1] "+&v"(l1), [o1] "+&v"(vo1), [o2] "+&v"(vo2), [o3] "+&v"(vo3)
        : [vperm] "v"(vperm), [a0] "v"(a0), [a1] "v"(a1), [o0] "v"(vo0), [vland] "v"(vland), [vfree] "v"(vfree),
          [kp] "s"(kp), [vp] "s"(vp), [nrec] "s"(kvbytes), [nt] "s"(NT), [ring] "s"(ring),
          [step] "s"(step), [due1] "s"(due0 + step + SOFT * 256), [duev] "s"(due0 + (to_sgpr(T16) >> 5) + (SOFT + GAP_TICKS) * 256), [lrows] "s"(lrows), [ntm] "s"(NT - 1), [fin] "s"(8 * NT), [u0] "n"(U0), [win] "s"(win), [late] "n"(LATE_TICKS * 256), [hshift] "n"(HALT_SHIFT * 256), [hmin] "s"(hmin), [srt] "n"(CTL_SORT - CTL_LAND)
        : "memory", "scc", "vcc", "s60", "s61", "s62", "s63", "s64", "s65", "s66", "s67", "s68", "s69", "s70", "s71", "s72", "s73", "s74", "s75",
          "s76", "s77", "s78", "s79", "s80", "s81", "s85", "s86");
    asm volatile("s_endpgm" ::: "memory");
  }

  int lane = lane0;
  asm volatile("" : "+v"(lane));
  if (wv == 7) {
#pragma unroll 1
    for (int i = 0; i < 2; ++i) {
      sort_pages(lane, i ? bp : b, i ? npl : nown, LDS_SRT + i * 2048, i ? LDS_PLIST : LDS_SRT + 4096);
      if (i == 0) {
        const unsigned lo = *(LAS unsigned*)(lds + LDS_SRT + 4096 + (nown - 1) * 4) + 1;
        LGKM0();
        ctl[CTL_SORT / 4 + (lane & 1)] = lo;
      }
    }
  }
  if (wv < 4) {
    VMWAIT(0);
    ctl[CTL_QOK / 4 + (wv & 1)] = 1;
  }
  const int tot = total();
  const int w = wv - 2;
  const int r32 = lane & 31, hi = lane >> 5;
  const int nmine = (NT > w) ? (NT - w + NC - 1) / NC : 0;
  const unsigned aland = lbase + LDS_CTL + CTL_LAND + (lane & 1) * 4;
  const int tots = __builtin_amdgcn_readfirstlane(tot);
  const bool pf_on = npl * 256 * 64 > tots * PF_X64 && npl > nown && (h & 3) == 0;
  const unsigned pf_last = 2 * NT - to_sgpr((int)((float)(2 * NT * (PF_LEAD + 23 * NC)) * 4096.0f * __builtin_amdgcn_rcpf((float)(tot * PACE_X16))));
  const unsigned pf_ratio = to_sgpr((int)((float)NTL * 65536.0f * __builtin_amdgcn_rcpf((float)NT)));
  const unsigned pf_per = PF_PER + (g & 7) * PF_PSTEP, pf_inv = (65536 + pf_per - 1) / pf_per;
  auto prefetch = [&](unsigned op) {
    const unsigned d = pf_last - op;
    if (d >= 1024u) return;
    const unsigned r = d - ((d * pf_inv) >> 16) * pf_per;
    if (r >= PF_RUN) return;
    unsigned tl = 2 * NTL - 1 - NC - (((d - r) * pf_ratio) >> 16) - r;
    if (tl >= 2u * NTL) return;
    const bool isv = tl >= (unsigned)NTL;
    tl -= isv ? NTL : 0;
    const __amdgpu_buffer_rsrc_t rp = __builtin_amdgcn_make_buffer_rsrc(isv ? (void*)vp : (void*)kp, 0, (int)kvbytes, 0x00020000);
    unsigned ln = lane;
    asm volatile("" : "+v"(ln));
    const unsigned vo = (*(LAS unsigned*)(lds + LDS_PLIST + min(2 * tl + (ln >> 5), (unsigned)npl - 1) * 4) & 0x7fff0000u) + (h + 1) * (HD * 2) +
                        ((ln >> 1) & 15) * 4096 + (ln & 1) * 128;
    asm volatile("s_mov_b32 m0, %0\n\ts_nop 4\n\tbuffer_load_dword %2, %1, 0 offen lds" :: "s"(lbase + LDS_DUMMY), "s"(rp), "v"(vo) : "memory");
  };
  const int pos = (w + NC * 22 - NT) % NC;
  const unsigned palp = LDS_WREG + pos * W_SIZE + W_ALP + r32 * 4;
  const unsigned ppst = LDS_WREG + pos * W_SIZE + lane * 16;
  asm volatile("" ::: "v128", "v255");
  unsigned resc = 0;
  float m = -3.0e38f, mprev = -3.0e38f, l = 0.f;

  {
    const unsigned fk = (((unsigned)r32 & 3) << 2) | (((unsigned)r32 >> 2) & 3);
    const unsigned kaddr0 = LDS_RING + r32 * 256 + (((unsigned)hi ^ fk) << 4);
    bf16x8 qf[8];
    wait_above<1>(aland + (CTL_QOK - CTL_LAND), 0);
#pragma unroll
    for (int s = 0; s < 8; ++s) qf[s] = *(LAS bf16x8*)(lds + ((kaddr0 + LDS_Q - LDS_RING) ^ (s << 5)));
#pragma unroll 1
    for (int k = 0; k < nmine; ++k) {
      const unsigned t = w + NC * k;
      wait_above<1>(aland, 4 * t);
      bf16x8 kf[8];
#pragma unroll
      for (int s = 0; s < 8; ++s) kf[s] = *(LAS bf16x8*)(lds + ((kaddr0 + ((t & 7) << 13)) ^ (s << 5)));
      asm volatile("s_waitcnt lgkmcnt(0)" : "+v"(kf[0]), "+v"(kf[1]), "+v"(kf[2]), "+v"(kf[3]), "+v"(kf[4]), "+v"(kf[5]), "+v"(kf[6]), "+v"(kf[7]) :: "memory");
      ctl[CTL_FREE / 4 + (t & 7)] = t + 8;
      if (pf_on) prefetch(t);
      f32x16 sc;
#pragma unroll
      for (int r = 0; r < 16; ++r) sc[r] = 0.f;
#pragma unroll
      for (int s = 0; s < 8; ++s) sc = __builtin_amdgcn_mfma_f32_32x32x16_bf16(kf[s], qf[s], sc, 0, 0, 0);
      if ((int)t == NT - 1) {
        const int kb = 2 * t * PAGE + 4 * hi;
#pragma unroll
        for (int r = 0; r < 16; ++r) sc[r] = (kb + 8 * (r >> 2) + (r & 3) < len) ? sc[r] : -__builtin_inff();
      }
      float mx = sc[0];
#pragma unroll
      for (int r = 1; r < 16; ++r) mx = fmaxf(mx, sc[r]);
      mx = swap_max(mx);
      const float mn = fmaxf(m, mx);
      const float alpha = __builtin_amdgcn_exp2f((m - mn) * CS);
      mprev = m;
      m = mn;
      float ps = 0.f;
#pragma unroll
      for (int r = 0; r < 16; ++r) { sc[r] = __builtin_amdgcn_exp2f((sc[r] - mn) * CS); ps += sc[r]; }
      l = fmaf(l, alpha, ps);
      u32x4 pf[2];
#pragma unroll
      for (int s = 0; s < 2; ++s)
        pf[s] = u32x4{pack2(sc[8 * s], sc[8 * s + 1]), pack2(sc[8 * s + 2], sc[8 * s + 3]), pack2(sc[8 * s + 4], sc[8 * s + 5]), pack2(sc[8 * s + 6], sc[8 * s + 7])};
      if (__builtin_amdgcn_ballot_w64(alpha != 1.0f)) resc |= 1u << k;
      *(LAS float*)(lds + palp + k * 128) = alpha;
      if (k >= KREG || k == nmine - 1) {
        const unsigned dst = ppst + ((k == nmine - 1) ? W_LAST : (k - KREG) * 2048);
        *(LAS u32x4*)(lds + dst) = pf[0];
        *(LAS u32x4*)(lds + dst + 1024) = pf[1];
      } else {
        asm volatile("s_set_gpr_idx_on %8, gpr_idx(DST)\n\t"
                     "v_mov_b32 v128, %0\n\tv_mov_b32 v129, %1\n\tv_mov_b32 v130, %2\n\tv_mov_b32 v131, %3\n\t"
                     "v_mov_b32 v132, %4\n\tv_mov_b32 v133, %5\n\tv_mov_b32 v134, %6\n\tv_mov_b32 v135, %7\n\t"
                     "s_set_gpr_idx_off"
                     :: "v"(pf[0][0]), "v"(pf[0][1]), "v"(pf[0][2]), "v"(pf[0][3]), "v"(pf[1][0]), "v"(pf[1][1]), "v"(pf[1][2]), "v"(pf[1][3]), "s"(8 * k));
      }
    }
  }
  l = swap_sum(l);
  *(LAS f32x2*)(lds + LDS_ML + w * 256 + r32 * 8) = f32x2{m, l};
  LGKM0();
  ctl[CTL_MLOK / 4 + w] = 1;

  const unsigned tq = (lane & 15) >> 2, tp = lane & 3, tblk = (lane >> 4) & 1;
  const unsigned vaddr0 = LDS_RING + (4 * hi + tq) * 256 + ((tq ^ (pos & 3)) << 6) + ((2 * tblk + (tp >> 1)) << 4) + ((tp & 1) << 3);
  f32x16 o[4];
#pragma unroll
  for (int i = 0; i < 4; ++i)
#pragma unroll
    for (int r = 0; r < 16; ++r) o[i][r] = 0.f;
  auto weights = [&]() {
    wait_above<1>(lbase + LDS_CTL + CTL_MLOK + min(lane, NC - 1) * 4, 0);
    unsigned a = LDS_ML + r32 * 8;
    asm volatile("" : "+v"(a) :: "memory");
    float M = -3.0e38f, L = 0.f;
    f32x2 ml[NC];
#pragma unroll
    for (int i = 0; i < NC; ++i) { ml[i] = *(LAS f32x2*)(lds + a + i * 256); M = fmaxf(M, ml[i][0]); }
#pragma unroll
    for (int i = 0; i < NC; ++i) L = fmaf(__builtin_amdgcn_exp2f((ml[i][0] - M) * CS), ml[i][1], L);
    const float inv = 1.0f / L;
    *(LAS float*)(lds + (a >> 1) + (LDS_WT - LDS_ML / 2) + pos * 128) = __builtin_amdgcn_exp2f((mprev - M) * CS) * inv;
    const float ct = __builtin_amdgcn_exp2f((m - M) * CS) * inv;
#pragma unroll
    for (int s = 0; s < 2; ++s) {
      u32x4 x = *(LAS u32x4*)(lds + ppst + W_LAST + s * 1024);
#pragma unroll
      for (int e = 0; e < 4; ++e) x[e] = pack2(__uint_as_float(x[e] << 16) * ct, __uint_as_float(x[e] & 0xffff0000u) * ct);
      *(LAS u32x4*)(lds + ppst + W_LAST + s * 1024) = x;
    }
    LGKM0();
    ctl[CTL_WTOK / 4 + w] = 1;
  };
  resc &= ~1u;
  if (nmine <= 3) weights();
#pragma unroll 1
  for (int k = 0; k < nmine - 1; ++k) {
    if (k == 2) weights();
    if ((resc >> k) & 1) {
      const float alpha = *(LAS float*)(lds + palp + k * 128);
#pragma unroll
      for (int i = 0; i < 4; ++i)
#pragma unroll
        for (int r = 0; r < 16; ++r) o[i][r] *= alpha;
    }
    const unsigned u = NT + ((k == 0 && NT >= 3 * NC) ? pos : w + NC * k);
    u32x4 pf[2];
    if (k >= KREG) {
      pf[0] = *(LAS u32x4*)(lds + ppst + (k - KREG) * 2048);
      pf[1] = *(LAS u32x4*)(lds + ppst + (k - KREG) * 2048 + 1024);
    } else {
      asm volatile("s_set_gpr_idx_on %8, gpr_idx(SRC0)\n\t"
                   "v_mov_b32 %0, v128\n\tv_mov_b32 %1, v129\n\tv_mov_b32 %2, v130\n\tv_mov_b32 %3, v131\n\t"
                   "v_mov_b32 %4, v132\n\tv_mov_b32 %5, v133\n\tv_mov_b32 %6, v134\n\tv_mov_b32 %7, v135\n\t"
                   "s_set_gpr_idx_off\n\ts_nop 1"
                   : "=v"(pf[0][0]), "=v"(pf[0][1]), "=v"(pf[0][2]), "=v"(pf[0][3]), "=v"(pf[1][0]), "=v"(pf[1][1]), "=v"(pf[1][2]), "=v"(pf[1][3])
                   : "s"(8 * k));
    }
    asm volatile("" : "+v"(pf[0]), "+v"(pf[1]), "+v"(o[0]), "+v"(o[1]), "+v"(o[2]), "+v"(o[3]));
    if (k == nmine - 2) wait_above<1>(lbase + LDS_CTL + CTL_WTOK + min(lane, NC - 1) * 4, 0);
    wait_above<1>(aland, 4 * u);
    bf16x8 vf[4][2];
#pragma unroll
    for (int i = 0; i < 4; ++i) {
      const unsigned a = (vaddr0 + ((u & 7) << 13)) ^ (i << 6);
#pragma unroll
      for (int s = 0; s < 2; ++s) {
        const s16x4 lo = __builtin_amdgcn_ds_read_tr16_b64_v4i16((LAS s16x4*)(lds + a + 4096 * s));
        const s16x4 up = __builtin_amdgcn_ds_read_tr16_b64_v4i16((LAS s16x4*)(lds + a + 4096 * s + 2048));
        vf[i][s] = __builtin_bit_cast(bf16x8, __builtin_shufflevector(lo, up, 0, 1, 2, 3, 4, 5, 6, 7));
      }
    }
    asm volatile("s_waitcnt lgkmcnt(0)" : "+v"(vf[0][0]), "+v"(vf[0][1]), "+v"(vf[1][0]), "+v"(vf[1][1]), "+v"(vf[2][0]), "+v"(vf[2][1]), "+v"(vf[3][0]), "+v"(vf[3][1]) :: "memory");
    ctl[CTL_FREE / 4 + (u & 7)] = u + 8;
    if (pf_on) prefetch(u);
#pragma unroll
    for (int s = 0; s < 2; ++s)
#pragma unroll
      for (int i = 0; i < 4; ++i) o[i] = __builtin_amdgcn_mfma_f32_32x32x16_bf16(vf[i][s], __builtin_bit_cast(bf16x8, pf[s]), o[i], 0, 0, 0);
  }

  {
    unsigned l16 = lane * 16;
    asm volatile("" : "+v"(l16));
    const unsigned l4 = l16 >> 2, awt = LDS_WT + (l4 & 127);
    auto put = [&](unsigned dst, const f32x16& x) {
#pragma unroll
      for (int j = 0; j < 4; ++j) *(LAS f32x4*)(lds + dst + j * 1024) = f32x4{x[4 * j], x[4 * j + 1], x[4 * j + 2], x[4 * j + 3]};
    };
    float co[6];
    bf16x8 pl[NC][2];
    auto ld_p = [&](int p) {
      pl[p][0] = *(LAS bf16x8*)(lds + LDS_WREG + p * W_SIZE + W_LAST + l16);
      pl[p][1] = *(LAS bf16x8*)(lds + LDS_WREG + p * W_SIZE + W_LAST + l16 + 1024);
    };
#pragma unroll
    for (int i = 0; i < 4; ++i) co[i] = *(LAS float*)(lds + awt + ((pos & 3) ^ i) * 128);
    co[4] = *(LAS float*)(lds + awt + 4 * 128);
    co[5] = *(LAS float*)(lds + awt + 5 * 128);
    ld_p(0); ld_p(1);
    asm volatile("" : "+v"(l16) :: "memory");
    if (nmine > 1) {
      if (pos > 3) put(LDS_RING + ((2 * NT - 2 * NC + pos) & 7) * 8192 + l16, o[0]);
#pragma unroll
      for (int i = 1; i < 4; ++i) put(LDS_WREG + pos * W_SIZE + (i - 1) * 4096 + l16, o[i]);
      LGKM0();
      ctl[CTL_ODONE / 4 + pos] = 1;
    }
    if (pos < 4) {
      f32x16 of = o[0], ot;
#pragma unroll
      for (int r = 0; r < 16; ++r) ot[r] = 0.f;
      auto src_o = [&](int p) -> unsigned {
        const int i = pos ^ (p & 3);
        return l16 + (i == 0 ? LDS_RING + ((2 * NT - 2 * NC + p) & 7) * 8192 : LDS_WREG + p * W_SIZE + (i - 1) * 4096);
      };
      auto fma_o = [&](unsigned src, float c) {
#pragma unroll
        for (int j = 0; j < 4; ++j) {
          const f32x4 x = *(LAS f32x4*)(lds + src + j * 1024);
#pragma unroll
          for (int e = 0; e < 4; ++e) of[4 * j + e] = fmaf(x[e], c, of[4 * j + e]);
        }
      };
      auto mma_t = [&](int p, bf16x8 p0, bf16x8 p1) {
        const unsigned a = vaddr0 + (((2 * NT - NC + p) & 7) << 13);
        bf16x8 vf[2];
#pragma unroll
        for (int s = 0; s < 2; ++s) {
          const s16x4 lo = __builtin_amdgcn_ds_read_tr16_b64_v4i16((LAS s16x4*)(lds + a + 4096 * s));
          const s16x4 up = __builtin_amdgcn_ds_read_tr16_b64_v4i16((LAS s16x4*)(lds + a + 4096 * s + 2048));
          vf[s] = __builtin_bit_cast(bf16x8, __builtin_shufflevector(lo, up, 0, 1, 2, 3, 4, 5, 6, 7));
        }
        ot = __builtin_amdgcn_mfma_f32_32x32x16_bf16(vf[0], p0, ot, 0, 0, 0);
        ot = __builtin_amdgcn_mfma_f32_32x32x16_bf16(vf[1], p1, ot, 0, 0, 0);
      };
      if (NT >= 2 * NC) {
#define PIN_OF() asm volatile("" : "+v"(of), "+v"(ot))
#pragma unroll
        for (int r = 0; r < 16; ++r) of[r] *= co[0];
        const bool wl = (l4 & 24) == 0;
        const unsigned ua = 4 * (2 * NT - NC);
        wait_above_v(wl ? aland : lbase + LDS_CTL + CTL_ODONE + (l4 & 12), wl ? ua : 0u);
        mma_t(0, pl[0][0], pl[0][1]);
#pragma unroll
        for (int i = 1; i < 4; ++i) fma_o(LDS_WREG + (pos ^ i) * W_SIZE + (i - 1) * 4096 + l16, co[i]);
        PIN_OF();
        ld_p(2); ld_p(3); ld_p(4); ld_p(5);
        wait_above_v(wl ? aland : lbase + LDS_CTL + CTL_ODONE + 16 + (l4 & 4), wl ? ua + 8 : 0u);
        mma_t(1, pl[1][0], pl[1][1]);
        mma_t(2, pl[2][0], pl[2][1]);
        fma_o(src_o(4), co[4]);
        fma_o(src_o(5), co[5]);
#pragma unroll
        for (int r = 0; r < 16; ++r) { ot[r] += of[r]; of[r] = 0.f; }
        asm volatile("" : "+v"(ot));
        wait_above<0>(aland, ua + 20);
        mma_t(3, pl[3][0], pl[3][1]);
        mma_t(4, pl[4][0], pl[4][1]);
        mma_t(5, pl[5][0], pl[5][1]);
        of = ot;
      } else {
        wait_above<1>(lbase + LDS_CTL + CTL_WTOK + min(l4, (NC - 1) * 4u), 0);
        const float cw = *(LAS float*)(lds + awt + pos * 128);
#pragma unroll
        for (int r = 0; r < 16; ++r) of[r] *= cw;
#pragma unroll 1
        for (int p = max(NC - NT, 0); p < NC; ++p) {
          if (p != pos && NT - 2 * NC + p >= 0) {
            wait_above<1>(lbase + LDS_CTL + CTL_ODONE + p * 4, 0);
            fma_o(src_o(p), *(LAS float*)(lds + awt + p * 128));
          }
          wait_above<1>(aland, 4 * (2 * NT - NC + p));
          mma_t(p, *(LAS bf16x8*)(lds + LDS_WREG + p * W_SIZE + W_LAST + l16), *(LAS bf16x8*)(lds + LDS_WREG + p * W_SIZE + W_LAST + l16 + 1024));
        }
#pragma unroll
        for (int r = 0; r < 16; ++r) of[r] += ot[r];
      }
      const __amdgpu_buffer_rsrc_t ro = __builtin_amdgcn_make_buffer_rsrc(outp, 0, NB * HQ * SQ * HD * 2, 0x00020000);
      const unsigned orow = (unsigned)(b * HQ + 2 * h) * SQ * HD * 2 + pos * 64 + ((l16 & 511) << 4) + ((l16 >> 5) & 16);
#pragma unroll
      for (int j = 0; j < 2; ++j) {
        const unsigned a0 = pack2(of[8 * j], of[8 * j + 1]), a1 = pack2(of[8 * j + 2], of[8 * j + 3]);
        const unsigned b0 = pack2(of[8 * j + 4], of[8 * j + 5]), b1 = pack2(of[8 * j + 6], of[8 * j + 7]);
        auto s0 = __builtin_amdgcn_permlane32_swap(a0, b0, false, false);
        auto s1 = __builtin_amdgcn_permlane32_swap(a1, b1, false, false);
        u32x4 val = {s0[0], s1[0], s0[1], s1[1]};
        __builtin_amdgcn_raw_buffer_store_b128(val, ro, orow + j * 32, 0, 0);
      }
    }
  }
}
}

extern "C" void kernel_launch(void* const* d_in, const int* in_sizes, int n_in, void* d_out, int out_size, void* d_ws,
                              size_t ws_size, hipStream_t stream) {
  (void)n_in; (void)out_size; (void)d_ws; (void)ws_size;
  const unsigned nphys = (unsigned)(in_sizes[1] / (PAGE * HKV * HD));
  const unsigned kvbytes = nphys * PAGE_BYTES;
  (void)hipFuncSetAttribute((const void*)paged_attn, hipFuncAttributeMaxDynamicSharedMemorySize, LDS_TOTAL);
  hipLaunchKernelGGL(paged_attn, dim3(256), dim3(512), LDS_TOTAL, stream, d_in[0], d_in[1], d_in[2], (const int*)d_in[3],
                     (const int*)d_in[4], d_out, kvbytes, nphys);
}
```

```cpp
#include <hip/hip_runtime.h>
#include <hip/hip_bf16.h>

#define PACE_X16 433
#define SOFT 15
#define GAP_TICKS 10
#define WIN_TICKS 82
#define WIN_C1 110
#define LATE_TICKS 100
#define HALT_SHIFT 240
#define HALT_FROM 400
#define ALIGN 1
#define ALIGN_OFF 0
#define CODEPF 11776
#define PF_LEAD 180
#define PF_X64 70
#define PF_PER 4
#define PF_PSTEP 2
#define PF_RUN 3

namespace {
constexpr int NB = 16, HQ = 32, HKV = 16, SQ = 16, HD = 128, PAGE = 16, NBPS = 256, SKV = NBPS * PAGE;
constexpr unsigned PAGE_BYTES = PAGE * HKV * HD * 2;
constexpr unsigned TLBG = 0x200000u;
constexpr int U0 = 4;
constexpr int NC = 6;
constexpr int KREG = 16, KLDS = 5;
static_assert(NC * (KREG + KLDS + 1) >= NBPS / 2, "P storage");
constexpr unsigned LDS_RING = 0;
constexpr unsigned LDS_WREG = 65536;
constexpr unsigned W_ALP = KLDS * 2048;
constexpr unsigned W_LAST = W_ALP + (KREG + KLDS + 1) * 128;
constexpr unsigned W_SIZE = W_LAST + 2048;
static_assert(W_LAST >= 3 * 4096 && W_SIZE % 256 == 0, "consumer region");
constexpr unsigned LDS_TBL = LDS_WREG + W_SIZE;
constexpr unsigned LDS_SRT = LDS_WREG + 3 * W_SIZE;
constexpr unsigned LDS_Q = LDS_WREG;
constexpr unsigned LDS_ML = LDS_WREG + NC * W_SIZE;
constexpr unsigned LDS_WT = LDS_ML + NC * 256;
constexpr unsigned LDS_CTL = LDS_WT + NC * 128;
constexpr unsigned CTL_LAND = 0;
constexpr unsigned CTL_ORG = 8;
constexpr unsigned CTL_QOK = 16;
constexpr unsigned CTL_SORT = 24;
constexpr unsigned CTL_FREE = 32;
constexpr unsigned CTL_MLOK = 64;
constexpr unsigned CTL_ODONE = 96;
constexpr unsigned CTL_WTOK = 128;
constexpr unsigned LDS_DUMMY = LDS_CTL + 256;
constexpr unsigned LDS_PLIST = LDS_DUMMY + 256;
constexpr unsigned LDS_TOTAL = LDS_PLIST + 1024;
static_assert(LDS_TOTAL <= 163840, "lds");

typedef __attribute__((ext_vector_type(8))) __bf16 bf16x8;
typedef __attribute__((ext_vector_type(2))) __bf16 bf16x2;
typedef __attribute__((ext_vector_type(4))) short s16x4;
typedef __attribute__((ext_vector_type(16))) float f32x16;
typedef __attribute__((ext_vector_type(4))) float f32x4;
typedef __attribute__((ext_vector_type(2))) float f32x2;
typedef __attribute__((ext_vector_type(4))) unsigned u32x4;
typedef __attribute__((ext_vector_type(2))) unsigned u32x2;
#define LAS __attribute__((address_space(3)))
#define GAS __attribute__((address_space(1)))

__device__ __forceinline__ float swap_max(float x) {
  auto r = __builtin_amdgcn_permlane32_swap(__float_as_uint(x), __float_as_uint(x), false, false);
  return fmaxf(__uint_as_float(r[0]), __uint_as_float(r[1]));
}
__device__ __forceinline__ float swap_sum(float x) {
  auto r = __builtin_amdgcn_permlane32_swap(__float_as_uint(x), __float_as_uint(x), false, false);
  return __uint_as_float(r[0]) + __uint_as_float(r[1]);
}
__device__ __forceinline__ unsigned pack2(float a, float b) {
  bf16x2 v = {(__bf16)a, (__bf16)b};
  return __builtin_bit_cast(unsigned, v);
}
template <int CTRL> __device__ __forceinline__ int dpp_add(int x) { return x + __builtin_amdgcn_update_dpp(0, x, CTRL, 0xf, 0xf, true); }
__device__ __forceinline__ int to_sgpr(int v) {
  int r;
  asm volatile("s_nop 1\n\tv_readfirstlane_b32 %0, %1\n\ts_nop 1" : "=s"(r) : "v"(v));
  return r;
}
#define LGKM0() asm volatile("s_waitcnt lgkmcnt(0)" ::: "memory")
#define VMWAIT(n) asm volatile("s_waitcnt vmcnt(" #n ")" ::: "memory")
#define CBAR() asm volatile("" ::: "memory")
template <int SLEEP> __device__ __forceinline__ void wait_above(unsigned addr, unsigned u) {
  unsigned v, cnt;
  if (SLEEP)
    asm volatile("s_mov_b32 %1, 0x800000\n\ts_branch 2f\n"
                 "1:\n\ts_sleep %4\n"
                 "2:\n\tds_read_b32 %0, %2\n\ts_sub_u32 %1, %1, 1\n\ts_waitcnt lgkmcnt(0)\n\t"
                 "v_cmp_ge_u32 vcc, %3, %0\n\ts_cbranch_scc1 3f\n\ts_nop 1\n\ts_cbranch_vccnz 1b\n3:"
                 : "=&v"(v), "=&s"(cnt) : "v"(addr), "s"(u), "n"(SLEEP) : "memory", "scc", "vcc");
  else
    asm volatile("s_mov_b32 %1, 0x800000\n"
                 "1:\n\tds_read_b32 %0, %2\n\ts_sub_u32 %1, %1, 1\n\ts_waitcnt lgkmcnt(0)\n\t"
                 "v_cmp_ge_u32 vcc, %3, %0\n\ts_cbranch_scc1 3f\n\ts_nop 1\n\ts_cbranch_vccnz 1b\n3:"
                 : "=&v"(v), "=&s"(cnt) : "v"(addr), "s"(u) : "memory", "scc", "vcc");
}

__device__ __forceinline__ void wait_above_v(unsigned addr, unsigned u) {
  unsigned v, cnt;
  asm volatile("s_mov_b32 %1, 0x800000\n"
               "1:\n\tds_read_b32 %0, %2\n\ts_sub_u32 %1, %1, 1\n\ts_waitcnt lgkmcnt(0)\n\t"
               "v_cmp_ge_u32 vcc, %3, %0\n\ts_cbranch_scc1 3f\n\ts_nop 1\n\ts_cbranch_vccnz 1b\n3:"
               : "=&v"(v), "=&s"(cnt) : "v"(addr), "v"(u) : "memory", "scc", "vcc");
}

__global__ void __launch_bounds__(512, 1) __attribute__((amdgpu_num_vgpr(64)))
paged_attn(const void* __restrict__ qp, const void* __restrict__ kp, const void* __restrict__ vp, const int* __restrict__ bt,
           const int* __restrict__ lens, void* __restrict__ outp, unsigned kvbytes, unsigned nphys, char* __restrict__ ws) {
  extern __shared__ __attribute__((aligned(16))) char smem[];
  LAS char* lds = (LAS char*)smem;
  const int lane0 = threadIdx.x & 63;
  const int wv = __builtin_amdgcn_readfirstlane(threadIdx.x >> 6);
  const int g = blockIdx.x;
  const int h = g >> 4;
  constexpr float CS = 0.08838834764831845f * 1.4426950408889634f;
  volatile LAS unsigned* ctl = (volatile LAS unsigned*)(lds + LDS_CTL);

  const unsigned lbase = (unsigned)(size_t)lds;

  unsigned lraw, first;
  u32x4 lq;
  {
    asm volatile("global_load_dword %0, %3, %4\n\t"
                 "global_load_dwordx4 %1, %5, %4\n\t"
                 "global_load_dword %2, %6, %7"
                 : "=&v"(lraw), "=&v"(lq), "=&v"(first)
                 : "v"((lane0 & 15) * 4), "s"(lens), "v"(lane0 & 48), "v"((lane0 & 15) * (wv < 2 ? NBPS * 4 : 0) + (lane0 >> 4) * 8 + (wv & 1) * 4), "s"(bt),
                   "s"(qp), "s"(kp), "s"(vp), "s"(outp), "s"(kvbytes), "s"(nphys)
                 : "memory");
    __builtin_amdgcn_sched_barrier(0);
  }
  if (wv >= 4) {
    if (wv == 7) {
      const __amdgpu_buffer_rsrc_t rb = __builtin_amdgcn_make_buffer_rsrc((void*)bt, 0, NB * NBPS * 4, 0x00020000);
#define TBL4(SO) "buffer_load_dwordx4 %2, %1, " SO " offen lds\n\tbuffer_load_dwordx4 %2, %1, " SO " offen offset:1024 lds\n\t" \
                 "buffer_load_dwordx4 %2, %1, " SO " offen offset:2048 lds\n\tbuffer_load_dwordx4 %2, %1, " SO " offen offset:3072 lds\n\t"
      asm volatile("s_mov_b32 m0, %0\n\ts_nop 0\n\t" TBL4("0") "s_add_u32 m0, m0, 4096\n\ts_nop 0\n\t" TBL4("%3")
                   "s_add_u32 m0, m0, %6\n\ts_nop 0\n\t" TBL4("%4") "s_add_u32 m0, m0, 4096\n\ts_nop 0\n\t" TBL4("%5")
                   :: "s"(lbase + LDS_TBL), "s"(rb), "v"(lane0 * 16), "s"(4096), "s"(8192), "s"(12288), "n"(W_SIZE - 4096) : "memory", "scc");
    }
    unsigned long long pc;
    asm volatile("s_getpc_b64 %0" : "=s"(pc));
    const __amdgpu_buffer_rsrc_t rt = __builtin_amdgcn_make_buffer_rsrc(
        wv == 5 ? (void*)pc : wv == 4 ? (void*)vp : (void*)kp, 0, wv == 5 ? CODEPF : (wv != 7 && (g & 8)) ? (int)kvbytes : 0, 0x00020000);
    const unsigned mul = wv == 5 ? 512u : 16 * TLBG + 4224u, add = wv == 5 ? ((g >> 3) & 3) * 128 : (g >> 4) * (TLBG + 33024u);
    asm volatile("s_mov_b32 m0, %0\n\ts_nop 0\n\tbuffer_load_dword %1, %2, 0 offen lds"
                 :: "s"(lbase + LDS_DUMMY), "v"((lane0 & 31) * mul + add), "s"(rt) : "memory");
    *(LAS u32x4*)(lds + LDS_SRT + (threadIdx.x - 256) * 16) = u32x4{0u, 0u, 0u, 0u};
    if (threadIdx.x < 296) ctl[lane0] = ((lane0 >> 3) == CTL_FREE / 32) ? (lane0 & 7) : 0u;
    asm volatile("s_waitcnt lgkmcnt(0)\n\ts_barrier" ::: "memory");
  }
  if (wv < 2) __builtin_amdgcn_s_setprio(3);
  const unsigned rstride = wv < 2 ? 4096 : 256;
  unsigned vo0 = (lane0 >> 4) * rstride + (((unsigned)(lane0 & 15) ^ ((lane0 >> 4) << 2)) << 4) + (wv < 2 ? h * (HD * 2) : 0);
  unsigned vo1 = (vo0 + 4 * rstride - 1024) ^ 16, vo2 = (vo0 + 8 * rstride - 2048) ^ 32, vo3 = (vo0 + 12 * rstride - 3072) ^ 48;
  const unsigned want = (g & 8) ? 15 - (g & 7) : (g & 7);
  const unsigned dst0 = lbase + (wv < 2 ? LDS_RING : LDS_Q) + (wv & 1) * 4096;
  asm volatile("s_cmp_ge_u32 %8, 4\n\ts_cbranch_scc1 1f\n\ts_waitcnt vmcnt(0)\n1:\n\ts_waitcnt vmcnt(1)" : "+v"(lraw), "+v"(lq), "+v"(first), "+v"(vo0), "+v"(vo1), "+v"(vo2), "+v"(vo3) : "s"(dst0), "s"(wv) : "memory", "scc");
  unsigned lm1 = min(lraw - 1u, (unsigned)SKV - 1u);
  unsigned rank = 0;
  {
    const unsigned key = lm1 * 16 + (lane0 & 15);
#pragma unroll
    for (int j = 0; j < 4; ++j) rank += (min(lq[j] - 1u, (unsigned)SKV - 1u) * 16 + ((lane0 >> 4) * 4 + j) > key) ? 1u : 0u;
    auto r1 = __builtin_amdgcn_permlane16_swap(rank, rank, false, false);
    rank = r1[0] + r1[1];
    auto r2 = __builtin_amdgcn_permlane32_swap(rank, rank, false, false);
    rank = r2[0] + r2[1];
  }
  first = min(first, nphys - 1) * PAGE_BYTES;
  const int b = __builtin_ctz((unsigned)__builtin_amdgcn_ballot_w64(rank == want) | 0x8000u);
  unsigned long long tstart = 0;
  if (wv < 4) {
    const bool qok = (int)__builtin_amdgcn_readlane(lraw, b) > 0;
    const __amdgpu_buffer_rsrc_t r0 = __builtin_amdgcn_make_buffer_rsrc(wv < 2 ? (void*)kp : (void*)qp, 0,
                                                                        wv < 2 ? (int)kvbytes : qok ? NB * HQ * SQ * HD * 2 : 0, 0x00020000);
    const unsigned so = wv < 2 ? __builtin_amdgcn_readlane(first, b) : qok ? (unsigned)((b * HQ + 2 * h) * SQ * HD * 2) + (wv & 1) * 4096 : 0u;
    asm volatile("s_waitcnt vmcnt(0)\n\t"
                 "s_mov_b32 m0, %2\n\t"
                 "s_memrealtime %0\n\t"
                 "s_nop 2\n\t"
                 "buffer_load_dwordx4 %4, %1, %3 offen nt lds\n\t"
                 "buffer_load_dwordx4 %5, %1, %3 offen offset:1024 nt lds\n\t"
                 "buffer_load_dwordx4 %6, %1, %3 offen offset:2048 nt lds\n\t"
                 "buffer_load_dwordx4 %7, %1, %3 offen offset:3072 nt lds\n\t"
                 "s_waitcnt lgkmcnt(0)\n\t"
                 "s_barrier"
                 : "=&s"(tstart) : "s"(r0), "s"(dst0), "s"(so), "v"(vo0), "v"(vo1), "v"(vo2), "v"(vo3) : "memory");
  }
  asm volatile("" : "+v"(lm1), "+v"(rank), "+v"(first) :: "memory");
  __builtin_amdgcn_sched_barrier(0);
  const int bp = __builtin_ctz((unsigned)__builtin_amdgcn_ballot_w64(rank == 15 - want) | 0x8000u);
  const int len = __builtin_amdgcn_readlane(lm1, b) + 1, nown = (len + PAGE - 1) / PAGE, NT = (nown + 1) >> 1;
  const int npl = (__builtin_amdgcn_readlane(lm1, bp) >> 4) + 1, NTL = (npl + 1) >> 1;
  auto total = [&]() __attribute__((always_inline)) {
    int t = lm1 + 1;
    t = dpp_add<0x121>(t); t = dpp_add<0x122>(t); t = dpp_add<0x124>(t); t = dpp_add<0x128>(t);
    return t;
  };

  auto sort_pages = [&](int lane, int bb, int n, unsigned scr, unsigned dst) __attribute__((always_inline)) {
    u32x4 e = *(LAS u32x4*)(lds + LDS_TBL + (bb >> 3) * W_SIZE + (bb & 7) * 1024 + lane * 16);
    bool in[4];
#pragma unroll
    for (int j = 0; j < 4; ++j) {
      e[j] = min(e[j], nphys - 1);
      in[j] = 4 * lane + j >= 2 * U0 && 4 * lane + j < n - 1;
      __hip_atomic_fetch_or((LAS unsigned*)(lds + scr + ((e[j] >> 5) & 255) * 8), in[j] ? 1u << (e[j] & 31) : 0u, __ATOMIC_RELAXED, __HIP_MEMORY_SCOPE_WAVEFRONT);
    }
    CBAR();
    u32x4 a = *(LAS u32x4*)(lds + scr + lane * 32), c = *(LAS u32x4*)(lds + scr + lane * 32 + 16);
    const int n0 = __builtin_popcount(a[0]), n1 = n0 + __builtin_popcount(a[2]), n2 = n1 + __builtin_popcount(c[0]), n3 = n2 + __builtin_popcount(c[2]);
    int sc = n3;
    sc = dpp_add<0x111>(sc); sc = dpp_add<0x112>(sc); sc = dpp_add<0x114>(sc); sc = dpp_add<0x118>(sc);
    sc += __builtin_amdgcn_update_dpp(0, sc, 0x142, 0xa, 0xf, false);
    sc += __builtin_amdgcn_update_dpp(0, sc, 0x143, 0xc, 0xf, false);
    const bool ok = nphys <= 8192u && __builtin_amdgcn_readlane(sc, 63) == n - 1 - 2 * U0;
    sc += 2 * U0 - n3;
    a[1] = sc; a[3] = sc + n0; c[1] = sc + n1; c[3] = sc + n2;
    *(LAS u32x4*)(lds + scr + lane * 32) = a;
    *(LAS u32x4*)(lds + scr + lane * 32 + 16) = c;
    CBAR();
    u32x2 wp[4];
#pragma unroll
    for (int j = 0; j < 4; ++j) wp[j] = *(LAS u32x2*)(lds + scr + ((e[j] >> 5) & 255) * 8);
#pragma unroll
    for (int j = 0; j < 4; ++j) {
      const unsigned r = wp[j][1] + __builtin_popcount(wp[j][0] & ((1u << (e[j] & 31)) - 1u));
      *(LAS unsigned*)(lds + dst + ((ok && in[j]) ? r : 4 * lane + j) * 4) = e[j] * PAGE_BYTES;
    }
    CBAR();
  };

  if (__builtin_expect(wv < 2, 1)) {
    const int half = wv, lane = lane0;
    const unsigned vland = lbase + LDS_CTL + CTL_LAND + half * 4;
    const unsigned vfree = lbase + LDS_CTL + CTL_FREE + (lane & 7) * 4;
    const unsigned ring = dst0;
    unsigned l0 = __builtin_amdgcn_ds_bpermute((b + 16 * lane) * 4, first), l1 = 0;
    const unsigned a0 = lbase + LDS_SRT + 4096 + min(2 * lane + half, nown - 1) * 4, a1 = lbase + LDS_SRT + 4096 + min(2 * lane + half + 128, nown - 1) * 4;
    const int rot = (NT >= 3 * NC) ? NT % NC : 0;
    const unsigned vperm = (lane < NC ? (lane + rot >= NC ? lane + rot - NC : lane + rot) : lane) * 4;
    const int T16 = total() * PACE_X16;
    const int step = to_sgpr((int)((float)T16 * __builtin_amdgcn_rcpf((float)max(len, 32))));
    const int win = to_sgpr((int)((float)(((h & 3) == 1 ? WIN_C1 : WIN_TICKS) * 1024) * __builtin_amdgcn_rcpf((float)step) + 0.5f));
    const int hmin = to_sgpr((int)((float)(HALT_FROM * 256) * __builtin_amdgcn_rcpf((float)step)));
    const unsigned due0 = ((unsigned)tstart << 8) + half * (step >> 1);
    const int lrows = len - (2 * (NT - 1) + half) * PAGE;
    unsigned vf, vt, lc;
    asm volatile(
        "s_waitcnt lgkmcnt(0)\n\t"
        "s_memrealtime s[68:69]\n\t"
        "ds_read_b32 %[vf], %[vfree]\n\t"
        "s_mov_b32 s60, 1\n\t"
        "s_mov_b32 s61, 0\n\t"
        "s_mov_b32 s62, 0x1000000\n\t"
        "s_mov_b32 s66, 0\n\t"
        "s_min_u32 s67, %[nt], %[u0]\n\t"
        "s_mov_b32 s75, %[nrec]\n\t"
        "s_mov_b32 s72, 0x2000\n\t"
        "s_mov_b32 s73, 1\n\t"
        "s_mov_b32 s74, %[due1]\n\t"
        "s_mov_b32 s85, %[duev]\n\t"
        "s_mov_b32 s86, %[late]\n\t"
        "s_mov_b64 s[76:77], %[kp]\n\t"
        "s_mov_b32 s78, %[nrec]\n\t"
        "s_mov_b32 s79, 0x20000\n\t"
        "s_and_b32 s77, s77, 0xffff\n\t"
        "v_mov_b32 %[lc], %[l0]\n\t"
        "s_getpc_b64 s[70:71]\n"
        ".LldA%=:\n\t"
        "s_add_u32 s80, s70, .LldL%=-.LldA%=\n\t"
        "s_addc_u32 s71, s71, 0\n\t"
        "s_branch .LldF%=\n"
        ".LldX%=:\n\t"
        "s_cmp_lt_i32 s64, 0\n\t"
        "s_cbranch_scc0 .LldH%=\n"
        ".LldR%=:\n\t"
        "s_sub_u32 s62, s62, 1\n\t"
        "s_cbranch_scc1 .LldE%=\n"
        ".LldT%=:\n\t"
        "s_waitcnt lgkmcnt(0)\n\t"
        "s_getreg_b32 s64, hwreg(HW_REG_IB_STS)\n\t"
        "s_and_b32 s65, s64, 15\n\t"
        "s_bfe_u32 s64, s64, 0x20016\n\t"
        "s_lshl4_add_u32 s81, s64, s65\n\t"
        "s_cmp_gt_i32 s81, s61\n\t"
        "s_cbranch_scc1 .LldC%=\n\t"
        "s_add_u32 s64, s81, 3\n\t"
        "s_and_b32 s64, s64, 28\n\t"
        "s_add_u32 s70, s80, s64\n\t"
        "s_add_u32 s66, s66, s61\n\t"
        "s_sub_u32 s61, s64, 4\n\t"
        "s_sub_u32 s66, s66, s61\n\t"
        "v_mov_b32 %[vt], s66\n\t"
        "s_setpc_b64 s[70:71]\n\t"
        ".p2align 5\n"
        ".LldL%=:\n\t"
        "s_waitcnt vmcnt(0)\n\t"
        "s_waitcnt vmcnt(4)\n\t"
        "s_waitcnt vmcnt(8)\n\t"
        "s_waitcnt vmcnt(12)\n\t"
        "s_waitcnt vmcnt(16)\n\t"
        "s_waitcnt vmcnt(20)\n\t"
        "s_waitcnt vmcnt(24)\n\t"
        "s_waitcnt vmcnt(28)\n\t"
        "ds_write_b32 %[vland], %[vt]\n"
        ".LldC%=:\n\t"
        "v_cmp_eq_u32 vcc, s60, %[vf]\n\t"
        "s_lshl_b32 s64, s68, 8\n\t"
        "s_memrealtime s[68:69]\n\t"
        "ds_read_b32 %[vf], %[vfree]\n\t"
        "s_sub_u32 s64, s64, s74\n\t"
        "s_cmp_gt_u32 s64, s86\n\t"
        "s_cbranch_scc1 .LldX%=\n"
        ".LldI%=:\n\t"
        "s_cbranch_vccz .LldR%=\n\t"
        "s_cmp_gt_u32 s81, %[win]\n\t"
        "s_cbranch_scc1 .LldR%=\n\t"
        "buffer_load_dwordx4 %[o0], s[76:79], s63 offen nt lds\n\t"
        "s_add_u32 s74, s74, %[step]\n\t"
        "s_add_u32 s73, s73, 1\n\t"
        "buffer_load_dwordx4 %[o1], s[76:79], s63 offen offset:1024 nt lds\n\t"
        "s_add_u32 s60, s60, 1\n\t"
        "s_add_u32 s61, s61, 4\n\t"
        "s_add_u32 s86, s64, %[late]\n\t"
        "buffer_load_dwordx4 %[o2], s[76:79], s63 offen offset:2048 nt lds\n\t"
        "s_add_u32 s72, s72, 0x2000\n\t"
        "s_and_b32 s72, s72, 0xe000\n\t"
        "buffer_load_dwordx4 %[o3], s[76:79], s63 offen offset:3072 nt lds\n"
        ".LldF%=:\n\t"
        "v_readlane_b32 s63, %[lc], s73\n\t"
        "s_add_u32 m0, s72, %[ring]\n\t"
        "s_cmp_lg_u32 s60, s67\n\t"
        "s_cbranch_scc1 .LldT%=\n\t"
        "s_cmp_eq_u32 s73, %[nt]\n\t"
        "s_cbranch_scc1 .LldV%=\n\t"
        "s_cmp_lg_u32 s60, %[u0]\n\t"
        "s_cbranch_scc1 .LldG%=\n\t"
        "s_cmp_le_u32 %[nt], %[u0]\n\t"
        "s_cbranch_scc1 .LldG%=\n"
        ".LldS%=:\n\t"
        "ds_read_b32 %[lc], %[vland] offset:%[srt]\n\t"
        "ds_read_b32 %[l0], %[a0]\n\t"
        "ds_read_b32 %[l1], %[a1]\n\t"
        "s_sub_u32 s62, s62, 1\n\t"
        "s_cbranch_scc1 .LldE%=\n\t"
        "s_waitcnt lgkmcnt(0)\n\t"
        "v_readfirstlane_b32 s64, %[lc]\n\t"
        "s_cmp_eq_u32 s64, 0\n\t"
        "s_cbranch_scc1 .LldS%=\n\t"
        "s_max_i32 s65, %[lrows], 0\n\t"
        "s_lshl_b32 s65, s65, 12\n\t"
        "s_add_u32 s64, s64, s65\n\t"
        "s_sub_u32 s64, s64, 1\n\t"
        "s_cmp_lt_i32 %[lrows], 16\n\t"
        "s_cselect_b32 s75, s64, %[nrec]\n\t"
        "v_mov_b32 %[lc], %[l0]\n"
        ".LldG%=:\n\t"
        "s_cmp_lg_u32 s73, 64\n\t"
        "s_cbranch_scc1 .LldN%=\n\t"
        "v_mov_b32 %[lc], %[l1]\n"
        ".LldN%=:\n\t"
        "s_cmp_eq_u32 s73, %[ntm]\n\t"
        "s_cselect_b32 s78, s75, %[nrec]\n\t"
        "s_cselect_b32 s64, %[nt], %[ntm]\n\t"
        "s_min_u32 s65, s64, 64\n\t"
        "s_cmp_lt_u32 s73, 64\n\t"
        "s_cselect_b32 s64, s65, s64\n\t"
        "s_sub_u32 s67, s60, s73\n\t"
        "s_add_u32 s67, s67, s64\n\t"
        "s_branch .LldF%=\n"
        ".LldV%=:\n\t"
        "s_cmp_lg_u32 s60, %[nt]\n\t"
        "s_cbranch_scc1 .LldD%=\n\t"
        "s_mov_b64 s[76:77], %[vp]\n\t"
        "s_and_b32 s77, s77, 0xffff\n\t"
        "s_mov_b32 s73, 0\n\t"
        "s_mov_b32 s74, s85\n\t"
        "ds_read_b32 %[lc], %[vland] offset:%[org]\n\t"
        "v_xor_b32 %[o1], 16, %[o1]\n\t"
        "v_xor_b32 %[o2], 32, %[o2]\n\t"
        "v_xor_b32 %[o3], 48, %[o3]\n\t"
        "s_waitcnt lgkmcnt(0)\n\t"
        "v_readfirstlane_b32 s64, %[lc]\n\t"
        "ds_bpermute_b32 %[lc], %[vperm], %[l0]\n\t"
        "s_sub_u32 s65, s64, %[ts]\n\t"
        "s_cmp_eq_u32 s64, 0\n\t"
        "s_cselect_b32 s65, 0, s65\n\t"
        "s_add_u32 s74, s74, s65\n\t"
        "s_sub_u32 s86, s86, s65\n\t"
        "s_max_i32 s86, s86, %[late]\n\t"
        "s_waitcnt lgkmcnt(0)\n\t"
        "s_branch .LldN%=\n"
        ".LldH%=:\n\t"
        "s_cmp_eq_u32 s66, %[fin]\n\t"
        "s_cbranch_scc1 .LldE%=\n\t"
        "s_cmp_lt_i32 s60, %[hmin]\n\t"
        "s_cbranch_scc1 .LldI%=\n\t"
        "s_add_u32 s74, s74, %[hshift]\n\t"
        "s_add_u32 s85, s85, %[hshift]\n\t"
        "s_branch .LldR%=\n"
        ".LldD%=:\n\t"
        "s_sub_u32 s74, s74, 0x10000000\n\t"
        "s_mov_b32 s86, 0\n\t"
        "s_mov_b32 s60, -1\n\t"
        "s_branch .LldT%=\n"
        ".LldE%=:\n\t"
        "s_waitcnt vmcnt(0) lgkmcnt(0)\n\t"
        : [vf] "=&v"(vf), [vt] "=&v"(vt), [lc] "=&v"(lc), [l0] "+&v"(l0), [l1] "+&v"(l1), [o1] "+&v"(vo1), [o2] "+&v"(vo2), [o3] "+&v"(vo3)
        : [vperm] "v"(vperm), [a0] "v"(a0), [a1] "v"(a1), [o0] "v"(vo0), [vland] "v"(vland), [vfree] "v"(vfree),
          [kp] "s"(kp), [vp] "s"(vp), [nrec] "s"(kvbytes), [nt] "s"(NT), [ring] "s"(ring),
          [step] "s"(step), [due1] "s"(due0 + step + SOFT * 256), [duev] "s"(due0 + (to_sgpr(T16) >> 5) + (SOFT + GAP_TICKS) * 256), [lrows] "s"(lrows), [ntm] "s"(NT - 1), [fin] "s"(8 * NT), [u0] "n"(U0), [win] "s"(win), [late] "n"(LATE_TICKS * 256), [hshift] "n"(HALT_SHIFT * 256), [hmin] "s"(hmin), [srt] "n"(CTL_SORT - CTL_LAND), [org] "n"(CTL_ORG - CTL_LAND), [ts] "s"((unsigned)tstart << 8)
        : "memory", "scc", "vcc", "s60", "s61", "s62", "s63", "s64", "s65", "s66", "s67", "s68", "s69", "s70", "s71", "s72", "s73", "s74", "s75",
          "s76", "s77", "s78", "s79", "s80", "s81", "s85", "s86");
    asm volatile("s_endpgm" ::: "memory");
  }

  int lane = lane0;
  asm volatile("" : "+v"(lane));
  if (wv == 7) {
#pragma unroll 1
    for (int i = 0; i < 2; ++i) {
      sort_pages(lane, i ? bp : b, i ? npl : nown, LDS_SRT + i * 2048, i ? LDS_PLIST : LDS_SRT + 4096);
      if (i == 0) {
        const unsigned lo = *(LAS unsigned*)(lds + LDS_SRT + 4096 + (nown - 1) * 4) + 1;
        LGKM0();
        ctl[CTL_SORT / 4 + (lane & 1)] = lo;
      }
    }
  }
  if (wv < 4) {
    VMWAIT(0);
    ctl[CTL_QOK / 4 + (wv & 1)] = 1;
  }
  GAS unsigned long long* torg = (GAS unsigned long long*)ws;
  if (ALIGN && wv == 2 && g < 8 && lane == 0) __hip_atomic_store(torg + g, tstart, __ATOMIC_RELAXED, __HIP_MEMORY_SCOPE_AGENT);
  const int tot = total();
  const int w = wv - 2;
  const int r32 = lane & 31, hi = lane >> 5;
  const int nmine = (NT > w) ? (NT - w + NC - 1) / NC : 0;
  const unsigned aland = lbase + LDS_CTL + CTL_LAND + (lane & 1) * 4;
  const int tots = __builtin_amdgcn_readfirstlane(tot);
  const bool pf_on = npl * 256 * 64 > tots * PF_X64 && npl > nown && (h & 3) == 0;
  const unsigned pf_last = 2 * NT - to_sgpr((int)((float)(2 * NT * (PF_LEAD + 23 * NC)) * 4096.0f * __builtin_amdgcn_rcpf((float)(tot * PACE_X16))));
  const unsigned pf_ratio = to_sgpr((int)((float)NTL * 65536.0f * __builtin_amdgcn_rcpf((float)NT)));
  const unsigned pf_per = PF_PER + (g & 7) * PF_PSTEP, pf_inv = (65536 + pf_per - 1) / pf_per;
  auto prefetch = [&](unsigned op) {
    const unsigned d = pf_last - op;
    if (d >= 1024u) return;
    const unsigned r = d - ((d * pf_inv) >> 16) * pf_per;
    if (r >= PF_RUN) return;
    unsigned tl = 2 * NTL - 1 - NC - (((d - r) * pf_ratio) >> 16) - r;
    if (tl >= 2u * NTL) return;
    const bool isv = tl >= (unsigned)NTL;
    tl -= isv ? NTL : 0;
    const __amdgpu_buffer_rsrc_t rp = __builtin_amdgcn_make_buffer_rsrc(isv ? (void*)vp : (void*)kp, 0, (int)kvbytes, 0x00020000);
    unsigned ln = lane;
    asm volatile("" : "+v"(ln));
    const unsigned vo = (*(LAS unsigned*)(lds + LDS_PLIST + min(2 * tl + (ln >> 5), (unsigned)npl - 1) * 4) & 0x7fff0000u) + (h + 1) * (HD * 2) +
                        ((ln >> 1) & 15) * 4096 + (ln & 1) * 128;
    asm volatile("s_mov_b32 m0, %0\n\ts_nop 4\n\tbuffer_load_dword %2, %1, 0 offen lds" :: "s"(lbase + LDS_DUMMY), "s"(rp), "v"(vo) : "memory");
  };
  const int pos = (w + NC * 22 - NT) % NC;
  const unsigned palp = LDS_WREG + pos * W_SIZE + W_ALP + r32 * 4;
  const unsigned ppst = LDS_WREG + pos * W_SIZE + lane * 16;
  asm volatile("" ::: "v128", "v255");
  unsigned resc = 0;
  float m = -3.0e38f, mprev = -3.0e38f, l = 0.f;

  {
    const unsigned fk = (((unsigned)r32 & 3) << 2) | (((unsigned)r32 >> 2) & 3);
    const unsigned kaddr0 = LDS_RING + r32 * 256 + (((unsigned)hi ^ fk) << 4);
    bf16x8 qf[8];
    wait_above<1>(aland + (CTL_QOK - CTL_LAND), 0);
#pragma unroll
    for (int s = 0; s < 8; ++s) qf[s] = *(LAS bf16x8*)(lds + ((kaddr0 + LDS_Q - LDS_RING) ^ (s << 5)));
#pragma unroll 1
    for (int k = 0; k < nmine; ++k) {
      const unsigned t = w + NC * k;
      wait_above<1>(aland, 4 * t);
      bf16x8 kf[8];
#pragma unroll
      for (int s = 0; s < 8; ++s) kf[s] = *(LAS bf16x8*)(lds + ((kaddr0 + ((t & 7) << 13)) ^ (s << 5)));
      asm volatile("s_waitcnt lgkmcnt(0)" : "+v"(kf[0]), "+v"(kf[1]), "+v"(kf[2]), "+v"(kf[3]), "+v"(kf[4]), "+v"(kf[5]), "+v"(kf[6]), "+v"(kf[7]) :: "memory");
      ctl[CTL_FREE / 4 + (t & 7)] = t + 8;
      if (pf_on) prefetch(t);
      unsigned long long tor = 0;
      if (ALIGN && t == 8 * NC) tor = __hip_atomic_load(torg + (lane & 7), __ATOMIC_RELAXED, __HIP_MEMORY_SCOPE_AGENT);
      f32x16 sc;
#pragma unroll
      for (int r = 0; r < 16; ++r) sc[r] = 0.f;
#pragma unroll
      for (int s = 0; s < 8; ++s) sc = __builtin_amdgcn_mfma_f32_32x32x16_bf16(kf[s], qf[s], sc, 0, 0, 0);
      if ((int)t == NT - 1) {
        const int kb = 2 * t * PAGE + 4 * hi;
#pragma unroll
        for (int r = 0; r < 16; ++r) sc[r] = (kb + 8 * (r >> 2) + (r & 3) < len) ? sc[r] : -__builtin_inff();
      }
      float mx = sc[0];
#pragma unroll
      for (int r = 1; r < 16; ++r) mx = fmaxf(mx, sc[r]);
      mx = swap_max(mx);
      const float mn = fmaxf(m, mx);
      const float alpha = __builtin_amdgcn_exp2f((m - mn) * CS);
      mprev = m;
      m = mn;
      float ps = 0.f;
#pragma unroll
      for (int r = 0; r < 16; ++r) { sc[r] = __builtin_amdgcn_exp2f((sc[r] - mn) * CS); ps += sc[r]; }
      l = fmaf(l, alpha, ps);
      u32x4 pf[2];
#pragma unroll
      for (int s = 0; s < 2; ++s)
        pf[s] = u32x4{pack2(sc[8 * s], sc[8 * s + 1]), pack2(sc[8 * s + 2], sc[8 * s + 3]), pack2(sc[8 * s + 4], sc[8 * s + 5]), pack2(sc[8 * s + 6], sc[8 * s + 7])};
      if (__builtin_amdgcn_ballot_w64(alpha != 1.0f)) resc |= 1u << k;
      if (ALIGN && t == 8 * NC) {
        const unsigned long long d = tor - tstart + 256;
        int e = (int)d;
        e = dpp_add<0x121>(e); e = dpp_add<0x122>(e); e = dpp_add<0x124>(e);
        if (__builtin_amdgcn_ballot_w64(d < 512) == ~0ull) ctl[CTL_ORG / 4 + (lane & 1)] = ((((unsigned)tstart - 256) << 8) + (e << 5) + ALIGN_OFF * 256) | 1u;
      }
      *(LAS float*)(lds + palp + k * 128) = alpha;
      if (k >= KREG || k == nmine - 1) {
        const unsigned dst = ppst + ((k == nmine - 1) ? W_LAST : (k - KREG) * 2048);
        *(LAS u32x4*)(lds + dst) = pf[0];
        *(LAS u32x4*)(lds + dst + 1024) = pf[1];
      } else {
        asm volatile("s_set_gpr_idx_on %8, gpr_idx(DST)\n\t"
                     "v_mov_b32 v128, %0\n\tv_mov_b32 v129, %1\n\tv_mov_b32 v130, %2\n\tv_mov_b32 v131, %3\n\t"
                     "v_mov_b32 v132, %4\n\tv_mov_b32 v133, %5\n\tv_mov_b32 v134, %6\n\tv_mov_b32 v135, %7\n\t"
                     "s_set_gpr_idx_off"
                     :: "v"(pf[0][0]), "v"(pf[0][1]), "v"(pf[0][2]), "v"(pf[0][3]), "v"(pf[1][0]), "v"(pf[1][1]), "v"(pf[1][2]), "v"(pf[1][3]), "s"(8 * k));
      }
    }
  }
  l = swap_sum(l);
  *(LAS f32x2*)(lds + LDS_ML + w * 256 + r32 * 8) = f32x2{m, l};
  LGKM0();
  ctl[CTL_MLOK / 4 + w] = 1;

  const unsigned tq = (lane & 15) >> 2, tp = lane & 3, tblk = (lane >> 4) & 1;
  const unsigned vaddr0 = LDS_RING + (4 * hi + tq) * 256 + ((tq ^ (pos & 3)) << 6) + ((2 * tblk + (tp >> 1)) << 4) + ((tp & 1) << 3);
  f32x16 o[4];
#pragma unroll
  for (int i = 0; i < 4; ++i)
#pragma unroll
    for (int r = 0; r < 16; ++r) o[i][r] = 0.f;
  auto weights = [&]() {
    wait_above<1>(lbase + LDS_CTL + CTL_MLOK + min(lane, NC - 1) * 4, 0);
    unsigned a = LDS_ML + r32 * 8;
    asm volatile("" : "+v"(a) :: "memory");
    float M = -3.0e38f, L = 0.f;
    f32x2 ml[NC];
#pragma unroll
    for (int i = 0; i < NC; ++i) { ml[i] = *(LAS f32x2*)(lds + a + i * 256); M = fmaxf(M, ml[i][0]); }
#pragma unroll
    for (int i = 0; i < NC; ++i) L = fmaf(__builtin_amdgcn_exp2f((ml[i][0] - M) * CS), ml[i][1], L);
    const float inv = 1.0f / L;
    *(LAS float*)(lds + (a >> 1) + (LDS_WT - LDS_ML / 2) + pos * 128) = __builtin_amdgcn_exp2f((mprev - M) * CS) * inv;
    const float ct = __builtin_amdgcn_exp2f((m - M) * CS) * inv;
#pragma unroll
    for (int s = 0; s < 2; ++s) {
      u32x4 x = *(LAS u32x4*)(lds + ppst + W_LAST + s * 1024);
#pragma unroll
      for (int e = 0; e < 4; ++e) x[e] = pack2(__uint_as_float(x[e] << 16) * ct, __uint_as_float(x[e] & 0xffff0000u) * ct);
      *(LAS u32x4*)(lds + ppst + W_LAST + s * 1024) = x;
    }
    LGKM0();
    ctl[CTL_WTOK / 4 + w] = 1;
  };
  resc &= ~1u;
  if (nmine <= 3) weights();
#pragma unroll 1
  for (int k = 0; k < nmine - 1; ++k) {
    if (k == 2) weights();
    if ((resc >> k) & 1) {
      const float alpha = *(LAS float*)(lds + palp + k * 128);
#pragma unroll
      for (int i = 0; i < 4; ++i)
#pragma unroll
        for (int r = 0; r < 16; ++r) o[i][r] *= alpha;
    }
    const unsigned u = NT + ((k == 0 && NT >= 3 * NC) ? pos : w + NC * k);
    u32x4 pf[2];
    if (k >= KREG) {
      pf[0] = *(LAS u32x4*)(lds + ppst + (k - KREG) * 2048);
      pf[1] = *(LAS u32x4*)(lds + ppst + (k - KREG) * 2048 + 1024);
    } else {
      asm volatile("s_set_gpr_idx_on %8, gpr_idx(SRC0)\n\t"
                   "v_mov_b32 %0, v128\n\tv_mov_b32 %1, v129\n\tv_mov_b32 %2, v130\n\tv_mov_b32 %3, v131\n\t"
                   "v_mov_b32 %4, v132\n\tv_mov_b32 %5, v133\n\tv_mov_b32 %6, v134\n\tv_mov_b32 %7, v135\n\t"
                   "s_set_gpr_idx_off\n\ts_nop 1"
                   : "=v"(pf[0][0]), "=v"(pf[0][1]), "=v"(pf[0][2]), "=v"(pf[0][3]), "=v"(pf[1][0]), "=v"(pf[1][1]), "=v"(pf[1][2]), "=v"(pf[1][3])
                   : "s"(8 * k));
    }
    asm volatile("" : "+v"(pf[0]), "+v"(pf[1]), "+v"(o[0]), "+v"(o[1]), "+v"(o[2]), "+v"(o[3]));
    if (k == nmine - 2) wait_above<1>(lbase + LDS_CTL + CTL_WTOK + min(lane, NC - 1) * 4, 0);
    wait_above<1>(aland, 4 * u);
    bf16x8 vf[4][2];
#pragma unroll
    for (int i = 0; i < 4; ++i) {
      const unsigned a = (vaddr0 + ((u & 7) << 13)) ^ (i << 6);
#pragma unroll
      for (int s = 0; s < 2; ++s) {
        const s16x4 lo = __builtin_amdgcn_ds_read_tr16_b64_v4i16((LAS s16x4*)(lds + a + 4096 * s));
        const s16x4 up = __builtin_amdgcn_ds_read_tr16_b64_v4i16((LAS s16x4*)(lds + a + 4096 * s + 2048));
        vf[i][s] = __builtin_bit_cast(bf16x8, __builtin_shufflevector(lo, up, 0, 1, 2, 3, 4, 5, 6, 7));
      }
    }
    asm volatile("s_waitcnt lgkmcnt(0)" : "+v"(vf[0][0]), "+v"(vf[0][1]), "+v"(vf[1][0]), "+v"(vf[1][1]), "+v"(vf[2][0]), "+v"(vf[2][1]), "+v"(vf[3][0]), "+v"(vf[3][1]) :: "memory");
    ctl[CTL_FREE / 4 + (u & 7)] = u + 8;
    if (pf_on) prefetch(u);
#pragma unroll
    for (int s = 0; s < 2; ++s)
#pragma unroll
      for (int i = 0; i < 4; ++i) o[i] = __builtin_amdgcn_mfma_f32_32x32x16_bf16(vf[i][s], __builtin_bit_cast(bf16x8, pf[s]), o[i], 0, 0, 0);
  }

  {
    unsigned l16 = lane * 16;
    asm volatile("" : "+v"(l16));
    const unsigned l4 = l16 >> 2, awt = LDS_WT + (l4 & 127);
    auto put = [&](unsigned dst, const f32x16& x) {
#pragma unroll
      for (int j = 0; j < 4; ++j) *(LAS f32x4*)(lds + dst + j * 1024) = f32x4{x[4 * j], x[4 * j + 1], x[4 * j + 2], x[4 * j + 3]};
    };
    float co[6];
    bf16x8 pl[NC][2];
    auto ld_p = [&](int p) {
      pl[p][0] = *(LAS bf16x8*)(lds + LDS_WREG + p * W_SIZE + W_LAST + l16);
      pl[p][1] = *(LAS bf16x8*)(lds + LDS_WREG + p * W_SIZE + W_LAST + l16 + 1024);
    };
#pragma unroll
    for (int i = 0; i < 4; ++i) co[i] = *(LAS float*)(lds + awt + ((pos & 3) ^ i) * 128);
    co[4] = *(LAS float*)(lds + awt + 4 * 128);
    co[5] = *(LAS float*)(lds + awt + 5 * 128);
    ld_p(0); ld_p(1);
    asm volatile("" : "+v"(l16) :: "memory");
    if (nmine > 1) {
      if (pos > 3) put(LDS_RING + ((2 * NT - 2 * NC + pos) & 7) * 8192 + l16, o[0]);
#pragma unroll
      for (int i = 1; i < 4; ++i) put(LDS_WREG + pos * W_SIZE + (i - 1) * 4096 + l16, o[i]);
      LGKM0();
      ctl[CTL_ODONE / 4 + pos] = 1;
    }
    if (pos < 4) {
      f32x16 of = o[0], ot;
#pragma unroll
      for (int r = 0; r < 16; ++r) ot[r] = 0.f;
      auto src_o = [&](int p) -> unsigned {
        const int i = pos ^ (p & 3);
        return l16 + (i == 0 ? LDS_RING + ((2 * NT - 2 * NC + p) & 7) * 8192 : LDS_WREG + p * W_SIZE + (i - 1) * 4096);
      };
      auto fma_o = [&](unsigned src, float c) {
#pragma unroll
        for (int j = 0; j < 4; ++j) {
          const f32x4 x = *(LAS f32x4*)(lds + src + j * 1024);
#pragma unroll
          for (int e = 0; e < 4; ++e) of[4 * j + e] = fmaf(x[e], c, of[4 * j + e]);
        }
      };
      auto mma_t = [&](int p, bf16x8 p0, bf16x8 p1) {
        const unsigned a = vaddr0 + (((2 * NT - NC + p) & 7) << 13);
        bf16x8 vf[2];
#pragma unroll
        for (int s = 0; s < 2; ++s) {
          const s16x4 lo = __builtin_amdgcn_ds_read_tr16_b64_v4i16((LAS s16x4*)(lds + a + 4096 * s));
          const s16x4 up = __builtin_amdgcn_ds_read_tr16_b64_v4i16((LAS s16x4*)(lds + a + 4096 * s + 2048));
          vf[s] = __builtin_bit_cast(bf16x8, __builtin_shufflevector(lo, up, 0, 1, 2, 3, 4, 5, 6, 7));
        }
        ot = __builtin_amdgcn_mfma_f32_32x32x16_bf16(vf[0], p0, ot, 0, 0, 0);
        ot = __builtin_amdgcn_mfma_f32_32x32x16_bf16(vf[1], p1, ot, 0, 0, 0);
      };
      if (NT >= 2 * NC) {
#define PIN_OF() asm volatile("" : "+v"(of), "+v"(ot))
#pragma unroll
        for (int r = 0; r < 16; ++r) of[r] *= co[0];
        const bool wl = (l4 & 24) == 0;
        const unsigned ua = 4 * (2 * NT - NC);
        wait_above_v(wl ? aland : lbase + LDS_CTL + CTL_ODONE + (l4 & 12), wl ? ua : 0u);
        mma_t(0, pl[0][0], pl[0][1]);
#pragma unroll
        for (int i = 1; i < 4; ++i) fma_o(LDS_WREG + (pos ^ i) * W_SIZE + (i - 1) * 4096 + l16, co[i]);
        PIN_OF();
        ld_p(2); ld_p(3); ld_p(4); ld_p(5);
        wait_above_v(wl ? aland : lbase + LDS_CTL + CTL_ODONE + 16 + (l4 & 4), wl ? ua + 8 : 0u);
        mma_t(1, pl[1][0], pl[1][1]);
        mma_t(2, pl[2][0], pl[2][1]);
        fma_o(src_o(4), co[4]);
        fma_o(src_o(5), co[5]);
#pragma unroll
        for (int r = 0; r < 16; ++r) { ot[r] += of[r]; of[r] = 0.f; }
        asm volatile("" : "+v"(ot));
        wait_above<0>(aland, ua + 20);
        mma_t(3, pl[3][0], pl[3][1]);
        mma_t(4, pl[4][0], pl[4][1]);
        mma_t(5, pl[5][0], pl[5][1]);
        of = ot;
      } else {
        wait_above<1>(lbase + LDS_CTL + CTL_WTOK + min(l4, (NC - 1) * 4u), 0);
        const float cw = *(LAS float*)(lds + awt + pos * 128);
#pragma unroll
        for (int r = 0; r < 16; ++r) of[r] *= cw;
#pragma unroll 1
        for (int p = max(NC - NT, 0); p < NC; ++p) {
          if (p != pos && NT - 2 * NC + p >= 0) {
            wait_above<1>(lbase + LDS_CTL + CTL_ODONE + p * 4, 0);
            fma_o(src_o(p), *(LAS float*)(lds + awt + p * 128));
          }
          wait_above<1>(aland, 4 * (2 * NT - NC + p));
          mma_t(p, *(LAS bf16x8*)(lds + LDS_WREG + p * W_SIZE + W_LAST + l16), *(LAS bf16x8*)(lds + LDS_WREG + p * W_SIZE + W_LAST + l16 + 1024));
        }
#pragma unroll
        for (int r = 0; r < 16; ++r) of[r] += ot[r];
      }
      const __amdgpu_buffer_rsrc_t ro = __builtin_amdgcn_make_buffer_rsrc(outp, 0, NB * HQ * SQ * HD * 2, 0x00020000);
      const unsigned orow = (unsigned)(b * HQ + 2 * h) * SQ * HD * 2 + pos * 64 + ((l16 & 511) << 4) + ((l16 >> 5) & 16);
#pragma unroll
      for (int j = 0; j < 2; ++j) {
        const unsigned a0 = pack2(of[8 * j], of[8 * j + 1]), a1 = pack2(of[8 * j + 2], of[8 * j + 3]);
        const unsigned b0 = pack2(of[8 * j + 4], of[8 * j + 5]), b1 = pack2(of[8 * j + 6], of[8 * j + 7]);
        auto s0 = __builtin_amdgcn_permlane32_swap(a0, b0, false, false);
        auto s1 = __builtin_amdgcn_permlane32_swap(a1, b1, false, false);
        u32x4 val = {s0[0], s1[0], s0[1], s1[1]};
        __builtin_amdgcn_raw_buffer_store_b128(val, ro, orow + j * 32, 0, 0);
      }
    }
  }
}
}

extern "C" void kernel_launch(void* const* d_in, const int* in_sizes, int n_in, void* d_out, int out_size, void* d_ws,
                              size_t ws_size, hipStream_t stream) {
  (void)n_in; (void)out_size; (void)ws_size;
  const unsigned nphys = (unsigned)(in_sizes[1] / (PAGE * HKV * HD));
  const unsigned kvbytes = nphys * PAGE_BYTES;
  (void)hipFuncSetAttribute((const void*)paged_attn, hipFuncAttributeMaxDynamicSharedMemorySize, LDS_TOTAL);
  hipLaunchKernelGGL(paged_attn, dim3(256), dim3(512), LDS_TOTAL, stream, d_in[0], d_in[1], d_in[2], (const int*)d_in[3],
                     (const int*)d_in[4], d_out, kvbytes, nphys, (char*)d_ws);
}
```

```cpp
#include <hip/hip_runtime.h>
#include <hip/hip_bf16.h>

#define PACE_X16 433
#define SOFT 15
#define GAP_TICKS 10
#define WIN_TICKS 87
#define WIN_C1 115
#define LATE_TICKS 100
#define HALT_SHIFT 240
#define HALT_FROM 400
#define CODEPF 11776
#define PF_LEAD 180
#define PF_X64 70
#define PF_PER 4
#define PF_PSTEP 2
#define PF_RUN 3

namespace {
constexpr int NB = 16, HQ = 32, HKV = 16, SQ = 16, HD = 128, PAGE = 16, NBPS = 256, SKV = NBPS * PAGE;
constexpr unsigned PAGE_BYTES = PAGE * HKV * HD * 2;
constexpr unsigned TLBG = 0x200000u;
constexpr int U0 = 4;
constexpr int NC = 6;
constexpr int KREG = 16, KLDS = 5;
static_assert(NC * (KREG + KLDS + 1) >= NBPS / 2, "P storage");
constexpr unsigned LDS_RING = 0;
constexpr unsigned LDS_WREG = 65536;
constexpr unsigned W_ALP = KLDS * 2048;
constexpr unsigned W_LAST = W_ALP + (KREG + KLDS + 1) * 128;
constexpr unsigned W_SIZE = W_LAST + 2048;
static_assert(W_LAST >= 3 * 4096 && W_SIZE % 256 == 0, "consumer region");
constexpr unsigned LDS_TBL = LDS_WREG + W_SIZE;
constexpr unsigned LDS_SRT = LDS_WREG + 3 * W_SIZE;
constexpr unsigned LDS_Q = LDS_WREG;
constexpr unsigned LDS_ML = LDS_WREG + NC * W_SIZE;
constexpr unsigned LDS_WT = LDS_ML + NC * 256;
constexpr unsigned LDS_CTL = LDS_WT + NC * 128;
constexpr unsigned CTL_LAND = 0;
constexpr unsigned CTL_QOK = 16;
constexpr unsigned CTL_SORT = 24;
constexpr unsigned CTL_FREE = 32;
constexpr unsigned CTL_MLOK = 64;
constexpr unsigned CTL_ODONE = 96;
constexpr unsigned CTL_WTOK = 128;
constexpr unsigned LDS_DUMMY = LDS_CTL + 256;
constexpr unsigned LDS_PLIST = LDS_DUMMY + 256;
constexpr unsigned LDS_TOTAL = LDS_PLIST + 1024;
static_assert(LDS_TOTAL <= 163840, "lds");

typedef __attribute__((ext_vector_type(8))) __bf16 bf16x8;
typedef __attribute__((ext_vector_type(2))) __bf16 bf16x2;
typedef __attribute__((ext_vector_type(4))) short s16x4;
typedef __attribute__((ext_vector_type(16))) float f32x16;
typedef __attribute__((ext_vector_type(4))) float f32x4;
typedef __attribute__((ext_vector_type(2))) float f32x2;
typedef __attribute__((ext_vector_type(4))) unsigned u32x4;
typedef __attribute__((ext_vector_type(2))) unsigned u32x2;
#define LAS __attribute__((address_space(3)))

__device__ __forceinline__ float swap_max(float x) {
  auto r = __builtin_amdgcn_permlane32_swap(__float_as_uint(x), __float_as_uint(x), false, false);
  return fmaxf(__uint_as_float(r[0]), __uint_as_float(r[1]));
}
__device__ __forceinline__ float swap_sum(float x) {
  auto r = __builtin_amdgcn_permlane32_swap(__float_as_uint(x), __float_as_uint(x), false, false);
  return __uint_as_float(r[0]) + __uint_as_float(r[1]);
}
__device__ __forceinline__ unsigned pack2(float a, float b) {
  bf16x2 v = {(__bf16)a, (__bf16)b};
  return __builtin_bit_cast(unsigned, v);
}
template <int CTRL> __device__ __forceinline__ int dpp_add(int x) { return x + __builtin_amdgcn_update_dpp(0, x, CTRL, 0xf, 0xf, true); }
__device__ __forceinline__ int to_sgpr(int v) {
  int r;
  asm volatile("s_nop 1\n\tv_readfirstlane_b32 %0, %1\n\ts_nop 1" : "=s"(r) : "v"(v));
  return r;
}
#define LGKM0() asm volatile("s_waitcnt lgkmcnt(0)" ::: "memory")
#define VMWAIT(n) asm volatile("s_waitcnt vmcnt(" #n ")" ::: "memory")
#define CBAR() asm volatile("" ::: "memory")
template <int SLEEP> __device__ __forceinline__ void wait_above(unsigned addr, unsigned u) {
  unsigned v, cnt;
  if (SLEEP)
    asm volatile("s_mov_b32 %1, 0x800000\n\ts_branch 2f\n"
                 "1:\n\ts_sleep %4\n"
                 "2:\n\tds_read_b32 %0, %2\n\ts_sub_u32 %1, %1, 1\n\ts_waitcnt lgkmcnt(0)\n\t"
                 "v_cmp_ge_u32 vcc, %3, %0\n\ts_cbranch_scc1 3f\n\ts_nop 1\n\ts_cbranch_vccnz 1b\n3:"
                 : "=&v"(v), "=&s"(cnt) : "v"(addr), "s"(u), "n"(SLEEP) : "memory", "scc", "vcc");
  else
    asm volatile("s_mov_b32 %1, 0x800000\n"
                 "1:\n\tds_read_b32 %0, %2\n\ts_sub_u32 %1, %1, 1\n\ts_waitcnt lgkmcnt(0)\n\t"
                 "v_cmp_ge_u32 vcc, %3, %0\n\ts_cbranch_scc1 3f\n\ts_nop 1\n\ts_cbranch_vccnz 1b\n3:"
                 : "=&v"(v), "=&s"(cnt) : "v"(addr), "s"(u) : "memory", "scc", "vcc");
}

__device__ __forceinline__ void wait_above_v(unsigned addr, unsigned u) {
  unsigned v, cnt;
  asm volatile("s_mov_b32 %1, 0x800000\n"
               "1:\n\tds_read_b32 %0, %2\n\ts_sub_u32 %1, %1, 1\n\ts_waitcnt lgkmcnt(0)\n\t"
               "v_cmp_ge_u32 vcc, %3, %0\n\ts_cbranch_scc1 3f\n\ts_nop 1\n\ts_cbranch_vccnz 1b\n3:"
               : "=&v"(v), "=&s"(cnt) : "v"(addr), "v"(u) : "memory", "scc", "vcc");
}

__global__ void __launch_bounds__(512, 1) __attribute__((amdgpu_num_vgpr(64)))
paged_attn(const void* __restrict__ qp, const void* __restrict__ kp, const void* __restrict__ vp, const int* __restrict__ bt,
           const int* __restrict__ lens, void* __restrict__ outp, unsigned kvbytes, unsigned nphys) {
  extern __shared__ __attribute__((aligned(16))) char smem[];
  LAS char* lds = (LAS char*)smem;
  const int lane0 = threadIdx.x & 63;
  const int wv = __builtin_amdgcn_readfirstlane(threadIdx.x >> 6);
  const int g = blockIdx.x;
  const int h = g >> 4;
  constexpr float CS = 0.08838834764831845f * 1.4426950408889634f;
  volatile LAS unsigned* ctl = (volatile LAS unsigned*)(lds + LDS_CTL);

  const unsigned lbase = (unsigned)(size_t)lds;

  unsigned lraw, first;
  u32x4 lq;
  {
    asm volatile("global_load_dword %0, %3, %4\n\t"
                 "global_load_dwordx4 %1, %5, %4\n\t"
                 "global_load_dword %2, %6, %7"
                 : "=&v"(lraw), "=&v"(lq), "=&v"(first)
                 : "v"((lane0 & 15) * 4), "s"(lens), "v"(lane0 & 48), "v"((lane0 & 15) * (wv < 2 ? NBPS * 4 : 0) + (lane0 >> 4) * 8 + (wv & 1) * 4), "s"(bt),
                   "s"(qp), "s"(kp), "s"(vp), "s"(outp), "s"(kvbytes), "s"(nphys)
                 : "memory");
    __builtin_amdgcn_sched_barrier(0);
  }
  if (wv >= 4) {
    if (wv == 7) {
      const __amdgpu_buffer_rsrc_t rb = __builtin_amdgcn_make_buffer_rsrc((void*)bt, 0, NB * NBPS * 4, 0x00020000);
#define TBL4(SO) "buffer_load_dwordx4 %2, %1, " SO " offen lds\n\tbuffer_load_dwordx4 %2, %1, " SO " offen offset:1024 lds\n\t" \
                 "buffer_load_dwordx4 %2, %1, " SO " offen offset:2048 lds\n\tbuffer_load_dwordx4 %2, %1, " SO " offen offset:3072 lds\n\t"
      asm volatile("s_mov_b32 m0, %0\n\ts_nop 0\n\t" TBL4("0") "s_add_u32 m0, m0, 4096\n\ts_nop 0\n\t" TBL4("%3")
                   "s_add_u32 m0, m0, %6\n\ts_nop 0\n\t" TBL4("%4") "s_add_u32 m0, m0, 4096\n\ts_nop 0\n\t" TBL4("%5")
                   :: "s"(lbase + LDS_TBL), "s"(rb), "v"(lane0 * 16), "s"(4096), "s"(8192), "s"(12288), "n"(W_SIZE - 4096) : "memory", "scc");
    }
    unsigned long long pc;
    asm volatile("s_getpc_b64 %0" : "=s"(pc));
    const __amdgpu_buffer_rsrc_t rt = __builtin_amdgcn_make_buffer_rsrc(
        wv == 5 ? (void*)pc : wv == 4 ? (void*)vp : (void*)kp, 0, wv == 5 ? CODEPF : (wv != 7 && (g & 8)) ? (int)kvbytes : 0, 0x00020000);
    const unsigned mul = wv == 5 ? 512u : 16 * TLBG + 4224u, add = wv == 5 ? ((g >> 3) & 3) * 128 : (g >> 4) * (TLBG + 33024u);
    asm volatile("s_mov_b32 m0, %0\n\ts_nop 0\n\tbuffer_load_dword %1, %2, 0 offen lds"
                 :: "s"(lbase + LDS_DUMMY), "v"((lane0 & 31) * mul + add), "s"(rt) : "memory");
    *(LAS u32x4*)(lds + LDS_SRT + (threadIdx.x - 256) * 16) = u32x4{0u, 0u, 0u, 0u};
    if (threadIdx.x < 296) ctl[lane0] = ((lane0 >> 3) == CTL_FREE / 32) ? (lane0 & 7) : 0u;
    asm volatile("s_waitcnt lgkmcnt(0)\n\ts_barrier" ::: "memory");
  }
  if (wv < 2) __builtin_amdgcn_s_setprio(3);
  const unsigned rstride = wv < 2 ? 4096 : 256;
  unsigned vo0 = (lane0 >> 4) * rstride + (((unsigned)(lane0 & 15) ^ ((lane0 >> 4) << 2)) << 4) + (wv < 2 ? h * (HD * 2) : 0);
  unsigned vo1 = (vo0 + 4 * rstride - 1024) ^ 16, vo2 = (vo0 + 8 * rstride - 2048) ^ 32, vo3 = (vo0 + 12 * rstride - 3072) ^ 48;
  const unsigned want = (g & 8) ? 15 - (g & 7) : (g & 7);
  const unsigned dst0 = lbase + (wv < 2 ? LDS_RING : LDS_Q) + (wv & 1) * 4096;
  asm volatile("s_cmp_ge_u32 %8, 4\n\ts_cbranch_scc1 1f\n\ts_waitcnt vmcnt(0)\n1:\n\ts_waitcnt vmcnt(1)" : "+v"(lraw), "+v"(lq), "+v"(first), "+v"(vo0), "+v"(vo1), "+v"(vo2), "+v"(vo3) : "s"(dst0), "s"(wv) : "memory", "scc");
  unsigned lm1 = min(lraw - 1u, (unsigned)SKV - 1u);
  unsigned rank = 0;
  {
    const unsigned key = lm1 * 16 + (lane0 & 15);
#pragma unroll
    for (int j = 0; j < 4; ++j) rank += (min(lq[j] - 1u, (unsigned)SKV - 1u) * 16 + ((lane0 >> 4) * 4 + j) > key) ? 1u : 0u;
    auto r1 = __builtin_amdgcn_permlane16_swap(rank, rank, false, false);
    rank = r1[0] + r1[1];
    auto r2 = __builtin_amdgcn_permlane32_swap(rank, rank, false, false);
    rank = r2[0] + r2[1];
  }
  first = min(first, nphys - 1) * PAGE_BYTES;
  const int b = __builtin_ctz((unsigned)__builtin_amdgcn_ballot_w64(rank == want) | 0x8000u);
  unsigned long long tstart = 0;
  if (wv < 4) {
    const bool qok = (int)__builtin_amdgcn_readlane(lraw, b) > 0;
    const __amdgpu_buffer_rsrc_t r0 = __builtin_amdgcn_make_buffer_rsrc(wv < 2 ? (void*)kp : (void*)qp, 0,
                                                                        wv < 2 ? (int)kvbytes : qok ? NB * HQ * SQ * HD * 2 : 0, 0x00020000);
    const unsigned so = wv < 2 ? __builtin_amdgcn_readlane(first, b) : qok ? (unsigned)((b * HQ + 2 * h) * SQ * HD * 2) + (wv & 1) * 4096 : 0u;
    asm volatile("s_waitcnt vmcnt(0)\n\t"
                 "s_mov_b32 m0, %2\n\t"
                 "s_memrealtime %0\n\t"
                 "s_nop 2\n\t"
                 "buffer_load_dwordx4 %4, %1, %3 offen nt lds\n\t"
                 "buffer_load_dwordx4 %5, %1, %3 offen offset:1024 nt lds\n\t"
                 "buffer_load_dwordx4 %6, %1, %3 offen offset:2048 nt lds\n\t"
                 "buffer_load_dwordx4 %7, %1, %3 offen offset:3072 nt lds\n\t"
                 "s_waitcnt lgkmcnt(0)\n\t"
                 "s_barrier"
                 : "=&s"(tstart) : "s"(r0), "s"(dst0), "s"(so), "v"(vo0), "v"(vo1), "v"(vo2), "v"(vo3) : "memory");
  }
  asm volatile("" : "+v"(lm1), "+v"(rank), "+v"(first) :: "memory");
  __builtin_amdgcn_sched_barrier(0);
  const int bp = __builtin_ctz((unsigned)__builtin_amdgcn_ballot_w64(rank == 15 - want) | 0x8000u);
  const int len = __builtin_amdgcn_readlane(lm1, b) + 1, nown = (len + PAGE - 1) / PAGE, NT = (nown + 1) >> 1;
  const int npl = (__builtin_amdgcn_readlane(lm1, bp) >> 4) + 1, NTL = (npl + 1) >> 1;
  auto total = [&]() __attribute__((always_inline)) {
    int t = lm1 + 1;
    t = dpp_add<0x121>(t); t = dpp_add<0x122>(t); t = dpp_add<0x124>(t); t = dpp_add<0x128>(t);
    return t;
  };

  auto sort_pages = [&](int lane, int bb, int n, unsigned scr, unsigned dst) __attribute__((always_inline)) {
    u32x4 e = *(LAS u32x4*)(lds + LDS_TBL + (bb >> 3) * W_SIZE + (bb & 7) * 1024 + lane * 16);
    bool in[4];
#pragma unroll
    for (int j = 0; j < 4; ++j) {
      e[j] = min(e[j], nphys - 1);
      in[j] = 4 * lane + j >= 2 * U0 && 4 * lane + j < n - 1;
      __hip_atomic_fetch_or((LAS unsigned*)(lds + scr + ((e[j] >> 5) & 255) * 8), in[j] ? 1u << (e[j] & 31) : 0u, __ATOMIC_RELAXED, __HIP_MEMORY_SCOPE_WAVEFRONT);
    }
    CBAR();
    u32x4 a = *(LAS u32x4*)(lds + scr + lane * 32), c = *(LAS u32x4*)(lds + scr + lane * 32 + 16);
    const int n0 = __builtin_popcount(a[0]), n1 = n0 + __builtin_popcount(a[2]), n2 = n1 + __builtin_popcount(c[0]), n3 = n2 + __builtin_popcount(c[2]);
    int sc = n3;
    sc = dpp_add<0x111>(sc); sc = dpp_add<0x112>(sc); sc = dpp_add<0x114>(sc); sc = dpp_add<0x118>(sc);
    sc += __builtin_amdgcn_update_dpp(0, sc, 0x142, 0xa, 0xf, false);
    sc += __builtin_amdgcn_update_dpp(0, sc, 0x143, 0xc, 0xf, false);
    const bool ok = nphys <= 8192u && __builtin_amdgcn_readlane(sc, 63) == n - 1 - 2 * U0;
    sc += 2 * U0 - n3;
    a[1] = sc; a[3] = sc + n0; c[1] = sc + n1; c[3] = sc + n2;
    *(LAS u32x4*)(lds + scr + lane * 32) = a;
    *(LAS u32x4*)(lds + scr + lane * 32 + 16) = c;
    CBAR();
    u32x2 wp[4];
#pragma unroll
    for (int j = 0; j < 4; ++j) wp[j] = *(LAS u32x2*)(lds + scr + ((e[j] >> 5) & 255) * 8);
#pragma unroll
    for (int j = 0; j < 4; ++j) {
      const unsigned r = wp[j][1] + __builtin_popcount(wp[j][0] & ((1u << (e[j] & 31)) - 1u));
      *(LAS unsigned*)(lds + dst + ((ok && in[j]) ? r : 4 * lane + j) * 4) = e[j] * PAGE_BYTES;
    }
    CBAR();
  };

  if (__builtin_expect(wv < 2, 1)) {
    const int half = wv, lane = lane0;
    const unsigned vland = lbase + LDS_CTL + CTL_LAND + half * 4;
    const unsigned vfree = lbase + LDS_CTL + CTL_FREE + (lane & 7) * 4;
    const unsigned ring = dst0;
    unsigned l0 = __builtin_amdgcn_ds_bpermute((b + 16 * lane) * 4, first), l1 = 0;
    const unsigned a0 = lbase + LDS_SRT + 4096 + min(2 * lane + half, nown - 1) * 4, a1 = lbase + LDS_SRT + 4096 + min(2 * lane + half + 128, nown - 1) * 4;
    const int rot = (NT >= 3 * NC) ? NT % NC : 0;
    const unsigned vperm = (lane < NC ? (lane + rot >= NC ? lane + rot - NC : lane + rot) : lane) * 4;
    const int T16 = total() * PACE_X16;
    const int step = to_sgpr((int)((float)T16 * __builtin_amdgcn_rcpf((float)max(len, 32))));
    const int win = to_sgpr((int)((float)(((h & 3) == 1 ? WIN_C1 : WIN_TICKS) * 1024) * __builtin_amdgcn_rcpf((float)step) + 0.5f));
    const int hmin = to_sgpr((int)((float)(HALT_FROM * 256) * __builtin_amdgcn_rcpf((float)step)));
    const unsigned due0 = ((unsigned)tstart << 8) + half * (step >> 1);
    const int lrows = len - (2 * (NT - 1) + half) * PAGE;
    unsigned vf, vt, lc;
    asm volatile(
        "s_waitcnt lgkmcnt(0)\n\t"
        "s_memrealtime s[68:69]\n\t"
        "ds_read_b32 %[vf], %[vfree]\n\t"
        "s_mov_b32 s60, 1\n\t"
        "s_mov_b32 s61, 0\n\t"
        "s_mov_b32 s62, 0x1000000\n\t"
        "s_mov_b32 s66, 0\n\t"
        "s_min_u32 s67, %[nt], %[u0]\n\t"
        "s_mov_b32 s75, %[nrec]\n\t"
        "s_mov_b32 s72, 0x2000\n\t"
        "s_mov_b32 s73, 1\n\t"
        "s_mov_b32 s74, %[due1]\n\t"
        "s_mov_b32 s85, %[duev]\n\t"
        "s_mov_b32 s86, %[late]\n\t"
        "s_mov_b64 s[76:77], %[kp]\n\t"
        "s_mov_b32 s78, %[nrec]\n\t"
        "s_mov_b32 s79, 0x20000\n\t"
        "s_and_b32 s77, s77, 0xffff\n\t"
        "v_mov_b32 %[lc], %[l0]\n\t"
        "s_getpc_b64 s[70:71]\n"
        ".LldA%=:\n\t"
        "s_add_u32 s80, s70, .LldL%=-.LldA%=\n\t"
        "s_addc_u32 s71, s71, 0\n\t"
        "s_branch .LldF%=\n"
        ".LldX%=:\n\t"
        "s_cmp_lt_i32 s64, 0\n\t"
        "s_cbranch_scc0 .LldH%=\n"
        ".LldR%=:\n\t"
        "s_sub_u32 s62, s62, 1\n\t"
        "s_cbranch_scc1 .LldE%=\n"
        ".LldT%=:\n\t"
        "s_waitcnt lgkmcnt(0)\n\t"
        "s_getreg_b32 s64, hwreg(HW_REG_IB_STS)\n\t"
        "s_and_b32 s65, s64, 15\n\t"
        "s_bfe_u32 s64, s64, 0x20016\n\t"
        "s_lshl4_add_u32 s81, s64, s65\n\t"
        "s_cmp_gt_i32 s81, s61\n\t"
        "s_cbranch_scc1 .LldC%=\n\t"
        "s_add_u32 s64, s81, 3\n\t"
        "s_and_b32 s64, s64, 28\n\t"
        "s_add_u32 s70, s80, s64\n\t"
        "s_add_u32 s66, s66, s61\n\t"
        "s_sub_u32 s61, s64, 4\n\t"
        "s_sub_u32 s66, s66, s61\n\t"
        "v_mov_b32 %[vt], s66\n\t"
        "s_setpc_b64 s[70:71]\n\t"
        ".p2align 5\n"
        ".LldL%=:\n\t"
        "s_waitcnt vmcnt(0)\n\t"
        "s_waitcnt vmcnt(4)\n\t"
        "s_waitcnt vmcnt(8)\n\t"
        "s_waitcnt vmcnt(12)\n\t"
        "s_waitcnt vmcnt(16)\n\t"
        "s_waitcnt vmcnt(20)\n\t"
        "s_waitcnt vmcnt(24)\n\t"
        "s_waitcnt vmcnt(28)\n\t"
        "ds_write_b32 %[vland], %[vt]\n"
        ".LldC%=:\n\t"
        "v_cmp_eq_u32 vcc, s60, %[vf]\n\t"
        "s_lshl_b32 s64, s68, 8\n\t"
        "s_memrealtime s[68:69]\n\t"
        "ds_read_b32 %[vf], %[vfree]\n\t"
        "s_sub_u32 s64, s64, s74\n\t"
        "s_cmp_gt_u32 s64, s86\n\t"
        "s_cbranch_scc1 .LldX%=\n"
        ".LldI%=:\n\t"
        "s_cbranch_vccz .LldR%=\n\t"
        "s_cmp_gt_u32 s81, %[win]\n\t"
        "s_cbranch_scc1 .LldR%=\n\t"
        "buffer_load_dwordx4 %[o0], s[76:79], s63 offen nt lds\n\t"
        "s_add_u32 s74, s74, %[step]\n\t"
        "s_add_u32 s73, s73, 1\n\t"
        "buffer_load_dwordx4 %[o1], s[76:79], s63 offen offset:1024 nt lds\n\t"
        "s_add_u32 s60, s60, 1\n\t"
        "s_add_u32 s61, s61, 4\n\t"
        "s_add_u32 s86, s64, %[late]\n\t"
        "buffer_load_dwordx4 %[o2], s[76:79], s63 offen offset:2048 nt lds\n\t"
        "s_add_u32 s72, s72, 0x2000\n\t"
        "s_and_b32 s72, s72, 0xe000\n\t"
        "buffer_load_dwordx4 %[o3], s[76:79], s63 offen offset:3072 nt lds\n"
        ".LldF%=:\n\t"
        "v_readlane_b32 s63, %[lc], s73\n\t"
        "s_add_u32 m0, s72, %[ring]\n\t"
        "s_cmp_lg_u32 s60, s67\n\t"
        "s_cbranch_scc1 .LldT%=\n\t"
        "s_cmp_eq_u32 s73, %[nt]\n\t"
        "s_cbranch_scc1 .LldV%=\n\t"
        "s_cmp_lg_u32 s60, %[u0]\n\t"
        "s_cbranch_scc1 .LldG%=\n\t"
        "s_cmp_le_u32 %[nt], %[u0]\n\t"
        "s_cbranch_scc1 .LldG%=\n"
        ".LldS%=:\n\t"
        "ds_read_b32 %[lc], %[vland] offset:%[srt]\n\t"
        "ds_read_b32 %[l0], %[a0]\n\t"
        "ds_read_b32 %[l1], %[a1]\n\t"
        "s_sub_u32 s62, s62, 1\n\t"
        "s_cbranch_scc1 .LldE%=\n\t"
        "s_waitcnt lgkmcnt(0)\n\t"
        "v_readfirstlane_b32 s64, %[lc]\n\t"
        "s_cmp_eq_u32 s64, 0\n\t"
        "s_cbranch_scc1 .LldS%=\n\t"
        "s_max_i32 s65, %[lrows], 0\n\t"
        "s_lshl_b32 s65, s65, 12\n\t"
        "s_add_u32 s64, s64, s65\n\t"
        "s_sub_u32 s64, s64, 1\n\t"
        "s_cmp_lt_i32 %[lrows], 16\n\t"
        "s_cselect_b32 s75, s64, %[nrec]\n\t"
        "v_mov_b32 %[lc], %[l0]\n"
        ".LldG%=:\n\t"
        "s_cmp_lg_u32 s73, 64\n\t"
        "s_cbranch_scc1 .LldN%=\n\t"
        "v_mov_b32 %[lc], %[l1]\n"
        ".LldN%=:\n\t"
        "s_cmp_eq_u32 s73, %[ntm]\n\t"
        "s_cselect_b32 s78, s75, %[nrec]\n\t"
        "s_cselect_b32 s64, %[nt], %[ntm]\n\t"
        "s_min_u32 s65, s64, 64\n\t"
        "s_cmp_lt_u32 s73, 64\n\t"
        "s_cselect_b32 s64, s65, s64\n\t"
        "s_sub_u32 s67, s60, s73\n\t"
        "s_add_u32 s67, s67, s64\n\t"
        "s_branch .LldF%=\n"
        ".LldV%=:\n\t"
        "s_cmp_lg_u32 s60, %[nt]\n\t"
        "s_cbranch_scc1 .LldD%=\n\t"
        "s_mov_b64 s[76:77], %[vp]\n\t"
        "s_and_b32 s77, s77, 0xffff\n\t"
        "s_mov_b32 s73, 0\n\t"
        "s_mov_b32 s74, s85\n\t"
        "ds_bpermute_b32 %[lc], %[vperm], %[l0]\n\t"
        "v_xor_b32 %[o1], 16, %[o1]\n\t"
        "v_xor_b32 %[o2], 32, %[o2]\n\t"
        "v_xor_b32 %[o3], 48, %[o3]\n\t"
        "s_waitcnt lgkmcnt(0)\n\t"
        "s_branch .LldN%=\n"
        ".LldH%=:\n\t"
        "s_cmp_eq_u32 s66, %[fin]\n\t"
        "s_cbranch_scc1 .LldE%=\n\t"
        "s_cmp_lt_i32 s60, %[hmin]\n\t"
        "s_cbranch_scc1 .LldI%=\n\t"
        "s_add_u32 s74, s74, %[hshift]\n\t"
        "s_add_u32 s85, s85, %[hshift]\n\t"
        "s_branch .LldR%=\n"
        ".LldD%=:\n\t"
        "s_sub_u32 s74, s74, 0x10000000\n\t"
        "s_mov_b32 s86, 0\n\t"
        "s_mov_b32 s60, -1\n\t"
        "s_branch .LldT%=\n"
        ".LldE%=:\n\t"
        "s_waitcnt vmcnt(0) lgkmcnt(0)\n\t"
        : [vf] "=&v"(vf), [vt] "=&v"(vt), [lc] "=&v"(lc), [l0] "+&v"(l0), [l1] "+&v"(l1), [o1] "+&v"(vo1), [o2] "+&v"(vo2), [o3] "+&v"(vo3)
        : [vperm] "v"(vperm), [a0] "v"(a0), [a1] "v"(a1), [o0] "v"(vo0), [vland] "v"(vland), [vfree] "v"(vfree),
          [kp] "s"(kp), [vp] "s"(vp), [nrec] "s"(kvbytes), [nt] "s"(NT), [ring] "s"(ring),
          [step] "s"(step), [due1] "s"(due0 + step + SOFT * 256), [duev] "s"(due0 + (to_sgpr(T16) >> 5) + (SOFT + GAP_TICKS) * 256), [lrows] "s"(lrows), [ntm] "s"(NT - 1), [fin] "s"(8 * NT), [u0] "n"(U0), [win] "s"(win), [late] "n"(LATE_TICKS * 256), [hshift] "n"(HALT_SHIFT * 256), [hmin] "s"(hmin), [srt] "n"(CTL_SORT - CTL_LAND)
        : "memory", "scc", "vcc", "s60", "s61", "s62", "s63", "s64", "s65", "s66", "s67", "s68", "s69", "s70", "s71", "s72", "s73", "s74", "s75",
          "s76", "s77", "s78", "s79", "s80", "s81", "s85", "s86");
    asm volatile("s_endpgm" ::: "memory");
  }

  int lane = lane0;
  asm volatile("" : "+v"(lane));
  if (wv == 7) {
#pragma unroll 1
    for (int i = 0; i < 2; ++i) {
      sort_pages(lane, i ? bp : b, i ? npl : nown, LDS_SRT + i * 2048, i ? LDS_PLIST : LDS_SRT + 4096);
      if (i == 0) {
        const unsigned lo = *(LAS unsigned*)(lds + LDS_SRT + 4096 + (nown - 1) * 4) + 1;
        LGKM0();
        ctl[CTL_SORT / 4 + (lane & 1)] = lo;
      }
    }
  }
  if (wv < 4) {
    VMWAIT(0);
    ctl[CTL_QOK / 4 + (wv & 1)] = 1;
  }
  const int tot = total();
  const int w = wv - 2;
  const int r32 = lane & 31, hi = lane >> 5;
  const int nmine = (NT > w) ? (NT - w + NC - 1) / NC : 0;
  const unsigned aland = lbase + LDS_CTL + CTL_LAND + (lane & 1) * 4;
  const int tots = __builtin_amdgcn_readfirstlane(tot);
  const bool pf_on = npl * 256 * 64 > tots * PF_X64 && npl > nown && (h & 3) == 0;
  const unsigned pf_last = 2 * NT - to_sgpr((int)((float)(2 * NT * (PF_LEAD + 23 * NC)) * 4096.0f * __builtin_amdgcn_rcpf((float)(tot * PACE_X16))));
  const unsigned pf_ratio = to_sgpr((int)((float)NTL * 65536.0f * __builtin_amdgcn_rcpf((float)NT)));
  const unsigned pf_per = PF_PER + (g & 7) * PF_PSTEP, pf_inv = (65536 + pf_per - 1) / pf_per;
  auto prefetch = [&](unsigned op) {
    const unsigned d = pf_last - op;
    if (d >= 1024u) return;
    const unsigned r = d - ((d * pf_inv) >> 16) * pf_per;
    if (r >= PF_RUN) return;
    unsigned tl = 2 * NTL - 1 - NC - (((d - r) * pf_ratio) >> 16) - r;
    if (tl >= 2u * NTL) return;
    const bool isv = tl >= (unsigned)NTL;
    tl -= isv ? NTL : 0;
    const __amdgpu_buffer_rsrc_t rp = __builtin_amdgcn_make_buffer_rsrc(isv ? (void*)vp : (void*)kp, 0, (int)kvbytes, 0x00020000);
    unsigned ln = lane;
    asm volatile("" : "+v"(ln));
    const unsigned vo = (*(LAS unsigned*)(lds + LDS_PLIST + min(2 * tl + (ln >> 5), (unsigned)npl - 1) * 4) & 0x7fff0000u) + (h + 1) * (HD * 2) +
                        ((ln >> 1) & 15) * 4096 + (ln & 1) * 128;
    asm volatile("s_mov_b32 m0, %0\n\ts_nop 4\n\tbuffer_load_dword %2, %1, 0 offen lds" :: "s"(lbase + LDS_DUMMY), "s"(rp), "v"(vo) : "memory");
  };
  const int pos = (w + NC * 22 - NT) % NC;
  const unsigned palp = LDS_WREG + pos * W_SIZE + W_ALP + r32 * 4;
  const unsigned ppst = LDS_WREG + pos * W_SIZE + lane * 16;
  asm volatile("" ::: "v128", "v255");
  unsigned resc = 0;
  float m = -3.0e38f, mprev = -3.0e38f, l = 0.f;

  {
    const unsigned fk = (((unsigned)r32 & 3) << 2) | (((unsigned)r32 >> 2) & 3);
    const unsigned kaddr0 = LDS_RING + r32 * 256 + (((unsigned)hi ^ fk) << 4);
    bf16x8 qf[8];
    wait_above<1>(aland + (CTL_QOK - CTL_LAND), 0);
#pragma unroll
    for (int s = 0; s < 8; ++s) qf[s] = *(LAS bf16x8*)(lds + ((kaddr0 + LDS_Q - LDS_RING) ^ (s << 5)));
#pragma unroll 1
    for (int k = 0; k < nmine; ++k) {
      const unsigned t = w + NC * k;
      wait_above<1>(aland, 4 * t);
      bf16x8 kf[8];
#pragma unroll
      for (int s = 0; s < 8; ++s) kf[s] = *(LAS bf16x8*)(lds + ((kaddr0 + ((t & 7) << 13)) ^ (s << 5)));
      asm volatile("s_waitcnt lgkmcnt(0)" : "+v"(kf[0]), "+v"(kf[1]), "+v"(kf[2]), "+v"(kf[3]), "+v"(kf[4]), "+v"(kf[5]), "+v"(kf[6]), "+v"(kf[7]) :: "memory");
      ctl[CTL_FREE / 4 + (t & 7)] = t + 8;
      if (pf_on) prefetch(t);
      f32x16 sc;
#pragma unroll
      for (int r = 0; r < 16; ++r) sc[r] = 0.f;
#pragma unroll
      for (int s = 0; s < 8; ++s) sc = __builtin_amdgcn_mfma_f32_32x32x16_bf16(kf[s], qf[s], sc, 0, 0, 0);
      if ((int)t == NT - 1) {
        const int kb = 2 * t * PAGE + 4 * hi;
#pragma unroll
        for (int r = 0; r < 16; ++r) sc[r] = (kb + 8 * (r >> 2) + (r & 3) < len) ? sc[r] : -__builtin_inff();
      }
      float mx = sc[0];
#pragma unroll
      for (int r = 1; r < 16; ++r) mx = fmaxf(mx, sc[r]);
      mx = swap_max(mx);
      const float mn = fmaxf(m, mx);
      const float alpha = __builtin_amdgcn_exp2f((m - mn) * CS);
      mprev = m;
      m = mn;
      float ps = 0.f;
#pragma unroll
      for (int r = 0; r < 16; ++r) { sc[r] = __builtin_amdgcn_exp2f((sc[r] - mn) * CS); ps += sc[r]; }
      l = fmaf(l, alpha, ps);
      u32x4 pf[2];
#pragma unroll
      for (int s = 0; s < 2; ++s)
        pf[s] = u32x4{pack2(sc[8 * s], sc[8 * s + 1]), pack2(sc[8 * s + 2], sc[8 * s + 3]), pack2(sc[8 * s + 4], sc[8 * s + 5]), pack2(sc[8 * s + 6], sc[8 * s + 7])};
      if (__builtin_amdgcn_ballot_w64(alpha != 1.0f)) resc |= 1u << k;
      *(LAS float*)(lds + palp + k * 128) = alpha;
      if (k >= KREG || k == nmine - 1) {
        const unsigned dst = ppst + ((k == nmine - 1) ? W_LAST : (k - KREG) * 2048);
        *(LAS u32x4*)(lds + dst) = pf[0];
        *(LAS u32x4*)(lds + dst + 1024) = pf[1];
      } else {
        asm volatile("s_set_gpr_idx_on %8, gpr_idx(DST)\n\t"
                     "v_mov_b32 v128, %0\n\tv_mov_b32 v129, %1\n\tv_mov_b32 v130, %2\n\tv_mov_b32 v131, %3\n\t"
                     "v_mov_b32 v132, %4\n\tv_mov_b32 v133, %5\n\tv_mov_b32 v134, %6\n\tv_mov_b32 v135, %7\n\t"
                     "s_set_gpr_idx_off"
                     :: "v"(pf[0][0]), "v"(pf[0][1]), "v"(pf[0][2]), "v"(pf[0][3]), "v"(pf[1][0]), "v"(pf[1][1]), "v"(pf[1][2]), "v"(pf[1][3]), "s"(8 * k));
      }
    }
  }
  l = swap_sum(l);
  *(LAS f32x2*)(lds + LDS_ML + w * 256 + r32 * 8) = f32x2{m, l};
  LGKM0();
  ctl[CTL_MLOK / 4 + w] = 1;

  const unsigned tq = (lane & 15) >> 2, tp = lane & 3, tblk = (lane >> 4) & 1;
  const unsigned vaddr0 = LDS_RING + (4 * hi + tq) * 256 + ((tq ^ (pos & 3)) << 6) + ((2 * tblk + (tp >> 1)) << 4) + ((tp & 1) << 3);
  f32x16 o[4];
#pragma unroll
  for (int i = 0; i < 4; ++i)
#pragma unroll
    for (int r = 0; r < 16; ++r) o[i][r] = 0.f;
  auto weights = [&]() {
    wait_above<1>(lbase + LDS_CTL + CTL_MLOK + min(lane, NC - 1) * 4, 0);
    unsigned a = LDS_ML + r32 * 8;
    asm volatile("" : "+v"(a) :: "memory");
    float M = -3.0e38f, L = 0.f;
    f32x2 ml[NC];
#pragma unroll
    for (int i = 0; i < NC; ++i) { ml[i] = *(LAS f32x2*)(lds + a + i * 256); M = fmaxf(M, ml[i][0]); }
#pragma unroll
    for (int i = 0; i < NC; ++i) L = fmaf(__builtin_amdgcn_exp2f((ml[i][0] - M) * CS), ml[i][1], L);
    const float inv = 1.0f / L;
    *(LAS float*)(lds + (a >> 1) + (LDS_WT - LDS_ML / 2) + pos * 128) = __builtin_amdgcn_exp2f((mprev - M) * CS) * inv;
    const float ct = __builtin_amdgcn_exp2f((m - M) * CS) * inv;
#pragma unroll
    for (int s = 0; s < 2; ++s) {
      u32x4 x = *(LAS u32x4*)(lds + ppst + W_LAST + s * 1024);
#pragma unroll
      for (int e = 0; e < 4; ++e) x[e] = pack2(__uint_as_float(x[e] << 16) * ct, __uint_as_float(x[e] & 0xffff0000u) * ct);
      *(LAS u32x4*)(lds + ppst + W_LAST + s * 1024) = x;
    }
    LGKM0();
    ctl[CTL_WTOK / 4 + w] = 1;
  };
  resc &= ~1u;
  if (nmine <= 3) weights();
#pragma unroll 1
  for (int k = 0; k < nmine - 1; ++k) {
    if (k == 2) weights();
    if ((resc >> k) & 1) {
      const float alpha = *(LAS float*)(lds + palp + k * 128);
#pragma unroll
      for (int i = 0; i < 4; ++i)
#pragma unroll
        for (int r = 0; r < 16; ++r) o[i][r] *= alpha;
    }
    const unsigned u = NT + ((k == 0 && NT >= 3 * NC) ? pos : w + NC * k);
    u32x4 pf[2];
    if (k >= KREG) {
      pf[0] = *(LAS u32x4*)(lds + ppst + (k - KREG) * 2048);
      pf[1] = *(LAS u32x4*)(lds + ppst + (k - KREG) * 2048 + 1024);
    } else {
      asm volatile("s_set_gpr_idx_on %8, gpr_idx(SRC0)\n\t"
                   "v_mov_b32 %0, v128\n\tv_mov_b32 %1, v129\n\tv_mov_b32 %2, v130\n\tv_mov_b32 %3, v131\n\t"
                   "v_mov_b32 %4, v132\n\tv_mov_b32 %5, v133\n\tv_mov_b32 %6, v134\n\tv_mov_b32 %7, v135\n\t"
                   "s_set_gpr_idx_off\n\ts_nop 1"
                   : "=v"(pf[0][0]), "=v"(pf[0][1]), "=v"(pf[0][2]), "=v"(pf[0][3]), "=v"(pf[1][0]), "=v"(pf[1][1]), "=v"(pf[1][2]), "=v"(pf[1][3])
                   : "s"(8 * k));
    }
    asm volatile("" : "+v"(pf[0]), "+v"(pf[1]), "+v"(o[0]), "+v"(o[1]), "+v"(o[2]), "+v"(o[3]));
    if (k == nmine - 2) wait_above<1>(lbase + LDS_CTL + CTL_WTOK + min(lane, NC - 1) * 4, 0);
    wait_above<1>(aland, 4 * u);
    bf16x8 vf[4][2];
#pragma unroll
    for (int i = 0; i < 4; ++i) {
      const unsigned a = (vaddr0 + ((u & 7) << 13)) ^ (i << 6);
#pragma unroll
      for (int s = 0; s < 2; ++s) {
        const s16x4 lo = __builtin_amdgcn_ds_read_tr16_b64_v4i16((LAS s16x4*)(lds + a + 4096 * s));
        const s16x4 up = __builtin_amdgcn_ds_read_tr16_b64_v4i16((LAS s16x4*)(lds + a + 4096 * s + 2048));
        vf[i][s] = __builtin_bit_cast(bf16x8, __builtin_shufflevector(lo, up, 0, 1, 2, 3, 4, 5, 6, 7));
      }
    }
    asm volatile("s_waitcnt lgkmcnt(0)" : "+v"(vf[0][0]), "+v"(vf[0][1]), "+v"(vf[1][0]), "+v"(vf[1][1]), "+v"(vf[2][0]), "+v"(vf[2][1]), "+v"(vf[3][0]), "+v"(vf[3][1]) :: "memory");
    ctl[CTL_FREE / 4 + (u & 7)] = u + 8;
    if (pf_on) prefetch(u);
#pragma unroll
    for (int s = 0; s < 2; ++s)
#pragma unroll
      for (int i = 0; i < 4; ++i) o[i] = __builtin_amdgcn_mfma_f32_32x32x16_bf16(vf[i][s], __builtin_bit_cast(bf16x8, pf[s]), o[i], 0, 0, 0);
  }

  {
    unsigned l16 = lane * 16;
    asm volatile("" : "+v"(l16));
    const unsigned l4 = l16 >> 2, awt = LDS_WT + (l4 & 127);
    auto put = [&](unsigned dst, const f32x16& x) {
#pragma unroll
      for (int j = 0; j < 4; ++j) *(LAS f32x4*)(lds + dst + j * 1024) = f32x4{x[4 * j], x[4 * j + 1], x[4 * j + 2], x[4 * j + 3]};
    };
    float co[6];
    bf16x8 pl[NC][2];
    auto ld_p = [&](int p) {
      pl[p][0] = *(LAS bf16x8*)(lds + LDS_WREG + p * W_SIZE + W_LAST + l16);
      pl[p][1] = *(LAS bf16x8*)(lds + LDS_WREG + p * W_SIZE + W_LAST + l16 + 1024);
    };
#pragma unroll
    for (int i = 0; i < 4; ++i) co[i] = *(LAS float*)(lds + awt + ((pos & 3) ^ i) * 128);
    co[4] = *(LAS float*)(lds + awt + 4 * 128);
    co[5] = *(LAS float*)(lds + awt + 5 * 128);
    ld_p(0); ld_p(1);
    asm volatile("" : "+v"(l16) :: "memory");
    if (nmine > 1) {
      if (pos > 3) put(LDS_RING + ((2 * NT - 2 * NC + pos) & 7) * 8192 + l16, o[0]);
#pragma unroll
      for (int i = 1; i < 4; ++i) put(LDS_WREG + pos * W_SIZE + (i - 1) * 4096 + l16, o[i]);
      LGKM0();
      ctl[CTL_ODONE / 4 + pos] = 1;
    }
    if (pos < 4) {
      f32x16 of = o[0], ot;
#pragma unroll
      for (int r = 0; r < 16; ++r) ot[r] = 0.f;
      auto src_o = [&](int p) -> unsigned {
        const int i = pos ^ (p & 3);
        return l16 + (i == 0 ? LDS_RING + ((2 * NT - 2 * NC + p) & 7) * 8192 : LDS_WREG + p * W_SIZE + (i - 1) * 4096);
      };
      auto fma_o = [&](unsigned src, float c) {
#pragma unroll
        for (int j = 0; j < 4; ++j) {
          const f32x4 x = *(LAS f32x4*)(lds + src + j * 1024);
#pragma unroll
          for (int e = 0; e < 4; ++e) of[4 * j + e] = fmaf(x[e], c, of[4 * j + e]);
        }
      };
      auto mma_t = [&](int p, bf16x8 p0, bf16x8 p1) {
        const unsigned a = vaddr0 + (((2 * NT - NC + p) & 7) << 13);
        bf16x8 vf[2];
#pragma unroll
        for (int s = 0; s < 2; ++s) {
          const s16x4 lo = __builtin_amdgcn_ds_read_tr16_b64_v4i16((LAS s16x4*)(lds + a + 4096 * s));
          const s16x4 up = __builtin_amdgcn_ds_read_tr16_b64_v4i16((LAS s16x4*)(lds + a + 4096 * s + 2048));
          vf[s] = __builtin_bit_cast(bf16x8, __builtin_shufflevector(lo, up, 0, 1, 2, 3, 4, 5, 6, 7));
        }
        ot = __builtin_amdgcn_mfma_f32_32x32x16_bf16(vf[0], p0, ot, 0, 0, 0);
        ot = __builtin_amdgcn_mfma_f32_32x32x16_bf16(vf[1], p1, ot, 0, 0, 0);
      };
      if (NT >= 2 * NC) {
#define PIN_OF() asm volatile("" : "+v"(of), "+v"(ot))
#pragma unroll
        for (int r = 0; r < 16; ++r) of[r] *= co[0];
        const bool wl = (l4 & 24) == 0;
        const unsigned ua = 4 * (2 * NT - NC);
        wait_above_v(wl ? aland : lbase + LDS_CTL + CTL_ODONE + (l4 & 12), wl ? ua : 0u);
        mma_t(0, pl[0][0], pl[0][1]);
#pragma unroll
        for (int i = 1; i < 4; ++i) fma_o(LDS_WREG + (pos ^ i) * W_SIZE + (i - 1) * 4096 + l16, co[i]);
        PIN_OF();
        ld_p(2); ld_p(3); ld_p(4); ld_p(5);
        wait_above_v(wl ? aland : lbase + LDS_CTL + CTL_ODONE + 16 + (l4 & 4), wl ? ua + 8 : 0u);
        mma_t(1, pl[1][0], pl[1][1]);
        mma_t(2, pl[2][0], pl[2][1]);
        fma_o(src_o(4), co[4]);
        fma_o(src_o(5), co[5]);
#pragma unroll
        for (int r = 0; r < 16; ++r) { ot[r] += of[r]; of[r] = 0.f; }
        asm volatile("" : "+v"(ot));
        wait_above<0>(aland, ua + 20);
        mma_t(3, pl[3][0], pl[3][1]);
        mma_t(4, pl[4][0], pl[4][1]);
        mma_t(5, pl[5][0], pl[5][1]);
        of = ot;
      } else {
        wait_above<1>(lbase + LDS_CTL + CTL_WTOK + min(l4, (NC - 1) * 4u), 0);
        const float cw = *(LAS float*)(lds + awt + pos * 128);
#pragma unroll
        for (int r = 0; r < 16; ++r) of[r] *= cw;
#pragma unroll 1
        for (int p = max(NC - NT, 0); p < NC; ++p) {
          if (p != pos && NT - 2 * NC + p >= 0) {
            wait_above<1>(lbase + LDS_CTL + CTL_ODONE + p * 4, 0);
            fma_o(src_o(p), *(LAS float*)(lds + awt + p * 128));
          }
          wait_above<1>(aland, 4 * (2 * NT - NC + p));
          mma_t(p, *(LAS bf16x8*)(lds + LDS_WREG + p * W_SIZE + W_LAST + l16), *(LAS bf16x8*)(lds + LDS_WREG + p * W_SIZE + W_LAST + l16 + 1024));
        }
#pragma unroll
        for (int r = 0; r < 16; ++r) of[r] += ot[r];
      }
      const __amdgpu_buffer_rsrc_t ro = __builtin_amdgcn_make_buffer_rsrc(outp, 0, NB * HQ * SQ * HD * 2, 0x00020000);
      const unsigned orow = (unsigned)(b * HQ + 2 * h) * SQ * HD * 2 + pos * 64 + ((l16 & 511) << 4) + ((l16 >> 5) & 16);
#pragma unroll
      for (int j = 0; j < 2; ++j) {
        const unsigned a0 = pack2(of[8 * j], of[8 * j + 1]), a1 = pack2(of[8 * j + 2], of[8 * j + 3]);
        const unsigned b0 = pack2(of[8 * j + 4], of[8 * j + 5]), b1 = pack2(of[8 * j + 6], of[8 * j + 7]);
        auto s0 = __builtin_amdgcn_permlane32_swap(a0, b0, false, false);
        auto s1 = __builtin_amdgcn_permlane32_swap(a1, b1, false, false);
        u32x4 val = {s0[0], s1[0], s0[1], s1[1]};
        __builtin_amdgcn_raw_buffer_store_b128(val, ro, orow + j * 32, 0, 0);
      }
    }
  }
}
}

extern "C" void kernel_launch(void* const* d_in, const int* in_sizes, int n_in, void* d_out, int out_size, void* d_ws,
                              size_t ws_size, hipStream_t stream) {
  (void)n_in; (void)out_size; (void)d_ws; (void)ws_size;
  const unsigned nphys = (unsigned)(in_sizes[1] / (PAGE * HKV * HD));
  const unsigned kvbytes = nphys * PAGE_BYTES;
  (void)hipFuncSetAttribute((const void*)paged_attn, hipFuncAttributeMaxDynamicSharedMemorySize, LDS_TOTAL);
  hipLaunchKernelGGL(paged_attn, dim3(256), dim3(512), LDS_TOTAL, stream, d_in[0], d_in[1], d_in[2], (const int*)d_in[3],
                     (const int*)d_in[4], d_out, kvbytes, nphys);
}
```
